```python
import torch

def _softmax(x, axis=-1):
    return torch.softmax(x, dim=axis)

def _gelu(x):
    return torch.nn.functional.gelu(x, approximate='tanh')

def _silu(x):
    return torch.nn.functional.silu(x)

def _sigmoid(x):
    return torch.sigmoid(x)

def _layernorm(x, axis=-1, eps=1e-05):
    m = x.mean(dim=axis, keepdim=True)
    v = x.var(dim=axis, keepdim=True, unbiased=False)
    return (x - m) / torch.sqrt(v + eps)

def _scatter_add(tgt, idx, src, axis=0):
    return tgt.clone().index_add_(axis, idx, src)

def _segment_sum(src, seg, n):
    out = torch.zeros((n,) + src.shape[1:], dtype=src.dtype)
    return out.index_add_(0, seg, src)

def _pad(x, pw):
    flat = tuple((v for pair in reversed(pw) for v in pair))
    return torch.nn.functional.pad(x, flat)
kernel_name = 'sequential_preLN__memory_aug_recurrent_mem_tr__mixer_s_mlp__prompt'

def setup_inputs():

    _w = lambda a: (lambda t: t.to(torch.bfloat16) if t.is_floating_point() else t)(torch.as_tensor(a))

    def _A_setup():
        B, S, Dm = (1, 2048, 1024)
        H, Dh = (32, 128)
        M, seg = (8, 32)
        seg = min(seg, S)
        S = S // seg * seg
        return {'x': _w((lambda *s: torch.randn(*s, dtype=torch.bfloat16) * 0.05)(B, S, Dm)), 'mem0': _w((lambda *s: torch.randn(*s, dtype=torch.bfloat16) * 0.2)(B, M, Dm)), 'Wq': _w((lambda *s: torch.randn(*s, dtype=torch.bfloat16) * 1.6)(Dm, H, Dh) * Dm ** (-0.5)), 'Wk': _w((lambda *s: torch.randn(*s, dtype=torch.bfloat16) * 1.6)(Dm, H, Dh) * Dm ** (-0.5)), 'Wv': _w((lambda *s: torch.randn(*s, dtype=torch.bfloat16) * 0.2)(Dm, H, Dh) * Dm ** (-0.5)), 'Wo': _w((lambda *s: torch.randn(*s, dtype=torch.bfloat16) * 0.2)(H, Dh, Dm) * (H * Dh) ** (-0.5)), 'seg_len': seg}
    _A_in = _A_setup()

    def _B_setup():
        B, S, D, F = (1, 2048, 1024, 3584)
        return {'x': _w((lambda *s: torch.randn(*s, dtype=torch.bfloat16) * 0.05)(B, S, D)), 'w1': _w((lambda *s: torch.randn(*s, dtype=torch.bfloat16) * 0.8)(D, D) * D ** (-0.5)), 'w2': _w((lambda *s: torch.randn(*s, dtype=torch.bfloat16) * 0.8)(D, D) * D ** (-0.5)), 'w3': _w((lambda *s: torch.randn(*s, dtype=torch.bfloat16) * 0.8)(D, F) * D ** (-0.5)), 'w4': _w((lambda *s: torch.randn(*s, dtype=torch.bfloat16) * 0.8)(F, D) * F ** (-0.5))}
    _B_in = _B_setup()
    return {'A_x': _A_in['x'], 'A_mem0': _A_in['mem0'], 'A_Wq': _A_in['Wq'], 'A_Wk': _A_in['Wk'], 'A_Wv': _A_in['Wv'], 'A_Wo': _A_in['Wo'], 'A_seg_len': _A_in['seg_len'], 'B_x': _B_in['x'], 'B_w1': _B_in['w1'], 'B_w2': _B_in['w2'], 'B_w3': _B_in['w3'], 'B_w4': _B_in['w4']}

def reference(A_x, A_mem0, A_Wq, A_Wk, A_Wv, A_Wo, A_seg_len, B_x, B_w1, B_w2, B_w3, B_w4):

    def _A_ref(x, mem0, Wq, Wk, Wv, Wo, seg_len):
        B, S, Dm = x.shape
        H, Dh = (Wq.shape[1], Wq.shape[2])
        M = mem0.shape[1]
        scale = Dh ** (-0.5)
        n_seg = S // seg_len
        L = M + seg_len
        idx = torch.arange(L)
        mask = (idx[:, None] < M) | (idx[:, None] >= idx[None, :])
        mem = mem0
        outs = []
        for s in range(n_seg):
            xs = x[:, s * seg_len:(s + 1) * seg_len, :].clone()
            h = torch.concatenate([mem, xs], axis=1)
            q = torch.einsum('btd,dhe->bhte', h, Wq)
            k = torch.einsum('btd,dhe->bhte', h, Wk)
            v = torch.einsum('btd,dhe->bhte', h, Wv)
            scores = torch.einsum('bhqe,bhke->bhqk', q, k) * scale
            scores = torch.where(mask[None, None], scores, -1000000000.0)
            p = _softmax(scores, axis=-1)
            o = torch.einsum('bhqk,bhke->bhqe', p, v)
            o = torch.einsum('bhte,hed->btd', o, Wo)
            mem = o[:, :M, :].clone()
            outs.append(o[:, M:, :])
        return torch.concatenate(outs, axis=1)

    def _B_ref(x, w1, w2, w3, w4):
        B, S, D = x.shape
        h = _gelu(_layernorm(x, axis=-1) @ w1)
        c = D // 4
        g0 = _pad(h[:, :, 0 * c:1 * c], ((0, 0), (1, 0), (0, 0)))[:, :S, :]
        g1 = _pad(h[:, :, 1 * c:2 * c], ((0, 0), (0, 1), (0, 0)))[:, 1:, :]
        g2 = _pad(h[:, :, 2 * c:3 * c], ((0, 0), (2, 0), (0, 0)))[:, :S, :]
        g3 = _pad(h[:, :, 3 * c:], ((0, 0), (0, 2), (0, 0)))[:, 2:, :]
        hs = torch.concatenate([g0, g1, g2, g3], axis=-1) @ w2
        x1 = x + hs
        h2 = _gelu(_layernorm(x1, axis=-1) @ w3) @ w4
        return x1 + h2
    x = A_x
    h = x + _A_ref(_layernorm(x, axis=-1), A_mem0, A_Wq, A_Wk, A_Wv, A_Wo, A_seg_len)
    h2 = h + _B_ref(_layernorm(h, axis=-1), B_w1, B_w2, B_w3, B_w4)
    return h2
```

```cpp
#include <hip/hip_runtime.h>
#include <hip/hip_bf16.h>
typedef unsigned short u16;
namespace nv {
constexpr int S = 2048, D = 1024, H = 32, E = 128, M = 8, SEG = 32, F = 3584, NSEG = S / SEG, HE = H * E;
__device__ __forceinline__ float b2f(u16 v) { return __uint_as_float((unsigned)v << 16); }
__device__ __forceinline__ u16 f2b(float f) { unsigned u = __float_as_uint(f); return (u16)((u + 0x7FFF + ((u >> 16) & 1)) >> 16); }
__device__ __forceinline__ float rb(float f) { return b2f(f2b(f)); }
#define MIMIC 1
__global__ __launch_bounds__(256) void k_ln(const u16* x, u16* y) {
  __shared__ float red[256];
  const int r = blockIdx.x, t = threadIdx.x;
  float v[4]; float s = 0;
  for (int i = 0; i < 4; ++i) { v[i] = b2f(x[r * D + t + 256 * i]); s += v[i]; }
  red[t] = s; __syncthreads();
  for (int o = 128; o > 0; o >>= 1) { if (t < o) red[t] += red[t + o]; __syncthreads(); }
  float mean = red[0] / D; __syncthreads();
  s = 0; for (int i = 0; i < 4; ++i) s += (v[i] - mean) * (v[i] - mean);
  red[t] = s; __syncthreads();
  for (int o = 128; o > 0; o >>= 1) { if (t < o) red[t] += red[t + o]; __syncthreads(); }
  float var = red[0] / D;
  float m = rb(mean), sd = rb(sqrtf(rb(rb(var) + 1e-5f)));
  for (int i = 0; i < 4; ++i) y[r * D + t + 256 * i] = f2b(rb(v[i] - m) / sd);
}
__device__ __forceinline__ float gelu(float x) { return 0.5f * x * (1.f + tanhf(0.7978845608028654f * (x + 0.044715f * x * x * x))); }
__global__ __launch_bounds__(256) void k_gemm(const u16* A, int lda, const u16* B, int ldb, u16* C, int ldc, int Mr, int N, int K, int mode, const u16* res, const u16* res2, int shift) {
  __shared__ float As[32][65], Bs[32][65];
  const int tx = threadIdx.x & 15, ty = threadIdx.x >> 4;
  const int m0 = blockIdx.y * 64, n0 = blockIdx.x * 64;
  float acc[4][4] = {};
  for (int k0 = 0; k0 < K; k0 += 32) {
    for (int i = threadIdx.x; i < 64 * 32; i += 256) {
      int r = i >> 5, k = i & 31; int row = m0 + r; float v = 0;
      if (row < Mr) {
        if (shift) { int g = (k0 + k) >> 8; int sr = row + (g == 0 ? -1 : g == 1 ? 1 : g == 2 ? -2 : 2); if (sr >= 0 && sr < S) v = b2f(A[(size_t)sr * lda + k0 + k]); }
        else v = b2f(A[(size_t)row * lda + k0 + k]);
      }
      As[k][r] = v;
      int kk = i >> 6, c = i & 63; Bs[kk][c] = b2f(B[(size_t)(k0 + kk) * ldb + n0 + c]);
    }
    __syncthreads();
    for (int k = 0; k < 32; ++k) {
      float a[4], b[4];
      for (int i = 0; i < 4; ++i) { a[i] = As[k][ty * 4 + i]; b[i] = Bs[k][tx * 4 + i]; }
      for (int i = 0; i < 4; ++i) for (int j = 0; j < 4; ++j) acc[i][j] += a[i] * b[j];
    }
    __syncthreads();
  }
  for (int i = 0; i < 4; ++i) for (int j = 0; j < 4; ++j) {
    int row = m0 + ty * 4 + i, col = n0 + tx * 4 + j; if (row >= Mr) continue;
    float v = acc[i][j];
    if (mode == 1) v = gelu(rb(v));
    else if (mode == 2) v = b2f(res[(size_t)row * ldc + col]) + rb(v);
    else if (mode == 3) v = b2f(res2[(size_t)row * ldc + col]) + rb(b2f(res[(size_t)row * ldc + col]) + rb(v));
    C[(size_t)row * ldc + col] = f2b(v);
  }
}
__global__ __launch_bounds__(64) void k_attn(const u16* qm, const u16* km, const u16* vm, const u16* qx, const u16* kx, const u16* vx, u16* om, u16* ox, int s) {
  const int h = blockIdx.x, r = blockIdx.y, l = threadIdx.x;
  const u16* q = r < M ? qm + r * HE + h * E : qx + (size_t)(s * SEG + r - M) * HE + h * E;
  float sc = -1e9f;
  if (l < 40) {
    const u16* k = l < M ? km + l * HE + h * E : kx + (size_t)(s * SEG + l - M) * HE + h * E;
    float a = 0; for (int e = 0; e < E; ++e) a += b2f(q[e]) * b2f(k[e]);
    a = rb(rb(a) * 0.08838834764831845f);
    if (r < M || r >= l) sc = a;
  } else sc = -3e38f;
  float mx = sc; for (int o = 32; o > 0; o >>= 1) mx = fmaxf(mx, __shfl_xor(mx, o));
  float p = l < 40 ? __expf(sc - mx) : 0.f; float sum = p; for (int o = 32; o > 0; o >>= 1) sum += __shfl_xor(sum, o);
  p = rb(p / sum);
  float a0 = 0, a1 = 0;
  for (int j = 0; j < 40; ++j) {
    float pj = __shfl(p, j);
    const u16* v = j < M ? vm + j * HE + h * E : vx + (size_t)(s * SEG + j - M) * HE + h * E;
    a0 += pj * b2f(v[l]); a1 += pj * b2f(v[l + 64]);
  }
  u16* o = r < M ? om + r * HE + h * E : ox + (size_t)(s * SEG + r - M) * HE + h * E;
  o[l] = f2b(a0); o[l + 64] = f2b(a1);
}
struct Bufs { u16 *xn, *qx, *kx, *vx, *ox, *mem, *qm, *km, *vm, *om, *h, *hn, *t, *g, *x1, *t2, *u; };
inline Bufs carve(void* ws) {
  u16* p = (u16*)ws; Bufs b;
  b.xn = p; p += S * D; b.qx = p; p += (size_t)S * HE; b.kx = p; p += (size_t)S * HE; b.vx = p; p += (size_t)S * HE; b.ox = p; p += (size_t)S * HE;
  b.mem = p; p += (NSEG + 1) * M * D; b.qm = p; p += NSEG * M * HE; b.km = p; p += NSEG * M * HE; b.vm = p; p += NSEG * M * HE; b.om = p; p += NSEG * M * HE;
  b.h = p; p += S * D; b.hn = p; p += S * D; b.t = p; p += S * D; b.g = p; p += S * D; b.x1 = p; p += S * D; b.t2 = p; p += S * D; b.u = p; p += (size_t)S * F;
  return b;
}
inline void run(void* const* d_in, void* d_out, void* d_ws, hipStream_t st) {
  const u16 *x = (const u16*)d_in[0], *mem0 = (const u16*)d_in[1], *Wq = (const u16*)d_in[2], *Wk = (const u16*)d_in[3], *Wv = (const u16*)d_in[4], *Wo = (const u16*)d_in[5];
  const u16 *w1 = (const u16*)d_in[8], *w2 = (const u16*)d_in[9], *w3 = (const u16*)d_in[10], *w4 = (const u16*)d_in[11];
  Bufs b = carve(d_ws);
  k_ln<<<S, 256, 0, st>>>(x, b.xn);
  k_gemm<<<dim3(HE / 64, S / 64), 256, 0, st>>>(b.xn, D, Wq, HE, b.qx, HE, S, HE, D, 0, nullptr, nullptr, 0);
  k_gemm<<<dim3(HE / 64, S / 64), 256, 0, st>>>(b.xn, D, Wk, HE, b.kx, HE, S, HE, D, 0, nullptr, nullptr, 0);
  k_gemm<<<dim3(HE / 64, S / 64), 256, 0, st>>>(b.xn, D, Wv, HE, b.vx, HE, S, HE, D, 0, nullptr, nullptr, 0);
  for (int s = 0; s < NSEG; ++s) {
    const u16* mem = s == 0 ? mem0 : b.mem + s * M * D;
    k_gemm<<<dim3(HE / 64, 1), 256, 0, st>>>(mem, D, Wq, HE, b.qm + s * M * HE, HE, M, HE, D, 0, nullptr, nullptr, 0);
    k_gemm<<<dim3(HE / 64, 1), 256, 0, st>>>(mem, D, Wk, HE, b.km + s * M * HE, HE, M, HE, D, 0, nullptr, nullptr, 0);
    k_gemm<<<dim3(HE / 64, 1), 256, 0, st>>>(mem, D, Wv, HE, b.vm + s * M * HE, HE, M, HE, D, 0, nullptr, nullptr, 0);
    k_attn<<<dim3(H, 40), 64, 0, st>>>(b.qm + s * M * HE, b.km + s * M * HE, b.vm + s * M * HE, b.qx, b.kx, b.vx, b.om + s * M * HE, b.ox, s);
    k_gemm<<<dim3(D / 64, 1), 256, 0, st>>>(b.om + s * M * HE, HE, Wo, D, b.mem + (s + 1) * M * D, D, M, D, HE, 0, nullptr, nullptr, 0);
  }
  k_gemm<<<dim3(D / 64, S / 64), 256, 0, st>>>(b.ox, HE, Wo, D, b.h, D, S, D, HE, 2, x, nullptr, 0);
  k_ln<<<S, 256, 0, st>>>(b.h, b.hn);
  k_ln<<<S, 256, 0, st>>>(b.hn, b.t);
  k_gemm<<<dim3(D / 64, S / 64), 256, 0, st>>>(b.t, D, w1, D, b.g, D, S, D, D, 1, nullptr, nullptr, 0);
  k_gemm<<<dim3(D / 64, S / 64), 256, 0, st>>>(b.g, D, w2, D, b.x1, D, S, D, D, 2, b.hn, nullptr, 1);
  k_ln<<<S, 256, 0, st>>>(b.x1, b.t2);
  k_gemm<<<dim3(F / 64, S / 64), 256, 0, st>>>(b.t2, D, w3, F, b.u, F, S, F, D, 1, nullptr, nullptr, 0);
  k_gemm<<<dim3(D / 64, S / 64), 256, 0, st>>>(b.u, F, w4, D, (u16*)d_out, D, S, D, F, 3, b.x1, b.h, 0);
}
}
extern "C" void kernel_launch(void* const* d_in, const int* in_sizes, int n_in, void* d_out, int out_size, void* d_ws, size_t ws_size, hipStream_t stream) {
  nv::run(d_in, d_out, d_ws, stream);
}
```

```cpp
#include <hip/hip_runtime.h>
typedef unsigned short u16;
typedef unsigned u2 __attribute__((ext_vector_type(2)));
typedef unsigned u4 __attribute__((ext_vector_type(4)));
typedef float f2 __attribute__((ext_vector_type(2)));
typedef float f4 __attribute__((ext_vector_type(4)));
typedef __bf16 bf8 __attribute__((ext_vector_type(8)));
typedef __amdgpu_buffer_rsrc_t rsrc_t;
#define LAS __attribute__((address_space(3)))
#define DI __device__ __forceinline__

constexpr unsigned SENT = 0xFFFFFFFFu;
constexpr int OOB = 0x40000000;
constexpr int SLOT = 512 << 10, BLK = 16 << 10, MB0 = 2 << 20, M_FIRST = MB0 - 4 * BLK, B_OM = 4, B_VM = 12, B_DB = 20, B_END = 21, WS_KM = 6 << 20, WS_VM = 10 << 20;
constexpr int FL0 = MB0 + 24 * BLK, FK = FL0, FV = FL0 + 4096, FQA = FL0 + 20480, FP1 = FL0 + 12288, FKM = FL0 + 16384, FH = FL0 + 24576, FG = FL0 + 28672;
constexpr int FX1 = FL0 + 40960, FU = FL0 + 45056, FP2 = FL0 + 57344, FL_SIZE = 65536, HS_R = 4096, HS_D = 6144;
DI int fa(int i) { return (i >> 2) * 128 + (i & 3) * 4; }
constexpr int WS_XN = 17 << 20, WS_KX = 21 << 20, WS_VX = 37 << 20, WS_OX = 53 << 20, WS_H = 70 << 20, WS_X1 = 75 << 20, WS_S1 = 80 << 20, WS_G = 85 << 20;
constexpr int WS_Y = 90 << 20, WS_U = 95 << 20, WS_S2 = 110 << 20, WS_P1 = 111 << 20, WS_P2 = 127 << 20, WS_DBG = 144 << 20, WS_END = 148 << 20;

struct Params {
  const u16 *x, *mem0, *Wq, *Wk, *Wv, *Wo, *w1, *w2, *w3, *w4;
  u16* out;
  unsigned char* ws;
};
DI rsrc_t mkrs(const void* p, int bytes) { return __builtin_amdgcn_make_buffer_rsrc((void*)p, 0, bytes, 0x00020000); }
DI int tok_of(int c) { return (c & 4) + ((c >> 3) & 1) * 2 + (c & 1); }
DI u4 ld16(rsrc_t r, int vo, int so) { return __builtin_amdgcn_raw_buffer_load_b128(r, vo, so, 16); }
DI u4 ld16p(rsrc_t r, int vo, int so) { return __builtin_amdgcn_raw_buffer_load_b128(r, vo, so, 0); }
DI unsigned ld4(rsrc_t r, int vo, int so) { return __builtin_amdgcn_raw_buffer_load_b32(r, vo, so, 16); }
DI void st16(rsrc_t r, int vo, int so, u4 v) { __builtin_amdgcn_raw_buffer_store_b128(v, r, vo + so, 0, 16); }
DI void st4(rsrc_t r, int vo, int so, unsigned v) { __builtin_amdgcn_raw_buffer_store_b32(v, r, vo, so, 16); }
DI int lane_id() { int l; asm volatile("v_mbcnt_lo_u32_b32 %0, -1, 0\n\tv_mbcnt_hi_u32_b32 %0, -1, %0" : "=v"(l)); return l & 63; }
DI void fence_c() { asm volatile("" ::: "memory"); }
DI void lds_barrier() { asm volatile("s_waitcnt lgkmcnt(0)\n\ts_barrier" ::: "memory"); }
DI void drain() { asm volatile("s_waitcnt vmcnt(0)" ::: "memory"); }
DI unsigned cvtpk(float lo, float hi) {
  typedef __bf16 b2 __attribute__((ext_vector_type(2)));
  f2 v = {lo, hi};
  return __builtin_bit_cast(unsigned, __builtin_convertvector(v, b2));
}
DI float f_(unsigned v) { return __uint_as_float(v); }
DI unsigned u_(float v) { return __float_as_uint(v); }
DI float blo(unsigned v) { return f_(v << 16); }
DI float bhi(unsigned v) { return f_(v & 0xFFFF0000u); }
DI float sum8(float v) {
  v += f_(__builtin_amdgcn_update_dpp(0, u_(v), 0x128, 0xf, 0xf, true));
  auto a = __builtin_amdgcn_permlane16_swap(u_(v), u_(v), false, false); v = f_(a[0]) + f_(a[1]);
  auto b = __builtin_amdgcn_permlane32_swap(u_(v), u_(v), false, false); return f_(b[0]) + f_(b[1]);
}
DI void row_stats(const float (&h)[32], LAS f2* st, int lane, int w, float& mean, float& var) {
  float s = 0;
#pragma unroll
  for (int i = 0; i < 32; ++i) s += h[i];
  const float m = sum8(s) * (1.f / 256);
  float q = 0;
#pragma unroll
  for (int i = 0; i < 32; ++i) q += (h[i] - m) * (h[i] - m);
  q = sum8(q);
  if (lane < 8) st[w * 8 + lane] = f2{m, q};
  lds_barrier();
  const f2 a = st[lane & 7], b = st[8 + (lane & 7)], c = st[16 + (lane & 7)], d = st[24 + (lane & 7)];
  mean = (a[0] + b[0] + c[0] + d[0]) * 0.25f;
  var = (a[1] + b[1] + c[1] + d[1]) * (1.f / 1024) + 0.25f * ((a[0] - mean) * (a[0] - mean) + (b[0] - mean) * (b[0] - mean) + (c[0] - mean) * (c[0] - mean) + (d[0] - mean) * (d[0] - mean));
}
#define DBS 16
#define DKV 1300
#define VFLIP 0
#define QK_ASM() asm volatile( \
"s_nop 4\n" \
"s_add_u32 m0, %[wbase], 16384\n" \
"s_add_u32 s66, %[so0], 524288\n" \
"buffer_load_dwordx4 %[wvo], %[rk], s66 offen lds\n" \
"s_add_u32 m0, %[wbase], 17408\n" \
"s_add_u32 s66, %[so0], 786432\n" \
"buffer_load_dwordx4 %[wvo], %[rk], s66 offen lds\n" \
"s_add_u32 m0, %[wbase], 18432\n" \
"s_add_u32 s66, %[so0], 540672\n" \
"buffer_load_dwordx4 %[wvo], %[rk], s66 offen lds\n" \
"s_add_u32 m0, %[wbase], 19456\n" \
"s_add_u32 s66, %[so0], 802816\n" \
"buffer_load_dwordx4 %[wvo], %[rk], s66 offen lds\n" \
"s_add_u32 m0, %[wbase], 20480\n" \
"s_add_u32 s66, %[so0], 524288\n" \
"buffer_load_dwordx4 %[wvo], %[rq], s66 offen lds\n" \
"s_add_u32 m0, %[wbase], 21504\n" \
"s_add_u32 s66, %[so0], 786432\n" \
"buffer_load_dwordx4 %[wvo], %[rq], s66 offen lds\n" \
"s_add_u32 m0, %[wbase], 22528\n" \
"s_add_u32 s66, %[so0], 540672\n" \
"buffer_load_dwordx4 %[wvo], %[rq], s66 offen lds\n" \
"s_add_u32 m0, %[wbase], 23552\n" \
"s_add_u32 s66, %[so0], 802816\n" \
"buffer_load_dwordx4 %[wvo], %[rq], s66 offen lds\n" \
"v_lshrrev_b32 v52, 9, %[wl]\n" \
"v_xor_b32 v52, v52, %[wl]\n" \
"v_and_b32 v52, 0xc0, v52\n" \
"v_and_b32 v53, 0x3c0, %[wl]\n" \
"v_lshl_or_b32 v52, v53, 2, v52\n" \
"v_and_or_b32 v52, %[wl], 48, v52\n" \
"v_add_u32 v53, 0x10000, v52\n" \
"s_bfe_u32 s66, %[hp], 0x10007\n" \
"s_lshr_b32 s66, 16, s66\n" \
"v_bfe_u32 v100, %[wl], 4, 6\n" \
"v_cmp_gt_u32 vcc, s66, v100\n" \
"v_mul_u32_u24 v100, 8192, v100\n" \
"s_nop 0\n" \
"v_cndmask_b32 v100, 2.0, v100, vcc\n" \
"s_waitcnt vmcnt(7)\n" \
"s_add_u32 m0, %[wbase], 24576\n" \
"s_add_u32 s66, %[so0], 1048576\n" \
"buffer_load_dwordx4 %[wvo], %[rq], s66 offen lds\n" \
"s_waitcnt vmcnt(7)\n" \
"s_add_u32 m0, %[wbase], 25600\n" \
"s_add_u32 s66, %[so0], 1310720\n" \
"buffer_load_dwordx4 %[wvo], %[rq], s66 offen lds\n" \
"s_waitcnt vmcnt(7)\n" \
"s_add_u32 m0, %[wbase], 26624\n" \
"s_add_u32 s66, %[so0], 1064960\n" \
"buffer_load_dwordx4 %[wvo], %[rq], s66 offen lds\n" \
"s_waitcnt vmcnt(7)\n" \
"s_add_u32 m0, %[wbase], 27648\n" \
"s_add_u32 s66, %[so0], 1327104\n" \
"buffer_load_dwordx4 %[wvo], %[rq], s66 offen lds\n" \
"s_barrier\n" \
"ds_read_b64_tr_b16 a[0:1], v52 offset:16384\n" \
"ds_read_b64_tr_b16 a[2:3], v52 offset:49152\n" \
"ds_read_b64_tr_b16 a[4:5], v53 offset:16384\n" \
"ds_read_b64_tr_b16 a[6:7], v53 offset:49152\n" \
"ds_read_b64_tr_b16 a[8:9], v52 offset:16392\n" \
"ds_read_b64_tr_b16 a[10:11], v52 offset:49160\n" \
"ds_read_b64_tr_b16 a[12:13], v53 offset:16392\n" \
"ds_read_b64_tr_b16 a[14:15], v53 offset:49160\n" \
"s_waitcnt vmcnt(7)\n" \
"s_add_u32 m0, %[wbase], 28672\n" \
"s_add_u32 s66, %[so0], 1572864\n" \
"buffer_load_dwordx4 %[wvo], %[rk], s66 offen lds\n" \
"s_waitcnt vmcnt(7)\n" \
"s_add_u32 m0, %[wbase], 29696\n" \
"s_add_u32 s66, %[so0], 1835008\n" \
"buffer_load_dwordx4 %[wvo], %[rk], s66 offen lds\n" \
"s_waitcnt vmcnt(7)\n" \
"s_add_u32 m0, %[wbase], 30720\n" \
"s_add_u32 s66, %[so0], 1589248\n" \
"buffer_load_dwordx4 %[wvo], %[rk], s66 offen lds\n" \
"s_waitcnt vmcnt(7)\n" \
"s_add_u32 m0, %[wbase], 31744\n" \
"s_add_u32 s66, %[so0], 1851392\n" \
"buffer_load_dwordx4 %[wvo], %[rk], s66 offen lds\n" \
"s_waitcnt lgkmcnt(0)\n" \
"s_barrier\n" \
"ds_read_b64_tr_b16 a[16:17], v52 offset:20480\n" \
"ds_read_b64_tr_b16 a[18:19], v52 offset:53248\n" \
"ds_read_b64_tr_b16 a[20:21], v53 offset:20480\n" \
"ds_read_b64_tr_b16 a[22:23], v53 offset:53248\n" \
"ds_read_b64_tr_b16 a[24:25], v52 offset:20488\n" \
"ds_read_b64_tr_b16 a[26:27], v52 offset:53256\n" \
"ds_read_b64_tr_b16 a[28:29], v53 offset:20488\n" \
"ds_read_b64_tr_b16 a[30:31], v53 offset:53256\n" \
"s_waitcnt vmcnt(7)\n" \
"s_add_u32 m0, %[wbase], 16384\n" \
"s_add_u32 s66, %[so0], 1572864\n" \
"buffer_load_dwordx4 %[wvo], %[rq], s66 offen lds\n" \
"s_waitcnt vmcnt(7)\n" \
"s_add_u32 m0, %[wbase], 17408\n" \
"s_add_u32 s66, %[so0], 1835008\n" \
"buffer_load_dwordx4 %[wvo], %[rq], s66 offen lds\n" \
"s_waitcnt vmcnt(7)\n" \
"s_add_u32 m0, %[wbase], 18432\n" \
"s_add_u32 s66, %[so0], 1589248\n" \
"buffer_load_dwordx4 %[wvo], %[rq], s66 offen lds\n" \
"s_waitcnt vmcnt(7)\n" \
"s_add_u32 m0, %[wbase], 19456\n" \
"s_add_u32 s66, %[so0], 1851392\n" \
"buffer_load_dwordx4 %[wvo], %[rq], s66 offen lds\n" \
"s_waitcnt lgkmcnt(0)\n" \
"s_barrier\n" \
"ds_read_b64_tr_b16 a[32:33], v52 offset:24576\n" \
"ds_read_b64_tr_b16 a[34:35], v52 offset:57344\n" \
"ds_read_b64_tr_b16 a[36:37], v53 offset:24576\n" \
"ds_read_b64_tr_b16 a[38:39], v53 offset:57344\n" \
"ds_read_b64_tr_b16 a[40:41], v52 offset:24584\n" \
"ds_read_b64_tr_b16 a[42:43], v52 offset:57352\n" \
"ds_read_b64_tr_b16 a[44:45], v53 offset:24584\n" \
"ds_read_b64_tr_b16 a[46:47], v53 offset:57352\n" \
"s_waitcnt vmcnt(7)\n" \
"s_add_u32 m0, %[wbase], 20480\n" \
"s_add_u32 s66, %[so0], 2097152\n" \
"buffer_load_dwordx4 %[wvo], %[rq], s66 offen lds\n" \
"s_waitcnt vmcnt(7)\n" \
"s_add_u32 m0, %[wbase], 21504\n" \
"s_add_u32 s66, %[so0], 2359296\n" \
"buffer_load_dwordx4 %[wvo], %[rq], s66 offen lds\n" \
"s_waitcnt vmcnt(7)\n" \
"s_add_u32 m0, %[wbase], 22528\n" \
"s_add_u32 s66, %[so0], 2113536\n" \
"buffer_load_dwordx4 %[wvo], %[rq], s66 offen lds\n" \
"s_waitcnt vmcnt(7)\n" \
"s_add_u32 m0, %[wbase], 23552\n" \
"s_add_u32 s66, %[so0], 2375680\n" \
"buffer_load_dwordx4 %[wvo], %[rq], s66 offen lds\n" \
"s_waitcnt lgkmcnt(0)\n" \
"s_barrier\n" \
"ds_read_b64_tr_b16 a[48:49], v52 offset:28672\n" \
"ds_read_b64_tr_b16 a[50:51], v52 offset:61440\n" \
"ds_read_b64_tr_b16 a[52:53], v53 offset:28672\n" \
"ds_read_b64_tr_b16 a[54:55], v53 offset:61440\n" \
"ds_read_b64_tr_b16 a[56:57], v52 offset:28680\n" \
"ds_read_b64_tr_b16 a[58:59], v52 offset:61448\n" \
"ds_read_b64_tr_b16 a[60:61], v53 offset:28680\n" \
"ds_read_b64_tr_b16 a[62:63], v53 offset:61448\n" \
"s_waitcnt vmcnt(7)\n" \
"s_add_u32 m0, %[wbase], 24576\n" \
"s_add_u32 s66, %[so0], 2621440\n" \
"buffer_load_dwordx4 %[wvo], %[rk], s66 offen lds\n" \
"s_waitcnt vmcnt(7)\n" \
"s_add_u32 m0, %[wbase], 25600\n" \
"s_add_u32 s66, %[so0], 2883584\n" \
"buffer_load_dwordx4 %[wvo], %[rk], s66 offen lds\n" \
"s_waitcnt vmcnt(7)\n" \
"s_add_u32 m0, %[wbase], 26624\n" \
"s_add_u32 s66, %[so0], 2637824\n" \
"buffer_load_dwordx4 %[wvo], %[rk], s66 offen lds\n" \
"s_waitcnt vmcnt(7)\n" \
"s_add_u32 m0, %[wbase], 27648\n" \
"s_add_u32 s66, %[so0], 2899968\n" \
"buffer_load_dwordx4 %[wvo], %[rk], s66 offen lds\n" \
"s_waitcnt lgkmcnt(0)\n" \
"s_barrier\n" \
"ds_read_b64_tr_b16 a[64:65], v52 offset:16384\n" \
"ds_read_b64_tr_b16 a[66:67], v52 offset:49152\n" \
"ds_read_b64_tr_b16 a[68:69], v53 offset:16384\n" \
"ds_read_b64_tr_b16 a[70:71], v53 offset:49152\n" \
"ds_read_b64_tr_b16 a[72:73], v52 offset:16392\n" \
"ds_read_b64_tr_b16 a[74:75], v52 offset:49160\n" \
"ds_read_b64_tr_b16 a[76:77], v53 offset:16392\n" \
"ds_read_b64_tr_b16 a[78:79], v53 offset:49160\n" \
"s_waitcnt vmcnt(7)\n" \
"s_add_u32 m0, %[wbase], 28672\n" \
"s_add_u32 s66, %[so0], 2621440\n" \
"buffer_load_dwordx4 %[wvo], %[rq], s66 offen lds\n" \
"s_waitcnt vmcnt(7)\n" \
"s_add_u32 m0, %[wbase], 29696\n" \
"s_add_u32 s66, %[so0], 2883584\n" \
"buffer_load_dwordx4 %[wvo], %[rq], s66 offen lds\n" \
"s_waitcnt vmcnt(7)\n" \
"s_add_u32 m0, %[wbase], 30720\n" \
"s_add_u32 s66, %[so0], 2637824\n" \
"buffer_load_dwordx4 %[wvo], %[rq], s66 offen lds\n" \
"s_waitcnt vmcnt(7)\n" \
"s_add_u32 m0, %[wbase], 31744\n" \
"s_add_u32 s66, %[so0], 2899968\n" \
"buffer_load_dwordx4 %[wvo], %[rq], s66 offen lds\n" \
"s_waitcnt lgkmcnt(0)\n" \
"s_barrier\n" \
"ds_read_b64_tr_b16 a[80:81], v52 offset:20480\n" \
"ds_read_b64_tr_b16 a[82:83], v52 offset:53248\n" \
"ds_read_b64_tr_b16 a[84:85], v53 offset:20480\n" \
"ds_read_b64_tr_b16 a[86:87], v53 offset:53248\n" \
"ds_read_b64_tr_b16 a[88:89], v52 offset:20488\n" \
"ds_read_b64_tr_b16 a[90:91], v52 offset:53256\n" \
"ds_read_b64_tr_b16 a[92:93], v53 offset:20488\n" \
"ds_read_b64_tr_b16 a[94:95], v53 offset:53256\n" \
"s_waitcnt vmcnt(7)\n" \
"s_add_u32 m0, %[wbase], 16384\n" \
"s_add_u32 s66, %[so0], 3145728\n" \
"buffer_load_dwordx4 %[wvo], %[rk], s66 offen lds\n" \
"s_waitcnt vmcnt(7)\n" \
"s_add_u32 m0, %[wbase], 17408\n" \
"s_add_u32 s66, %[so0], 3407872\n" \
"buffer_load_dwordx4 %[wvo], %[rk], s66 offen lds\n" \
"s_waitcnt vmcnt(7)\n" \
"s_add_u32 m0, %[wbase], 18432\n" \
"s_add_u32 s66, %[so0], 3162112\n" \
"buffer_load_dwordx4 %[wvo], %[rk], s66 offen lds\n" \
"s_waitcnt vmcnt(7)\n" \
"s_add_u32 m0, %[wbase], 19456\n" \
"s_add_u32 s66, %[so0], 3424256\n" \
"buffer_load_dwordx4 %[wvo], %[rk], s66 offen lds\n" \
"s_waitcnt lgkmcnt(0)\n" \
"s_barrier\n" \
"ds_read_b64_tr_b16 a[96:97], v52 offset:24576\n" \
"ds_read_b64_tr_b16 a[98:99], v52 offset:57344\n" \
"ds_read_b64_tr_b16 a[100:101], v53 offset:24576\n" \
"ds_read_b64_tr_b16 a[102:103], v53 offset:57344\n" \
"ds_read_b64_tr_b16 a[104:105], v52 offset:24584\n" \
"ds_read_b64_tr_b16 a[106:107], v52 offset:57352\n" \
"ds_read_b64_tr_b16 a[108:109], v53 offset:24584\n" \
"ds_read_b64_tr_b16 a[110:111], v53 offset:57352\n" \
"s_waitcnt vmcnt(7)\n" \
"s_add_u32 m0, %[wbase], 20480\n" \
"s_add_u32 s66, %[so0], 3145728\n" \
"buffer_load_dwordx4 %[wvo], %[rq], s66 offen lds\n" \
"s_waitcnt vmcnt(7)\n" \
"s_add_u32 m0, %[wbase], 21504\n" \
"s_add_u32 s66, %[so0], 3407872\n" \
"buffer_load_dwordx4 %[wvo], %[rq], s66 offen lds\n" \
"s_waitcnt vmcnt(7)\n" \
"s_add_u32 m0, %[wbase], 22528\n" \
"s_add_u32 s66, %[so0], 3162112\n" \
"buffer_load_dwordx4 %[wvo], %[rq], s66 offen lds\n" \
"s_waitcnt vmcnt(7)\n" \
"s_add_u32 m0, %[wbase], 23552\n" \
"s_add_u32 s66, %[so0], 3424256\n" \
"buffer_load_dwordx4 %[wvo], %[rq], s66 offen lds\n" \
"s_waitcnt lgkmcnt(0)\n" \
"s_barrier\n" \
"ds_read_b64_tr_b16 a[112:113], v52 offset:28672\n" \
"ds_read_b64_tr_b16 a[114:115], v52 offset:61440\n" \
"ds_read_b64_tr_b16 a[116:117], v53 offset:28672\n" \
"ds_read_b64_tr_b16 a[118:119], v53 offset:61440\n" \
"ds_read_b64_tr_b16 a[120:121], v52 offset:28680\n" \
"ds_read_b64_tr_b16 a[122:123], v52 offset:61448\n" \
"ds_read_b64_tr_b16 a[124:125], v53 offset:28680\n" \
"ds_read_b64_tr_b16 a[126:127], v53 offset:61448\n" \
"s_waitcnt vmcnt(7)\n" \
"s_add_u32 m0, %[wbase], 24576\n" \
"s_add_u32 s66, %[so0], 3670016\n" \
"buffer_load_dwordx4 %[wvo], %[rk], s66 offen lds\n" \
"s_waitcnt vmcnt(7)\n" \
"s_add_u32 m0, %[wbase], 25600\n" \
"s_add_u32 s66, %[so0], 3932160\n" \
"buffer_load_dwordx4 %[wvo], %[rk], s66 offen lds\n" \
"s_waitcnt vmcnt(7)\n" \
"s_add_u32 m0, %[wbase], 26624\n" \
"s_add_u32 s66, %[so0], 3686400\n" \
"buffer_load_dwordx4 %[wvo], %[rk], s66 offen lds\n" \
"s_waitcnt vmcnt(7)\n" \
"s_add_u32 m0, %[wbase], 27648\n" \
"s_add_u32 s66, %[so0], 3948544\n" \
"buffer_load_dwordx4 %[wvo], %[rk], s66 offen lds\n" \
"s_waitcnt lgkmcnt(0)\n" \
"s_barrier\n" \
"ds_read_b64_tr_b16 a[128:129], v52 offset:16384\n" \
"ds_read_b64_tr_b16 a[130:131], v52 offset:49152\n" \
"ds_read_b64_tr_b16 a[132:133], v53 offset:16384\n" \
"ds_read_b64_tr_b16 a[134:135], v53 offset:49152\n" \
"ds_read_b64_tr_b16 a[136:137], v52 offset:16392\n" \
"ds_read_b64_tr_b16 a[138:139], v52 offset:49160\n" \
"ds_read_b64_tr_b16 a[140:141], v53 offset:16392\n" \
"ds_read_b64_tr_b16 a[142:143], v53 offset:49160\n" \
"s_waitcnt vmcnt(7)\n" \
"s_add_u32 m0, %[wbase], 28672\n" \
"s_add_u32 s66, %[so0], 3670016\n" \
"buffer_load_dwordx4 %[wvo], %[rq], s66 offen lds\n" \
"s_waitcnt vmcnt(7)\n" \
"s_add_u32 m0, %[wbase], 29696\n" \
"s_add_u32 s66, %[so0], 3932160\n" \
"buffer_load_dwordx4 %[wvo], %[rq], s66 offen lds\n" \
"s_waitcnt vmcnt(7)\n" \
"s_add_u32 m0, %[wbase], 30720\n" \
"s_add_u32 s66, %[so0], 3686400\n" \
"buffer_load_dwordx4 %[wvo], %[rq], s66 offen lds\n" \
"s_waitcnt vmcnt(7)\n" \
"s_add_u32 m0, %[wbase], 31744\n" \
"s_add_u32 s66, %[so0], 3948544\n" \
"buffer_load_dwordx4 %[wvo], %[rq], s66 offen lds\n" \
"s_waitcnt lgkmcnt(0)\n" \
"s_barrier\n" \
"ds_read_b64_tr_b16 a[144:145], v52 offset:20480\n" \
"ds_read_b64_tr_b16 a[146:147], v52 offset:53248\n" \
"ds_read_b64_tr_b16 a[148:149], v53 offset:20480\n" \
"ds_read_b64_tr_b16 a[150:151], v53 offset:53248\n" \
"ds_read_b64_tr_b16 a[152:153], v52 offset:20488\n" \
"ds_read_b64_tr_b16 a[154:155], v52 offset:53256\n" \
"ds_read_b64_tr_b16 a[156:157], v53 offset:20488\n" \
"ds_read_b64_tr_b16 a[158:159], v53 offset:53256\n" \
"s_waitcnt vmcnt(7)\n" \
"s_add_u32 m0, %[wbase], 16384\n" \
"s_add_u32 s66, %[so0], 4194304\n" \
"buffer_load_dwordx4 %[wvo], %[rk], s66 offen lds\n" \
"s_waitcnt vmcnt(7)\n" \
"s_add_u32 m0, %[wbase], 17408\n" \
"s_add_u32 s66, %[so0], 4456448\n" \
"buffer_load_dwordx4 %[wvo], %[rk], s66 offen lds\n" \
"s_waitcnt vmcnt(7)\n" \
"s_add_u32 m0, %[wbase], 18432\n" \
"s_add_u32 s66, %[so0], 4210688\n" \
"buffer_load_dwordx4 %[wvo], %[rk], s66 offen lds\n" \
"s_waitcnt vmcnt(7)\n" \
"s_add_u32 m0, %[wbase], 19456\n" \
"s_add_u32 s66, %[so0], 4472832\n" \
"buffer_load_dwordx4 %[wvo], %[rk], s66 offen lds\n" \
"s_waitcnt lgkmcnt(0)\n" \
"s_barrier\n" \
"ds_read_b64_tr_b16 a[160:161], v52 offset:24576\n" \
"ds_read_b64_tr_b16 a[162:163], v52 offset:57344\n" \
"ds_read_b64_tr_b16 a[164:165], v53 offset:24576\n" \
"ds_read_b64_tr_b16 a[166:167], v53 offset:57344\n" \
"ds_read_b64_tr_b16 a[168:169], v52 offset:24584\n" \
"ds_read_b64_tr_b16 a[170:171], v52 offset:57352\n" \
"ds_read_b64_tr_b16 a[172:173], v53 offset:24584\n" \
"ds_read_b64_tr_b16 a[174:175], v53 offset:57352\n" \
"s_waitcnt vmcnt(7)\n" \
"s_add_u32 m0, %[wbase], 20480\n" \
"s_add_u32 s66, %[so0], 4718592\n" \
"buffer_load_dwordx4 %[wvo], %[rk], s66 offen lds\n" \
"s_waitcnt vmcnt(7)\n" \
"s_add_u32 m0, %[wbase], 21504\n" \
"s_add_u32 s66, %[so0], 4980736\n" \
"buffer_load_dwordx4 %[wvo], %[rk], s66 offen lds\n" \
"s_waitcnt vmcnt(7)\n" \
"s_add_u32 m0, %[wbase], 22528\n" \
"s_add_u32 s66, %[so0], 4734976\n" \
"buffer_load_dwordx4 %[wvo], %[rk], s66 offen lds\n" \
"s_waitcnt vmcnt(7)\n" \
"s_add_u32 m0, %[wbase], 23552\n" \
"s_add_u32 s66, %[so0], 4997120\n" \
"buffer_load_dwordx4 %[wvo], %[rk], s66 offen lds\n" \
"s_waitcnt lgkmcnt(0)\n" \
"s_barrier\n" \
"ds_read_b64_tr_b16 a[176:177], v52 offset:28672\n" \
"ds_read_b64_tr_b16 a[178:179], v52 offset:61440\n" \
"ds_read_b64_tr_b16 a[180:181], v53 offset:28672\n" \
"ds_read_b64_tr_b16 a[182:183], v53 offset:61440\n" \
"ds_read_b64_tr_b16 a[184:185], v52 offset:28680\n" \
"ds_read_b64_tr_b16 a[186:187], v52 offset:61448\n" \
"ds_read_b64_tr_b16 a[188:189], v53 offset:28680\n" \
"ds_read_b64_tr_b16 a[190:191], v53 offset:61448\n" \
"s_waitcnt vmcnt(7)\n" \
"s_add_u32 m0, %[wbase], 24576\n" \
"s_add_u32 s66, %[so0], 4718592\n" \
"buffer_load_dwordx4 %[wvo], %[rq], s66 offen lds\n" \
"s_waitcnt vmcnt(7)\n" \
"s_add_u32 m0, %[wbase], 25600\n" \
"s_add_u32 s66, %[so0], 4980736\n" \
"buffer_load_dwordx4 %[wvo], %[rq], s66 offen lds\n" \
"s_waitcnt vmcnt(7)\n" \
"s_add_u32 m0, %[wbase], 26624\n" \
"s_add_u32 s66, %[so0], 4734976\n" \
"buffer_load_dwordx4 %[wvo], %[rq], s66 offen lds\n" \
"s_waitcnt vmcnt(7)\n" \
"s_add_u32 m0, %[wbase], 27648\n" \
"s_add_u32 s66, %[so0], 4997120\n" \
"buffer_load_dwordx4 %[wvo], %[rq], s66 offen lds\n" \
"s_waitcnt lgkmcnt(0)\n" \
"s_barrier\n" \
"ds_read_b64_tr_b16 a[192:193], v52 offset:16384\n" \
"ds_read_b64_tr_b16 a[194:195], v52 offset:49152\n" \
"ds_read_b64_tr_b16 a[196:197], v53 offset:16384\n" \
"ds_read_b64_tr_b16 a[198:199], v53 offset:49152\n" \
"ds_read_b64_tr_b16 a[200:201], v52 offset:16392\n" \
"ds_read_b64_tr_b16 a[202:203], v52 offset:49160\n" \
"ds_read_b64_tr_b16 a[204:205], v53 offset:16392\n" \
"ds_read_b64_tr_b16 a[206:207], v53 offset:49160\n" \
"s_waitcnt vmcnt(7)\n" \
"s_add_u32 m0, %[wbase], 28672\n" \
"s_add_u32 s66, %[so0], 5242880\n" \
"buffer_load_dwordx4 %[wvo], %[rk], s66 offen lds\n" \
"s_waitcnt vmcnt(7)\n" \
"s_add_u32 m0, %[wbase], 29696\n" \
"s_add_u32 s66, %[so0], 5505024\n" \
"buffer_load_dwordx4 %[wvo], %[rk], s66 offen lds\n" \
"s_waitcnt vmcnt(7)\n" \
"s_add_u32 m0, %[wbase], 30720\n" \
"s_add_u32 s66, %[so0], 5259264\n" \
"buffer_load_dwordx4 %[wvo], %[rk], s66 offen lds\n" \
"s_waitcnt vmcnt(7)\n" \
"s_add_u32 m0, %[wbase], 31744\n" \
"s_add_u32 s66, %[so0], 5521408\n" \
"buffer_load_dwordx4 %[wvo], %[rk], s66 offen lds\n" \
"s_waitcnt lgkmcnt(0)\n" \
"s_barrier\n" \
"ds_read_b64_tr_b16 a[208:209], v52 offset:20480\n" \
"ds_read_b64_tr_b16 a[210:211], v52 offset:53248\n" \
"ds_read_b64_tr_b16 a[212:213], v53 offset:20480\n" \
"ds_read_b64_tr_b16 a[214:215], v53 offset:53248\n" \
"ds_read_b64_tr_b16 a[216:217], v52 offset:20488\n" \
"ds_read_b64_tr_b16 a[218:219], v52 offset:53256\n" \
"ds_read_b64_tr_b16 a[220:221], v53 offset:20488\n" \
"ds_read_b64_tr_b16 a[222:223], v53 offset:53256\n" \
"s_waitcnt vmcnt(7)\n" \
"s_add_u32 m0, %[wbase], 16384\n" \
"s_add_u32 s66, %[so0], 5242880\n" \
"buffer_load_dwordx4 %[wvo], %[rq], s66 offen lds\n" \
"s_waitcnt vmcnt(7)\n" \
"s_add_u32 m0, %[wbase], 17408\n" \
"s_add_u32 s66, %[so0], 5505024\n" \
"buffer_load_dwordx4 %[wvo], %[rq], s66 offen lds\n" \
"s_waitcnt vmcnt(7)\n" \
"s_add_u32 m0, %[wbase], 18432\n" \
"s_add_u32 s66, %[so0], 5259264\n" \
"buffer_load_dwordx4 %[wvo], %[rq], s66 offen lds\n" \
"s_waitcnt vmcnt(7)\n" \
"s_add_u32 m0, %[wbase], 19456\n" \
"s_add_u32 s66, %[so0], 5521408\n" \
"buffer_load_dwordx4 %[wvo], %[rq], s66 offen lds\n" \
"s_waitcnt lgkmcnt(0)\n" \
"s_barrier\n" \
"ds_read_b64_tr_b16 a[224:225], v52 offset:24576\n" \
"ds_read_b64_tr_b16 a[226:227], v52 offset:57344\n" \
"ds_read_b64_tr_b16 a[228:229], v53 offset:24576\n" \
"ds_read_b64_tr_b16 a[230:231], v53 offset:57344\n" \
"ds_read_b64_tr_b16 a[232:233], v52 offset:24584\n" \
"ds_read_b64_tr_b16 a[234:235], v52 offset:57352\n" \
"ds_read_b64_tr_b16 a[236:237], v53 offset:24584\n" \
"ds_read_b64_tr_b16 a[238:239], v53 offset:57352\n" \
"s_waitcnt vmcnt(7)\n" \
"s_add_u32 m0, %[wbase], 20480\n" \
"s_add_u32 s66, %[so0], 5767168\n" \
"buffer_load_dwordx4 %[wvo], %[rk], s66 offen lds\n" \
"s_waitcnt vmcnt(7)\n" \
"s_add_u32 m0, %[wbase], 21504\n" \
"s_add_u32 s66, %[so0], 6029312\n" \
"buffer_load_dwordx4 %[wvo], %[rk], s66 offen lds\n" \
"s_waitcnt vmcnt(7)\n" \
"s_add_u32 m0, %[wbase], 22528\n" \
"s_add_u32 s66, %[so0], 5783552\n" \
"buffer_load_dwordx4 %[wvo], %[rk], s66 offen lds\n" \
"s_waitcnt vmcnt(7)\n" \
"s_add_u32 m0, %[wbase], 23552\n" \
"s_add_u32 s66, %[so0], 6045696\n" \
"buffer_load_dwordx4 %[wvo], %[rk], s66 offen lds\n" \
"s_waitcnt lgkmcnt(0)\n" \
"s_barrier\n" \
"ds_read_b64_tr_b16 a[240:241], v52 offset:28672\n" \
"ds_read_b64_tr_b16 a[242:243], v52 offset:61440\n" \
"ds_read_b64_tr_b16 a[244:245], v53 offset:28672\n" \
"ds_read_b64_tr_b16 a[246:247], v53 offset:61440\n" \
"ds_read_b64_tr_b16 a[248:249], v52 offset:28680\n" \
"ds_read_b64_tr_b16 a[250:251], v52 offset:61448\n" \
"ds_read_b64_tr_b16 a[252:253], v53 offset:28680\n" \
"ds_read_b64_tr_b16 a[254:255], v53 offset:61448\n" \
"s_waitcnt vmcnt(7)\n" \
"s_add_u32 m0, %[wbase], 24576\n" \
"s_add_u32 s66, %[so0], 5767168\n" \
"buffer_load_dwordx4 %[wvo], %[rq], s66 offen lds\n" \
"s_waitcnt vmcnt(7)\n" \
"s_add_u32 m0, %[wbase], 25600\n" \
"s_add_u32 s66, %[so0], 6029312\n" \
"buffer_load_dwordx4 %[wvo], %[rq], s66 offen lds\n" \
"s_waitcnt vmcnt(7)\n" \
"s_add_u32 m0, %[wbase], 26624\n" \
"s_add_u32 s66, %[so0], 5783552\n" \
"buffer_load_dwordx4 %[wvo], %[rq], s66 offen lds\n" \
"s_waitcnt vmcnt(7)\n" \
"s_add_u32 m0, %[wbase], 27648\n" \
"s_add_u32 s66, %[so0], 6045696\n" \
"buffer_load_dwordx4 %[wvo], %[rq], s66 offen lds\n" \
"s_waitcnt lgkmcnt(0)\n" \
"s_barrier\n" \
"ds_read_b64_tr_b16 v[128:129], v52 offset:16384\n" \
"ds_read_b64_tr_b16 v[130:131], v52 offset:49152\n" \
"ds_read_b64_tr_b16 v[132:133], v53 offset:16384\n" \
"ds_read_b64_tr_b16 v[134:135], v53 offset:49152\n" \
"ds_read_b64_tr_b16 v[136:137], v52 offset:16392\n" \
"ds_read_b64_tr_b16 v[138:139], v52 offset:49160\n" \
"ds_read_b64_tr_b16 v[140:141], v53 offset:16392\n" \
"ds_read_b64_tr_b16 v[142:143], v53 offset:49160\n" \
"s_waitcnt vmcnt(7)\n" \
"s_add_u32 m0, %[wbase], 28672\n" \
"s_add_u32 s66, %[so0], 6291456\n" \
"buffer_load_dwordx4 %[wvo], %[rk], s66 offen lds\n" \
"s_waitcnt vmcnt(7)\n" \
"s_add_u32 m0, %[wbase], 29696\n" \
"s_add_u32 s66, %[so0], 6553600\n" \
"buffer_load_dwordx4 %[wvo], %[rk], s66 offen lds\n" \
"s_waitcnt vmcnt(7)\n" \
"s_add_u32 m0, %[wbase], 30720\n" \
"s_add_u32 s66, %[so0], 6307840\n" \
"buffer_load_dwordx4 %[wvo], %[rk], s66 offen lds\n" \
"s_waitcnt vmcnt(7)\n" \
"s_add_u32 m0, %[wbase], 31744\n" \
"s_add_u32 s66, %[so0], 6569984\n" \
"buffer_load_dwordx4 %[wvo], %[rk], s66 offen lds\n" \
"s_waitcnt lgkmcnt(0)\n" \
"s_barrier\n" \
"ds_read_b64_tr_b16 v[144:145], v52 offset:20480\n" \
"ds_read_b64_tr_b16 v[146:147], v52 offset:53248\n" \
"ds_read_b64_tr_b16 v[148:149], v53 offset:20480\n" \
"ds_read_b64_tr_b16 v[150:151], v53 offset:53248\n" \
"ds_read_b64_tr_b16 v[152:153], v52 offset:20488\n" \
"ds_read_b64_tr_b16 v[154:155], v52 offset:53256\n" \
"ds_read_b64_tr_b16 v[156:157], v53 offset:20488\n" \
"ds_read_b64_tr_b16 v[158:159], v53 offset:53256\n" \
"s_waitcnt vmcnt(7)\n" \
"s_add_u32 m0, %[wbase], 16384\n" \
"s_add_u32 s66, %[so0], 6291456\n" \
"buffer_load_dwordx4 %[wvo], %[rq], s66 offen lds\n" \
"s_waitcnt vmcnt(7)\n" \
"s_add_u32 m0, %[wbase], 17408\n" \
"s_add_u32 s66, %[so0], 6553600\n" \
"buffer_load_dwordx4 %[wvo], %[rq], s66 offen lds\n" \
"s_waitcnt vmcnt(7)\n" \
"s_add_u32 m0, %[wbase], 18432\n" \
"s_add_u32 s66, %[so0], 6307840\n" \
"buffer_load_dwordx4 %[wvo], %[rq], s66 offen lds\n" \
"s_waitcnt vmcnt(7)\n" \
"s_add_u32 m0, %[wbase], 19456\n" \
"s_add_u32 s66, %[so0], 6569984\n" \
"buffer_load_dwordx4 %[wvo], %[rq], s66 offen lds\n" \
"s_waitcnt lgkmcnt(0)\n" \
"s_barrier\n" \
"ds_read_b64_tr_b16 v[160:161], v52 offset:24576\n" \
"ds_read_b64_tr_b16 v[162:163], v52 offset:57344\n" \
"ds_read_b64_tr_b16 v[164:165], v53 offset:24576\n" \
"ds_read_b64_tr_b16 v[166:167], v53 offset:57344\n" \
"ds_read_b64_tr_b16 v[168:169], v52 offset:24584\n" \
"ds_read_b64_tr_b16 v[170:171], v52 offset:57352\n" \
"ds_read_b64_tr_b16 v[172:173], v53 offset:24584\n" \
"ds_read_b64_tr_b16 v[174:175], v53 offset:57352\n" \
"s_waitcnt vmcnt(7)\n" \
"s_add_u32 m0, %[wbase], 20480\n" \
"s_add_u32 s66, %[so0], 7340032\n" \
"buffer_load_dwordx4 %[wvo], %[rk], s66 offen lds\n" \
"s_waitcnt vmcnt(7)\n" \
"s_add_u32 m0, %[wbase], 21504\n" \
"s_add_u32 s66, %[so0], 7602176\n" \
"buffer_load_dwordx4 %[wvo], %[rk], s66 offen lds\n" \
"s_waitcnt vmcnt(7)\n" \
"s_add_u32 m0, %[wbase], 22528\n" \
"s_add_u32 s66, %[so0], 7356416\n" \
"buffer_load_dwordx4 %[wvo], %[rk], s66 offen lds\n" \
"s_waitcnt vmcnt(7)\n" \
"s_add_u32 m0, %[wbase], 23552\n" \
"s_add_u32 s66, %[so0], 7618560\n" \
"buffer_load_dwordx4 %[wvo], %[rk], s66 offen lds\n" \
"s_waitcnt lgkmcnt(0)\n" \
"s_barrier\n" \
"ds_read_b64_tr_b16 v[176:177], v52 offset:28672\n" \
"ds_read_b64_tr_b16 v[178:179], v52 offset:61440\n" \
"ds_read_b64_tr_b16 v[180:181], v53 offset:28672\n" \
"ds_read_b64_tr_b16 v[182:183], v53 offset:61440\n" \
"ds_read_b64_tr_b16 v[184:185], v52 offset:28680\n" \
"ds_read_b64_tr_b16 v[186:187], v52 offset:61448\n" \
"ds_read_b64_tr_b16 v[188:189], v53 offset:28680\n" \
"ds_read_b64_tr_b16 v[190:191], v53 offset:61448\n" \
"s_waitcnt vmcnt(7)\n" \
"s_add_u32 m0, %[wbase], 24576\n" \
"s_add_u32 s66, %[so0], 7340032\n" \
"buffer_load_dwordx4 %[wvo], %[rq], s66 offen lds\n" \
"s_add_u32 s66, %[hp], 0\n" \
"buffer_load_dword v101, v100, %[rq], s66 offen\n" \
"buffer_load_dword v125, off, %[ws], %[hs] sc1\n" \
"s_waitcnt vmcnt(9)\n" \
"s_add_u32 m0, %[wbase], 25600\n" \
"s_add_u32 s66, %[so0], 7602176\n" \
"buffer_load_dwordx4 %[wvo], %[rq], s66 offen lds\n" \
"s_waitcnt vmcnt(9)\n" \
"s_add_u32 m0, %[wbase], 26624\n" \
"s_add_u32 s66, %[so0], 7356416\n" \
"buffer_load_dwordx4 %[wvo], %[rq], s66 offen lds\n" \
"s_waitcnt vmcnt(9)\n" \
"s_add_u32 m0, %[wbase], 27648\n" \
"s_add_u32 s66, %[so0], 7618560\n" \
"buffer_load_dwordx4 %[wvo], %[rq], s66 offen lds\n" \
"s_waitcnt lgkmcnt(0)\n" \
"s_barrier\n" \
"ds_read_b64_tr_b16 v[192:193], v52 offset:16384\n" \
"ds_read_b64_tr_b16 v[194:195], v52 offset:49152\n" \
"ds_read_b64_tr_b16 v[196:197], v53 offset:16384\n" \
"ds_read_b64_tr_b16 v[198:199], v53 offset:49152\n" \
"ds_read_b64_tr_b16 v[200:201], v52 offset:16392\n" \
"ds_read_b64_tr_b16 v[202:203], v52 offset:49160\n" \
"ds_read_b64_tr_b16 v[204:205], v53 offset:16392\n" \
"ds_read_b64_tr_b16 v[206:207], v53 offset:49160\n" \
"s_waitcnt vmcnt(9)\n" \
"s_add_u32 m0, %[wbase], 28672\n" \
"s_add_u32 s66, %[so0], 7864320\n" \
"buffer_load_dwordx4 %[wvo], %[rq], s66 offen lds\n" \
"s_add_u32 s66, %[hp], 0\n" \
"buffer_load_dword v101, v100, %[rk], s66 offen\n" \
"s_waitcnt vmcnt(10)\n" \
"s_add_u32 m0, %[wbase], 29696\n" \
"s_add_u32 s66, %[so0], 8126464\n" \
"buffer_load_dwordx4 %[wvo], %[rq], s66 offen lds\n" \
"s_waitcnt vmcnt(10)\n" \
"s_add_u32 m0, %[wbase], 30720\n" \
"s_add_u32 s66, %[so0], 7880704\n" \
"buffer_load_dwordx4 %[wvo], %[rq], s66 offen lds\n" \
"s_waitcnt vmcnt(10)\n" \
"s_add_u32 m0, %[wbase], 31744\n" \
"s_add_u32 s66, %[so0], 8142848\n" \
"buffer_load_dwordx4 %[wvo], %[rq], s66 offen lds\n" \
"s_waitcnt lgkmcnt(0)\n" \
"s_barrier\n" \
"ds_read_b64_tr_b16 v[208:209], v52 offset:20480\n" \
"ds_read_b64_tr_b16 v[210:211], v52 offset:53248\n" \
"ds_read_b64_tr_b16 v[212:213], v53 offset:20480\n" \
"ds_read_b64_tr_b16 v[214:215], v53 offset:53248\n" \
"ds_read_b64_tr_b16 v[216:217], v52 offset:20488\n" \
"ds_read_b64_tr_b16 v[218:219], v52 offset:53256\n" \
"ds_read_b64_tr_b16 v[220:221], v53 offset:20488\n" \
"ds_read_b64_tr_b16 v[222:223], v53 offset:53256\n" \
"s_waitcnt vmcnt(10)\n" \
"s_add_u32 m0, %[wbase], 0\n" \
"s_add_u32 s66, %[so0], 0\n" \
"buffer_load_dwordx4 %[wvo], %[rq], s66 offen lds\n" \
"s_add_u32 s66, %[hp], 1048576\n" \
"buffer_load_dword v101, v100, %[rk], s66 offen\n" \
"s_waitcnt vmcnt(9)\n" \
"s_add_u32 m0, %[wbase], 1024\n" \
"s_add_u32 s66, %[so0], 262144\n" \
"buffer_load_dwordx4 %[wvo], %[rq], s66 offen lds\n" \
"s_waitcnt vmcnt(9)\n" \
"s_add_u32 m0, %[wbase], 2048\n" \
"s_add_u32 s66, %[so0], 16384\n" \
"buffer_load_dwordx4 %[wvo], %[rq], s66 offen lds\n" \
"s_waitcnt vmcnt(9)\n" \
"s_add_u32 m0, %[wbase], 3072\n" \
"s_add_u32 s66, %[so0], 278528\n" \
"buffer_load_dwordx4 %[wvo], %[rq], s66 offen lds\n" \
"s_waitcnt lgkmcnt(0)\n" \
"s_barrier\n" \
"ds_read_b64_tr_b16 v[224:225], v52 offset:24576\n" \
"ds_read_b64_tr_b16 v[226:227], v52 offset:57344\n" \
"ds_read_b64_tr_b16 v[228:229], v53 offset:24576\n" \
"ds_read_b64_tr_b16 v[230:231], v53 offset:57344\n" \
"ds_read_b64_tr_b16 v[232:233], v52 offset:24584\n" \
"ds_read_b64_tr_b16 v[234:235], v52 offset:57352\n" \
"ds_read_b64_tr_b16 v[236:237], v53 offset:24584\n" \
"ds_read_b64_tr_b16 v[238:239], v53 offset:57352\n" \
"s_mov_b32 s64, 262144\n" \
"Lqk_hs_%=:\n" \
"v_readfirstlane_b32 s62, v125\n" \
"s_cmp_eq_u32 s62, 0\n" \
"s_cbranch_scc1 Lqk_shs_%=\n" \
"v_mov_b32 v125, 2\n" \
"s_mov_b64 exec, 1\n" \
"buffer_store_dword v125, off, %[ws], %[hs] offset:2048 sc1\n" \
"s_mov_b64 exec, -1\n" \
"s_and_b32 s62, s62, 8192\n" \
"v_xor_b32 %[fvo], s62, %[fvo]\n" \
"s_xor_b32 %[fo], %[fo], s62\n" \
"s_waitcnt vmcnt(10)\n" \
"s_add_u32 m0, %[wbase], 4096\n" \
"s_add_u32 s66, %[so0], 0\n" \
"buffer_load_dwordx4 %[wvo], %[rk], s66 offen lds\n" \
"s_add_u32 s66, %[hp], 4194304\n" \
"buffer_load_dword v101, v100, %[rq], s66 offen\n" \
"v_lshlrev_b32 v102, 3, %[wl]\n" \
"v_and_b32 v102, 0x1f80, v102\n" \
"v_min_u32 v102, Lqk_last_%=-Lqk_top_%=, v102\n" \
"s_getpc_b64 s[94:95]\n" \
"Lqk_pc100_%=:\n" \
"s_add_u32 s94, s94, Lqk_top_%=-Lqk_pc100_%=\n" \
"s_addc_u32 s95, s95, 0\n" \
"global_load_dword v103, v102, s[94:95]\n" \
"s_waitcnt vmcnt(11)\n" \
"s_add_u32 m0, %[wbase], 5120\n" \
"s_add_u32 s66, %[so0], 262144\n" \
"buffer_load_dwordx4 %[wvo], %[rk], s66 offen lds\n" \
"s_waitcnt vmcnt(11)\n" \
"s_add_u32 m0, %[wbase], 6144\n" \
"s_add_u32 s66, %[so0], 16384\n" \
"buffer_load_dwordx4 %[wvo], %[rk], s66 offen lds\n" \
"s_waitcnt vmcnt(11)\n" \
"s_add_u32 m0, %[wbase], 7168\n" \
"s_add_u32 s66, %[so0], 278528\n" \
"buffer_load_dwordx4 %[wvo], %[rk], s66 offen lds\n" \
"s_waitcnt lgkmcnt(0)\n" \
"s_barrier\n" \
"ds_read_b64_tr_b16 v[240:241], v52 offset:28672\n" \
"ds_read_b64_tr_b16 v[242:243], v52 offset:61440\n" \
"ds_read_b64_tr_b16 v[244:245], v53 offset:28672\n" \
"ds_read_b64_tr_b16 v[246:247], v53 offset:61440\n" \
"ds_read_b64_tr_b16 v[248:249], v52 offset:28680\n" \
"ds_read_b64_tr_b16 v[250:251], v52 offset:61448\n" \
"ds_read_b64_tr_b16 v[252:253], v53 offset:28680\n" \
"ds_read_b64_tr_b16 v[254:255], v53 offset:61448\n" \
"s_waitcnt vmcnt(11)\n" \
"s_add_u32 m0, %[wbase], 8192\n" \
"s_add_u32 s66, %[so0], 1048576\n" \
"buffer_load_dwordx4 %[wvo], %[rk], s66 offen lds\n" \
"s_add_u32 s66, %[hp], 6815744\n" \
"buffer_load_dword v101, v100, %[rq], s66 offen\n" \
"s_waitcnt vmcnt(11)\n" \
"s_add_u32 m0, %[wbase], 9216\n" \
"s_add_u32 s66, %[so0], 1310720\n" \
"buffer_load_dwordx4 %[wvo], %[rk], s66 offen lds\n" \
"s_waitcnt vmcnt(11)\n" \
"s_add_u32 m0, %[wbase], 10240\n" \
"s_add_u32 s66, %[so0], 1064960\n" \
"buffer_load_dwordx4 %[wvo], %[rk], s66 offen lds\n" \
"s_waitcnt vmcnt(11)\n" \
"s_add_u32 m0, %[wbase], 11264\n" \
"s_add_u32 s66, %[so0], 1327104\n" \
"buffer_load_dwordx4 %[wvo], %[rk], s66 offen lds\n" \
"s_waitcnt lgkmcnt(0)\n" \
"s_barrier\n" \
"ds_read_b64_tr_b16 v[20:21], v52 offset:0\n" \
"ds_read_b64_tr_b16 v[22:23], v52 offset:32768\n" \
"ds_read_b64_tr_b16 v[24:25], v53 offset:0\n" \
"ds_read_b64_tr_b16 v[26:27], v53 offset:32768\n" \
"ds_read_b64_tr_b16 v[28:29], v52 offset:8\n" \
"ds_read_b64_tr_b16 v[30:31], v52 offset:32776\n" \
"ds_read_b64_tr_b16 v[32:33], v53 offset:8\n" \
"ds_read_b64_tr_b16 v[34:35], v53 offset:32776\n" \
"s_waitcnt vmcnt(10)\n" \
"s_add_u32 m0, %[wbase], 12288\n" \
"s_add_u32 s66, %[so0], 4194304\n" \
"buffer_load_dwordx4 %[wvo], %[rq], s66 offen lds\n" \
"s_add_u32 s66, %[hp], 6815744\n" \
"buffer_load_dword v101, v100, %[rk], s66 offen\n" \
"s_mov_b32 s64, 262144\n" \
"s_mov_b32 s65, 1\n" \
"s_add_u32 s62, s62, 3670016\n" \
"s_mov_b32 s74, %[okm]\n" \
"s_mov_b32 s75, %[okx]\n" \
"s_mov_b32 s77, %[ovx]\n" \
"s_mov_b32 s78, 0x3e0293ee\n" \
"s_mov_b32 s59, 0\n" \
"s_mov_b32 s60, 0\n" \
"s_mov_b32 s72, s62\n" \
"s_add_u32 s62, s62, 524288\n" \
"s_and_b32 s62, s62, 2097151\n" \
"s_bitset1_b32 s62, 21\n" \
"s_cmp_eq_u32 s65, 1\n" \
"s_cselect_b32 s68, 2031616, s62\n" \
"s_add_u32 s69, s68, 16384\n" \
"s_add_u32 s70, s68, 32768\n" \
"s_add_u32 s71, s68, 49152\n" \
"s_add_u32 s63, s62, %[hb]\n" \
"s_add_u32 s73, s62, %[ovm]\n" \
"v_mov_b32 v116, -1\n" \
"v_mov_b32 v117, -1\n" \
"v_mov_b32 v118, -1\n" \
"v_mov_b32 v119, -1\n" \
"s_xor_b32 s66, s62, 1048576\n" \
"s_add_u32 s66, s66, %[op0]\n" \
"buffer_store_dwordx4 v[116:119], %[wl], %[ws], s66 offen sc1\n" \
"s_xor_b32 s66, s62, 1048576\n" \
"s_add_u32 s66, s66, %[op1]\n" \
"buffer_store_dwordx4 v[116:119], %[wl], %[ws], s66 offen sc1\n" \
"v_lshlrev_b32 v104, 20, %[mvo]\n" \
"v_and_or_b32 v104, v104, 2.0, %[mvo]\n" \
"buffer_load_dwordx4 v[68:71], v104, %[ws], s68 offen sc1\n" \
"buffer_load_dwordx4 v[72:75], v104, %[ws], s68 offen offset:512 sc1\n" \
"s_waitcnt vmcnt(13)\n" \
"s_add_u32 m0, %[wbase], 13312\n" \
"s_add_u32 s66, %[so0], 4456448\n" \
"buffer_load_dwordx4 %[wvo], %[rq], s66 offen lds\n" \
"buffer_load_dwordx4 v[76:79], v104, %[ws], s69 offen sc1\n" \
"buffer_load_dwordx4 v[80:83], v104, %[ws], s69 offen offset:512 sc1\n" \
"s_waitcnt vmcnt(15)\n" \
"s_add_u32 m0, %[wbase], 14336\n" \
"s_add_u32 s66, %[so0], 4210688\n" \
"buffer_load_dwordx4 %[wvo], %[rq], s66 offen lds\n" \
"buffer_load_dwordx4 v[84:87], v104, %[ws], s70 offen sc1\n" \
"buffer_load_dwordx4 v[88:91], v104, %[ws], s70 offen offset:512 sc1\n" \
"s_waitcnt vmcnt(17)\n" \
"s_add_u32 m0, %[wbase], 15360\n" \
"s_add_u32 s66, %[so0], 4472832\n" \
"buffer_load_dwordx4 %[wvo], %[rq], s66 offen lds\n" \
"buffer_load_dwordx4 v[92:95], v104, %[ws], s71 offen sc1\n" \
"buffer_load_dwordx4 v[96:99], v104, %[ws], s71 offen offset:512 sc1\n" \
"s_waitcnt lgkmcnt(0)\n" \
"s_barrier\n" \
"ds_write_b128 %[wl], v[20:23] offset:0\n" \
"ds_write_b128 %[wl], v[24:27] offset:1024\n" \
"ds_write_b128 %[wl], v[28:31] offset:2048\n" \
"ds_write_b128 %[wl], v[32:35] offset:3072\n" \
"ds_read_b64_tr_b16 v[36:37], v52 offset:4096\n" \
"ds_read_b64_tr_b16 v[38:39], v52 offset:36864\n" \
"ds_read_b64_tr_b16 v[40:41], v53 offset:4096\n" \
"ds_read_b64_tr_b16 v[42:43], v53 offset:36864\n" \
"ds_read_b64_tr_b16 v[44:45], v52 offset:4104\n" \
"ds_read_b64_tr_b16 v[46:47], v52 offset:36872\n" \
"ds_read_b64_tr_b16 v[48:49], v53 offset:4104\n" \
"ds_read_b64_tr_b16 v[50:51], v53 offset:36872\n" \
"s_waitcnt vmcnt(19)\n" \
"s_add_u32 m0, %[wbase], 16384\n" \
"s_add_u32 s66, %[so0], 6815744\n" \
"buffer_load_dwordx4 %[wvo], %[rq], s66 offen lds\n" \
"s_add_u32 s66, %[hp], 7864320\n" \
"buffer_load_dword v101, v100, %[rk], s66 offen\n" \
"s_waitcnt vmcnt(19)\n" \
"s_add_u32 m0, %[wbase], 17408\n" \
"s_add_u32 s66, %[so0], 7077888\n" \
"buffer_load_dwordx4 %[wvo], %[rq], s66 offen lds\n" \
"s_waitcnt vmcnt(19)\n" \
"s_add_u32 m0, %[wbase], 18432\n" \
"s_add_u32 s66, %[so0], 6832128\n" \
"buffer_load_dwordx4 %[wvo], %[rq], s66 offen lds\n" \
"s_waitcnt vmcnt(19)\n" \
"s_add_u32 m0, %[wbase], 19456\n" \
"s_add_u32 s66, %[so0], 7094272\n" \
"buffer_load_dwordx4 %[wvo], %[rq], s66 offen lds\n" \
"s_waitcnt lgkmcnt(0)\n" \
"s_barrier\n" \
"ds_write_b128 %[wl], v[36:39] offset:4096\n" \
"ds_write_b128 %[wl], v[40:43] offset:5120\n" \
"ds_write_b128 %[wl], v[44:47] offset:6144\n" \
"ds_write_b128 %[wl], v[48:51] offset:7168\n" \
"ds_read_b64_tr_b16 v[20:21], v52 offset:8192\n" \
"ds_read_b64_tr_b16 v[22:23], v52 offset:40960\n" \
"ds_read_b64_tr_b16 v[24:25], v53 offset:8192\n" \
"ds_read_b64_tr_b16 v[26:27], v53 offset:40960\n" \
"ds_read_b64_tr_b16 v[28:29], v52 offset:8200\n" \
"ds_read_b64_tr_b16 v[30:31], v52 offset:40968\n" \
"ds_read_b64_tr_b16 v[32:33], v53 offset:8200\n" \
"ds_read_b64_tr_b16 v[34:35], v53 offset:40968\n" \
"s_waitcnt vmcnt(19)\n" \
"s_add_u32 m0, %[wbase], 20480\n" \
"s_add_u32 s66, %[so0], 6815744\n" \
"buffer_load_dwordx4 %[wvo], %[rk], s66 offen lds\n" \
"s_add_u32 s66, %[hp], 2097152\n" \
"buffer_load_dword v101, v100, %[rk], s66 offen\n" \
"s_waitcnt vmcnt(15)\n" \
"s_add_u32 m0, %[wbase], 21504\n" \
"s_add_u32 s66, %[so0], 7077888\n" \
"buffer_load_dwordx4 %[wvo], %[rk], s66 offen lds\n" \
"s_waitcnt vmcnt(13)\n" \
"s_add_u32 m0, %[wbase], 22528\n" \
"s_add_u32 s66, %[so0], 6832128\n" \
"buffer_load_dwordx4 %[wvo], %[rk], s66 offen lds\n" \
"s_waitcnt vmcnt(11)\n" \
"s_add_u32 m0, %[wbase], 23552\n" \
"s_add_u32 s66, %[so0], 7094272\n" \
"buffer_load_dwordx4 %[wvo], %[rk], s66 offen lds\n" \
"buffer_load_dword v125, %[fvo], %[ws], 0 offen sc1\n" \
"s_waitcnt lgkmcnt(0)\n" \
"s_barrier\n" \
"ds_write_b128 %[wl], v[20:23] offset:8192\n" \
"ds_write_b128 %[wl], v[24:27] offset:9216\n" \
"ds_write_b128 %[wl], v[28:31] offset:10240\n" \
"ds_write_b128 %[wl], v[32:35] offset:11264\n" \
"ds_read_b64_tr_b16 v[36:37], v52 offset:12288\n" \
"ds_read_b64_tr_b16 v[38:39], v52 offset:45056\n" \
"ds_read_b64_tr_b16 v[40:41], v53 offset:12288\n" \
"ds_read_b64_tr_b16 v[42:43], v53 offset:45056\n" \
"ds_read_b64_tr_b16 v[44:45], v52 offset:12296\n" \
"ds_read_b64_tr_b16 v[46:47], v52 offset:45064\n" \
"ds_read_b64_tr_b16 v[48:49], v53 offset:12296\n" \
"ds_read_b64_tr_b16 v[50:51], v53 offset:45064\n" \
"s_waitcnt vmcnt(10)\n" \
"s_add_u32 m0, %[wbase], 24576\n" \
"s_add_u32 s66, %[so0], 7864320\n" \
"buffer_load_dwordx4 %[wvo], %[rk], s66 offen lds\n" \
"s_waitcnt vmcnt(9)\n" \
"s_add_u32 m0, %[wbase], 25600\n" \
"s_add_u32 s66, %[so0], 8126464\n" \
"buffer_load_dwordx4 %[wvo], %[rk], s66 offen lds\n" \
"s_waitcnt vmcnt(9)\n" \
"s_add_u32 m0, %[wbase], 26624\n" \
"s_add_u32 s66, %[so0], 7880704\n" \
"buffer_load_dwordx4 %[wvo], %[rk], s66 offen lds\n" \
"s_waitcnt vmcnt(9)\n" \
"s_add_u32 m0, %[wbase], 27648\n" \
"s_add_u32 s66, %[so0], 8142848\n" \
"buffer_load_dwordx4 %[wvo], %[rk], s66 offen lds\n" \
"s_waitcnt lgkmcnt(0)\n" \
"s_barrier\n" \
"ds_write_b128 %[wl], v[36:39] offset:12288\n" \
"ds_write_b128 %[wl], v[40:43] offset:13312\n" \
"ds_write_b128 %[wl], v[44:47] offset:14336\n" \
"ds_write_b128 %[wl], v[48:51] offset:15360\n" \
"ds_read_b64_tr_b16 v[20:21], v52 offset:16384\n" \
"ds_read_b64_tr_b16 v[22:23], v52 offset:49152\n" \
"ds_read_b64_tr_b16 v[24:25], v53 offset:16384\n" \
"ds_read_b64_tr_b16 v[26:27], v53 offset:49152\n" \
"ds_read_b64_tr_b16 v[28:29], v52 offset:16392\n" \
"ds_read_b64_tr_b16 v[30:31], v52 offset:49160\n" \
"ds_read_b64_tr_b16 v[32:33], v53 offset:16392\n" \
"ds_read_b64_tr_b16 v[34:35], v53 offset:49160\n" \
"s_waitcnt vmcnt(9)\n" \
"s_add_u32 m0, %[wbase], 28672\n" \
"s_add_u32 s66, %[so0], 2097152\n" \
"buffer_load_dwordx4 %[wvo], %[rk], s66 offen lds\n" \
"s_waitcnt vmcnt(8)\n" \
"s_add_u32 m0, %[wbase], 29696\n" \
"s_add_u32 s66, %[so0], 2359296\n" \
"buffer_load_dwordx4 %[wvo], %[rk], s66 offen lds\n" \
"s_waitcnt vmcnt(8)\n" \
"s_add_u32 m0, %[wbase], 30720\n" \
"s_add_u32 s66, %[so0], 2113536\n" \
"buffer_load_dwordx4 %[wvo], %[rk], s66 offen lds\n" \
"s_waitcnt vmcnt(8)\n" \
"s_add_u32 m0, %[wbase], 31744\n" \
"s_add_u32 s66, %[so0], 2375680\n" \
"buffer_load_dwordx4 %[wvo], %[rk], s66 offen lds\n" \
"s_waitcnt vmcnt(8)\n" \
"Lqk_flag0_%=:\n" \
"v_cmp_gt_u32 vcc, s65, v125\n" \
"s_cbranch_vccnz Lqk_sflag0_%=\n" \
"s_lshr_b32 m0, %[wbase], 5\n" \
"s_nop 0\n" \
"s_add_u32 m0, m0, 139264\n" \
"s_nop 0\n" \
"buffer_load_dwordx4 %[wl], %[ws], s77 offen offset:0 sc1 lds\n" \
"s_add_u32 m0, m0, 3072\n" \
"s_add_u32 s66, s75, 4096\n" \
"buffer_load_dwordx4 %[wl], %[ws], s77 offen offset:1024 sc1 lds\n" \
"s_add_u32 m0, m0, 5120\n" \
"s_add_u32 s66, s75, 4096\n" \
"buffer_load_dwordx4 %[wl], %[ws], s75 offen offset:0 sc1 lds\n" \
"s_add_u32 m0, m0, 4096\n" \
"s_add_u32 s66, s75, 4096\n" \
"buffer_load_dwordx4 %[wl], %[ws], s66 offen offset:0 sc1 lds\n" \
"s_waitcnt lgkmcnt(0)\n" \
"s_barrier\n" \
"ds_write_b128 %[wl], v[20:23] offset:16384\n" \
"ds_write_b128 %[wl], v[24:27] offset:17408\n" \
"ds_write_b128 %[wl], v[28:31] offset:18432\n" \
"ds_write_b128 %[wl], v[32:35] offset:19456\n" \
"ds_read_b64_tr_b16 v[36:37], v52 offset:20480\n" \
"ds_read_b64_tr_b16 v[38:39], v52 offset:53248\n" \
"ds_read_b64_tr_b16 v[40:41], v53 offset:20480\n" \
"ds_read_b64_tr_b16 v[42:43], v53 offset:53248\n" \
"ds_read_b64_tr_b16 v[44:45], v52 offset:20488\n" \
"ds_read_b64_tr_b16 v[46:47], v52 offset:53256\n" \
"ds_read_b64_tr_b16 v[48:49], v53 offset:20488\n" \
"ds_read_b64_tr_b16 v[50:51], v53 offset:53256\n" \
"s_waitcnt vmcnt(8)\n" \
"s_waitcnt lgkmcnt(0)\n" \
"s_barrier\n" \
"ds_write_b128 %[wl], v[36:39] offset:20480\n" \
"ds_write_b128 %[wl], v[40:43] offset:21504\n" \
"ds_write_b128 %[wl], v[44:47] offset:22528\n" \
"ds_write_b128 %[wl], v[48:51] offset:23552\n" \
"ds_read_b64_tr_b16 v[20:21], v52 offset:24576\n" \
"ds_read_b64_tr_b16 v[22:23], v52 offset:57344\n" \
"ds_read_b64_tr_b16 v[24:25], v53 offset:24576\n" \
"ds_read_b64_tr_b16 v[26:27], v53 offset:57344\n" \
"ds_read_b64_tr_b16 v[28:29], v52 offset:24584\n" \
"ds_read_b64_tr_b16 v[30:31], v52 offset:57352\n" \
"ds_read_b64_tr_b16 v[32:33], v53 offset:24584\n" \
"ds_read_b64_tr_b16 v[34:35], v53 offset:57352\n" \
"s_waitcnt vmcnt(4)\n" \
"s_waitcnt lgkmcnt(0)\n" \
"s_barrier\n" \
"ds_write_b128 %[wl], v[20:23] offset:24576\n" \
"ds_write_b128 %[wl], v[24:27] offset:25600\n" \
"ds_write_b128 %[wl], v[28:31] offset:26624\n" \
"ds_write_b128 %[wl], v[32:35] offset:27648\n" \
"ds_read_b64_tr_b16 v[36:37], v52 offset:28672\n" \
"ds_read_b64_tr_b16 v[38:39], v52 offset:61440\n" \
"ds_read_b64_tr_b16 v[40:41], v53 offset:28672\n" \
"ds_read_b64_tr_b16 v[42:43], v53 offset:61440\n" \
"ds_read_b64_tr_b16 v[44:45], v52 offset:28680\n" \
"ds_read_b64_tr_b16 v[46:47], v52 offset:61448\n" \
"ds_read_b64_tr_b16 v[48:49], v53 offset:28680\n" \
"ds_read_b64_tr_b16 v[50:51], v53 offset:61448\n" \
"s_waitcnt lgkmcnt(0)\n" \
"s_barrier\n" \
"ds_write_b128 %[wl], v[36:39] offset:28672\n" \
"ds_write_b128 %[wl], v[40:43] offset:29696\n" \
"ds_write_b128 %[wl], v[44:47] offset:30720\n" \
"ds_write_b128 %[wl], v[48:51] offset:31744\n" \
"s_waitcnt lgkmcnt(0)\n" \
"v_mov_b32 v20, v68\n" \
"v_mov_b32 v21, v69\n" \
"v_mov_b32 v22, v70\n" \
"v_mov_b32 v23, v71\n" \
"v_mov_b32 v24, v72\n" \
"v_mov_b32 v25, v73\n" \
"v_mov_b32 v26, v74\n" \
"v_mov_b32 v27, v75\n" \
"v_mov_b32 v28, v76\n" \
"v_mov_b32 v29, v77\n" \
"v_mov_b32 v30, v78\n" \
"v_mov_b32 v31, v79\n" \
"v_mov_b32 v32, v80\n" \
"v_mov_b32 v33, v81\n" \
"v_mov_b32 v34, v82\n" \
"v_mov_b32 v35, v83\n" \
"v_mov_b32 v36, v84\n" \
"v_mov_b32 v37, v85\n" \
"v_mov_b32 v38, v86\n" \
"v_mov_b32 v39, v87\n" \
"v_mov_b32 v40, v88\n" \
"v_mov_b32 v41, v89\n" \
"v_mov_b32 v42, v90\n" \
"v_mov_b32 v43, v91\n" \
"v_mov_b32 v44, v92\n" \
"v_mov_b32 v45, v93\n" \
"v_mov_b32 v46, v94\n" \
"v_mov_b32 v47, v95\n" \
"v_mov_b32 v48, v96\n" \
"v_mov_b32 v49, v97\n" \
"v_mov_b32 v50, v98\n" \
"v_mov_b32 v51, v99\n" \
"s_waitcnt vmcnt(4)\n" \
"ds_read_b128 v[68:71], %[wl] offset:0\n" \
"ds_read_b128 v[72:75], %[wl] offset:1024\n" \
"ds_read_b128 v[76:79], %[wl] offset:2048\n" \
"ds_read_b128 v[80:83], %[wl] offset:3072\n" \
"ds_read_b128 v[84:87], %[wl] offset:4096\n" \
"ds_read_b128 v[88:91], %[wl] offset:5120\n" \
"ds_read_b128 v[92:95], %[wl] offset:6144\n" \
"ds_read_b128 v[96:99], %[wl] offset:7168\n" \
"v_mov_b32 v100, 0\n" \
"v_mov_b32 v101, 0\n" \
"v_mov_b32 v102, 0\n" \
"v_mov_b32 v103, 0\n" \
"v_mov_b32 v104, 0\n" \
"v_mov_b32 v105, 0\n" \
"v_mov_b32 v106, 0\n" \
"v_mov_b32 v107, 0\n" \
"v_mov_b32 v108, 0\n" \
"v_mov_b32 v109, 0\n" \
"v_mov_b32 v110, 0\n" \
"v_mov_b32 v111, 0\n" \
"v_mov_b32 v112, 0\n" \
"v_mov_b32 v113, 0\n" \
"v_mov_b32 v114, 0\n" \
"v_mov_b32 v115, 0\n" \
"s_memtime s[92:93]\n" \
"s_waitcnt lgkmcnt(0)\n" \
"s_sub_u32 s92, s92, 0x100000\n" \
"s_branch Lqk_first_%=\n" \
"Lqk_top_%=:\n" \
"s_nop 1\n" \
"s_mov_b32 s72, s62\n" \
"s_add_u32 s62, s62, 524288\n" \
"s_and_b32 s62, s62, 2097151\n" \
"s_bitset1_b32 s62, 21\n" \
"s_mov_b32 s68, s62\n" \
"s_add_u32 s69, s68, 16384\n" \
"s_add_u32 s70, s68, 32768\n" \
"s_add_u32 s71, s68, 49152\n" \
"s_add_u32 s63, s62, %[hb]\n" \
"s_add_u32 s73, s62, %[ovm]\n" \
"s_waitcnt vmcnt(4)\n" \
"Lqk_db_%=:\n" \
"v_cmp_eq_u32 vcc, -1, v64\n" \
"s_cbranch_vccnz Lqk_sdb_%=\n" \
"v_subrev_u32 v64, s67, v64\n" \
"s_nop 1\n" \
"v_max_i32_dpp v64, v64, v64 quad_perm:[1,0,3,2] row_mask:0xf bank_mask:0xf\n" \
"s_nop 1\n" \
"v_max_i32_dpp v64, v64, v64 quad_perm:[2,3,0,1] row_mask:0xf bank_mask:0xf\n" \
"s_nop 1\n" \
"v_max_i32_dpp v64, v64, v64 row_half_mirror row_mask:0xf bank_mask:0xf\n" \
"s_nop 1\n" \
"v_max_i32_dpp v64, v64, v64 row_mirror row_mask:0xf bank_mask:0xf\n" \
"v_mov_b32 v65, v64\n" \
"s_nop 1\n" \
"v_permlane16_swap_b32 v64, v65\n" \
"v_max_i32 v64, v64, v65\n" \
"v_mov_b32 v65, v64\n" \
"s_nop 1\n" \
"v_permlane32_swap_b32 v64, v65\n" \
"v_max_i32 v64, v64, v65\n" \
"s_nop 0\n" \
"v_readfirstlane_b32 s94, v64\n" \
"s_min_i32 s94, s94, 1000\n" \
"s_max_i32 s94, s94, -1000\n" \
"s_mul_i32 s94, s94, 23\n" \
"s_ashr_i32 s93, s94, 1\n" \
"s_add_u32 s92, s92, s93\n" \
"v_mov_b32 v116, -1\n" \
"v_mov_b32 v117, -1\n" \
"v_mov_b32 v118, -1\n" \
"v_mov_b32 v119, -1\n" \
"s_waitcnt vmcnt(3)\n" \
"Lqk_flag_%=:\n" \
"v_cmp_gt_u32 vcc, s65, v125\n" \
"s_cbranch_vccnz Lqk_sflag_%=\n" \
"s_waitcnt lgkmcnt(0)\n" \
"Lqk_d1a_%=:\n" \
"s_memtime s[94:95]\n" \
"s_waitcnt lgkmcnt(0)\n" \
"s_sub_u32 s66, s92, s94\n" \
"s_add_u32 s66, s66, %[d1]\n" \
"s_cmp_gt_i32 s66, 200\n" \
"s_cbranch_scc1 Lqk_d1a_%=\n" \
"s_max_i32 s66, s66, 0\n" \
"Lqk_d1_%=:\n" \
"s_sub_u32 s66, s66, 28\n" \
"s_cbranch_scc0 Lqk_d1_%=\n" \
"s_lshr_b32 m0, %[wbase], 5\n" \
"s_nop 0\n" \
"s_add_u32 m0, m0, 139264\n" \
"s_nop 0\n" \
"buffer_load_dwordx4 %[wl], %[ws], s77 offen offset:0 sc1 lds\n" \
"s_add_u32 m0, m0, 3072\n" \
"s_add_u32 s66, s75, 4096\n" \
"buffer_load_dwordx4 %[wl], %[ws], s77 offen offset:1024 sc1 lds\n" \
"s_add_u32 m0, m0, 5120\n" \
"s_add_u32 s66, s75, 4096\n" \
"buffer_load_dwordx4 %[wl], %[ws], s75 offen offset:0 sc1 lds\n" \
"s_add_u32 m0, m0, 4096\n" \
"s_add_u32 s66, s75, 4096\n" \
"buffer_load_dwordx4 %[wl], %[ws], s66 offen offset:0 sc1 lds\n" \
"s_xor_b32 s66, s62, 1048576\n" \
"s_add_u32 s66, s66, %[op0]\n" \
"buffer_store_dwordx4 v[116:119], %[wl], %[ws], s66 offen sc1\n" \
"s_xor_b32 s66, s62, 1048576\n" \
"s_add_u32 s66, s66, %[op1]\n" \
"buffer_store_dwordx4 v[116:119], %[wl], %[ws], s66 offen sc1\n" \
".p2align 6\n" \
"s_waitcnt lgkmcnt(0)\n" \
"Lqk_d0a_%=:\n" \
"s_memtime s[94:95]\n" \
"s_waitcnt lgkmcnt(0)\n" \
"s_sub_u32 s66, s92, s94\n" \
"s_add_u32 s66, s66, %[d0]\n" \
"s_cmp_gt_i32 s66, 200\n" \
"s_cbranch_scc1 Lqk_d0a_%=\n" \
"s_max_i32 s66, s66, 0\n" \
"Lqk_d0_%=:\n" \
"s_sub_u32 s66, s66, 28\n" \
"s_cbranch_scc0 Lqk_d0_%=\n" \
"Lqk_go_%=:\n" \
"buffer_load_dwordx4 v[20:23], %[mvo], %[ws], s68 offen sc1\n" \
"s_nop 0\n" \
"buffer_load_dwordx4 v[24:27], %[mvo], %[ws], s68 offen offset:512 sc1\n" \
"s_nop 0\n" \
"s_nop 7\n" \
"s_nop 1\n" \
"buffer_load_dwordx4 v[28:31], %[mvo], %[ws], s69 offen sc1\n" \
"s_nop 0\n" \
"buffer_load_dwordx4 v[32:35], %[mvo], %[ws], s69 offen offset:512 sc1\n" \
"s_nop 0\n" \
"s_nop 7\n" \
"s_nop 1\n" \
"buffer_load_dwordx4 v[36:39], %[mvo], %[ws], s70 offen sc1\n" \
"s_nop 0\n" \
"buffer_load_dwordx4 v[40:43], %[mvo], %[ws], s70 offen offset:512 sc1\n" \
"s_nop 0\n" \
"s_nop 7\n" \
"s_nop 1\n" \
"buffer_load_dwordx4 v[44:47], %[mvo], %[ws], s71 offen sc1\n" \
"s_nop 0\n" \
"buffer_load_dwordx4 v[48:51], %[mvo], %[ws], s71 offen offset:512 sc1\n" \
"s_nop 0\n" \
"Lqk_first_%=:\n" \
"s_waitcnt vmcnt(7)\n" \
"v_mfma_f32_16x16x32_bf16 v[116:119], %[sel], v[20:23], 0\n" \
"s_nop 6\n" \
"s_waitcnt vmcnt(6)\n" \
"v_mfma_f32_16x16x32_bf16 v[120:123], %[sel], v[24:27], 0\n" \
"v_cvt_pk_bf16_f32 v116, v116, v117\n" \
"v_cvt_pk_bf16_f32 v117, v118, v119\n" \
"s_nop 5\n" \
"v_cvt_pk_bf16_f32 v118, v120, v121\n" \
"v_cvt_pk_bf16_f32 v119, v122, v123\n" \
"ds_write_b128 %[stw], v[116:119] offset:0\n" \
"v_cmp_u_f32 vcc, v116, v119\n" \
"s_cbranch_vccnz Lqk_s0_%=\n" \
"Lqk_c0_%=:\n" \
"s_waitcnt lgkmcnt(0)\n" \
"s_barrier\n" \
"ds_read_b128 v[52:55], %[str] offset:0\n" \
"ds_read_b128 v[56:59], %[str] offset:1024\n" \
"ds_read_b128 v[60:63], %[str] offset:2048\n" \
"ds_read_b128 v[64:67], %[str] offset:3072\n" \
"s_waitcnt vmcnt(5)\n" \
"v_mfma_f32_16x16x32_bf16 v[116:119], %[sel], v[28:31], 0\n" \
"s_waitcnt vmcnt(4)\n" \
"s_nop 1\n" \
"v_mfma_f32_16x16x32_bf16 v[120:123], %[sel], v[32:35], 0\n" \
"s_waitcnt lgkmcnt(3)\n" \
"s_nop 1\n" \
"v_smfmac_f32_16x16x64_bf16 v[108:111], v[52:55], v[68:75], %[idx]\n" \
"s_nop 0\n" \
"ds_read_b128 v[68:71], %[wl] offset:8192\n" \
"v_cvt_pk_bf16_f32 v116, v116, v117\n" \
"v_smfmac_f32_16x16x64_bf16 v[112:115], v[52:55], v[76:83], %[idx]\n" \
"s_nop 0\n" \
"ds_read_b128 v[72:75], %[wl] offset:9216\n" \
"v_cvt_pk_bf16_f32 v117, v118, v119\n" \
"v_smfmac_f32_16x16x64_bf16 v[100:103], v[52:55], v[84:91], %[idx]\n" \
"s_nop 0\n" \
"ds_read_b128 v[76:79], %[wl] offset:10240\n" \
"v_cvt_pk_bf16_f32 v118, v120, v121\n" \
"v_smfmac_f32_16x16x64_bf16 v[104:107], v[52:55], v[92:99], %[idx]\n" \
"s_waitcnt lgkmcnt(5)\n" \
"ds_read_b128 v[80:83], %[wl] offset:11264\n" \
"v_cvt_pk_bf16_f32 v119, v122, v123\n" \
"v_smfmac_f32_16x16x64_bf16 v[108:111], v[56:59], a[16:23], %[idx]\n" \
"s_nop 0\n" \
"ds_write_b128 %[stw], v[116:119] offset:4096\n" \
"v_cmp_u_f32 vcc, v116, v119\n" \
"v_smfmac_f32_16x16x64_bf16 v[112:115], v[56:59], a[24:31], %[idx]\n" \
"s_nop 0\n" \
"ds_read_b128 v[84:87], %[wl] offset:12288\n" \
"ds_read_b128 v[88:91], %[wl] offset:13312\n" \
"v_smfmac_f32_16x16x64_bf16 v[100:103], v[56:59], a[0:7], %[idx]\n" \
"s_waitcnt vmcnt(3)\n" \
"s_cbranch_vccnz Lqk_s1_%=\n" \
"Lqk_c1_%=:\n" \
"v_mfma_f32_16x16x32_bf16 v[116:119], %[sel], v[36:39], 0\n" \
"s_waitcnt vmcnt(2)\n" \
"ds_read_b128 v[28:31], %[stw] offset:8192\n" \
"ds_read_b128 v[92:95], %[wl] offset:14336\n" \
"v_mfma_f32_16x16x32_bf16 v[120:123], %[sel], v[40:43], 0\n" \
"s_waitcnt lgkmcnt(4)\n" \
"s_barrier\n" \
"v_smfmac_f32_16x16x64_bf16 v[104:107], v[56:59], a[8:15], %[idx]\n" \
"s_nop 0\n" \
"ds_read_b128 v[52:55], %[str] offset:4096\n" \
"v_cvt_pk_bf16_f32 v116, v116, v117\n" \
"v_smfmac_f32_16x16x64_bf16 v[108:111], v[60:63], a[32:39], %[idx]\n" \
"s_nop 0\n" \
"ds_read_b128 v[56:59], %[str] offset:5120\n" \
"v_cvt_pk_bf16_f32 v117, v118, v119\n" \
"v_smfmac_f32_16x16x64_bf16 v[112:115], v[60:63], a[40:47], %[idx]\n" \
"s_nop 0\n" \
"ds_read_b128 v[96:99], %[wl] offset:15360\n" \
"v_cvt_pk_bf16_f32 v118, v120, v121\n" \
"v_smfmac_f32_16x16x64_bf16 v[100:103], v[60:63], v[68:75], %[idx]\n" \
"s_nop 0\n" \
"ds_read_b128 v[68:71], %[wl] offset:16384\n" \
"v_cvt_pk_bf16_f32 v119, v122, v123\n" \
"v_smfmac_f32_16x16x64_bf16 v[104:107], v[60:63], v[76:83], %[idx]\n" \
"s_nop 0\n" \
"ds_write_b128 %[stw], v[116:119] offset:8192\n" \
"v_cmp_u_f32 vcc, v116, v119\n" \
"v_smfmac_f32_16x16x64_bf16 v[108:111], v[64:67], a[64:71], %[idx]\n" \
"s_nop 0\n" \
"ds_read_b128 v[60:63], %[str] offset:6144\n" \
"ds_read_b128 v[72:75], %[wl] offset:17408\n" \
"v_smfmac_f32_16x16x64_bf16 v[112:115], v[64:67], a[72:79], %[idx]\n" \
"s_waitcnt vmcnt(1)\n" \
"s_cbranch_vccnz Lqk_s2_%=\n" \
"Lqk_c2_%=:\n" \
"v_mfma_f32_16x16x32_bf16 v[116:119], %[sel], v[44:47], 0\n" \
"s_waitcnt vmcnt(0)\n" \
"ds_read_b128 v[32:35], %[stw] offset:12288\n" \
"ds_read_b128 v[64:67], %[str] offset:7168\n" \
"v_mfma_f32_16x16x32_bf16 v[120:123], %[sel], v[48:51], 0\n" \
"s_waitcnt lgkmcnt(4)\n" \
"s_barrier\n" \
"v_smfmac_f32_16x16x64_bf16 v[108:111], v[52:55], a[80:87], %[idx]\n" \
"s_nop 0\n" \
"ds_read_b128 v[76:79], %[wl] offset:18432\n" \
"v_cvt_pk_bf16_f32 v116, v116, v117\n" \
"v_smfmac_f32_16x16x64_bf16 v[112:115], v[52:55], a[88:95], %[idx]\n" \
"s_nop 0\n" \
"ds_read_b128 v[52:55], %[str] offset:8192\n" \
"v_cvt_pk_bf16_f32 v117, v118, v119\n" \
"v_smfmac_f32_16x16x64_bf16 v[108:111], v[56:59], a[112:119], %[idx]\n" \
"s_nop 0\n" \
"ds_read_b128 v[80:83], %[wl] offset:19456\n" \
"v_cvt_pk_bf16_f32 v118, v120, v121\n" \
"v_smfmac_f32_16x16x64_bf16 v[112:115], v[56:59], a[120:127], %[idx]\n" \
"s_waitcnt lgkmcnt(6)\n" \
"ds_read_b128 v[56:59], %[str] offset:9216\n" \
"v_cvt_pk_bf16_f32 v119, v122, v123\n" \
"v_smfmac_f32_16x16x64_bf16 v[108:111], v[60:63], a[144:151], %[idx]\n" \
"s_nop 0\n" \
"ds_write_b128 %[stw], v[116:119] offset:12288\n" \
"v_cmp_u_f32 vcc, v116, v119\n" \
"v_smfmac_f32_16x16x64_bf16 v[112:115], v[60:63], a[152:159], %[idx]\n" \
"s_waitcnt lgkmcnt(5)\n" \
"ds_read_b128 v[60:63], %[str] offset:10240\n" \
"v_smfmac_f32_16x16x64_bf16 v[108:111], v[64:67], a[176:183], %[idx]\n" \
"s_cbranch_vccnz Lqk_s3_%=\n" \
"Lqk_c3_%=:\n" \
"v_smfmac_f32_16x16x64_bf16 v[112:115], v[64:67], a[184:191], %[idx]\n" \
"s_waitcnt lgkmcnt(4)\n" \
"ds_read_b128 v[64:67], %[str] offset:11264\n" \
"v_smfmac_f32_16x16x64_bf16 v[108:111], v[52:55], v[84:91], %[idx]\n" \
"s_waitcnt lgkmcnt(2)\n" \
"s_barrier\n" \
"v_smfmac_f32_16x16x64_bf16 v[112:115], v[52:55], v[92:99], %[idx]\n" \
"s_nop 0\n" \
"ds_read_b128 v[52:55], %[str] offset:12288\n" \
"ds_read_b128 v[84:87], %[wl] offset:20480\n" \
"v_smfmac_f32_16x16x64_bf16 v[108:111], v[56:59], a[224:231], %[idx]\n" \
"s_nop 0\n" \
"ds_read_b128 v[88:91], %[wl] offset:21504\n" \
"v_smfmac_f32_16x16x64_bf16 v[112:115], v[56:59], a[232:239], %[idx]\n" \
"s_waitcnt lgkmcnt(4)\n" \
"ds_read_b128 v[56:59], %[str] offset:13312\n" \
"ds_read_b128 v[92:95], %[wl] offset:22528\n" \
"v_smfmac_f32_16x16x64_bf16 v[108:111], v[60:63], v[128:135], %[idx]\n" \
"s_nop 0\n" \
"ds_read_b128 v[96:99], %[wl] offset:23552\n" \
"v_smfmac_f32_16x16x64_bf16 v[112:115], v[60:63], v[136:143], %[idx]\n" \
"s_waitcnt lgkmcnt(6)\n" \
"ds_read_b128 v[60:63], %[str] offset:14336\n" \
"ds_read_b128 v[20:23], %[stw] offset:16384\n" \
"v_smfmac_f32_16x16x64_bf16 v[108:111], v[64:67], v[160:167], %[idx]\n" \
"s_nop 0\n" \
"ds_read_b128 v[24:27], %[stw] offset:20480\n" \
"v_smfmac_f32_16x16x64_bf16 v[112:115], v[64:67], v[168:175], %[idx]\n" \
"s_waitcnt lgkmcnt(8)\n" \
"ds_read_b128 v[64:67], %[str] offset:15360\n" \
"v_smfmac_f32_16x16x64_bf16 v[108:111], v[52:55], v[192:199], %[idx]\n" \
"s_nop 2\n" \
"v_smfmac_f32_16x16x64_bf16 v[112:115], v[52:55], v[200:207], %[idx]\n" \
"s_waitcnt lgkmcnt(6)\n" \
"s_memrealtime s[94:95]\n" \
"s_memtime s[92:93]\n" \
"v_smfmac_f32_16x16x64_bf16 v[108:111], v[56:59], v[68:75], %[idx]\n" \
"s_nop 2\n" \
"v_smfmac_f32_16x16x64_bf16 v[112:115], v[56:59], v[76:83], %[idx]\n" \
"s_waitcnt lgkmcnt(3)\n" \
"s_add_u32 s66, s62, %[odb]\n" \
"s_nop 0\n" \
"v_smfmac_f32_16x16x64_bf16 v[108:111], v[60:63], v[224:231], %[idx]\n" \
"s_waitcnt lgkmcnt(0)\n" \
"s_lshl_b32 s67, s94, 1\n" \
"v_mov_b32 v124, s67\n" \
"v_smfmac_f32_16x16x64_bf16 v[108:111], v[64:67], v[240:247], %[idx]\n" \
"s_nop 0\n" \
"ds_read_b128 v[68:71], %[wl] offset:24576\n" \
"ds_read_b128 v[72:75], %[wl] offset:25600\n" \
"v_smfmac_f32_16x16x64_bf16 v[112:115], v[60:63], v[232:239], %[idx]\n" \
"s_mov_b64 exec, 1\n" \
"buffer_store_dword v124, off, %[ws], s66 sc1\n" \
"s_mov_b64 exec, -1\n" \
"v_smfmac_f32_16x16x64_bf16 v[112:115], v[64:67], v[248:255], %[idx]\n" \
"s_nop 0\n" \
"ds_read_b128 v[76:79], %[wl] offset:26624\n" \
"v_cvt_pk_bf16_f32 v36, v108, v109\n" \
"v_smfmac_f32_16x16x64_bf16 v[100:103], v[52:55], v[176:183], %[idx]\n" \
"s_nop 0\n" \
"ds_read_b128 v[80:83], %[wl] offset:27648\n" \
"v_cvt_pk_bf16_f32 v37, v110, v111\n" \
"v_smfmac_f32_16x16x64_bf16 v[104:107], v[52:55], v[184:191], %[idx]\n" \
"s_nop 0\n" \
"ds_read_b128 v[52:55], %[str] offset:3072\n" \
"v_cvt_pk_bf16_f32 v38, v112, v113\n" \
"v_smfmac_f32_16x16x64_bf16 v[100:103], v[56:59], v[84:91], %[idx]\n" \
"s_nop 0\n" \
"ds_read_b128 v[84:87], %[wl] offset:28672\n" \
"v_cvt_pk_bf16_f32 v39, v114, v115\n" \
"v_mfma_f32_16x16x16_bf16 v[108:111], v[36:37], %[selT], 0\n" \
"s_nop 0\n" \
"ds_read_b128 v[88:91], %[wl] offset:29696\n" \
"v_mfma_f32_16x16x16_bf16 v[112:115], v[38:39], %[selT], 0\n" \
"s_nop 2\n" \
"v_smfmac_f32_16x16x64_bf16 v[104:107], v[56:59], v[92:99], %[idx]\n" \
"s_nop 0\n" \
"ds_read_b128 v[56:59], %[str] offset:4096\n" \
"v_cvt_pk_bf16_f32 v40, v108, v109\n" \
"v_smfmac_f32_16x16x64_bf16 v[100:103], v[60:63], v[208:215], %[idx]\n" \
"s_nop 0\n" \
"ds_read_b128 v[92:95], %[wl] offset:30720\n" \
"v_cvt_pk_bf16_f32 v41, v110, v111\n" \
"v_smfmac_f32_16x16x64_bf16 v[104:107], v[60:63], v[216:223], %[idx]\n" \
"s_waitcnt lgkmcnt(7)\n" \
"ds_read_b128 v[96:99], %[wl] offset:31744\n" \
"v_cvt_pk_bf16_f32 v42, v112, v113\n" \
"v_smfmac_f32_16x16x64_bf16 v[100:103], v[64:67], v[68:75], %[idx]\n" \
"s_waitcnt lgkmcnt(6)\n" \
"ds_read_b128 v[60:63], %[str] offset:5120\n" \
"v_cvt_pk_bf16_f32 v43, v114, v115\n" \
"v_smfmac_f32_16x16x64_bf16 v[104:107], v[64:67], v[76:83], %[idx]\n" \
"s_nop 0\n" \
"ds_read_b128 v[64:67], %[str] offset:6144\n" \
"v_mfma_f32_16x16x32_bf16 v[116:119], v[20:23], v[40:43], 0\n" \
"s_nop 2\n" \
"v_mfma_f32_16x16x32_bf16 v[120:123], v[24:27], v[40:43], 0\n" \
"s_waitcnt lgkmcnt(7)\n" \
"s_nop 1\n" \
"v_smfmac_f32_16x16x64_bf16 v[100:103], v[52:55], a[48:55], %[idx]\n" \
"s_nop 0\n" \
"ds_write_b128 %[stw], v[116:119] offset:16384\n" \
"v_smfmac_f32_16x16x64_bf16 v[104:107], v[52:55], a[56:63], %[idx]\n" \
"s_waitcnt lgkmcnt(5)\n" \
"ds_write_b128 %[stw], v[120:123] offset:20480\n" \
"v_smfmac_f32_16x16x64_bf16 v[100:103], v[56:59], v[84:91], %[idx]\n" \
"s_waitcnt lgkmcnt(4)\n" \
"ds_read_b128 v[52:55], %[str] offset:7168\n" \
"v_smfmac_f32_16x16x64_bf16 v[104:107], v[56:59], v[92:99], %[idx]\n" \
"s_waitcnt lgkmcnt(4)\n" \
"ds_read_b128 v[56:59], %[str] offset:8192\n" \
"v_smfmac_f32_16x16x64_bf16 v[100:103], v[60:63], a[96:103], %[idx]\n" \
"s_nop 0\n" \
"buffer_load_dwordx2 v[116:117], %[vmo], %[ws], s73 offen sc1\n" \
"v_mov_b32 v120, 0x3f803f80\n" \
"v_smfmac_f32_16x16x64_bf16 v[104:107], v[60:63], a[104:111], %[idx]\n" \
"s_waitcnt lgkmcnt(4)\n" \
"ds_read_b128 v[60:63], %[str] offset:9216\n" \
"v_mov_b32 v121, 0x3f803f80\n" \
"v_smfmac_f32_16x16x64_bf16 v[100:103], v[64:67], a[128:135], %[idx]\n" \
"s_waitcnt lgkmcnt(3)\n" \
"s_barrier\n" \
"v_smfmac_f32_16x16x64_bf16 v[104:107], v[64:67], a[136:143], %[idx]\n" \
"s_waitcnt lgkmcnt(2)\n" \
"ds_read_b128 v[84:87], %[xr] offset:0\n" \
"ds_read_b128 v[88:91], %[xr] offset:1024\n" \
"v_smfmac_f32_16x16x64_bf16 v[100:103], v[52:55], a[160:167], %[idx]\n" \
"s_nop 0\n" \
"ds_read_b128 v[92:95], %[xr] offset:2048\n" \
"ds_read_b128 v[96:99], %[xr] offset:3072\n" \
"v_smfmac_f32_16x16x64_bf16 v[104:107], v[52:55], a[168:175], %[idx]\n" \
"s_waitcnt lgkmcnt(5)\n" \
"ds_read_b128 v[64:67], %[str] offset:10240\n" \
"v_mov_b32 v122, 0x3f803f80\n" \
"v_smfmac_f32_16x16x64_bf16 v[100:103], v[56:59], a[192:199], %[idx]\n" \
"s_nop 0\n" \
"ds_read_b128 v[52:55], %[str] offset:11264\n" \
"v_mov_b32 v123, 0x3f803f80\n" \
"v_smfmac_f32_16x16x64_bf16 v[104:107], v[56:59], a[200:207], %[idx]\n" \
"s_waitcnt lgkmcnt(6)\n" \
"s_nop 1\n" \
"v_smfmac_f32_16x16x64_bf16 v[100:103], v[60:63], a[208:215], %[idx]\n" \
"s_waitcnt lgkmcnt(4)\n" \
"buffer_load_dwordx2 v[116:117], %[vmo], %[ws], s73 offen sc1\n" \
"v_add_f32 v84, v84, v88\n" \
"v_smfmac_f32_16x16x64_bf16 v[104:107], v[60:63], a[216:223], %[idx]\n" \
"s_waitcnt lgkmcnt(1)\n" \
"v_add_f32 v85, v85, v89\n" \
"v_smfmac_f32_16x16x64_bf16 v[100:103], v[64:67], a[240:247], %[idx]\n" \
"s_waitcnt lgkmcnt(0)\n" \
"v_add_f32 v86, v86, v90\n" \
"v_smfmac_f32_16x16x64_bf16 v[100:103], v[52:55], v[144:151], %[idx]\n" \
"s_nop 1\n" \
"v_add_f32 v87, v87, v91\n" \
"v_smfmac_f32_16x16x64_bf16 v[104:107], v[64:67], a[248:255], %[idx]\n" \
"s_nop 1\n" \
"v_add_f32 v92, v92, v96\n" \
"v_smfmac_f32_16x16x64_bf16 v[104:107], v[52:55], v[152:159], %[idx]\n" \
"s_nop 1\n" \
"v_cvt_pk_bf16_f32 v44, v100, v101\n" \
"v_cvt_pk_bf16_f32 v45, v102, v103\n" \
"v_add_f32 v93, v93, v97\n" \
"v_add_f32 v94, v94, v98\n" \
"v_mfma_f32_16x16x16_bf16 v[100:103], v[44:45], %[selT], 0\n" \
"v_add_f32 v95, v95, v99\n" \
"v_cvt_pk_bf16_f32 v46, v104, v105\n" \
"v_cvt_pk_bf16_f32 v47, v106, v107\n" \
"v_add_f32 v84, v84, v92\n" \
"v_add_f32 v85, v85, v93\n" \
"v_mfma_f32_16x16x16_bf16 v[104:107], v[46:47], %[selT], 0\n" \
"s_nop 0\n" \
"v_add_f32 v86, v86, v94\n" \
"v_add_f32 v87, v87, v95\n" \
"v_max3_f32 v126, v84, v85, v86\n" \
"v_cvt_pk_bf16_f32 v48, v100, v101\n" \
"v_cvt_pk_bf16_f32 v49, v102, v103\n" \
"v_max_f32 v126, v126, v87\n" \
"s_nop 1\n" \
"v_max_f32_dpp v114, v126, v126 quad_perm:[2,3,0,1] row_mask:0xf bank_mask:0xf\n" \
"v_cvt_pk_bf16_f32 v50, v104, v105\n" \
"v_cvt_pk_bf16_f32 v51, v106, v107\n" \
"v_max_f32_dpp v115, v126, v126 quad_perm:[2,3,0,1] row_mask:0xf bank_mask:0xf\n" \
"s_nop 1\n" \
"v_permlane16_swap_b32 v114, v115\n" \
"v_mfma_f32_16x16x32_bf16 v[76:79], v[48:51], v[40:43], 0\n" \
"s_nop 0\n" \
"buffer_load_dwordx2 v[116:117], %[vmo], %[ws], s73 offen sc1\n" \
"v_max_f32 v126, v114, v115\n" \
"v_max_f32 v115, v114, v115\n" \
"s_nop 1\n" \
"v_permlane32_swap_b32 v126, v115\n" \
"v_max_f32 v126, v126, v115\n" \
"s_nop 1\n" \
"ds_write_b64 %[vmo], v[76:77]\n" \
"v_mul_f32 v126, s78, v126\n" \
"v_fma_f32 v108, v84, s78, -v126\n" \
"v_fma_f32 v109, v85, s78, -v126\n" \
"v_fma_f32 v110, v86, s78, -v126\n" \
"v_fma_f32 v111, v87, s78, -v126\n" \
"v_exp_f32 v108, v108\n" \
"v_exp_f32 v109, v109\n" \
"s_waitcnt lgkmcnt(0)\n" \
"s_barrier\n" \
"ds_read2st64_b32 v[80:81], %[ma] offset0:0 offset1:2\n" \
"ds_read2st64_b32 v[82:83], %[ma] offset0:4 offset1:6\n" \
"v_exp_f32 v110, v110\n" \
"v_exp_f32 v111, v111\n" \
"v_cvt_pk_bf16_f32 v36, v108, v109\n" \
"v_cvt_pk_bf16_f32 v37, v110, v111\n" \
"s_nop 0\n" \
"v_mov_b32_dpp v38, v36 quad_perm:[2,3,0,1] row_mask:0xf bank_mask:0xf\n" \
"v_mov_b32_dpp v39, v37 quad_perm:[2,3,0,1] row_mask:0xf bank_mask:0xf\n" \
"s_nop 1\n" \
"v_mfma_f32_16x16x32_bf16 v[60:63], v[120:123], v[36:39], 0\n" \
"s_nop 0\n" \
"v_mov_b32 v45, 0\n" \
"v_mov_b32 v46, 0\n" \
"v_mfma_f32_16x16x32_bf16 v[52:55], v[28:31], v[36:39], 0\n" \
"s_waitcnt lgkmcnt(1)\n" \
"v_add_f32 v80, v80, v81\n" \
"s_waitcnt lgkmcnt(0)\n" \
"v_add_f32 v82, v82, v83\n" \
"v_mfma_f32_16x16x32_bf16 v[56:59], v[32:35], v[36:39], 0\n" \
"s_nop 0\n" \
"v_add_f32 v80, v80, v82\n" \
"v_fma_f32 v115, v80, s78, -v126\n" \
"v_exp_f32 v112, v115\n" \
"s_nop 1\n" \
"v_mov_b32_dpp v113, v112 quad_perm:[2,3,0,1] row_mask:0xf bank_mask:0xf\n" \
"v_cvt_pk_bf16_f32 v44, v112, v113\n" \
"v_cvt_pk_bf16_f32 v47, v112, v113\n" \
"s_nop 0\n" \
"v_mfma_f32_16x16x16_bf16 v[60:63], v[120:121], v[44:45], v[60:63]\n" \
"s_waitcnt vmcnt(2)\n" \
"v_max_u32 v124, v116, v117\n" \
"v_fma_f32 v124, v124, 0, v115\n" \
"v_mfma_f32_16x16x16_bf16 v[52:55], v[116:117], v[44:45], v[52:55]\n" \
"s_nop 0\n" \
"v_cmp_nge_f32 vcc, 0x41f00000, v124\n" \
"v_mfma_f32_16x16x16_bf16 v[56:59], v[116:117], v[46:47], v[56:59]\n" \
"s_cbranch_vccnz Lqk_sv_%=\n" \
"Lqk_cm_%=:\n" \
"v_rcp_f32 v60, v60\n" \
"s_nop 2\n" \
"v_mul_f32 v52, v52, v60\n" \
"v_mul_f32 v53, v53, v60\n" \
"v_mul_f32 v54, v54, v60\n" \
"v_mul_f32 v55, v55, v60\n" \
"v_cvt_pk_bf16_f32 v52, v52, v53\n" \
"v_cvt_pk_bf16_f32 v53, v54, v55\n" \
"v_pk_mul_f32 v[56:57], v[56:57], v[60:61] op_sel_hi:[1,0]\n" \
"v_pk_mul_f32 v[58:59], v[58:59], v[60:61] op_sel_hi:[1,0]\n" \
"v_cvt_pk_bf16_f32 v54, v56, v57\n" \
"v_cvt_pk_bf16_f32 v55, v58, v59\n" \
"buffer_store_dwordx4 v[52:55], %[omo], %[ws], s63 offen sc1\n" \
"Lqk_done_%=:\n" \
"v_bfe_u32 v65, %[wl], 4, 6\n" \
"s_add_u32 s66, s62, 327680\n" \
"v_mul_u32_u24 v65, 16, v65\n" \
"buffer_load_dword v64, v65, %[ws], s66 offen sc1\n" \
"buffer_load_dword v125, %[fvo], %[ws], 0 offen sc1\n" \
"buffer_store_dwordx4 v[48:51], %[kmo], %[ws], s74 offen sc1\n" \
"s_add_u32 s66, s74, %[dvm]\n" \
"buffer_store_dwordx2 v[116:117], %[vmo], %[ws], s66 offen sc1\n" \
"v_mov_b32 v124, s65\n" \
"s_mov_b64 exec, 1\n" \
"buffer_store_dword v124, off, %[ws], %[fo] sc1\n" \
"s_mov_b64 exec, -1\n" \
"ds_read_b128 v[68:71], %[wl] offset:0\n" \
"ds_read_b128 v[72:75], %[wl] offset:1024\n" \
"ds_read_b128 v[76:79], %[wl] offset:2048\n" \
"ds_read_b128 v[80:83], %[wl] offset:3072\n" \
"ds_read_b128 v[84:87], %[wl] offset:4096\n" \
"ds_read_b128 v[88:91], %[wl] offset:5120\n" \
"ds_read_b128 v[92:95], %[wl] offset:6144\n" \
"ds_read_b128 v[96:99], %[wl] offset:7168\n" \
"v_mov_b32 v100, 0\n" \
"v_mov_b32 v101, 0\n" \
"v_mov_b32 v102, 0\n" \
"v_mov_b32 v103, 0\n" \
"v_mov_b32 v104, 0\n" \
"v_mov_b32 v105, 0\n" \
"v_mov_b32 v106, 0\n" \
"v_mov_b32 v107, 0\n" \
"v_mov_b32 v108, 0\n" \
"v_mov_b32 v109, 0\n" \
"v_mov_b32 v110, 0\n" \
"v_mov_b32 v111, 0\n" \
"v_mov_b32 v112, 0\n" \
"v_mov_b32 v113, 0\n" \
"v_mov_b32 v114, 0\n" \
"v_mov_b32 v115, 0\n" \
"s_add_u32 s74, s74, 65536\n" \
"s_add_u32 s65, s65, 1\n" \
"s_add_u32 s75, s75, 262144\n" \
"s_add_u32 s77, s77, 262144\n" \
"s_waitcnt lgkmcnt(0)\n" \
"s_cmp_le_u32 s65, 64\n" \
"s_cbranch_scc1 Lqk_top_%=\n" \
"s_endpgm\n" \
"Lqk_shs_%=:\n" \
"s_sub_u32 s64, s64, 1\n" \
"s_cbranch_scc1 Lqk_dead_%=\n" \
"s_sleep 8\n" \
"buffer_load_dword v125, off, %[ws], %[hs] sc1\n" \
"s_waitcnt vmcnt(0)\n" \
"s_branch Lqk_hs_%=\n" \
"Lqk_sflag0_%=:\n" \
"s_sub_u32 s64, s64, 1\n" \
"s_cbranch_scc1 Lqk_dead_%=\n" \
"buffer_load_dword v125, %[fvo], %[ws], 0 offen sc1\n" \
"s_waitcnt vmcnt(0)\n" \
"s_branch Lqk_flag0_%=\n" \
"Lqk_sdb_%=:\n" \
"s_sub_u32 s64, s64, 1\n" \
"s_cbranch_scc1 Lqk_dead_%=\n" \
"s_sleep 1\n" \
"v_bfe_u32 v65, %[wl], 4, 6\n" \
"s_add_u32 s66, s72, 327680\n" \
"v_mul_u32_u24 v65, 16, v65\n" \
"buffer_load_dword v64, v65, %[ws], s66 offen sc1\n" \
"s_waitcnt vmcnt(0)\n" \
"s_branch Lqk_db_%=\n" \
"Lqk_sflag_%=:\n" \
"s_sub_u32 s64, s64, 1\n" \
"s_cbranch_scc1 Lqk_dead_%=\n" \
"buffer_load_dword v125, %[fvo], %[ws], 0 offen sc1\n" \
"s_waitcnt vmcnt(0)\n" \
"s_branch Lqk_flag_%=\n" \
"Lqk_s0_%=:\n" \
"s_nop 7\n" \
"s_nop 7\n" \
"v_max3_u32 v124, v20, v21, v22\n" \
"v_max3_u32 v124, v124, v23, v24\n" \
"v_max3_u32 v124, v124, v25, v26\n" \
"v_max_u32 v124, v124, v27\n" \
"v_cmp_eq_u32 vcc, -1, v124\n" \
"s_cbranch_vccz Lqk_c0_%=\n" \
"Lqk_r0_%=:\n" \
"s_sub_u32 s64, s64, 1\n" \
"s_cbranch_scc1 Lqk_dead_%=\n" \
"buffer_load_dwordx4 v[20:23], %[mvo], %[ws], s68 offen sc1\n" \
"s_nop 0\n" \
"buffer_load_dwordx4 v[24:27], %[mvo], %[ws], s68 offen offset:512 sc1\n" \
"s_nop 0\n" \
"s_waitcnt vmcnt(2)\n" \
"v_max3_u32 v124, v28, v29, v30\n" \
"v_max3_u32 v124, v124, v31, v32\n" \
"v_max3_u32 v124, v124, v33, v34\n" \
"v_max_u32 v124, v124, v35\n" \
"v_cmp_eq_u32 vcc, -1, v124\n" \
"s_cbranch_vccz Lqk_k01_%=\n" \
"buffer_load_dwordx4 v[28:31], %[mvo], %[ws], s69 offen sc1\n" \
"s_nop 0\n" \
"buffer_load_dwordx4 v[32:35], %[mvo], %[ws], s69 offen offset:512 sc1\n" \
"s_nop 0\n" \
"Lqk_k01_%=:\n" \
"v_max3_u32 v124, v36, v37, v38\n" \
"v_max3_u32 v124, v124, v39, v40\n" \
"v_max3_u32 v124, v124, v41, v42\n" \
"v_max_u32 v124, v124, v43\n" \
"v_cmp_eq_u32 vcc, -1, v124\n" \
"s_cbranch_vccz Lqk_k02_%=\n" \
"buffer_load_dwordx4 v[36:39], %[mvo], %[ws], s70 offen sc1\n" \
"s_nop 0\n" \
"buffer_load_dwordx4 v[40:43], %[mvo], %[ws], s70 offen offset:512 sc1\n" \
"s_nop 0\n" \
"Lqk_k02_%=:\n" \
"v_max3_u32 v124, v44, v45, v46\n" \
"v_max3_u32 v124, v124, v47, v48\n" \
"v_max3_u32 v124, v124, v49, v50\n" \
"v_max_u32 v124, v124, v51\n" \
"v_cmp_eq_u32 vcc, -1, v124\n" \
"s_cbranch_vccz Lqk_k03_%=\n" \
"buffer_load_dwordx4 v[44:47], %[mvo], %[ws], s71 offen sc1\n" \
"s_nop 0\n" \
"buffer_load_dwordx4 v[48:51], %[mvo], %[ws], s71 offen offset:512 sc1\n" \
"s_nop 0\n" \
"Lqk_k03_%=:\n" \
"s_waitcnt vmcnt(0)\n" \
"v_max3_u32 v124, v20, v21, v22\n" \
"v_max3_u32 v124, v124, v23, v24\n" \
"v_max3_u32 v124, v124, v25, v26\n" \
"v_max_u32 v124, v124, v27\n" \
"v_cmp_eq_u32 vcc, -1, v124\n" \
"s_cbranch_vccnz Lqk_r0_%=\n" \
"v_mfma_f32_16x16x32_bf16 v[116:119], %[sel], v[20:23], 0\n" \
"v_mfma_f32_16x16x32_bf16 v[120:123], %[sel], v[24:27], 0\n" \
"s_nop 6\n" \
"v_cvt_pk_bf16_f32 v116, v116, v117\n" \
"v_cvt_pk_bf16_f32 v117, v118, v119\n" \
"v_cvt_pk_bf16_f32 v118, v120, v121\n" \
"v_cvt_pk_bf16_f32 v119, v122, v123\n" \
"ds_write_b128 %[stw], v[116:119] offset:0\n" \
"s_waitcnt lgkmcnt(0)\n" \
"s_branch Lqk_c0_%=\n" \
"Lqk_s1_%=:\n" \
"s_nop 7\n" \
"s_nop 7\n" \
"v_max3_u32 v124, v28, v29, v30\n" \
"v_max3_u32 v124, v124, v31, v32\n" \
"v_max3_u32 v124, v124, v33, v34\n" \
"v_max_u32 v124, v124, v35\n" \
"v_cmp_eq_u32 vcc, -1, v124\n" \
"s_cbranch_vccz Lqk_c1_%=\n" \
"Lqk_r1_%=:\n" \
"s_sub_u32 s64, s64, 1\n" \
"s_cbranch_scc1 Lqk_dead_%=\n" \
"buffer_load_dwordx4 v[28:31], %[mvo], %[ws], s69 offen sc1\n" \
"s_nop 0\n" \
"buffer_load_dwordx4 v[32:35], %[mvo], %[ws], s69 offen offset:512 sc1\n" \
"s_nop 0\n" \
"s_waitcnt vmcnt(2)\n" \
"v_max3_u32 v124, v36, v37, v38\n" \
"v_max3_u32 v124, v124, v39, v40\n" \
"v_max3_u32 v124, v124, v41, v42\n" \
"v_max_u32 v124, v124, v43\n" \
"v_cmp_eq_u32 vcc, -1, v124\n" \
"s_cbranch_vccz Lqk_k12_%=\n" \
"buffer_load_dwordx4 v[36:39], %[mvo], %[ws], s70 offen sc1\n" \
"s_nop 0\n" \
"buffer_load_dwordx4 v[40:43], %[mvo], %[ws], s70 offen offset:512 sc1\n" \
"s_nop 0\n" \
"Lqk_k12_%=:\n" \
"v_max3_u32 v124, v44, v45, v46\n" \
"v_max3_u32 v124, v124, v47, v48\n" \
"v_max3_u32 v124, v124, v49, v50\n" \
"v_max_u32 v124, v124, v51\n" \
"v_cmp_eq_u32 vcc, -1, v124\n" \
"s_cbranch_vccz Lqk_k13_%=\n" \
"buffer_load_dwordx4 v[44:47], %[mvo], %[ws], s71 offen sc1\n" \
"s_nop 0\n" \
"buffer_load_dwordx4 v[48:51], %[mvo], %[ws], s71 offen offset:512 sc1\n" \
"s_nop 0\n" \
"Lqk_k13_%=:\n" \
"s_waitcnt vmcnt(0)\n" \
"v_max3_u32 v124, v28, v29, v30\n" \
"v_max3_u32 v124, v124, v31, v32\n" \
"v_max3_u32 v124, v124, v33, v34\n" \
"v_max_u32 v124, v124, v35\n" \
"v_cmp_eq_u32 vcc, -1, v124\n" \
"s_cbranch_vccnz Lqk_r1_%=\n" \
"v_mfma_f32_16x16x32_bf16 v[116:119], %[sel], v[28:31], 0\n" \
"v_mfma_f32_16x16x32_bf16 v[120:123], %[sel], v[32:35], 0\n" \
"s_nop 6\n" \
"v_cvt_pk_bf16_f32 v116, v116, v117\n" \
"v_cvt_pk_bf16_f32 v117, v118, v119\n" \
"v_cvt_pk_bf16_f32 v118, v120, v121\n" \
"v_cvt_pk_bf16_f32 v119, v122, v123\n" \
"ds_write_b128 %[stw], v[116:119] offset:4096\n" \
"s_waitcnt lgkmcnt(0)\n" \
"s_branch Lqk_c1_%=\n" \
"Lqk_s2_%=:\n" \
"s_nop 7\n" \
"s_nop 7\n" \
"v_max3_u32 v124, v36, v37, v38\n" \
"v_max3_u32 v124, v124, v39, v40\n" \
"v_max3_u32 v124, v124, v41, v42\n" \
"v_max_u32 v124, v124, v43\n" \
"v_cmp_eq_u32 vcc, -1, v124\n" \
"s_cbranch_vccz Lqk_c2_%=\n" \
"Lqk_r2_%=:\n" \
"s_sub_u32 s64, s64, 1\n" \
"s_cbranch_scc1 Lqk_dead_%=\n" \
"buffer_load_dwordx4 v[36:39], %[mvo], %[ws], s70 offen sc1\n" \
"s_nop 0\n" \
"buffer_load_dwordx4 v[40:43], %[mvo], %[ws], s70 offen offset:512 sc1\n" \
"s_nop 0\n" \
"s_waitcnt vmcnt(2)\n" \
"v_max3_u32 v124, v44, v45, v46\n" \
"v_max3_u32 v124, v124, v47, v48\n" \
"v_max3_u32 v124, v124, v49, v50\n" \
"v_max_u32 v124, v124, v51\n" \
"v_cmp_eq_u32 vcc, -1, v124\n" \
"s_cbranch_vccz Lqk_k23_%=\n" \
"buffer_load_dwordx4 v[44:47], %[mvo], %[ws], s71 offen sc1\n" \
"s_nop 0\n" \
"buffer_load_dwordx4 v[48:51], %[mvo], %[ws], s71 offen offset:512 sc1\n" \
"s_nop 0\n" \
"Lqk_k23_%=:\n" \
"s_waitcnt vmcnt(0)\n" \
"v_max3_u32 v124, v36, v37, v38\n" \
"v_max3_u32 v124, v124, v39, v40\n" \
"v_max3_u32 v124, v124, v41, v42\n" \
"v_max_u32 v124, v124, v43\n" \
"v_cmp_eq_u32 vcc, -1, v124\n" \
"s_cbranch_vccnz Lqk_r2_%=\n" \
"v_mfma_f32_16x16x32_bf16 v[116:119], %[sel], v[36:39], 0\n" \
"v_mfma_f32_16x16x32_bf16 v[120:123], %[sel], v[40:43], 0\n" \
"s_nop 6\n" \
"v_cvt_pk_bf16_f32 v116, v116, v117\n" \
"v_cvt_pk_bf16_f32 v117, v118, v119\n" \
"v_cvt_pk_bf16_f32 v118, v120, v121\n" \
"v_cvt_pk_bf16_f32 v119, v122, v123\n" \
"ds_write_b128 %[stw], v[116:119] offset:8192\n" \
"s_waitcnt lgkmcnt(0)\n" \
"s_branch Lqk_c2_%=\n" \
"Lqk_s3_%=:\n" \
"s_nop 7\n" \
"s_nop 7\n" \
"v_max3_u32 v124, v44, v45, v46\n" \
"v_max3_u32 v124, v124, v47, v48\n" \
"v_max3_u32 v124, v124, v49, v50\n" \
"v_max_u32 v124, v124, v51\n" \
"v_cmp_eq_u32 vcc, -1, v124\n" \
"s_cbranch_vccz Lqk_c3_%=\n" \
"Lqk_r3_%=:\n" \
"s_sub_u32 s64, s64, 1\n" \
"s_cbranch_scc1 Lqk_dead_%=\n" \
"buffer_load_dwordx4 v[44:47], %[mvo], %[ws], s71 offen sc1\n" \
"s_nop 0\n" \
"buffer_load_dwordx4 v[48:51], %[mvo], %[ws], s71 offen offset:512 sc1\n" \
"s_nop 0\n" \
"s_waitcnt vmcnt(2)\n" \
"s_waitcnt vmcnt(0)\n" \
"v_max3_u32 v124, v44, v45, v46\n" \
"v_max3_u32 v124, v124, v47, v48\n" \
"v_max3_u32 v124, v124, v49, v50\n" \
"v_max_u32 v124, v124, v51\n" \
"v_cmp_eq_u32 vcc, -1, v124\n" \
"s_cbranch_vccnz Lqk_r3_%=\n" \
"v_mfma_f32_16x16x32_bf16 v[116:119], %[sel], v[44:47], 0\n" \
"v_mfma_f32_16x16x32_bf16 v[120:123], %[sel], v[48:51], 0\n" \
"s_nop 6\n" \
"v_cvt_pk_bf16_f32 v116, v116, v117\n" \
"v_cvt_pk_bf16_f32 v117, v118, v119\n" \
"v_cvt_pk_bf16_f32 v118, v120, v121\n" \
"v_cvt_pk_bf16_f32 v119, v122, v123\n" \
"ds_write_b128 %[stw], v[116:119] offset:12288\n" \
"s_waitcnt lgkmcnt(0)\n" \
"s_branch Lqk_c3_%=\n" \
"Lqk_rv_%=:\n" \
"s_sub_u32 s64, s64, 1\n" \
"s_cbranch_scc1 Lqk_dead_%=\n" \
"buffer_load_dwordx2 v[116:117], %[vmo], %[ws], s73 offen sc1\n" \
"s_waitcnt vmcnt(2)\n" \
"Lqk_sv_%=:\n" \
"v_max_u32 v124, v116, v117\n" \
"v_cmp_eq_u32 vcc, -1, v124\n" \
"s_cbranch_vccnz Lqk_rv_%=\n" \
"v_cmp_nge_f32 vcc, 0x41f00000, v115\n" \
"s_cbranch_vccz Lqk_so_%=\n" \
"Lqk_sm_%=:\n" \
"s_nop 7\n" \
"s_nop 7\n" \
"v_mov_b32 v108, v80\n" \
"s_nop 1\n" \
"v_max_f32_dpp v114, v108, v108 quad_perm:[2,3,0,1] row_mask:0xf bank_mask:0xf\n" \
"v_max_f32_dpp v115, v108, v108 quad_perm:[2,3,0,1] row_mask:0xf bank_mask:0xf\n" \
"s_nop 1\n" \
"v_permlane16_swap_b32 v114, v115\n" \
"v_max_f32 v108, v114, v115\n" \
"v_max_f32 v115, v114, v115\n" \
"s_nop 1\n" \
"v_permlane32_swap_b32 v108, v115\n" \
"v_max_f32 v108, v108, v115\n" \
"v_mul_f32 v108, s78, v108\n" \
"v_max_f32 v126, v126, v108\n" \
"v_fma_f32 v108, v84, s78, -v126\n" \
"v_fma_f32 v109, v85, s78, -v126\n" \
"v_fma_f32 v110, v86, s78, -v126\n" \
"v_fma_f32 v111, v87, s78, -v126\n" \
"v_exp_f32 v108, v108\n" \
"v_exp_f32 v109, v109\n" \
"v_exp_f32 v110, v110\n" \
"v_exp_f32 v111, v111\n" \
"v_cvt_pk_bf16_f32 v36, v108, v109\n" \
"v_cvt_pk_bf16_f32 v37, v110, v111\n" \
"s_nop 0\n" \
"v_mov_b32_dpp v38, v36 quad_perm:[2,3,0,1] row_mask:0xf bank_mask:0xf\n" \
"v_mov_b32_dpp v39, v37 quad_perm:[2,3,0,1] row_mask:0xf bank_mask:0xf\n" \
"s_nop 1\n" \
"v_mfma_f32_16x16x32_bf16 v[60:63], v[120:123], v[36:39], 0\n" \
"v_fma_f32 v115, v80, s78, -v126\n" \
"v_exp_f32 v112, v115\n" \
"s_nop 1\n" \
"v_mov_b32_dpp v113, v112 quad_perm:[2,3,0,1] row_mask:0xf bank_mask:0xf\n" \
"v_cvt_pk_bf16_f32 v44, v112, v113\n" \
"v_cvt_pk_bf16_f32 v47, v112, v113\n" \
"s_nop 0\n" \
"v_mfma_f32_16x16x16_bf16 v[60:63], v[120:121], v[44:45], v[60:63]\n" \
"Lqk_so_%=:\n" \
"s_nop 3\n" \
"v_mfma_f32_16x16x32_bf16 v[52:55], v[28:31], v[36:39], 0\n" \
"v_mfma_f32_16x16x32_bf16 v[56:59], v[32:35], v[36:39], 0\n" \
"s_nop 5\n" \
"v_mfma_f32_16x16x16_bf16 v[52:55], v[116:117], v[44:45], v[52:55]\n" \
"v_mfma_f32_16x16x16_bf16 v[56:59], v[116:117], v[46:47], v[56:59]\n" \
"s_nop 7\n" \
"s_branch Lqk_cm_%=\n" \
"Lqk_dead_%=:\n" \
"v_mov_b32 v124, 0x7777\n" \
"buffer_store_dword v124, off, %[ws], 0 sc1\n" \
"Lqk_last_%=:\n" \
"s_endpgm\n" \
    : [fvo] "+v"(fvo), [fo] "+s"(fo) : [wl] "v"(wl), [str] "v"(str), [stw] "v"(stw), [mvo] "v"(mvo), [sel] "v"(sel), [selT] "v"(selT), [idx] "v"(idx), [xr] "v"(xr), [ma] "v"(ma), \
       [vmo] "v"(vmo), [kmo] "v"(kmo), [omo] "v"(omo), [wvo] "v"(wvo), [ws] "s"(ws), [rq] "s"(rq), [rk] "s"(rk), [so0] "s"(so0), [wbase] "s"(wbase), \
       [hs] "s"(hs), [hp] "s"(hp), [hb] "s"(hb), [odb] "s"(odb), [okm] "s"(okm), [ovm] "s"(ovm), [okx] "s"(okx), [ovx] "s"(ovx), [op0] "s"(op0), [op1] "s"(op1), [dvm] "s"(dvm), [d0] "s"(d0), [d1] "s"(d1) \
    : "v20", "v21", "v22", "v23", "v24", "v25", "v26", "v27", "v28", "v29", "v30", "v31", "v32", "v33", "v34", "v35", "v36", "v37", "v38", "v39", "v40", "v41", "v42", "v43", "v44", "v45", "v46", "v47", "v48", "v49", "v50", "v51", "v52", "v53", "v54", "v55", "v56", "v57", "v58", "v59", "v60", "v61", "v62", "v63", "v64", "v65", "v66", "v67", "v68", "v69", "v70", "v71", "v72", "v73", "v74", "v75", "v76", "v77", "v78", "v79", "v80", "v81", "v82", "v83", "v84", "v85", "v86", "v87", "v88", "v89", "v90", "v91", "v92", "v93", "v94", "v95", "v96", "v97", "v98", "v99", "v100", "v101", "v102", "v103", "v104", "v105", "v106", "v107", "v108", "v109", "v110", "v111", "v112", "v113", "v114", "v115", "v116", "v117", "v118", "v119", "v120", "v121", "v122", "v123", "v124", "v125", "v126", "v127", "v128", "v129", "v130", "v131", "v132", "v133", "v134", "v135", "v136", "v137", "v138", "v139", "v140", "v141", "v142", "v143", "v144", "v145", "v146", "v147", "v148", "v149", "v150", "v151", "v152", "v153", "v154", "v155", "v156", "v157", "v158", "v159", "v160", "v161", "v162", "v163", "v164", "v165", "v166", "v167", "v168", "v169", "v170", "v171", "v172", "v173", "v174", "v175", "v176", "v177", "v178", "v179", "v180", "v181", "v182", "v183", "v184", "v185", "v186", "v187", "v188", "v189", "v190", "v191", "v192", "v193", "v194", "v195", "v196", "v197", "v198", "v199", "v200", "v201", "v202", "v203", "v204", "v205", "v206", "v207", "v208", "v209", "v210", "v211", "v212", "v213", "v214", "v215", "v216", "v217", "v218", "v219", "v220", "v221", "v222", "v223", "v224", "v225", "v226", "v227", "v228", "v229", "v230", "v231", "v232", "v233", "v234", "v235", "v236", "v237", "v238", "v239", "v240", "v241", "v242", "v243", "v244", "v245", "v246", "v247", "v248", "v249", "v250", "v251", "v252", "v253", "v254", "v255", "a0", "a1", "a2", "a3", "a4", "a5", "a6", "a7", "a8", "a9", "a10", "a11", "a12", "a13", "a14", "a15", "a16", "a17", "a18", "a19", "a20", "a21", "a22", "a23", "a24", "a25", "a26", "a27", "a28", "a29", "a30", "a31", "a32", "a33", "a34", "a35", "a36", "a37", "a38", "a39", "a40", "a41", "a42", "a43", "a44", "a45", "a46", "a47", "a48", "a49", "a50", "a51", "a52", "a53", "a54", "a55", "a56", "a57", "a58", "a59", "a60", "a61", "a62", "a63", "a64", "a65", "a66", "a67", "a68", "a69", "a70", "a71", "a72", "a73", "a74", "a75", "a76", "a77", "a78", "a79", "a80", "a81", "a82", "a83", "a84", "a85", "a86", "a87", "a88", "a89", "a90", "a91", "a92", "a93", "a94", "a95", "a96", "a97", "a98", "a99", "a100", "a101", "a102", "a103", "a104", "a105", "a106", "a107", "a108", "a109", "a110", "a111", "a112", "a113", "a114", "a115", "a116", "a117", "a118", "a119", "a120", "a121", "a122", "a123", "a124", "a125", "a126", "a127", "a128", "a129", "a130", "a131", "a132", "a133", "a134", "a135", "a136", "a137", "a138", "a139", "a140", "a141", "a142", "a143", "a144", "a145", "a146", "a147", "a148", "a149", "a150", "a151", "a152", "a153", "a154", "a155", "a156", "a157", "a158", "a159", "a160", "a161", "a162", "a163", "a164", "a165", "a166", "a167", "a168", "a169", "a170", "a171", "a172", "a173", "a174", "a175", "a176", "a177", "a178", "a179", "a180", "a181", "a182", "a183", "a184", "a185", "a186", "a187", "a188", "a189", "a190", "a191", "a192", "a193", "a194", "a195", "a196", "a197", "a198", "a199", "a200", "a201", "a202", "a203", "a204", "a205", "a206", "a207", "a208", "a209", "a210", "a211", "a212", "a213", "a214", "a215", "a216", "a217", "a218", "a219", "a220", "a221", "a222", "a223", "a224", "a225", "a226", "a227", "a228", "a229", "a230", "a231", "a232", "a233", "a234", "a235", "a236", "a237", "a238", "a239", "a240", "a241", "a242", "a243", "a244", "a245", "a246", "a247", "a248", "a249", "a250", "a251", "a252", "a253", "a254", "a255", "s57", "s58", "s59", "s60", "s61", "s62", "s63", "s64", "s65", "s66", "s67", "s68", "s69", "s70", "s71", "s72", "s73", "s74", "s75", "s76", "s77", "s78", "s79", "s80", "s81", "s82", "s83", "s84", "s85", "s86", "s87", "s88", "s89", "s90", "s91", "s92", "s93", "s94", "s95", "vcc", "scc", "memory")
#define VO_ASM() asm volatile( \
"s_nop 4\n" \
"s_add_u32 m0, %[wbase], 0\n" \
"s_add_u32 s66, %[sov], 0\n" \
"buffer_load_dwordx4 %[vov], %[rv], s66 offen lds\n" \
"s_add_u32 m0, %[wbase], 1024\n" \
"s_add_u32 s66, %[sov], 262144\n" \
"buffer_load_dwordx4 %[vov], %[rv], s66 offen lds\n" \
"s_add_u32 m0, %[wbase], 2048\n" \
"s_add_u32 s66, %[sov], 16384\n" \
"buffer_load_dwordx4 %[vov], %[rv], s66 offen lds\n" \
"s_add_u32 m0, %[wbase], 3072\n" \
"s_add_u32 s66, %[sov], 278528\n" \
"buffer_load_dwordx4 %[vov], %[rv], s66 offen lds\n" \
"s_add_u32 m0, %[wbase], 4096\n" \
"s_add_u32 s66, %[sov], 524288\n" \
"buffer_load_dwordx4 %[vov], %[rv], s66 offen lds\n" \
"s_add_u32 m0, %[wbase], 5120\n" \
"s_add_u32 s66, %[sov], 786432\n" \
"buffer_load_dwordx4 %[vov], %[rv], s66 offen lds\n" \
"s_add_u32 m0, %[wbase], 6144\n" \
"s_add_u32 s66, %[sov], 540672\n" \
"buffer_load_dwordx4 %[vov], %[rv], s66 offen lds\n" \
"v_lshrrev_b32 v48, 9, %[wl]\n" \
"v_xor_b32 v48, v48, %[wl]\n" \
"v_and_b32 v48, 0xc0, v48\n" \
"v_and_b32 v49, 0x3c0, %[wl]\n" \
"v_lshl_or_b32 v48, v49, 2, v48\n" \
"v_and_or_b32 v48, %[wl], 48, v48\n" \
"v_add_u32 v49, 0x10000, v48\n" \
"v_and_b32 v50, 0x3c0, %[wl]\n" \
"v_add_u32 v50, v50, %[wl]\n" \
"v_lshrrev_b32 v51, 9, %[wl]\n" \
"v_xor_b32 v51, v51, %[wl]\n" \
"v_lshrrev_b32 v51, 1, v51\n" \
"v_and_or_b32 v51, v51, 64, v50\n" \
"v_xor_b32 v50, 64, v51\n" \
"v_bfe_u32 v104, %[wl], 4, 6\n" \
"v_cmp_gt_u32 vcc, 8, v104\n" \
"v_mul_u32_u24 v104, 2048, v104\n" \
"s_nop 0\n" \
"v_cndmask_b32 v104, 2.0, v104, vcc\n" \
"s_bfe_u32 s66, %[hp], 0x10007\n" \
"s_lshr_b32 s66, 16, s66\n" \
"v_bfe_u32 v100, %[wl], 4, 6\n" \
"v_cmp_gt_u32 vcc, s66, v100\n" \
"v_mul_u32_u24 v100, 8192, v100\n" \
"s_nop 0\n" \
"v_cndmask_b32 v100, 2.0, v100, vcc\n" \
"s_waitcnt vmcnt(6)\n" \
"s_add_u32 m0, %[wbase], 7168\n" \
"s_add_u32 s66, %[sov], 802816\n" \
"buffer_load_dwordx4 %[vov], %[rv], s66 offen lds\n" \
"s_waitcnt vmcnt(6)\n" \
"s_add_u32 m0, %[wbase], 16384\n" \
"s_add_u32 s66, %[sov], 1048576\n" \
"buffer_load_dwordx4 %[vov], %[rv], s66 offen lds\n" \
"s_waitcnt vmcnt(6)\n" \
"s_add_u32 m0, %[wbase], 17408\n" \
"s_add_u32 s66, %[sov], 1310720\n" \
"buffer_load_dwordx4 %[vov], %[rv], s66 offen lds\n" \
"s_waitcnt vmcnt(6)\n" \
"s_add_u32 m0, %[wbase], 18432\n" \
"s_add_u32 s66, %[sov], 1064960\n" \
"buffer_load_dwordx4 %[vov], %[rv], s66 offen lds\n" \
"s_barrier\n" \
"ds_read_b64_tr_b16 v[16:17], v48 offset:0\n" \
"ds_read_b64_tr_b16 v[18:19], v48 offset:32768\n" \
"ds_read_b64_tr_b16 v[20:21], v49 offset:0\n" \
"ds_read_b64_tr_b16 v[22:23], v49 offset:32768\n" \
"ds_read_b64_tr_b16 v[24:25], v48 offset:8\n" \
"ds_read_b64_tr_b16 v[26:27], v48 offset:32776\n" \
"ds_read_b64_tr_b16 v[28:29], v49 offset:8\n" \
"ds_read_b64_tr_b16 v[30:31], v49 offset:32776\n" \
"s_waitcnt vmcnt(6)\n" \
"s_add_u32 m0, %[wbase], 19456\n" \
"s_add_u32 s66, %[sov], 1327104\n" \
"buffer_load_dwordx4 %[vov], %[rv], s66 offen lds\n" \
"s_waitcnt vmcnt(6)\n" \
"s_add_u32 m0, %[wbase], 20480\n" \
"s_add_u32 s66, %[sov], 1572864\n" \
"buffer_load_dwordx4 %[vov], %[rv], s66 offen lds\n" \
"s_waitcnt vmcnt(6)\n" \
"s_add_u32 m0, %[wbase], 21504\n" \
"s_add_u32 s66, %[sov], 1835008\n" \
"buffer_load_dwordx4 %[vov], %[rv], s66 offen lds\n" \
"s_waitcnt vmcnt(6)\n" \
"s_add_u32 m0, %[wbase], 22528\n" \
"s_add_u32 s66, %[sov], 1589248\n" \
"buffer_load_dwordx4 %[vov], %[rv], s66 offen lds\n" \
"s_waitcnt lgkmcnt(0)\n" \
"s_barrier\n" \
"ds_write_b128 %[wl], v[16:19] offset:0\n" \
"ds_write_b128 %[wl], v[20:23] offset:1024\n" \
"ds_write_b128 %[wl], v[24:27] offset:2048\n" \
"ds_write_b128 %[wl], v[28:31] offset:3072\n" \
"ds_read_b64_tr_b16 v[32:33], v48 offset:4096\n" \
"ds_read_b64_tr_b16 v[34:35], v48 offset:36864\n" \
"ds_read_b64_tr_b16 v[36:37], v49 offset:4096\n" \
"ds_read_b64_tr_b16 v[38:39], v49 offset:36864\n" \
"ds_read_b64_tr_b16 v[40:41], v48 offset:4104\n" \
"ds_read_b64_tr_b16 v[42:43], v48 offset:36872\n" \
"ds_read_b64_tr_b16 v[44:45], v49 offset:4104\n" \
"ds_read_b64_tr_b16 v[46:47], v49 offset:36872\n" \
"s_waitcnt vmcnt(6)\n" \
"s_add_u32 m0, %[wbase], 23552\n" \
"s_add_u32 s66, %[sov], 1851392\n" \
"buffer_load_dwordx4 %[vov], %[rv], s66 offen lds\n" \
"s_waitcnt vmcnt(6)\n" \
"s_add_u32 m0, %[wbase], 24576\n" \
"s_add_u32 s66, %[sov], 2097152\n" \
"buffer_load_dwordx4 %[vov], %[rv], s66 offen lds\n" \
"s_waitcnt vmcnt(6)\n" \
"s_add_u32 m0, %[wbase], 25600\n" \
"s_add_u32 s66, %[sov], 2359296\n" \
"buffer_load_dwordx4 %[vov], %[rv], s66 offen lds\n" \
"s_waitcnt vmcnt(6)\n" \
"s_add_u32 m0, %[wbase], 26624\n" \
"s_add_u32 s66, %[sov], 2113536\n" \
"buffer_load_dwordx4 %[vov], %[rv], s66 offen lds\n" \
"s_waitcnt lgkmcnt(0)\n" \
"s_barrier\n" \
"ds_write_b128 %[wl], v[32:35] offset:4096\n" \
"ds_write_b128 %[wl], v[36:39] offset:5120\n" \
"ds_write_b128 %[wl], v[40:43] offset:6144\n" \
"ds_write_b128 %[wl], v[44:47] offset:7168\n" \
"ds_read_b64_tr_b16 a[16:17], v48 offset:16384\n" \
"ds_read_b64_tr_b16 a[18:19], v48 offset:49152\n" \
"ds_read_b64_tr_b16 a[20:21], v49 offset:16384\n" \
"ds_read_b64_tr_b16 a[22:23], v49 offset:49152\n" \
"ds_read_b64_tr_b16 a[24:25], v48 offset:16392\n" \
"ds_read_b64_tr_b16 a[26:27], v48 offset:49160\n" \
"ds_read_b64_tr_b16 a[28:29], v49 offset:16392\n" \
"ds_read_b64_tr_b16 a[30:31], v49 offset:49160\n" \
"s_waitcnt vmcnt(6)\n" \
"s_add_u32 m0, %[wbase], 27648\n" \
"s_add_u32 s66, %[sov], 2375680\n" \
"buffer_load_dwordx4 %[vov], %[rv], s66 offen lds\n" \
"s_waitcnt vmcnt(6)\n" \
"s_add_u32 m0, %[wbase], 28672\n" \
"s_add_u32 s66, %[sov], 2621440\n" \
"buffer_load_dwordx4 %[vov], %[rv], s66 offen lds\n" \
"s_waitcnt vmcnt(6)\n" \
"s_add_u32 m0, %[wbase], 29696\n" \
"s_add_u32 s66, %[sov], 2883584\n" \
"buffer_load_dwordx4 %[vov], %[rv], s66 offen lds\n" \
"s_waitcnt vmcnt(6)\n" \
"s_add_u32 m0, %[wbase], 30720\n" \
"s_add_u32 s66, %[sov], 2637824\n" \
"buffer_load_dwordx4 %[vov], %[rv], s66 offen lds\n" \
"s_waitcnt lgkmcnt(0)\n" \
"s_barrier\n" \
"ds_read_b64_tr_b16 a[48:49], v48 offset:20480\n" \
"ds_read_b64_tr_b16 a[50:51], v48 offset:53248\n" \
"ds_read_b64_tr_b16 a[52:53], v49 offset:20480\n" \
"ds_read_b64_tr_b16 a[54:55], v49 offset:53248\n" \
"ds_read_b64_tr_b16 a[56:57], v48 offset:20488\n" \
"ds_read_b64_tr_b16 a[58:59], v48 offset:53256\n" \
"ds_read_b64_tr_b16 a[60:61], v49 offset:20488\n" \
"ds_read_b64_tr_b16 a[62:63], v49 offset:53256\n" \
"s_waitcnt vmcnt(6)\n" \
"s_add_u32 m0, %[wbase], 31744\n" \
"s_add_u32 s66, %[sov], 2899968\n" \
"buffer_load_dwordx4 %[vov], %[rv], s66 offen lds\n" \
"s_waitcnt vmcnt(6)\n" \
"s_add_u32 m0, %[wbase], 16384\n" \
"s_add_u32 s66, %[sov], 3145728\n" \
"buffer_load_dwordx4 %[vov], %[rv], s66 offen lds\n" \
"s_waitcnt vmcnt(6)\n" \
"s_add_u32 m0, %[wbase], 17408\n" \
"s_add_u32 s66, %[sov], 3407872\n" \
"buffer_load_dwordx4 %[vov], %[rv], s66 offen lds\n" \
"s_waitcnt vmcnt(6)\n" \
"s_add_u32 m0, %[wbase], 18432\n" \
"s_add_u32 s66, %[sov], 3162112\n" \
"buffer_load_dwordx4 %[vov], %[rv], s66 offen lds\n" \
"s_waitcnt lgkmcnt(0)\n" \
"s_barrier\n" \
"ds_read_b64_tr_b16 a[80:81], v48 offset:24576\n" \
"ds_read_b64_tr_b16 a[82:83], v48 offset:57344\n" \
"ds_read_b64_tr_b16 a[84:85], v49 offset:24576\n" \
"ds_read_b64_tr_b16 a[86:87], v49 offset:57344\n" \
"ds_read_b64_tr_b16 a[88:89], v48 offset:24584\n" \
"ds_read_b64_tr_b16 a[90:91], v48 offset:57352\n" \
"ds_read_b64_tr_b16 a[92:93], v49 offset:24584\n" \
"ds_read_b64_tr_b16 a[94:95], v49 offset:57352\n" \
"s_waitcnt vmcnt(6)\n" \
"s_add_u32 m0, %[wbase], 19456\n" \
"s_add_u32 s66, %[sov], 3424256\n" \
"buffer_load_dwordx4 %[vov], %[rv], s66 offen lds\n" \
"s_waitcnt vmcnt(6)\n" \
"s_add_u32 m0, %[wbase], 20480\n" \
"s_add_u32 s66, %[sov], 3670016\n" \
"buffer_load_dwordx4 %[vov], %[rv], s66 offen lds\n" \
"s_waitcnt vmcnt(6)\n" \
"s_add_u32 m0, %[wbase], 21504\n" \
"s_add_u32 s66, %[sov], 3932160\n" \
"buffer_load_dwordx4 %[vov], %[rv], s66 offen lds\n" \
"s_waitcnt vmcnt(6)\n" \
"s_add_u32 m0, %[wbase], 22528\n" \
"s_add_u32 s66, %[sov], 3686400\n" \
"buffer_load_dwordx4 %[vov], %[rv], s66 offen lds\n" \
"s_waitcnt lgkmcnt(0)\n" \
"s_barrier\n" \
"ds_read_b64_tr_b16 a[112:113], v48 offset:28672\n" \
"ds_read_b64_tr_b16 a[114:115], v48 offset:61440\n" \
"ds_read_b64_tr_b16 a[116:117], v49 offset:28672\n" \
"ds_read_b64_tr_b16 a[118:119], v49 offset:61440\n" \
"ds_read_b64_tr_b16 a[120:121], v48 offset:28680\n" \
"ds_read_b64_tr_b16 a[122:123], v48 offset:61448\n" \
"ds_read_b64_tr_b16 a[124:125], v49 offset:28680\n" \
"ds_read_b64_tr_b16 a[126:127], v49 offset:61448\n" \
"s_waitcnt vmcnt(6)\n" \
"s_add_u32 m0, %[wbase], 23552\n" \
"s_add_u32 s66, %[sov], 3948544\n" \
"buffer_load_dwordx4 %[vov], %[rv], s66 offen lds\n" \
"s_waitcnt vmcnt(6)\n" \
"s_add_u32 m0, %[wbase], 24576\n" \
"s_add_u32 s66, %[sov], 4194304\n" \
"buffer_load_dwordx4 %[vov], %[rv], s66 offen lds\n" \
"s_waitcnt vmcnt(6)\n" \
"s_add_u32 m0, %[wbase], 25600\n" \
"s_add_u32 s66, %[sov], 4456448\n" \
"buffer_load_dwordx4 %[vov], %[rv], s66 offen lds\n" \
"s_waitcnt vmcnt(6)\n" \
"s_add_u32 m0, %[wbase], 26624\n" \
"s_add_u32 s66, %[sov], 4210688\n" \
"buffer_load_dwordx4 %[vov], %[rv], s66 offen lds\n" \
"s_waitcnt lgkmcnt(0)\n" \
"s_barrier\n" \
"ds_read_b64_tr_b16 a[144:145], v48 offset:16384\n" \
"ds_read_b64_tr_b16 a[146:147], v48 offset:49152\n" \
"ds_read_b64_tr_b16 a[148:149], v49 offset:16384\n" \
"ds_read_b64_tr_b16 a[150:151], v49 offset:49152\n" \
"ds_read_b64_tr_b16 a[152:153], v48 offset:16392\n" \
"ds_read_b64_tr_b16 a[154:155], v48 offset:49160\n" \
"ds_read_b64_tr_b16 a[156:157], v49 offset:16392\n" \
"ds_read_b64_tr_b16 a[158:159], v49 offset:49160\n" \
"s_waitcnt vmcnt(6)\n" \
"s_add_u32 m0, %[wbase], 27648\n" \
"s_add_u32 s66, %[sov], 4472832\n" \
"buffer_load_dwordx4 %[vov], %[rv], s66 offen lds\n" \
"s_waitcnt vmcnt(6)\n" \
"s_add_u32 m0, %[wbase], 28672\n" \
"s_add_u32 s66, %[sov], 4718592\n" \
"buffer_load_dwordx4 %[vov], %[rv], s66 offen lds\n" \
"s_waitcnt vmcnt(6)\n" \
"s_add_u32 m0, %[wbase], 29696\n" \
"s_add_u32 s66, %[sov], 4980736\n" \
"buffer_load_dwordx4 %[vov], %[rv], s66 offen lds\n" \
"s_waitcnt vmcnt(6)\n" \
"s_add_u32 m0, %[wbase], 30720\n" \
"s_add_u32 s66, %[sov], 4734976\n" \
"buffer_load_dwordx4 %[vov], %[rv], s66 offen lds\n" \
"s_waitcnt lgkmcnt(0)\n" \
"s_barrier\n" \
"ds_read_b64_tr_b16 a[176:177], v48 offset:20480\n" \
"ds_read_b64_tr_b16 a[178:179], v48 offset:53248\n" \
"ds_read_b64_tr_b16 a[180:181], v49 offset:20480\n" \
"ds_read_b64_tr_b16 a[182:183], v49 offset:53248\n" \
"ds_read_b64_tr_b16 a[184:185], v48 offset:20488\n" \
"ds_read_b64_tr_b16 a[186:187], v48 offset:53256\n" \
"ds_read_b64_tr_b16 a[188:189], v49 offset:20488\n" \
"ds_read_b64_tr_b16 a[190:191], v49 offset:53256\n" \
"s_waitcnt vmcnt(6)\n" \
"s_add_u32 m0, %[wbase], 31744\n" \
"s_add_u32 s66, %[sov], 4997120\n" \
"buffer_load_dwordx4 %[vov], %[rv], s66 offen lds\n" \
"s_waitcnt vmcnt(6)\n" \
"s_add_u32 m0, %[wbase], 16384\n" \
"s_add_u32 s66, %[sov], 5242880\n" \
"buffer_load_dwordx4 %[vov], %[rv], s66 offen lds\n" \
"s_waitcnt vmcnt(6)\n" \
"s_add_u32 m0, %[wbase], 17408\n" \
"s_add_u32 s66, %[sov], 5505024\n" \
"buffer_load_dwordx4 %[vov], %[rv], s66 offen lds\n" \
"s_waitcnt vmcnt(6)\n" \
"s_add_u32 m0, %[wbase], 18432\n" \
"s_add_u32 s66, %[sov], 5259264\n" \
"buffer_load_dwordx4 %[vov], %[rv], s66 offen lds\n" \
"s_waitcnt lgkmcnt(0)\n" \
"s_barrier\n" \
"ds_read_b64_tr_b16 a[208:209], v48 offset:24576\n" \
"ds_read_b64_tr_b16 a[210:211], v48 offset:57344\n" \
"ds_read_b64_tr_b16 a[212:213], v49 offset:24576\n" \
"ds_read_b64_tr_b16 a[214:215], v49 offset:57344\n" \
"ds_read_b64_tr_b16 a[216:217], v48 offset:24584\n" \
"ds_read_b64_tr_b16 a[218:219], v48 offset:57352\n" \
"ds_read_b64_tr_b16 a[220:221], v49 offset:24584\n" \
"ds_read_b64_tr_b16 a[222:223], v49 offset:57352\n" \
"s_waitcnt vmcnt(6)\n" \
"s_add_u32 m0, %[wbase], 19456\n" \
"s_add_u32 s66, %[sov], 5521408\n" \
"buffer_load_dwordx4 %[vov], %[rv], s66 offen lds\n" \
"s_waitcnt vmcnt(6)\n" \
"s_add_u32 m0, %[wbase], 20480\n" \
"s_add_u32 s66, %[sov], 5767168\n" \
"buffer_load_dwordx4 %[vov], %[rv], s66 offen lds\n" \
"s_waitcnt vmcnt(6)\n" \
"s_add_u32 m0, %[wbase], 21504\n" \
"s_add_u32 s66, %[sov], 6029312\n" \
"buffer_load_dwordx4 %[vov], %[rv], s66 offen lds\n" \
"s_waitcnt vmcnt(6)\n" \
"s_add_u32 m0, %[wbase], 22528\n" \
"s_add_u32 s66, %[sov], 5783552\n" \
"buffer_load_dwordx4 %[vov], %[rv], s66 offen lds\n" \
"s_waitcnt lgkmcnt(0)\n" \
"s_barrier\n" \
"ds_read_b64_tr_b16 a[240:241], v48 offset:28672\n" \
"ds_read_b64_tr_b16 a[242:243], v48 offset:61440\n" \
"ds_read_b64_tr_b16 a[244:245], v49 offset:28672\n" \
"ds_read_b64_tr_b16 a[246:247], v49 offset:61440\n" \
"ds_read_b64_tr_b16 a[248:249], v48 offset:28680\n" \
"ds_read_b64_tr_b16 a[250:251], v48 offset:61448\n" \
"ds_read_b64_tr_b16 a[252:253], v49 offset:28680\n" \
"ds_read_b64_tr_b16 a[254:255], v49 offset:61448\n" \
"s_waitcnt vmcnt(6)\n" \
"s_add_u32 m0, %[wbase], 23552\n" \
"s_add_u32 s66, %[sov], 6045696\n" \
"buffer_load_dwordx4 %[vov], %[rv], s66 offen lds\n" \
"s_waitcnt vmcnt(6)\n" \
"s_add_u32 m0, %[wbase], 24576\n" \
"s_add_u32 s66, %[sov], 6291456\n" \
"buffer_load_dwordx4 %[vov], %[rv], s66 offen lds\n" \
"s_waitcnt vmcnt(6)\n" \
"s_add_u32 m0, %[wbase], 25600\n" \
"s_add_u32 s66, %[sov], 6553600\n" \
"buffer_load_dwordx4 %[vov], %[rv], s66 offen lds\n" \
"s_waitcnt vmcnt(6)\n" \
"s_add_u32 m0, %[wbase], 26624\n" \
"s_add_u32 s66, %[sov], 6307840\n" \
"buffer_load_dwordx4 %[vov], %[rv], s66 offen lds\n" \
"s_waitcnt lgkmcnt(0)\n" \
"s_barrier\n" \
"ds_read_b64_tr_b16 v[144:145], v48 offset:16384\n" \
"ds_read_b64_tr_b16 v[146:147], v48 offset:49152\n" \
"ds_read_b64_tr_b16 v[148:149], v49 offset:16384\n" \
"ds_read_b64_tr_b16 v[150:151], v49 offset:49152\n" \
"ds_read_b64_tr_b16 v[152:153], v48 offset:16392\n" \
"ds_read_b64_tr_b16 v[154:155], v48 offset:49160\n" \
"ds_read_b64_tr_b16 v[156:157], v49 offset:16392\n" \
"ds_read_b64_tr_b16 v[158:159], v49 offset:49160\n" \
"s_waitcnt vmcnt(6)\n" \
"s_add_u32 m0, %[wbase], 27648\n" \
"s_add_u32 s66, %[sov], 6569984\n" \
"buffer_load_dwordx4 %[vov], %[rv], s66 offen lds\n" \
"s_waitcnt vmcnt(6)\n" \
"s_add_u32 m0, %[wbase], 28672\n" \
"s_add_u32 s66, %[sov], 6815744\n" \
"buffer_load_dwordx4 %[vov], %[rv], s66 offen lds\n" \
"s_waitcnt vmcnt(6)\n" \
"s_add_u32 m0, %[wbase], 29696\n" \
"s_add_u32 s66, %[sov], 7077888\n" \
"buffer_load_dwordx4 %[vov], %[rv], s66 offen lds\n" \
"s_waitcnt vmcnt(6)\n" \
"s_add_u32 m0, %[wbase], 30720\n" \
"s_add_u32 s66, %[sov], 6832128\n" \
"buffer_load_dwordx4 %[vov], %[rv], s66 offen lds\n" \
"s_waitcnt lgkmcnt(0)\n" \
"s_barrier\n" \
"ds_read_b64_tr_b16 v[176:177], v48 offset:20480\n" \
"ds_read_b64_tr_b16 v[178:179], v48 offset:53248\n" \
"ds_read_b64_tr_b16 v[180:181], v49 offset:20480\n" \
"ds_read_b64_tr_b16 v[182:183], v49 offset:53248\n" \
"ds_read_b64_tr_b16 v[184:185], v48 offset:20488\n" \
"ds_read_b64_tr_b16 v[186:187], v48 offset:53256\n" \
"ds_read_b64_tr_b16 v[188:189], v49 offset:20488\n" \
"ds_read_b64_tr_b16 v[190:191], v49 offset:53256\n" \
"s_waitcnt vmcnt(6)\n" \
"s_add_u32 m0, %[wbase], 31744\n" \
"s_add_u32 s66, %[sov], 7094272\n" \
"buffer_load_dwordx4 %[vov], %[rv], s66 offen lds\n" \
"s_waitcnt vmcnt(6)\n" \
"s_add_u32 m0, %[wbase], 16384\n" \
"s_add_u32 s66, %[sov], 7340032\n" \
"buffer_load_dwordx4 %[vov], %[rv], s66 offen lds\n" \
"s_waitcnt vmcnt(6)\n" \
"s_add_u32 m0, %[wbase], 17408\n" \
"s_add_u32 s66, %[sov], 7602176\n" \
"buffer_load_dwordx4 %[vov], %[rv], s66 offen lds\n" \
"s_waitcnt vmcnt(6)\n" \
"s_add_u32 m0, %[wbase], 18432\n" \
"s_add_u32 s66, %[sov], 7356416\n" \
"buffer_load_dwordx4 %[vov], %[rv], s66 offen lds\n" \
"s_waitcnt lgkmcnt(0)\n" \
"s_barrier\n" \
"ds_read_b64_tr_b16 v[192:193], v48 offset:24576\n" \
"ds_read_b64_tr_b16 v[194:195], v48 offset:57344\n" \
"ds_read_b64_tr_b16 v[196:197], v49 offset:24576\n" \
"ds_read_b64_tr_b16 v[198:199], v49 offset:57344\n" \
"ds_read_b64_tr_b16 v[200:201], v48 offset:24584\n" \
"ds_read_b64_tr_b16 v[202:203], v48 offset:57352\n" \
"ds_read_b64_tr_b16 v[204:205], v49 offset:24584\n" \
"ds_read_b64_tr_b16 v[206:207], v49 offset:57352\n" \
"s_waitcnt vmcnt(6)\n" \
"s_add_u32 m0, %[wbase], 19456\n" \
"s_add_u32 s66, %[sov], 7618560\n" \
"buffer_load_dwordx4 %[vov], %[rv], s66 offen lds\n" \
"s_waitcnt vmcnt(6)\n" \
"s_add_u32 m0, %[wbase], 20480\n" \
"s_add_u32 s66, %[sov], 7864320\n" \
"buffer_load_dwordx4 %[vov], %[rv], s66 offen lds\n" \
"s_waitcnt vmcnt(6)\n" \
"s_add_u32 m0, %[wbase], 21504\n" \
"s_add_u32 s66, %[sov], 8126464\n" \
"buffer_load_dwordx4 %[vov], %[rv], s66 offen lds\n" \
"s_waitcnt vmcnt(6)\n" \
"s_add_u32 m0, %[wbase], 22528\n" \
"s_add_u32 s66, %[sov], 7880704\n" \
"buffer_load_dwordx4 %[vov], %[rv], s66 offen lds\n" \
"s_waitcnt lgkmcnt(0)\n" \
"s_barrier\n" \
"ds_read_b64_tr_b16 v[208:209], v48 offset:28672\n" \
"ds_read_b64_tr_b16 v[210:211], v48 offset:61440\n" \
"ds_read_b64_tr_b16 v[212:213], v49 offset:28672\n" \
"ds_read_b64_tr_b16 v[214:215], v49 offset:61440\n" \
"ds_read_b64_tr_b16 v[216:217], v48 offset:28680\n" \
"ds_read_b64_tr_b16 v[218:219], v48 offset:61448\n" \
"ds_read_b64_tr_b16 v[220:221], v49 offset:28680\n" \
"ds_read_b64_tr_b16 v[222:223], v49 offset:61448\n" \
"s_waitcnt vmcnt(6)\n" \
"s_add_u32 m0, %[wbase], 23552\n" \
"s_add_u32 s66, %[sov], 8142848\n" \
"buffer_load_dwordx4 %[vov], %[rv], s66 offen lds\n" \
"s_waitcnt vmcnt(6)\n" \
"s_add_u32 m0, %[wbase], 8192\n" \
"s_add_u32 s66, %[soo], 0\n" \
"buffer_load_dwordx4 %[voo], %[ro], s66 offen lds\n" \
"s_waitcnt vmcnt(6)\n" \
"s_add_u32 m0, %[wbase], 9216\n" \
"s_add_u32 s66, %[soo], 16384\n" \
"buffer_load_dwordx4 %[voo], %[ro], s66 offen lds\n" \
"s_waitcnt vmcnt(6)\n" \
"s_add_u32 m0, %[wbase], 10240\n" \
"s_add_u32 s66, %[soo], 4096\n" \
"buffer_load_dwordx4 %[voo], %[ro], s66 offen lds\n" \
"s_waitcnt lgkmcnt(0)\n" \
"s_barrier\n" \
"ds_read_b64_tr_b16 v[224:225], v48 offset:16384\n" \
"ds_read_b64_tr_b16 v[226:227], v48 offset:49152\n" \
"ds_read_b64_tr_b16 v[228:229], v49 offset:16384\n" \
"ds_read_b64_tr_b16 v[230:231], v49 offset:49152\n" \
"ds_read_b64_tr_b16 v[232:233], v48 offset:16392\n" \
"ds_read_b64_tr_b16 v[234:235], v48 offset:49160\n" \
"ds_read_b64_tr_b16 v[236:237], v49 offset:16392\n" \
"ds_read_b64_tr_b16 v[238:239], v49 offset:49160\n" \
"s_waitcnt vmcnt(6)\n" \
"s_add_u32 m0, %[wbase], 11264\n" \
"s_add_u32 s66, %[soo], 20480\n" \
"buffer_load_dwordx4 %[voo], %[ro], s66 offen lds\n" \
"s_waitcnt vmcnt(6)\n" \
"s_add_u32 m0, %[wbase], 12288\n" \
"s_add_u32 s66, %[soo], 32768\n" \
"buffer_load_dwordx4 %[voo], %[ro], s66 offen lds\n" \
"s_waitcnt vmcnt(6)\n" \
"s_add_u32 m0, %[wbase], 13312\n" \
"s_add_u32 s66, %[soo], 49152\n" \
"buffer_load_dwordx4 %[voo], %[ro], s66 offen lds\n" \
"s_waitcnt vmcnt(6)\n" \
"s_add_u32 m0, %[wbase], 14336\n" \
"s_add_u32 s66, %[soo], 36864\n" \
"buffer_load_dwordx4 %[voo], %[ro], s66 offen lds\n" \
"s_waitcnt lgkmcnt(0)\n" \
"s_barrier\n" \
"ds_read_b64_tr_b16 v[240:241], v48 offset:20480\n" \
"ds_read_b64_tr_b16 v[242:243], v48 offset:53248\n" \
"ds_read_b64_tr_b16 v[244:245], v49 offset:20480\n" \
"ds_read_b64_tr_b16 v[246:247], v49 offset:53248\n" \
"ds_read_b64_tr_b16 v[248:249], v48 offset:20488\n" \
"ds_read_b64_tr_b16 v[250:251], v48 offset:53256\n" \
"ds_read_b64_tr_b16 v[252:253], v49 offset:20488\n" \
"ds_read_b64_tr_b16 v[254:255], v49 offset:53256\n" \
"s_waitcnt vmcnt(6)\n" \
"s_add_u32 m0, %[wbase], 15360\n" \
"s_add_u32 s66, %[soo], 53248\n" \
"buffer_load_dwordx4 %[voo], %[ro], s66 offen lds\n" \
"s_waitcnt vmcnt(6)\n" \
"s_add_u32 m0, %[wbase], 24576\n" \
"s_add_u32 s66, %[soo], 262144\n" \
"buffer_load_dwordx4 %[voo], %[ro], s66 offen lds\n" \
"s_waitcnt vmcnt(6)\n" \
"s_add_u32 m0, %[wbase], 25600\n" \
"s_add_u32 s66, %[soo], 278528\n" \
"buffer_load_dwordx4 %[voo], %[ro], s66 offen lds\n" \
"s_waitcnt vmcnt(6)\n" \
"s_add_u32 m0, %[wbase], 26624\n" \
"s_add_u32 s66, %[soo], 266240\n" \
"buffer_load_dwordx4 %[voo], %[ro], s66 offen lds\n" \
"s_waitcnt lgkmcnt(0)\n" \
"s_barrier\n" \
"ds_read_b64_tr_b16 v[16:17], v50 offset:8192\n" \
"ds_read_b64_tr_b16 v[18:19], v50 offset:10240\n" \
"ds_read_b64_tr_b16 v[20:21], v50 offset:8200\n" \
"ds_read_b64_tr_b16 v[22:23], v50 offset:10248\n" \
"ds_read_b64_tr_b16 v[24:25], v51 offset:8192\n" \
"ds_read_b64_tr_b16 v[26:27], v51 offset:10240\n" \
"ds_read_b64_tr_b16 v[28:29], v51 offset:8200\n" \
"ds_read_b64_tr_b16 v[30:31], v51 offset:10248\n" \
"s_waitcnt vmcnt(6)\n" \
"s_add_u32 m0, %[wbase], 27648\n" \
"s_add_u32 s66, %[soo], 282624\n" \
"buffer_load_dwordx4 %[voo], %[ro], s66 offen lds\n" \
"s_waitcnt vmcnt(6)\n" \
"s_add_u32 m0, %[wbase], 28672\n" \
"s_add_u32 s66, %[soo], 294912\n" \
"buffer_load_dwordx4 %[voo], %[ro], s66 offen lds\n" \
"buffer_load_dword v125, off, %[ws], %[hs] sc1\n" \
"v_lshlrev_b32 v102, 3, %[wl]\n" \
"v_and_b32 v102, 0x1f80, v102\n" \
"v_min_u32 v102, Lvo_last_%=-Lvo_top_%=, v102\n" \
"s_getpc_b64 s[94:95]\n" \
"Lvo_pc76_%=:\n" \
"s_add_u32 s94, s94, Lvo_top_%=-Lvo_pc76_%=\n" \
"s_addc_u32 s95, s95, 0\n" \
"global_load_dword v103, v102, s[94:95]\n" \
"v_lshlrev_b32 v102, 3, %[wl]\n" \
"v_and_b32 v102, 0x1f80, v102\n" \
"v_min_u32 v102, Lvo_fin_%=-Lvo_rest_%=, v102\n" \
"s_getpc_b64 s[94:95]\n" \
"Lvo_pc200_%=:\n" \
"s_add_u32 s94, s94, Lvo_rest_%=-Lvo_pc200_%=\n" \
"s_addc_u32 s95, s95, 0\n" \
"global_load_dword v103, v102, s[94:95]\n" \
"s_waitcnt vmcnt(9)\n" \
"s_add_u32 m0, %[wbase], 29696\n" \
"s_add_u32 s66, %[soo], 311296\n" \
"buffer_load_dwordx4 %[voo], %[ro], s66 offen lds\n" \
"s_waitcnt vmcnt(9)\n" \
"s_add_u32 m0, %[wbase], 30720\n" \
"s_add_u32 s66, %[soo], 299008\n" \
"buffer_load_dwordx4 %[voo], %[ro], s66 offen lds\n" \
"s_waitcnt lgkmcnt(0)\n" \
"s_barrier\n" \
"ds_write_b128 %[wl], v[16:19] offset:8192\n" \
"ds_write_b128 %[wl], v[20:23] offset:9216\n" \
"ds_write_b128 %[wl], v[24:27] offset:10240\n" \
"ds_write_b128 %[wl], v[28:31] offset:11264\n" \
"ds_read_b64_tr_b16 v[32:33], v50 offset:12288\n" \
"ds_read_b64_tr_b16 v[34:35], v50 offset:14336\n" \
"ds_read_b64_tr_b16 v[36:37], v50 offset:12296\n" \
"ds_read_b64_tr_b16 v[38:39], v50 offset:14344\n" \
"ds_read_b64_tr_b16 v[40:41], v51 offset:12288\n" \
"ds_read_b64_tr_b16 v[42:43], v51 offset:14336\n" \
"ds_read_b64_tr_b16 v[44:45], v51 offset:12296\n" \
"ds_read_b64_tr_b16 v[46:47], v51 offset:14344\n" \
"s_waitcnt vmcnt(9)\n" \
"s_add_u32 m0, %[wbase], 31744\n" \
"s_add_u32 s66, %[soo], 315392\n" \
"buffer_load_dwordx4 %[voo], %[ro], s66 offen lds\n" \
"s_waitcnt vmcnt(9)\n" \
"s_add_u32 m0, %[wbase], 16384\n" \
"s_add_u32 s66, %[soo], 393216\n" \
"buffer_load_dwordx4 %[voo], %[ro], s66 offen lds\n" \
"s_waitcnt vmcnt(9)\n" \
"s_add_u32 m0, %[wbase], 17408\n" \
"s_add_u32 s66, %[soo], 409600\n" \
"buffer_load_dwordx4 %[voo], %[ro], s66 offen lds\n" \
"s_waitcnt vmcnt(9)\n" \
"s_add_u32 m0, %[wbase], 18432\n" \
"s_add_u32 s66, %[soo], 397312\n" \
"buffer_load_dwordx4 %[voo], %[ro], s66 offen lds\n" \
"s_waitcnt lgkmcnt(0)\n" \
"s_barrier\n" \
"ds_write_b128 %[wl], v[32:35] offset:12288\n" \
"ds_write_b128 %[wl], v[36:39] offset:13312\n" \
"ds_write_b128 %[wl], v[40:43] offset:14336\n" \
"ds_write_b128 %[wl], v[44:47] offset:15360\n" \
"ds_read_b64_tr_b16 a[0:1], v50 offset:24576\n" \
"ds_read_b64_tr_b16 a[2:3], v50 offset:26624\n" \
"ds_read_b64_tr_b16 a[8:9], v50 offset:24584\n" \
"ds_read_b64_tr_b16 a[10:11], v50 offset:26632\n" \
"ds_read_b64_tr_b16 a[32:33], v51 offset:24576\n" \
"ds_read_b64_tr_b16 a[34:35], v51 offset:26624\n" \
"ds_read_b64_tr_b16 a[40:41], v51 offset:24584\n" \
"ds_read_b64_tr_b16 a[42:43], v51 offset:26632\n" \
"s_waitcnt vmcnt(9)\n" \
"s_add_u32 m0, %[wbase], 19456\n" \
"s_add_u32 s66, %[soo], 413696\n" \
"buffer_load_dwordx4 %[voo], %[ro], s66 offen lds\n" \
"s_waitcnt vmcnt(6)\n" \
"s_add_u32 m0, %[wbase], 20480\n" \
"s_add_u32 s66, %[soo], 425984\n" \
"buffer_load_dwordx4 %[voo], %[ro], s66 offen lds\n" \
"s_waitcnt vmcnt(6)\n" \
"s_add_u32 m0, %[wbase], 21504\n" \
"s_add_u32 s66, %[soo], 442368\n" \
"buffer_load_dwordx4 %[voo], %[ro], s66 offen lds\n" \
"s_waitcnt vmcnt(6)\n" \
"s_add_u32 m0, %[wbase], 22528\n" \
"s_add_u32 s66, %[soo], 430080\n" \
"buffer_load_dwordx4 %[voo], %[ro], s66 offen lds\n" \
"s_waitcnt lgkmcnt(0)\n" \
"s_barrier\n" \
"ds_read_b64_tr_b16 a[4:5], v50 offset:28672\n" \
"ds_read_b64_tr_b16 a[6:7], v50 offset:30720\n" \
"ds_read_b64_tr_b16 a[12:13], v50 offset:28680\n" \
"ds_read_b64_tr_b16 a[14:15], v50 offset:30728\n" \
"ds_read_b64_tr_b16 a[36:37], v51 offset:28672\n" \
"ds_read_b64_tr_b16 a[38:39], v51 offset:30720\n" \
"ds_read_b64_tr_b16 a[44:45], v51 offset:28680\n" \
"ds_read_b64_tr_b16 a[46:47], v51 offset:30728\n" \
"s_mov_b32 s64, 262144\n" \
"Lvo_hs_%=:\n" \
"v_readfirstlane_b32 s62, v125\n" \
"s_cmp_eq_u32 s62, 0\n" \
"s_cbranch_scc1 Lvo_shs_%=\n" \
"v_mov_b32 v125, 2\n" \
"s_mov_b64 exec, 1\n" \
"buffer_store_dword v125, off, %[ws], %[hs] offset:2048 sc1\n" \
"s_mov_b64 exec, -1\n" \
"s_and_b32 s62, s62, 8192\n" \
"s_mov_b32 s64, 262144\n" \
"s_mov_b32 s65, 1\n" \
"s_add_u32 s62, s62, 3670016\n" \
"s_mov_b32 s72, s62\n" \
"s_add_u32 s62, s62, 524288\n" \
"s_and_b32 s62, s62, 2097151\n" \
"s_bitset1_b32 s62, 21\n" \
"s_cmp_eq_u32 s65, 1\n" \
"s_cselect_b32 s68, 2031616, s62\n" \
"s_add_u32 s69, s68, 16384\n" \
"s_add_u32 s70, s68, 32768\n" \
"s_add_u32 s71, s68, 49152\n" \
"s_add_u32 s63, s62, %[vb]\n" \
"s_mov_b32 s59, 0\n" \
"s_mov_b32 s60, 0\n" \
"s_memtime s[92:93]\n" \
"s_waitcnt lgkmcnt(0)\n" \
"s_sub_u32 s92, s92, 0x100000\n" \
"s_branch Lvo_top_%=\n" \
".p2align 6\n" \
".fill 7, 4, 0xbf800000\n" \
"Lvo_top_%=:\n" \
"s_nop 1\n" \
"s_waitcnt lgkmcnt(0)\n" \
"Lvo_d0a_%=:\n" \
"s_memtime s[94:95]\n" \
"s_waitcnt lgkmcnt(0)\n" \
"s_sub_u32 s66, s92, s94\n" \
"s_add_u32 s66, s66, %[d0]\n" \
"s_cmp_gt_i32 s66, 200\n" \
"s_cbranch_scc1 Lvo_d0a_%=\n" \
"s_max_i32 s66, s66, 0\n" \
"Lvo_d0_%=:\n" \
"s_sub_u32 s66, s66, 28\n" \
"s_cbranch_scc0 Lvo_d0_%=\n" \
"Lvo_go_%=:\n" \
"buffer_load_dwordx4 v[16:19], %[mvo], %[ws], s68 offen sc1\n" \
"s_nop 0\n" \
"buffer_load_dwordx4 v[20:23], %[mvo], %[ws], s68 offen offset:512 sc1\n" \
"s_nop 0\n" \
"s_nop 7\n" \
"s_nop 1\n" \
"buffer_load_dwordx4 v[24:27], %[mvo], %[ws], s69 offen sc1\n" \
"s_nop 0\n" \
"buffer_load_dwordx4 v[28:31], %[mvo], %[ws], s69 offen offset:512 sc1\n" \
"s_nop 0\n" \
"s_nop 7\n" \
"s_nop 1\n" \
"buffer_load_dwordx4 v[32:35], %[mvo], %[ws], s70 offen sc1\n" \
"s_nop 0\n" \
"buffer_load_dwordx4 v[36:39], %[mvo], %[ws], s70 offen offset:512 sc1\n" \
"s_nop 0\n" \
"s_nop 7\n" \
"s_nop 1\n" \
"buffer_load_dwordx4 v[40:43], %[mvo], %[ws], s71 offen sc1\n" \
"s_nop 0\n" \
"buffer_load_dwordx4 v[44:47], %[mvo], %[ws], s71 offen offset:512 sc1\n" \
"s_nop 0\n" \
"ds_read_b128 v[80:83], %[wl] offset:0\n" \
"ds_read_b128 v[84:87], %[wl] offset:1024\n" \
"ds_read_b128 v[88:91], %[wl] offset:2048\n" \
"ds_read_b128 v[92:95], %[wl] offset:3072\n" \
"ds_read_b128 v[96:99], %[wl] offset:4096\n" \
"ds_read_b128 v[100:103], %[wl] offset:5120\n" \
"ds_read_b128 v[104:107], %[wl] offset:6144\n" \
"ds_read_b128 v[108:111], %[wl] offset:7168\n" \
"v_mov_b32 v112, 0\n" \
"v_mov_b32 v113, 0\n" \
"v_mov_b32 v114, 0\n" \
"v_mov_b32 v115, 0\n" \
"v_mov_b32 v116, 0\n" \
"v_mov_b32 v117, 0\n" \
"v_mov_b32 v118, 0\n" \
"v_mov_b32 v119, 0\n" \
"Lvo_first_%=:\n" \
"s_waitcnt vmcnt(7)\n" \
"v_mfma_f32_16x16x32_bf16 v[120:123], %[sel], v[16:19], 0\n" \
"s_nop 6\n" \
"s_waitcnt vmcnt(6)\n" \
"v_mfma_f32_16x16x32_bf16 v[124:127], %[sel], v[20:23], 0\n" \
"v_cvt_pk_bf16_f32 v120, v120, v121\n" \
"v_cvt_pk_bf16_f32 v121, v122, v123\n" \
"s_nop 5\n" \
"v_cvt_pk_bf16_f32 v122, v124, v125\n" \
"v_cvt_pk_bf16_f32 v123, v126, v127\n" \
"ds_write_b128 %[stw], v[120:123] offset:0\n" \
"v_cmp_u_f32 vcc, v120, v123\n" \
"s_cbranch_vccnz Lvo_s0_%=\n" \
"Lvo_c0_%=:\n" \
"s_waitcnt lgkmcnt(0)\n" \
"s_barrier\n" \
"ds_read_b128 v[48:51], %[str] offset:0\n" \
"ds_read_b128 v[52:55], %[str] offset:1024\n" \
"ds_read_b128 v[56:59], %[str] offset:2048\n" \
"ds_read_b128 v[60:63], %[str] offset:3072\n" \
"s_waitcnt vmcnt(5)\n" \
"v_mfma_f32_16x16x32_bf16 v[120:123], %[sel], v[24:27], 0\n" \
"s_waitcnt vmcnt(4)\n" \
"s_nop 1\n" \
"v_mfma_f32_16x16x32_bf16 v[124:127], %[sel], v[28:31], 0\n" \
"s_waitcnt lgkmcnt(3)\n" \
"s_nop 1\n" \
"v_smfmac_f32_16x16x64_bf16 v[112:115], v[48:51], v[80:87], %[idx]\n" \
"s_nop 1\n" \
"v_cvt_pk_bf16_f32 v120, v120, v121\n" \
"v_cvt_pk_bf16_f32 v121, v122, v123\n" \
"v_smfmac_f32_16x16x64_bf16 v[116:119], v[48:51], v[88:95], %[idx]\n" \
"s_waitcnt lgkmcnt(2)\n" \
"v_cvt_pk_bf16_f32 v122, v124, v125\n" \
"v_cvt_pk_bf16_f32 v123, v126, v127\n" \
"v_smfmac_f32_16x16x64_bf16 v[112:115], v[52:55], v[96:103], %[idx]\n" \
"s_nop 0\n" \
"ds_write_b128 %[stw], v[120:123] offset:4096\n" \
"v_cmp_u_f32 vcc, v120, v123\n" \
"v_smfmac_f32_16x16x64_bf16 v[116:119], v[52:55], v[104:111], %[idx]\n" \
"s_waitcnt lgkmcnt(2)\n" \
"s_nop 1\n" \
"v_smfmac_f32_16x16x64_bf16 v[112:115], v[56:59], a[16:23], %[idx]\n" \
"s_waitcnt vmcnt(3)\n" \
"s_cbranch_vccnz Lvo_s1_%=\n" \
"Lvo_c1_%=:\n" \
"v_mfma_f32_16x16x32_bf16 v[120:123], %[sel], v[32:35], 0\n" \
"s_waitcnt vmcnt(2)\n" \
"s_nop 1\n" \
"v_mfma_f32_16x16x32_bf16 v[124:127], %[sel], v[36:39], 0\n" \
"s_waitcnt lgkmcnt(0)\n" \
"s_barrier\n" \
"v_smfmac_f32_16x16x64_bf16 v[116:119], v[56:59], a[24:31], %[idx]\n" \
"s_nop 0\n" \
"ds_read_b128 v[64:67], %[str] offset:4096\n" \
"ds_read_b128 v[68:71], %[str] offset:5120\n" \
"v_cvt_pk_bf16_f32 v120, v120, v121\n" \
"v_cvt_pk_bf16_f32 v121, v122, v123\n" \
"v_smfmac_f32_16x16x64_bf16 v[112:115], v[60:63], a[48:55], %[idx]\n" \
"s_nop 0\n" \
"ds_read_b128 v[72:75], %[str] offset:6144\n" \
"ds_read_b128 v[76:79], %[str] offset:7168\n" \
"v_cvt_pk_bf16_f32 v122, v124, v125\n" \
"v_cvt_pk_bf16_f32 v123, v126, v127\n" \
"v_smfmac_f32_16x16x64_bf16 v[116:119], v[60:63], a[56:63], %[idx]\n" \
"s_waitcnt lgkmcnt(3)\n" \
"ds_write_b128 %[stw], v[120:123] offset:8192\n" \
"v_cmp_u_f32 vcc, v120, v123\n" \
"v_smfmac_f32_16x16x64_bf16 v[112:115], v[64:67], a[80:87], %[idx]\n" \
"s_nop 2\n" \
"v_smfmac_f32_16x16x64_bf16 v[116:119], v[64:67], a[88:95], %[idx]\n" \
"s_waitcnt lgkmcnt(3)\n" \
"s_cbranch_vccnz Lvo_s2_%=\n" \
"Lvo_c2_%=:\n" \
"v_smfmac_f32_16x16x64_bf16 v[112:115], v[68:71], a[112:119], %[idx]\n" \
"s_waitcnt vmcnt(1)\n" \
"s_nop 1\n" \
"v_mfma_f32_16x16x32_bf16 v[120:123], %[sel], v[40:43], 0\n" \
"s_waitcnt vmcnt(0) lgkmcnt(0)\n" \
"s_barrier\n" \
"v_mfma_f32_16x16x32_bf16 v[124:127], %[sel], v[44:47], 0\n" \
"s_nop 0\n" \
"ds_read_b128 v[48:51], %[str] offset:8192\n" \
"ds_read_b128 v[52:55], %[str] offset:9216\n" \
"v_smfmac_f32_16x16x64_bf16 v[116:119], v[68:71], a[120:127], %[idx]\n" \
"s_nop 0\n" \
"ds_read_b128 v[56:59], %[str] offset:10240\n" \
"ds_read_b128 v[60:63], %[str] offset:11264\n" \
"v_cvt_pk_bf16_f32 v120, v120, v121\n" \
"v_cvt_pk_bf16_f32 v121, v122, v123\n" \
"v_smfmac_f32_16x16x64_bf16 v[112:115], v[72:75], a[144:151], %[idx]\n" \
"s_nop 1\n" \
"v_cvt_pk_bf16_f32 v122, v124, v125\n" \
"v_cvt_pk_bf16_f32 v123, v126, v127\n" \
"v_smfmac_f32_16x16x64_bf16 v[116:119], v[72:75], a[152:159], %[idx]\n" \
"s_nop 0\n" \
"ds_write_b128 %[stw], v[120:123] offset:12288\n" \
"v_cmp_u_f32 vcc, v120, v123\n" \
"v_smfmac_f32_16x16x64_bf16 v[112:115], v[76:79], a[176:183], %[idx]\n" \
"s_nop 2\n" \
"v_smfmac_f32_16x16x64_bf16 v[116:119], v[76:79], a[184:191], %[idx]\n" \
"s_waitcnt lgkmcnt(4)\n" \
"s_cbranch_vccnz Lvo_s3_%=\n" \
"Lvo_c3_%=:\n" \
"v_smfmac_f32_16x16x64_bf16 v[112:115], v[48:51], a[208:215], %[idx]\n" \
"s_waitcnt lgkmcnt(0)\n" \
"s_barrier\n" \
"v_smfmac_f32_16x16x64_bf16 v[116:119], v[48:51], a[216:223], %[idx]\n" \
"s_nop 0\n" \
"ds_read_b128 v[64:67], %[str] offset:12288\n" \
"ds_read_b128 v[68:71], %[str] offset:13312\n" \
"v_smfmac_f32_16x16x64_bf16 v[112:115], v[52:55], a[240:247], %[idx]\n" \
"s_nop 0\n" \
"ds_read_b128 v[72:75], %[str] offset:14336\n" \
"ds_read_b128 v[76:79], %[str] offset:15360\n" \
"v_smfmac_f32_16x16x64_bf16 v[116:119], v[52:55], a[248:255], %[idx]\n" \
"s_nop 2\n" \
"v_smfmac_f32_16x16x64_bf16 v[112:115], v[56:59], v[144:151], %[idx]\n" \
"s_nop 2\n" \
"v_smfmac_f32_16x16x64_bf16 v[116:119], v[56:59], v[152:159], %[idx]\n" \
"s_nop 2\n" \
"v_smfmac_f32_16x16x64_bf16 v[112:115], v[60:63], v[176:183], %[idx]\n" \
"s_nop 2\n" \
"v_smfmac_f32_16x16x64_bf16 v[116:119], v[60:63], v[184:191], %[idx]\n" \
"s_waitcnt lgkmcnt(3)\n" \
"s_nop 1\n" \
"v_smfmac_f32_16x16x64_bf16 v[112:115], v[64:67], v[192:199], %[idx]\n" \
"s_nop 2\n" \
"v_smfmac_f32_16x16x64_bf16 v[116:119], v[64:67], v[200:207], %[idx]\n" \
"s_waitcnt lgkmcnt(2)\n" \
"s_nop 1\n" \
"v_smfmac_f32_16x16x64_bf16 v[112:115], v[68:71], v[208:215], %[idx]\n" \
"s_nop 2\n" \
"v_smfmac_f32_16x16x64_bf16 v[116:119], v[68:71], v[216:223], %[idx]\n" \
"s_waitcnt lgkmcnt(1)\n" \
"s_nop 1\n" \
"v_smfmac_f32_16x16x64_bf16 v[112:115], v[72:75], v[224:231], %[idx]\n" \
"s_waitcnt lgkmcnt(0)\n" \
"s_nop 1\n" \
"v_smfmac_f32_16x16x64_bf16 v[112:115], v[76:79], v[240:247], %[idx]\n" \
"s_nop 2\n" \
"v_smfmac_f32_16x16x64_bf16 v[116:119], v[72:75], v[232:239], %[idx]\n" \
"s_nop 2\n" \
"v_smfmac_f32_16x16x64_bf16 v[116:119], v[76:79], v[248:255], %[idx]\n" \
"s_nop 1\n" \
"v_add_f32 v112, v112, v114\n" \
"v_add_f32 v113, v113, v115\n" \
"v_cvt_pk_bf16_f32 v16, v112, v113\n" \
"s_nop 2\n" \
"v_pk_add_f32 v[116:117], v[116:117], v[118:119]\n" \
"v_cvt_pk_bf16_f32 v17, v116, v117\n" \
"buffer_store_dwordx2 v[16:17], %[l8], %[ws], s63 offen sc1\n" \
"Lvo_vdone_%=:\n" \
"s_memtime s[92:93]\n" \
"s_memrealtime s[94:95]\n" \
"s_and_b32 s66, s65, 62\n" \
"s_cbranch_scc0 Lvo_end_%=\n" \
"s_movk_i32 s74, 400\n" \
"s_nop 0\n" \
"s_add_u32 s66, s62, %[odb]\n" \
"s_waitcnt lgkmcnt(0)\n" \
"s_sub_u32 s94, s94, 8\n" \
"s_lshl_b32 s67, s94, 1\n" \
"v_mov_b32 %[vov], s67\n" \
"s_sub_u32 s92, s92, 184\n" \
"s_mov_b64 exec, 1\n" \
"buffer_store_dword %[vov], off, %[ws], s66 sc1\n" \
"s_mov_b64 exec, -1\n" \
"Lvo_posted_%=:\n" \
"s_add_u32 s75, s62, %[oom]\n" \
"s_add_u32 s76, s75, 4096\n" \
"s_mov_b32 s72, s62\n" \
"s_add_u32 s62, s62, 524288\n" \
"s_and_b32 s62, s62, 2097151\n" \
"s_bitset1_b32 s62, 21\n" \
"s_mov_b32 s68, s62\n" \
"s_add_u32 s69, s68, 16384\n" \
"s_add_u32 s70, s68, 32768\n" \
"s_add_u32 s71, s68, 49152\n" \
"s_add_u32 s63, s62, %[vb]\n" \
"ds_read_b128 v[80:83], %[wl] offset:8192\n" \
"ds_read_b128 v[84:87], %[wl] offset:12288\n" \
"ds_read_b128 v[88:91], %[wl] offset:9216\n" \
"ds_read_b128 v[92:95], %[wl] offset:13312\n" \
"ds_read_b128 v[96:99], %[wl] offset:10240\n" \
"ds_read_b128 v[100:103], %[wl] offset:14336\n" \
"ds_read_b128 v[104:107], %[wl] offset:11264\n" \
"ds_read_b128 v[108:111], %[wl] offset:15360\n" \
"ds_read_b128 v[48:51], %[wl] offset:16384\n" \
"ds_read_b128 v[52:55], %[wl] offset:20480\n" \
"ds_read_b128 v[56:59], %[wl] offset:17408\n" \
"ds_read_b128 v[60:63], %[wl] offset:21504\n" \
"ds_read_b128 v[64:67], %[wl] offset:18432\n" \
"ds_read_b128 v[68:71], %[wl] offset:22528\n" \
"ds_read_b128 v[72:75], %[wl] offset:19456\n" \
"ds_read_b128 v[76:79], %[wl] offset:23552\n" \
"v_mov_b32 v112, 0\n" \
"v_mov_b32 v113, 0\n" \
"v_mov_b32 v114, 0\n" \
"v_mov_b32 v115, 0\n" \
"v_mov_b32 v116, 0\n" \
"v_mov_b32 v117, 0\n" \
"v_mov_b32 v118, 0\n" \
"v_mov_b32 v119, 0\n" \
"v_mov_b32 v120, 0\n" \
"v_mov_b32 v121, 0\n" \
"v_mov_b32 v122, 0\n" \
"v_mov_b32 v123, 0\n" \
"v_mov_b32 v124, 0\n" \
"v_mov_b32 v125, 0\n" \
"v_mov_b32 v126, 0\n" \
"v_mov_b32 v127, 0\n" \
"Lvo_dbd_%=:\n" \
"s_sub_u32 s74, s74, 28\n" \
"s_cbranch_scc0 Lvo_dbd_%=\n" \
"v_bfe_u32 v21, %[wl], 4, 6\n" \
"s_add_u32 s66, s72, 327680\n" \
"v_mul_u32_u24 v21, 16, v21\n" \
"buffer_load_dword v20, v21, %[ws], s66 offen sc1\n" \
"s_waitcnt vmcnt(0)\n" \
"Lvo_db_%=:\n" \
"v_cmp_eq_u32 vcc, -1, v20\n" \
"s_cbranch_vccnz Lvo_sdb_%=\n" \
"v_subrev_u32 v20, s67, v20\n" \
"s_nop 1\n" \
"v_max_i32_dpp v20, v20, v20 quad_perm:[1,0,3,2] row_mask:0xf bank_mask:0xf\n" \
"s_nop 1\n" \
"v_max_i32_dpp v20, v20, v20 quad_perm:[2,3,0,1] row_mask:0xf bank_mask:0xf\n" \
"s_nop 1\n" \
"v_max_i32_dpp v20, v20, v20 row_half_mirror row_mask:0xf bank_mask:0xf\n" \
"s_nop 1\n" \
"v_max_i32_dpp v20, v20, v20 row_mirror row_mask:0xf bank_mask:0xf\n" \
"v_mov_b32 v21, v20\n" \
"s_nop 1\n" \
"v_permlane16_swap_b32 v20, v21\n" \
"v_max_i32 v20, v20, v21\n" \
"v_mov_b32 v21, v20\n" \
"s_nop 1\n" \
"v_permlane32_swap_b32 v20, v21\n" \
"v_max_i32 v20, v20, v21\n" \
"s_nop 0\n" \
"v_readfirstlane_b32 s94, v20\n" \
"s_min_i32 s94, s94, 1000\n" \
"s_max_i32 s94, s94, -1000\n" \
"s_mul_i32 s94, s94, 23\n" \
"s_ashr_i32 s93, s94, 1\n" \
"s_add_u32 s92, s92, s93\n" \
".p2align 6\n" \
"s_waitcnt lgkmcnt(0)\n" \
"Lvo_d1a_%=:\n" \
"s_memtime s[94:95]\n" \
"s_waitcnt lgkmcnt(0)\n" \
"s_sub_u32 s66, s92, s94\n" \
"s_add_u32 s66, s66, %[d1]\n" \
"s_cmp_gt_i32 s66, 200\n" \
"s_cbranch_scc1 Lvo_d1a_%=\n" \
"s_max_i32 s66, s66, 0\n" \
"Lvo_d1_%=:\n" \
"s_sub_u32 s66, s66, 28\n" \
"s_cbranch_scc0 Lvo_d1_%=\n" \
"Lvo_ogo_%=:\n" \
"buffer_load_dwordx4 v[16:19], %[ol], %[ws], s75 offen offset:0 sc1\n" \
"s_nop 0\n" \
"buffer_load_dwordx4 v[20:23], %[ol], %[ws], s75 offen offset:1024 sc1\n" \
"s_nop 0\n" \
"buffer_load_dwordx4 v[24:27], %[ol], %[ws], s75 offen offset:2048 sc1\n" \
"s_nop 0\n" \
"buffer_load_dwordx4 v[28:31], %[ol], %[ws], s75 offen offset:3072 sc1\n" \
"s_nop 0\n" \
"buffer_load_dwordx4 v[32:35], %[ol], %[ws], s76 offen offset:0 sc1\n" \
"s_nop 0\n" \
"buffer_load_dwordx4 v[36:39], %[ol], %[ws], s76 offen offset:1024 sc1\n" \
"s_nop 0\n" \
"buffer_load_dwordx4 v[40:43], %[ol], %[ws], s76 offen offset:2048 sc1\n" \
"s_nop 0\n" \
"buffer_load_dwordx4 v[44:47], %[ol], %[ws], s76 offen offset:3072 sc1\n" \
"s_nop 0\n" \
"Lvo_o_%=:\n" \
"s_waitcnt vmcnt(7)\n" \
"v_smfmac_f32_16x16x64_bf16 v[112:115], v[16:19], v[80:87], %[idx]\n" \
"s_nop 1\n" \
"v_cmp_eq_u32 vcc, -1, v16\n" \
"v_smfmac_f32_16x16x64_bf16 v[116:119], v[16:19], v[88:95], %[idx]\n" \
"s_nop 2\n" \
"v_smfmac_f32_16x16x64_bf16 v[120:123], v[16:19], v[96:103], %[idx]\n" \
"s_cbranch_vccnz Lvo_so0_%=\n" \
"v_smfmac_f32_16x16x64_bf16 v[124:127], v[16:19], v[104:111], %[idx]\n" \
"s_waitcnt vmcnt(6)\n" \
"s_nop 1\n" \
"v_smfmac_f32_16x16x64_bf16 v[112:115], v[20:23], v[48:55], %[idx]\n" \
"s_nop 0\n" \
"ds_read_b128 v[80:83], %[wl] offset:24576\n" \
"v_cmp_eq_u32 vcc, -1, v20\n" \
"v_smfmac_f32_16x16x64_bf16 v[116:119], v[20:23], v[56:63], %[idx]\n" \
"s_nop 0\n" \
"ds_read_b128 v[84:87], %[wl] offset:28672\n" \
"v_smfmac_f32_16x16x64_bf16 v[120:123], v[20:23], v[64:71], %[idx]\n" \
"s_cbranch_vccnz Lvo_so1_%=\n" \
"v_smfmac_f32_16x16x64_bf16 v[124:127], v[20:23], v[72:79], %[idx]\n" \
"s_waitcnt vmcnt(5)\n" \
"s_nop 1\n" \
"v_smfmac_f32_16x16x64_bf16 v[112:115], v[24:27], a[0:7], %[idx]\n" \
"s_nop 0\n" \
"ds_read_b128 v[88:91], %[wl] offset:25600\n" \
"v_cmp_eq_u32 vcc, -1, v24\n" \
"v_smfmac_f32_16x16x64_bf16 v[116:119], v[24:27], a[8:15], %[idx]\n" \
"s_nop 0\n" \
"ds_read_b128 v[92:95], %[wl] offset:29696\n" \
"v_smfmac_f32_16x16x64_bf16 v[120:123], v[24:27], a[32:39], %[idx]\n" \
"s_cbranch_vccnz Lvo_so2_%=\n" \
"v_smfmac_f32_16x16x64_bf16 v[124:127], v[24:27], a[40:47], %[idx]\n" \
"s_waitcnt vmcnt(4)\n" \
"s_nop 1\n" \
"v_smfmac_f32_16x16x64_bf16 v[112:115], v[28:31], a[64:71], %[idx]\n" \
"s_nop 0\n" \
"ds_read_b128 v[96:99], %[wl] offset:26624\n" \
"v_cmp_eq_u32 vcc, -1, v28\n" \
"v_smfmac_f32_16x16x64_bf16 v[116:119], v[28:31], a[72:79], %[idx]\n" \
"s_nop 0\n" \
"ds_read_b128 v[100:103], %[wl] offset:30720\n" \
"v_smfmac_f32_16x16x64_bf16 v[120:123], v[28:31], a[96:103], %[idx]\n" \
"s_cbranch_vccnz Lvo_so3_%=\n" \
"v_smfmac_f32_16x16x64_bf16 v[124:127], v[28:31], a[104:111], %[idx]\n" \
"s_waitcnt vmcnt(3)\n" \
"s_nop 1\n" \
"v_smfmac_f32_16x16x64_bf16 v[112:115], v[32:35], a[128:135], %[idx]\n" \
"s_nop 0\n" \
"ds_read_b128 v[104:107], %[wl] offset:27648\n" \
"v_cmp_eq_u32 vcc, -1, v32\n" \
"v_smfmac_f32_16x16x64_bf16 v[116:119], v[32:35], a[136:143], %[idx]\n" \
"s_nop 0\n" \
"ds_read_b128 v[108:111], %[wl] offset:31744\n" \
"v_smfmac_f32_16x16x64_bf16 v[120:123], v[32:35], a[160:167], %[idx]\n" \
"s_cbranch_vccnz Lvo_so4_%=\n" \
"v_smfmac_f32_16x16x64_bf16 v[124:127], v[32:35], a[168:175], %[idx]\n" \
"s_waitcnt vmcnt(2)\n" \
"s_nop 1\n" \
"v_smfmac_f32_16x16x64_bf16 v[112:115], v[36:39], a[192:199], %[idx]\n" \
"s_nop 1\n" \
"v_cmp_eq_u32 vcc, -1, v36\n" \
"v_smfmac_f32_16x16x64_bf16 v[116:119], v[36:39], a[200:207], %[idx]\n" \
"s_nop 2\n" \
"v_smfmac_f32_16x16x64_bf16 v[120:123], v[36:39], a[224:231], %[idx]\n" \
"s_cbranch_vccnz Lvo_so5_%=\n" \
"v_smfmac_f32_16x16x64_bf16 v[124:127], v[36:39], a[232:239], %[idx]\n" \
"s_waitcnt vmcnt(1) lgkmcnt(6)\n" \
"s_nop 1\n" \
"v_smfmac_f32_16x16x64_bf16 v[112:115], v[40:43], v[80:87], %[idx]\n" \
"s_waitcnt lgkmcnt(4)\n" \
"v_cmp_eq_u32 vcc, -1, v40\n" \
"v_smfmac_f32_16x16x64_bf16 v[116:119], v[40:43], v[88:95], %[idx]\n" \
"s_waitcnt lgkmcnt(2)\n" \
"s_nop 1\n" \
"v_smfmac_f32_16x16x64_bf16 v[120:123], v[40:43], v[96:103], %[idx]\n" \
"s_waitcnt lgkmcnt(0)\n" \
"s_cbranch_vccnz Lvo_so6_%=\n" \
"v_smfmac_f32_16x16x64_bf16 v[124:127], v[40:43], v[104:111], %[idx]\n" \
"s_waitcnt vmcnt(0)\n" \
"s_nop 1\n" \
"v_smfmac_f32_16x16x64_bf16 v[112:115], v[44:47], v[128:135], %[idx]\n" \
"s_nop 1\n" \
"v_cmp_eq_u32 vcc, -1, v44\n" \
"v_smfmac_f32_16x16x64_bf16 v[116:119], v[44:47], v[136:143], %[idx]\n" \
"s_cbranch_vccnz Lvo_so7_%=\n" \
"v_smfmac_f32_16x16x64_bf16 v[120:123], v[44:47], v[160:167], %[idx]\n" \
"s_nop 1\n" \
"v_add_f32 v48, v112, v114\n" \
"v_smfmac_f32_16x16x64_bf16 v[124:127], v[44:47], v[168:175], %[idx]\n" \
"s_nop 1\n" \
"v_add_f32 v50, v113, v115\n" \
"v_add_f32 v49, v116, v118\n" \
"v_add_f32 v51, v117, v119\n" \
"s_nop 1\n" \
"ds_write2st64_b64 %[xw], v[48:49], v[50:51] offset1:1\n" \
"v_add_f32 v52, v121, v123\n" \
"v_add_f32 v53, v125, v127\n" \
"ds_write_b64 %[xw], v[52:53] offset:1024\n" \
"v_cmp_u_f32 vcc, v48, v50\n" \
"v_add_f32 v54, v120, v122\n" \
"v_add_f32 v55, v124, v126\n" \
"s_cbranch_vccnz Lvo_sn_%=\n" \
"Lvo_cn_%=:\n" \
"v_xor_b32 v56, 4096, %[xw]\n" \
"v_xor_b32 v58, 6144, %[xw]\n" \
"v_xor_b32 v60, 2048, %[xw]\n" \
"s_waitcnt lgkmcnt(0)\n" \
"s_barrier\n" \
"ds_read_b64 v[56:57], v56 offset:0\n" \
"ds_read_b64 v[58:59], v58 offset:512\n" \
"ds_read_b64 v[60:61], v60 offset:1024\n" \
"s_waitcnt lgkmcnt(2)\n" \
"v_pk_add_f32 v[54:55], v[54:55], v[56:57]\n" \
"s_waitcnt lgkmcnt(1)\n" \
"v_pk_add_f32 v[54:55], v[54:55], v[58:59]\n" \
"s_waitcnt lgkmcnt(0)\n" \
"v_pk_add_f32 v[54:55], v[54:55], v[60:61]\n" \
"v_cvt_pk_bf16_f32 v54, v54, v55\n" \
"buffer_store_dword v54, %[l4], %[ws], s62 offen sc1\n" \
"Lvo_done_%=:\n" \
"s_add_u32 s65, s65, 1\n" \
"s_branch Lvo_top_%=\n" \
"Lvo_end_%=:\n" \
"s_cmp_eq_u32 s65, 1\n" \
"s_cbranch_scc1 Lvo_rest_%=\n" \
"s_endpgm\n" \
"Lvo_shs_%=:\n" \
"s_sub_u32 s64, s64, 1\n" \
"s_cbranch_scc1 Lvo_dead_%=\n" \
"s_sleep 8\n" \
"buffer_load_dword v125, off, %[ws], %[hs] sc1\n" \
"s_waitcnt vmcnt(0)\n" \
"s_branch Lvo_hs_%=\n" \
"Lvo_s0_%=:\n" \
"s_nop 7\n" \
"s_nop 7\n" \
"v_max3_u32 %[vov], v16, v17, v18\n" \
"v_max3_u32 %[vov], %[vov], v19, v20\n" \
"v_max3_u32 %[vov], %[vov], v21, v22\n" \
"v_max_u32 %[vov], %[vov], v23\n" \
"v_cmp_eq_u32 vcc, -1, %[vov]\n" \
"s_cbranch_vccz Lvo_c0_%=\n" \
"Lvo_r0_%=:\n" \
"s_sub_u32 s64, s64, 1\n" \
"s_cbranch_scc1 Lvo_dead_%=\n" \
"buffer_load_dwordx4 v[16:19], %[mvo], %[ws], s68 offen sc1\n" \
"s_nop 0\n" \
"buffer_load_dwordx4 v[20:23], %[mvo], %[ws], s68 offen offset:512 sc1\n" \
"s_nop 0\n" \
"s_waitcnt vmcnt(2)\n" \
"v_max3_u32 %[vov], v24, v25, v26\n" \
"v_max3_u32 %[vov], %[vov], v27, v28\n" \
"v_max3_u32 %[vov], %[vov], v29, v30\n" \
"v_max_u32 %[vov], %[vov], v31\n" \
"v_cmp_eq_u32 vcc, -1, %[vov]\n" \
"s_cbranch_vccz Lvo_k01_%=\n" \
"buffer_load_dwordx4 v[24:27], %[mvo], %[ws], s69 offen sc1\n" \
"s_nop 0\n" \
"buffer_load_dwordx4 v[28:31], %[mvo], %[ws], s69 offen offset:512 sc1\n" \
"s_nop 0\n" \
"Lvo_k01_%=:\n" \
"v_max3_u32 %[vov], v32, v33, v34\n" \
"v_max3_u32 %[vov], %[vov], v35, v36\n" \
"v_max3_u32 %[vov], %[vov], v37, v38\n" \
"v_max_u32 %[vov], %[vov], v39\n" \
"v_cmp_eq_u32 vcc, -1, %[vov]\n" \
"s_cbranch_vccz Lvo_k02_%=\n" \
"buffer_load_dwordx4 v[32:35], %[mvo], %[ws], s70 offen sc1\n" \
"s_nop 0\n" \
"buffer_load_dwordx4 v[36:39], %[mvo], %[ws], s70 offen offset:512 sc1\n" \
"s_nop 0\n" \
"Lvo_k02_%=:\n" \
"v_max3_u32 %[vov], v40, v41, v42\n" \
"v_max3_u32 %[vov], %[vov], v43, v44\n" \
"v_max3_u32 %[vov], %[vov], v45, v46\n" \
"v_max_u32 %[vov], %[vov], v47\n" \
"v_cmp_eq_u32 vcc, -1, %[vov]\n" \
"s_cbranch_vccz Lvo_k03_%=\n" \
"buffer_load_dwordx4 v[40:43], %[mvo], %[ws], s71 offen sc1\n" \
"s_nop 0\n" \
"buffer_load_dwordx4 v[44:47], %[mvo], %[ws], s71 offen offset:512 sc1\n" \
"s_nop 0\n" \
"Lvo_k03_%=:\n" \
"s_waitcnt vmcnt(0)\n" \
"v_max3_u32 %[vov], v16, v17, v18\n" \
"v_max3_u32 %[vov], %[vov], v19, v20\n" \
"v_max3_u32 %[vov], %[vov], v21, v22\n" \
"v_max_u32 %[vov], %[vov], v23\n" \
"v_cmp_eq_u32 vcc, -1, %[vov]\n" \
"s_cbranch_vccnz Lvo_r0_%=\n" \
"v_mfma_f32_16x16x32_bf16 v[120:123], %[sel], v[16:19], 0\n" \
"v_mfma_f32_16x16x32_bf16 v[124:127], %[sel], v[20:23], 0\n" \
"s_nop 6\n" \
"v_cvt_pk_bf16_f32 v120, v120, v121\n" \
"v_cvt_pk_bf16_f32 v121, v122, v123\n" \
"v_cvt_pk_bf16_f32 v122, v124, v125\n" \
"v_cvt_pk_bf16_f32 v123, v126, v127\n" \
"ds_write_b128 %[stw], v[120:123] offset:0\n" \
"s_waitcnt lgkmcnt(0)\n" \
"s_branch Lvo_c0_%=\n" \
"Lvo_s1_%=:\n" \
"s_nop 7\n" \
"s_nop 7\n" \
"v_max3_u32 %[vov], v24, v25, v26\n" \
"v_max3_u32 %[vov], %[vov], v27, v28\n" \
"v_max3_u32 %[vov], %[vov], v29, v30\n" \
"v_max_u32 %[vov], %[vov], v31\n" \
"v_cmp_eq_u32 vcc, -1, %[vov]\n" \
"s_cbranch_vccz Lvo_c1_%=\n" \
"Lvo_r1_%=:\n" \
"s_sub_u32 s64, s64, 1\n" \
"s_cbranch_scc1 Lvo_dead_%=\n" \
"buffer_load_dwordx4 v[24:27], %[mvo], %[ws], s69 offen sc1\n" \
"s_nop 0\n" \
"buffer_load_dwordx4 v[28:31], %[mvo], %[ws], s69 offen offset:512 sc1\n" \
"s_nop 0\n" \
"s_waitcnt vmcnt(2)\n" \
"v_max3_u32 %[vov], v32, v33, v34\n" \
"v_max3_u32 %[vov], %[vov], v35, v36\n" \
"v_max3_u32 %[vov], %[vov], v37, v38\n" \
"v_max_u32 %[vov], %[vov], v39\n" \
"v_cmp_eq_u32 vcc, -1, %[vov]\n" \
"s_cbranch_vccz Lvo_k12_%=\n" \
"buffer_load_dwordx4 v[32:35], %[mvo], %[ws], s70 offen sc1\n" \
"s_nop 0\n" \
"buffer_load_dwordx4 v[36:39], %[mvo], %[ws], s70 offen offset:512 sc1\n" \
"s_nop 0\n" \
"Lvo_k12_%=:\n" \
"v_max3_u32 %[vov], v40, v41, v42\n" \
"v_max3_u32 %[vov], %[vov], v43, v44\n" \
"v_max3_u32 %[vov], %[vov], v45, v46\n" \
"v_max_u32 %[vov], %[vov], v47\n" \
"v_cmp_eq_u32 vcc, -1, %[vov]\n" \
"s_cbranch_vccz Lvo_k13_%=\n" \
"buffer_load_dwordx4 v[40:43], %[mvo], %[ws], s71 offen sc1\n" \
"s_nop 0\n" \
"buffer_load_dwordx4 v[44:47], %[mvo], %[ws], s71 offen offset:512 sc1\n" \
"s_nop 0\n" \
"Lvo_k13_%=:\n" \
"s_waitcnt vmcnt(0)\n" \
"v_max3_u32 %[vov], v24, v25, v26\n" \
"v_max3_u32 %[vov], %[vov], v27, v28\n" \
"v_max3_u32 %[vov], %[vov], v29, v30\n" \
"v_max_u32 %[vov], %[vov], v31\n" \
"v_cmp_eq_u32 vcc, -1, %[vov]\n" \
"s_cbranch_vccnz Lvo_r1_%=\n" \
"v_mfma_f32_16x16x32_bf16 v[120:123], %[sel], v[24:27], 0\n" \
"v_mfma_f32_16x16x32_bf16 v[124:127], %[sel], v[28:31], 0\n" \
"s_nop 6\n" \
"v_cvt_pk_bf16_f32 v120, v120, v121\n" \
"v_cvt_pk_bf16_f32 v121, v122, v123\n" \
"v_cvt_pk_bf16_f32 v122, v124, v125\n" \
"v_cvt_pk_bf16_f32 v123, v126, v127\n" \
"ds_write_b128 %[stw], v[120:123] offset:4096\n" \
"s_waitcnt lgkmcnt(0)\n" \
"s_branch Lvo_c1_%=\n" \
"Lvo_s2_%=:\n" \
"s_nop 7\n" \
"s_nop 7\n" \
"v_max3_u32 %[vov], v32, v33, v34\n" \
"v_max3_u32 %[vov], %[vov], v35, v36\n" \
"v_max3_u32 %[vov], %[vov], v37, v38\n" \
"v_max_u32 %[vov], %[vov], v39\n" \
"v_cmp_eq_u32 vcc, -1, %[vov]\n" \
"s_cbranch_vccz Lvo_c2_%=\n" \
"Lvo_r2_%=:\n" \
"s_sub_u32 s64, s64, 1\n" \
"s_cbranch_scc1 Lvo_dead_%=\n" \
"buffer_load_dwordx4 v[32:35], %[mvo], %[ws], s70 offen sc1\n" \
"s_nop 0\n" \
"buffer_load_dwordx4 v[36:39], %[mvo], %[ws], s70 offen offset:512 sc1\n" \
"s_nop 0\n" \
"s_waitcnt vmcnt(2)\n" \
"v_max3_u32 %[vov], v40, v41, v42\n" \
"v_max3_u32 %[vov], %[vov], v43, v44\n" \
"v_max3_u32 %[vov], %[vov], v45, v46\n" \
"v_max_u32 %[vov], %[vov], v47\n" \
"v_cmp_eq_u32 vcc, -1, %[vov]\n" \
"s_cbranch_vccz Lvo_k23_%=\n" \
"buffer_load_dwordx4 v[40:43], %[mvo], %[ws], s71 offen sc1\n" \
"s_nop 0\n" \
"buffer_load_dwordx4 v[44:47], %[mvo], %[ws], s71 offen offset:512 sc1\n" \
"s_nop 0\n" \
"Lvo_k23_%=:\n" \
"s_waitcnt vmcnt(0)\n" \
"v_max3_u32 %[vov], v32, v33, v34\n" \
"v_max3_u32 %[vov], %[vov], v35, v36\n" \
"v_max3_u32 %[vov], %[vov], v37, v38\n" \
"v_max_u32 %[vov], %[vov], v39\n" \
"v_cmp_eq_u32 vcc, -1, %[vov]\n" \
"s_cbranch_vccnz Lvo_r2_%=\n" \
"v_mfma_f32_16x16x32_bf16 v[120:123], %[sel], v[32:35], 0\n" \
"v_mfma_f32_16x16x32_bf16 v[124:127], %[sel], v[36:39], 0\n" \
"s_nop 6\n" \
"v_cvt_pk_bf16_f32 v120, v120, v121\n" \
"v_cvt_pk_bf16_f32 v121, v122, v123\n" \
"v_cvt_pk_bf16_f32 v122, v124, v125\n" \
"v_cvt_pk_bf16_f32 v123, v126, v127\n" \
"ds_write_b128 %[stw], v[120:123] offset:8192\n" \
"s_waitcnt lgkmcnt(0)\n" \
"s_branch Lvo_c2_%=\n" \
"Lvo_s3_%=:\n" \
"s_nop 7\n" \
"s_nop 7\n" \
"v_max3_u32 %[vov], v40, v41, v42\n" \
"v_max3_u32 %[vov], %[vov], v43, v44\n" \
"v_max3_u32 %[vov], %[vov], v45, v46\n" \
"v_max_u32 %[vov], %[vov], v47\n" \
"v_cmp_eq_u32 vcc, -1, %[vov]\n" \
"s_cbranch_vccz Lvo_c3_%=\n" \
"Lvo_r3_%=:\n" \
"s_sub_u32 s64, s64, 1\n" \
"s_cbranch_scc1 Lvo_dead_%=\n" \
"buffer_load_dwordx4 v[40:43], %[mvo], %[ws], s71 offen sc1\n" \
"s_nop 0\n" \
"buffer_load_dwordx4 v[44:47], %[mvo], %[ws], s71 offen offset:512 sc1\n" \
"s_nop 0\n" \
"s_waitcnt vmcnt(2)\n" \
"s_waitcnt vmcnt(0)\n" \
"v_max3_u32 %[vov], v40, v41, v42\n" \
"v_max3_u32 %[vov], %[vov], v43, v44\n" \
"v_max3_u32 %[vov], %[vov], v45, v46\n" \
"v_max_u32 %[vov], %[vov], v47\n" \
"v_cmp_eq_u32 vcc, -1, %[vov]\n" \
"s_cbranch_vccnz Lvo_r3_%=\n" \
"v_mfma_f32_16x16x32_bf16 v[120:123], %[sel], v[40:43], 0\n" \
"v_mfma_f32_16x16x32_bf16 v[124:127], %[sel], v[44:47], 0\n" \
"s_nop 6\n" \
"v_cvt_pk_bf16_f32 v120, v120, v121\n" \
"v_cvt_pk_bf16_f32 v121, v122, v123\n" \
"v_cvt_pk_bf16_f32 v122, v124, v125\n" \
"v_cvt_pk_bf16_f32 v123, v126, v127\n" \
"ds_write_b128 %[stw], v[120:123] offset:12288\n" \
"s_waitcnt lgkmcnt(0)\n" \
"s_branch Lvo_c3_%=\n" \
"Lvo_sdb_%=:\n" \
"s_sub_u32 s64, s64, 1\n" \
"s_cbranch_scc1 Lvo_dead_%=\n" \
"s_sleep 1\n" \
"v_bfe_u32 v21, %[wl], 4, 6\n" \
"s_add_u32 s66, s72, 327680\n" \
"v_mul_u32_u24 v21, 16, v21\n" \
"buffer_load_dword v20, v21, %[ws], s66 offen sc1\n" \
"s_waitcnt vmcnt(0)\n" \
"s_branch Lvo_db_%=\n" \
"Lvo_so0_%=:\n" \
"buffer_load_dwordx4 v[16:19], %[ol], %[ws], s75 offen offset:0 sc1\n" \
"s_nop 0\n" \
"Lvo_so1_%=:\n" \
"buffer_load_dwordx4 v[20:23], %[ol], %[ws], s75 offen offset:1024 sc1\n" \
"s_nop 0\n" \
"Lvo_so2_%=:\n" \
"buffer_load_dwordx4 v[24:27], %[ol], %[ws], s75 offen offset:2048 sc1\n" \
"s_nop 0\n" \
"Lvo_so3_%=:\n" \
"buffer_load_dwordx4 v[28:31], %[ol], %[ws], s75 offen offset:3072 sc1\n" \
"s_nop 0\n" \
"Lvo_so4_%=:\n" \
"buffer_load_dwordx4 v[32:35], %[ol], %[ws], s76 offen offset:0 sc1\n" \
"s_nop 0\n" \
"Lvo_so5_%=:\n" \
"buffer_load_dwordx4 v[36:39], %[ol], %[ws], s76 offen offset:1024 sc1\n" \
"s_nop 0\n" \
"Lvo_so6_%=:\n" \
"buffer_load_dwordx4 v[40:43], %[ol], %[ws], s76 offen offset:2048 sc1\n" \
"s_nop 0\n" \
"Lvo_so7_%=:\n" \
"buffer_load_dwordx4 v[44:47], %[ol], %[ws], s76 offen offset:3072 sc1\n" \
"s_nop 0\n" \
"s_sub_u32 s64, s64, 1\n" \
"s_cbranch_scc1 Lvo_dead_%=\n" \
"s_nop 7\n" \
"s_nop 7\n" \
"s_waitcnt lgkmcnt(0)\n" \
"ds_read_b128 v[80:83], %[wl] offset:8192\n" \
"ds_read_b128 v[84:87], %[wl] offset:12288\n" \
"ds_read_b128 v[88:91], %[wl] offset:9216\n" \
"ds_read_b128 v[92:95], %[wl] offset:13312\n" \
"ds_read_b128 v[96:99], %[wl] offset:10240\n" \
"ds_read_b128 v[100:103], %[wl] offset:14336\n" \
"ds_read_b128 v[104:107], %[wl] offset:11264\n" \
"ds_read_b128 v[108:111], %[wl] offset:15360\n" \
"v_mov_b32 v112, 0\n" \
"v_mov_b32 v113, 0\n" \
"v_mov_b32 v114, 0\n" \
"v_mov_b32 v115, 0\n" \
"v_mov_b32 v116, 0\n" \
"v_mov_b32 v117, 0\n" \
"v_mov_b32 v118, 0\n" \
"v_mov_b32 v119, 0\n" \
"v_mov_b32 v120, 0\n" \
"v_mov_b32 v121, 0\n" \
"v_mov_b32 v122, 0\n" \
"v_mov_b32 v123, 0\n" \
"v_mov_b32 v124, 0\n" \
"v_mov_b32 v125, 0\n" \
"v_mov_b32 v126, 0\n" \
"v_mov_b32 v127, 0\n" \
"s_waitcnt lgkmcnt(0)\n" \
"s_branch Lvo_o_%=\n" \
"Lvo_sn_%=:\n" \
"s_waitcnt vmcnt(7)\n" \
"v_max_u32 %[vov], v16, v17\n" \
"v_max3_u32 %[vov], %[vov], v18, v19\n" \
"s_waitcnt vmcnt(6)\n" \
"v_max3_u32 %[vov], %[vov], v20, v21\n" \
"v_max3_u32 %[vov], %[vov], v22, v23\n" \
"s_waitcnt vmcnt(5)\n" \
"v_max3_u32 %[vov], %[vov], v24, v25\n" \
"v_max3_u32 %[vov], %[vov], v26, v27\n" \
"s_waitcnt vmcnt(4)\n" \
"v_max3_u32 %[vov], %[vov], v28, v29\n" \
"v_max3_u32 %[vov], %[vov], v30, v31\n" \
"s_waitcnt vmcnt(3)\n" \
"v_max3_u32 %[vov], %[vov], v32, v33\n" \
"v_max3_u32 %[vov], %[vov], v34, v35\n" \
"s_waitcnt vmcnt(2)\n" \
"v_max3_u32 %[vov], %[vov], v36, v37\n" \
"v_max3_u32 %[vov], %[vov], v38, v39\n" \
"s_waitcnt vmcnt(1)\n" \
"v_max3_u32 %[vov], %[vov], v40, v41\n" \
"v_max3_u32 %[vov], %[vov], v42, v43\n" \
"s_waitcnt vmcnt(0)\n" \
"v_max3_u32 %[vov], %[vov], v44, v45\n" \
"v_max3_u32 %[vov], %[vov], v46, v47\n" \
"v_cmp_eq_u32 vcc, -1, %[vov]\n" \
"s_cbranch_vccz Lvo_cn_%=\n" \
"s_waitcnt lgkmcnt(0)\n" \
"ds_read_b128 v[48:51], %[wl] offset:16384\n" \
"ds_read_b128 v[52:55], %[wl] offset:20480\n" \
"ds_read_b128 v[56:59], %[wl] offset:17408\n" \
"ds_read_b128 v[60:63], %[wl] offset:21504\n" \
"s_branch Lvo_so0_%=\n" \
"Lvo_dead_%=:\n" \
"v_mov_b32 %[vov], 0x7777\n" \
"buffer_store_dword %[vov], off, %[ws], 0 sc1\n" \
"Lvo_last_%=:\n" \
"s_endpgm\n" \
"Lvo_rest_%=:\n" \
"s_waitcnt lgkmcnt(0)\n" \
"v_and_b32 v50, 0x3c0, %[wl]\n" \
"v_add_u32 v50, v50, %[wl]\n" \
"v_lshrrev_b32 v51, 9, %[wl]\n" \
"v_xor_b32 v51, v51, %[wl]\n" \
"v_lshrrev_b32 v51, 1, v51\n" \
"v_and_or_b32 v51, v51, 64, v50\n" \
"v_xor_b32 v50, 64, v51\n" \
"v_bfe_u32 v104, %[wl], 4, 6\n" \
"v_cmp_gt_u32 vcc, 8, v104\n" \
"v_mul_u32_u24 v104, 2048, v104\n" \
"s_nop 0\n" \
"v_cndmask_b32 v104, 2.0, v104, vcc\n" \
"s_add_u32 m0, %[wbase], 23552\n" \
"s_add_u32 s66, %[soo], 446464\n" \
"buffer_load_dwordx4 %[voo], %[ro], s66 offen lds\n" \
"s_add_u32 m0, %[wbase], 24576\n" \
"s_add_u32 s66, %[soo], 524288\n" \
"buffer_load_dwordx4 %[voo], %[ro], s66 offen lds\n" \
"s_add_u32 s66, %[ho], 655360\n" \
"buffer_load_dword v101, v104, %[ro], s66 offen\n" \
"s_add_u32 m0, %[wbase], 25600\n" \
"s_add_u32 s66, %[soo], 540672\n" \
"buffer_load_dwordx4 %[voo], %[ro], s66 offen lds\n" \
"s_add_u32 m0, %[wbase], 26624\n" \
"s_add_u32 s66, %[soo], 528384\n" \
"buffer_load_dwordx4 %[voo], %[ro], s66 offen lds\n" \
"s_add_u32 m0, %[wbase], 27648\n" \
"s_add_u32 s66, %[soo], 544768\n" \
"buffer_load_dwordx4 %[voo], %[ro], s66 offen lds\n" \
"s_waitcnt lgkmcnt(0)\n" \
"s_barrier\n" \
"ds_read_b64_tr_b16 a[64:65], v50 offset:16384\n" \
"ds_read_b64_tr_b16 a[66:67], v50 offset:18432\n" \
"ds_read_b64_tr_b16 a[72:73], v50 offset:16392\n" \
"ds_read_b64_tr_b16 a[74:75], v50 offset:18440\n" \
"ds_read_b64_tr_b16 a[96:97], v51 offset:16384\n" \
"ds_read_b64_tr_b16 a[98:99], v51 offset:18432\n" \
"ds_read_b64_tr_b16 a[104:105], v51 offset:16392\n" \
"ds_read_b64_tr_b16 a[106:107], v51 offset:18440\n" \
"s_add_u32 m0, %[wbase], 28672\n" \
"s_add_u32 s66, %[soo], 557056\n" \
"buffer_load_dwordx4 %[voo], %[ro], s66 offen lds\n" \
"s_add_u32 s66, %[ho], 688128\n" \
"buffer_load_dword v101, v104, %[ro], s66 offen\n" \
"s_add_u32 m0, %[wbase], 29696\n" \
"s_add_u32 s66, %[soo], 573440\n" \
"buffer_load_dwordx4 %[voo], %[ro], s66 offen lds\n" \
"s_add_u32 m0, %[wbase], 30720\n" \
"s_add_u32 s66, %[soo], 561152\n" \
"buffer_load_dwordx4 %[voo], %[ro], s66 offen lds\n" \
"s_waitcnt vmcnt(9)\n" \
"s_add_u32 m0, %[wbase], 31744\n" \
"s_add_u32 s66, %[soo], 577536\n" \
"buffer_load_dwordx4 %[voo], %[ro], s66 offen lds\n" \
"s_waitcnt lgkmcnt(0)\n" \
"s_barrier\n" \
"ds_read_b64_tr_b16 a[68:69], v50 offset:20480\n" \
"ds_read_b64_tr_b16 a[70:71], v50 offset:22528\n" \
"ds_read_b64_tr_b16 a[76:77], v50 offset:20488\n" \
"ds_read_b64_tr_b16 a[78:79], v50 offset:22536\n" \
"ds_read_b64_tr_b16 a[100:101], v51 offset:20480\n" \
"ds_read_b64_tr_b16 a[102:103], v51 offset:22528\n" \
"ds_read_b64_tr_b16 a[108:109], v51 offset:20488\n" \
"ds_read_b64_tr_b16 a[110:111], v51 offset:22536\n" \
"s_waitcnt vmcnt(9)\n" \
"s_add_u32 m0, %[wbase], 16384\n" \
"s_add_u32 s66, %[soo], 655360\n" \
"buffer_load_dwordx4 %[voo], %[ro], s66 offen lds\n" \
"s_add_u32 s66, %[ho], 917504\n" \
"buffer_load_dword v101, v104, %[ro], s66 offen\n" \
"s_waitcnt vmcnt(9)\n" \
"s_add_u32 m0, %[wbase], 17408\n" \
"s_add_u32 s66, %[soo], 671744\n" \
"buffer_load_dwordx4 %[voo], %[ro], s66 offen lds\n" \
"s_waitcnt vmcnt(9)\n" \
"s_add_u32 m0, %[wbase], 18432\n" \
"s_add_u32 s66, %[soo], 659456\n" \
"buffer_load_dwordx4 %[voo], %[ro], s66 offen lds\n" \
"s_waitcnt vmcnt(9)\n" \
"s_add_u32 m0, %[wbase], 19456\n" \
"s_add_u32 s66, %[soo], 675840\n" \
"buffer_load_dwordx4 %[voo], %[ro], s66 offen lds\n" \
"s_waitcnt lgkmcnt(0)\n" \
"s_barrier\n" \
"ds_read_b64_tr_b16 a[128:129], v50 offset:24576\n" \
"ds_read_b64_tr_b16 a[130:131], v50 offset:26624\n" \
"ds_read_b64_tr_b16 a[136:137], v50 offset:24584\n" \
"ds_read_b64_tr_b16 a[138:139], v50 offset:26632\n" \
"ds_read_b64_tr_b16 a[160:161], v51 offset:24576\n" \
"ds_read_b64_tr_b16 a[162:163], v51 offset:26624\n" \
"ds_read_b64_tr_b16 a[168:169], v51 offset:24584\n" \
"ds_read_b64_tr_b16 a[170:171], v51 offset:26632\n" \
"s_waitcnt vmcnt(9)\n" \
"s_add_u32 m0, %[wbase], 20480\n" \
"s_add_u32 s66, %[soo], 688128\n" \
"buffer_load_dwordx4 %[voo], %[ro], s66 offen lds\n" \
"s_add_u32 s66, %[ho], 950272\n" \
"buffer_load_dword v101, v104, %[ro], s66 offen\n" \
"v_lshlrev_b32 v102, 3, %[wl]\n" \
"v_and_b32 v102, 0x1f80, v102\n" \
"v_min_u32 v102, Lvo_last_%=-Lvo_top_%=, v102\n" \
"s_getpc_b64 s[94:95]\n" \
"Lvo_pc100_%=:\n" \
"s_add_u32 s94, s94, Lvo_top_%=-Lvo_pc100_%=\n" \
"s_addc_u32 s95, s95, -1\n" \
"global_load_dword v103, v102, s[94:95]\n" \
"s_waitcnt vmcnt(10)\n" \
"s_add_u32 m0, %[wbase], 21504\n" \
"s_add_u32 s66, %[soo], 704512\n" \
"buffer_load_dwordx4 %[voo], %[ro], s66 offen lds\n" \
"s_waitcnt vmcnt(10)\n" \
"s_add_u32 m0, %[wbase], 22528\n" \
"s_add_u32 s66, %[soo], 692224\n" \
"buffer_load_dwordx4 %[voo], %[ro], s66 offen lds\n" \
"s_waitcnt vmcnt(10)\n" \
"s_add_u32 m0, %[wbase], 23552\n" \
"s_add_u32 s66, %[soo], 708608\n" \
"buffer_load_dwordx4 %[voo], %[ro], s66 offen lds\n" \
"s_waitcnt lgkmcnt(0)\n" \
"s_barrier\n" \
"ds_read_b64_tr_b16 a[132:133], v50 offset:28672\n" \
"ds_read_b64_tr_b16 a[134:135], v50 offset:30720\n" \
"ds_read_b64_tr_b16 a[140:141], v50 offset:28680\n" \
"ds_read_b64_tr_b16 a[142:143], v50 offset:30728\n" \
"ds_read_b64_tr_b16 a[164:165], v51 offset:28672\n" \
"ds_read_b64_tr_b16 a[166:167], v51 offset:30720\n" \
"ds_read_b64_tr_b16 a[172:173], v51 offset:28680\n" \
"ds_read_b64_tr_b16 a[174:175], v51 offset:30728\n" \
"s_waitcnt vmcnt(10)\n" \
"s_add_u32 m0, %[wbase], 24576\n" \
"s_add_u32 s66, %[soo], 917504\n" \
"buffer_load_dwordx4 %[voo], %[ro], s66 offen lds\n" \
"s_add_u32 s66, %[ho], 131072\n" \
"buffer_load_dword v101, v104, %[ro], s66 offen\n" \
"s_waitcnt vmcnt(10)\n" \
"s_add_u32 m0, %[wbase], 25600\n" \
"s_add_u32 s66, %[soo], 933888\n" \
"buffer_load_dwordx4 %[voo], %[ro], s66 offen lds\n" \
"s_waitcnt vmcnt(10)\n" \
"s_add_u32 m0, %[wbase], 26624\n" \
"s_add_u32 s66, %[soo], 921600\n" \
"buffer_load_dwordx4 %[voo], %[ro], s66 offen lds\n" \
"s_waitcnt vmcnt(10)\n" \
"s_add_u32 m0, %[wbase], 27648\n" \
"s_add_u32 s66, %[soo], 937984\n" \
"buffer_load_dwordx4 %[voo], %[ro], s66 offen lds\n" \
"s_waitcnt lgkmcnt(0)\n" \
"s_barrier\n" \
"ds_read_b64_tr_b16 a[192:193], v50 offset:16384\n" \
"ds_read_b64_tr_b16 a[194:195], v50 offset:18432\n" \
"ds_read_b64_tr_b16 a[200:201], v50 offset:16392\n" \
"ds_read_b64_tr_b16 a[202:203], v50 offset:18440\n" \
"ds_read_b64_tr_b16 a[224:225], v51 offset:16384\n" \
"ds_read_b64_tr_b16 a[226:227], v51 offset:18432\n" \
"ds_read_b64_tr_b16 a[232:233], v51 offset:16392\n" \
"ds_read_b64_tr_b16 a[234:235], v51 offset:18440\n" \
"s_waitcnt vmcnt(10)\n" \
"s_add_u32 m0, %[wbase], 28672\n" \
"s_add_u32 s66, %[soo], 950272\n" \
"buffer_load_dwordx4 %[voo], %[ro], s66 offen lds\n" \
"s_add_u32 s66, %[ho], 163840\n" \
"buffer_load_dword v101, v104, %[ro], s66 offen\n" \
"s_waitcnt vmcnt(9)\n" \
"s_add_u32 m0, %[wbase], 29696\n" \
"s_add_u32 s66, %[soo], 966656\n" \
"buffer_load_dwordx4 %[voo], %[ro], s66 offen lds\n" \
"s_waitcnt vmcnt(9)\n" \
"s_add_u32 m0, %[wbase], 30720\n" \
"s_add_u32 s66, %[soo], 954368\n" \
"buffer_load_dwordx4 %[voo], %[ro], s66 offen lds\n" \
"s_waitcnt vmcnt(9)\n" \
"s_add_u32 m0, %[wbase], 31744\n" \
"s_add_u32 s66, %[soo], 970752\n" \
"buffer_load_dwordx4 %[voo], %[ro], s66 offen lds\n" \
"s_waitcnt lgkmcnt(0)\n" \
"s_barrier\n" \
"ds_read_b64_tr_b16 a[196:197], v50 offset:20480\n" \
"ds_read_b64_tr_b16 a[198:199], v50 offset:22528\n" \
"ds_read_b64_tr_b16 a[204:205], v50 offset:20488\n" \
"ds_read_b64_tr_b16 a[206:207], v50 offset:22536\n" \
"ds_read_b64_tr_b16 a[228:229], v51 offset:20480\n" \
"ds_read_b64_tr_b16 a[230:231], v51 offset:22528\n" \
"ds_read_b64_tr_b16 a[236:237], v51 offset:20488\n" \
"ds_read_b64_tr_b16 a[238:239], v51 offset:22536\n" \
"s_waitcnt vmcnt(9)\n" \
"s_add_u32 m0, %[wbase], 16384\n" \
"s_add_u32 s66, %[soo], 131072\n" \
"buffer_load_dwordx4 %[voo], %[ro], s66 offen lds\n" \
"s_add_u32 s66, %[ho], 786432\n" \
"buffer_load_dword v101, v104, %[ro], s66 offen\n" \
"s_waitcnt vmcnt(9)\n" \
"s_add_u32 m0, %[wbase], 17408\n" \
"s_add_u32 s66, %[soo], 147456\n" \
"buffer_load_dwordx4 %[voo], %[ro], s66 offen lds\n" \
"s_waitcnt vmcnt(9)\n" \
"s_add_u32 m0, %[wbase], 18432\n" \
"s_add_u32 s66, %[soo], 135168\n" \
"buffer_load_dwordx4 %[voo], %[ro], s66 offen lds\n" \
"s_waitcnt vmcnt(9)\n" \
"s_add_u32 m0, %[wbase], 19456\n" \
"s_add_u32 s66, %[soo], 151552\n" \
"buffer_load_dwordx4 %[voo], %[ro], s66 offen lds\n" \
"s_waitcnt lgkmcnt(0)\n" \
"s_barrier\n" \
"ds_read_b64_tr_b16 v[128:129], v50 offset:24576\n" \
"ds_read_b64_tr_b16 v[130:131], v50 offset:26624\n" \
"ds_read_b64_tr_b16 v[136:137], v50 offset:24584\n" \
"ds_read_b64_tr_b16 v[138:139], v50 offset:26632\n" \
"ds_read_b64_tr_b16 v[160:161], v51 offset:24576\n" \
"ds_read_b64_tr_b16 v[162:163], v51 offset:26624\n" \
"ds_read_b64_tr_b16 v[168:169], v51 offset:24584\n" \
"ds_read_b64_tr_b16 v[170:171], v51 offset:26632\n" \
"s_waitcnt vmcnt(9)\n" \
"s_add_u32 m0, %[wbase], 20480\n" \
"s_add_u32 s66, %[soo], 163840\n" \
"buffer_load_dwordx4 %[voo], %[ro], s66 offen lds\n" \
"s_add_u32 s66, %[ho], 819200\n" \
"buffer_load_dword v101, v104, %[ro], s66 offen\n" \
"s_waitcnt vmcnt(9)\n" \
"s_add_u32 m0, %[wbase], 21504\n" \
"s_add_u32 s66, %[soo], 180224\n" \
"buffer_load_dwordx4 %[voo], %[ro], s66 offen lds\n" \
"s_waitcnt vmcnt(9)\n" \
"s_add_u32 m0, %[wbase], 22528\n" \
"s_add_u32 s66, %[soo], 167936\n" \
"buffer_load_dwordx4 %[voo], %[ro], s66 offen lds\n" \
"s_waitcnt vmcnt(9)\n" \
"s_add_u32 m0, %[wbase], 23552\n" \
"s_add_u32 s66, %[soo], 184320\n" \
"buffer_load_dwordx4 %[voo], %[ro], s66 offen lds\n" \
"s_waitcnt lgkmcnt(0)\n" \
"s_barrier\n" \
"ds_read_b64_tr_b16 v[132:133], v50 offset:28672\n" \
"ds_read_b64_tr_b16 v[134:135], v50 offset:30720\n" \
"ds_read_b64_tr_b16 v[140:141], v50 offset:28680\n" \
"ds_read_b64_tr_b16 v[142:143], v50 offset:30728\n" \
"ds_read_b64_tr_b16 v[164:165], v51 offset:28672\n" \
"ds_read_b64_tr_b16 v[166:167], v51 offset:30720\n" \
"ds_read_b64_tr_b16 v[172:173], v51 offset:28680\n" \
"ds_read_b64_tr_b16 v[174:175], v51 offset:30728\n" \
"s_waitcnt vmcnt(9)\n" \
"s_add_u32 m0, %[wbase], 24576\n" \
"s_add_u32 s66, %[soo], 786432\n" \
"buffer_load_dwordx4 %[voo], %[ro], s66 offen lds\n" \
"s_waitcnt vmcnt(8)\n" \
"s_add_u32 m0, %[wbase], 25600\n" \
"s_add_u32 s66, %[soo], 802816\n" \
"buffer_load_dwordx4 %[voo], %[ro], s66 offen lds\n" \
"s_waitcnt vmcnt(8)\n" \
"s_add_u32 m0, %[wbase], 26624\n" \
"s_add_u32 s66, %[soo], 790528\n" \
"buffer_load_dwordx4 %[voo], %[ro], s66 offen lds\n" \
"s_waitcnt vmcnt(8)\n" \
"s_add_u32 m0, %[wbase], 27648\n" \
"s_add_u32 s66, %[soo], 806912\n" \
"buffer_load_dwordx4 %[voo], %[ro], s66 offen lds\n" \
"s_waitcnt lgkmcnt(0)\n" \
"s_barrier\n" \
"ds_read_b64_tr_b16 v[16:17], v50 offset:16384\n" \
"ds_read_b64_tr_b16 v[18:19], v50 offset:18432\n" \
"ds_read_b64_tr_b16 v[20:21], v50 offset:16392\n" \
"ds_read_b64_tr_b16 v[22:23], v50 offset:18440\n" \
"ds_read_b64_tr_b16 v[24:25], v51 offset:16384\n" \
"ds_read_b64_tr_b16 v[26:27], v51 offset:18432\n" \
"ds_read_b64_tr_b16 v[28:29], v51 offset:16392\n" \
"ds_read_b64_tr_b16 v[30:31], v51 offset:18440\n" \
"s_waitcnt vmcnt(8)\n" \
"s_add_u32 m0, %[wbase], 28672\n" \
"s_add_u32 s66, %[soo], 819200\n" \
"buffer_load_dwordx4 %[voo], %[ro], s66 offen lds\n" \
"s_waitcnt vmcnt(7)\n" \
"s_add_u32 m0, %[wbase], 29696\n" \
"s_add_u32 s66, %[soo], 835584\n" \
"buffer_load_dwordx4 %[voo], %[ro], s66 offen lds\n" \
"s_waitcnt vmcnt(7)\n" \
"s_add_u32 m0, %[wbase], 30720\n" \
"s_add_u32 s66, %[soo], 823296\n" \
"buffer_load_dwordx4 %[voo], %[ro], s66 offen lds\n" \
"s_waitcnt vmcnt(7)\n" \
"s_add_u32 m0, %[wbase], 31744\n" \
"s_add_u32 s66, %[soo], 839680\n" \
"buffer_load_dwordx4 %[voo], %[ro], s66 offen lds\n" \
"s_memtime s[92:93]\n" \
"s_memrealtime s[94:95]\n" \
"s_add_u32 s66, s62, %[odb]\n" \
"s_waitcnt lgkmcnt(0)\n" \
"s_sub_u32 s94, s94, -70\n" \
"s_lshl_b32 s67, s94, 1\n" \
"v_mov_b32 %[vov], s67\n" \
"s_sub_u32 s92, s92, -1610\n" \
"s_mov_b64 exec, 1\n" \
"buffer_store_dword %[vov], off, %[ws], s66 sc1\n" \
"s_mov_b64 exec, -1\n" \
"s_waitcnt lgkmcnt(0)\n" \
"s_barrier\n" \
"ds_write_b128 %[wl], v[16:19] offset:16384\n" \
"ds_write_b128 %[wl], v[20:23] offset:17408\n" \
"ds_write_b128 %[wl], v[24:27] offset:18432\n" \
"ds_write_b128 %[wl], v[28:31] offset:19456\n" \
"ds_read_b64_tr_b16 v[32:33], v50 offset:20480\n" \
"ds_read_b64_tr_b16 v[34:35], v50 offset:22528\n" \
"ds_read_b64_tr_b16 v[36:37], v50 offset:20488\n" \
"ds_read_b64_tr_b16 v[38:39], v50 offset:22536\n" \
"ds_read_b64_tr_b16 v[40:41], v51 offset:20480\n" \
"ds_read_b64_tr_b16 v[42:43], v51 offset:22528\n" \
"ds_read_b64_tr_b16 v[44:45], v51 offset:20488\n" \
"ds_read_b64_tr_b16 v[46:47], v51 offset:22536\n" \
"s_waitcnt vmcnt(5)\n" \
"s_waitcnt lgkmcnt(0)\n" \
"s_barrier\n" \
"ds_write_b128 %[wl], v[32:35] offset:20480\n" \
"ds_write_b128 %[wl], v[36:39] offset:21504\n" \
"ds_write_b128 %[wl], v[40:43] offset:22528\n" \
"ds_write_b128 %[wl], v[44:47] offset:23552\n" \
"ds_read_b64_tr_b16 v[16:17], v50 offset:24576\n" \
"ds_read_b64_tr_b16 v[18:19], v50 offset:26624\n" \
"ds_read_b64_tr_b16 v[20:21], v50 offset:24584\n" \
"ds_read_b64_tr_b16 v[22:23], v50 offset:26632\n" \
"ds_read_b64_tr_b16 v[24:25], v51 offset:24576\n" \
"ds_read_b64_tr_b16 v[26:27], v51 offset:26624\n" \
"ds_read_b64_tr_b16 v[28:29], v51 offset:24584\n" \
"ds_read_b64_tr_b16 v[30:31], v51 offset:26632\n" \
"s_waitcnt vmcnt(1)\n" \
"s_waitcnt lgkmcnt(0)\n" \
"s_barrier\n" \
"ds_write_b128 %[wl], v[16:19] offset:24576\n" \
"ds_write_b128 %[wl], v[20:23] offset:25600\n" \
"ds_write_b128 %[wl], v[24:27] offset:26624\n" \
"ds_write_b128 %[wl], v[28:31] offset:27648\n" \
"ds_read_b64_tr_b16 v[32:33], v50 offset:28672\n" \
"ds_read_b64_tr_b16 v[34:35], v50 offset:30720\n" \
"ds_read_b64_tr_b16 v[36:37], v50 offset:28680\n" \
"ds_read_b64_tr_b16 v[38:39], v50 offset:30728\n" \
"ds_read_b64_tr_b16 v[40:41], v51 offset:28672\n" \
"ds_read_b64_tr_b16 v[42:43], v51 offset:30720\n" \
"ds_read_b64_tr_b16 v[44:45], v51 offset:28680\n" \
"ds_read_b64_tr_b16 v[46:47], v51 offset:30728\n" \
"s_waitcnt lgkmcnt(0)\n" \
"s_barrier\n" \
"ds_write_b128 %[wl], v[32:35] offset:28672\n" \
"ds_write_b128 %[wl], v[36:39] offset:29696\n" \
"ds_write_b128 %[wl], v[40:43] offset:30720\n" \
"ds_write_b128 %[wl], v[44:47] offset:31744\n" \
"s_waitcnt lgkmcnt(0)\n" \
"s_mov_b32 s74, 0\n" \
"Lvo_fin_%=:\n" \
"s_branch Lvo_posted_%=\n" \
    :: [wl] "v"(wl), [str] "v"(str), [stw] "v"(stw), [mvo] "v"(mvo), [sel] "v"(sel), [idx] "v"(idx), [xw] "v"(xw), [ol] "v"(ol), [l8] "v"(l8), [l4] "v"(l4), \
       [vov] "v"(vov), [voo] "v"(voo), [ws] "s"(ws), [rv] "s"(rv), [ro] "s"(ro), [sov] "s"(sov), [soo] "s"(soo), [wbase] "s"(wbase), \
       [hs] "s"(hs), [hp] "s"(hp), [ho] "s"(ho), [vb] "s"(vb), [odb] "s"(odb), [oom] "s"(oom), [d0] "s"(d0), [d1] "s"(d1) \
    : "v16", "v17", "v18", "v19", "v20", "v21", "v22", "v23", "v24", "v25", "v26", "v27", "v28", "v29", "v30", "v31", "v32", "v33", "v34", "v35", "v36", "v37", "v38", "v39", "v40", "v41", "v42", "v43", "v44", "v45", "v46", "v47", "v48", "v49", "v50", "v51", "v52", "v53", "v54", "v55", "v56", "v57", "v58", "v59", "v60", "v61", "v62", "v63", "v64", "v65", "v66", "v67", "v68", "v69", "v70", "v71", "v72", "v73", "v74", "v75", "v76", "v77", "v78", "v79", "v80", "v81", "v82", "v83", "v84", "v85", "v86", "v87", "v88", "v89", "v90", "v91", "v92", "v93", "v94", "v95", "v96", "v97", "v98", "v99", "v100", "v101", "v102", "v103", "v104", "v105", "v106", "v107", "v108", "v109", "v110", "v111", "v112", "v113", "v114", "v115", "v116", "v117", "v118", "v119", "v120", "v121", "v122", "v123", "v124", "v125", "v126", "v127", "v128", "v129", "v130", "v131", "v132", "v133", "v134", "v135", "v136", "v137", "v138", "v139", "v140", "v141", "v142", "v143", "v144", "v145", "v146", "v147", "v148", "v149", "v150", "v151", "v152", "v153", "v154", "v155", "v156", "v157", "v158", "v159", "v160", "v161", "v162", "v163", "v164", "v165", "v166", "v167", "v168", "v169", "v170", "v171", "v172", "v173", "v174", "v175", "v176", "v177", "v178", "v179", "v180", "v181", "v182", "v183", "v184", "v185", "v186", "v187", "v188", "v189", "v190", "v191", "v192", "v193", "v194", "v195", "v196", "v197", "v198", "v199", "v200", "v201", "v202", "v203", "v204", "v205", "v206", "v207", "v208", "v209", "v210", "v211", "v212", "v213", "v214", "v215", "v216", "v217", "v218", "v219", "v220", "v221", "v222", "v223", "v224", "v225", "v226", "v227", "v228", "v229", "v230", "v231", "v232", "v233", "v234", "v235", "v236", "v237", "v238", "v239", "v240", "v241", "v242", "v243", "v244", "v245", "v246", "v247", "v248", "v249", "v250", "v251", "v252", "v253", "v254", "v255", "a0", "a1", "a2", "a3", "a4", "a5", "a6", "a7", "a8", "a9", "a10", "a11", "a12", "a13", "a14", "a15", "a16", "a17", "a18", "a19", "a20", "a21", "a22", "a23", "a24", "a25", "a26", "a27", "a28", "a29", "a30", "a31", "a32", "a33", "a34", "a35", "a36", "a37", "a38", "a39", "a40", "a41", "a42", "a43", "a44", "a45", "a46", "a47", "a48", "a49", "a50", "a51", "a52", "a53", "a54", "a55", "a56", "a57", "a58", "a59", "a60", "a61", "a62", "a63", "a64", "a65", "a66", "a67", "a68", "a69", "a70", "a71", "a72", "a73", "a74", "a75", "a76", "a77", "a78", "a79", "a80", "a81", "a82", "a83", "a84", "a85", "a86", "a87", "a88", "a89", "a90", "a91", "a92", "a93", "a94", "a95", "a96", "a97", "a98", "a99", "a100", "a101", "a102", "a103", "a104", "a105", "a106", "a107", "a108", "a109", "a110", "a111", "a112", "a113", "a114", "a115", "a116", "a117", "a118", "a119", "a120", "a121", "a122", "a123", "a124", "a125", "a126", "a127", "a128", "a129", "a130", "a131", "a132", "a133", "a134", "a135", "a136", "a137", "a138", "a139", "a140", "a141", "a142", "a143", "a144", "a145", "a146", "a147", "a148", "a149", "a150", "a151", "a152", "a153", "a154", "a155", "a156", "a157", "a158", "a159", "a160", "a161", "a162", "a163", "a164", "a165", "a166", "a167", "a168", "a169", "a170", "a171", "a172", "a173", "a174", "a175", "a176", "a177", "a178", "a179", "a180", "a181", "a182", "a183", "a184", "a185", "a186", "a187", "a188", "a189", "a190", "a191", "a192", "a193", "a194", "a195", "a196", "a197", "a198", "a199", "a200", "a201", "a202", "a203", "a204", "a205", "a206", "a207", "a208", "a209", "a210", "a211", "a212", "a213", "a214", "a215", "a216", "a217", "a218", "a219", "a220", "a221", "a222", "a223", "a224", "a225", "a226", "a227", "a228", "a229", "a230", "a231", "a232", "a233", "a234", "a235", "a236", "a237", "a238", "a239", "a240", "a241", "a242", "a243", "a244", "a245", "a246", "a247", "a248", "a249", "a250", "a251", "a252", "a253", "a254", "a255", "s57", "s58", "s59", "s60", "s61", "s62", "s63", "s64", "s65", "s66", "s67", "s68", "s69", "s70", "s71", "s72", "s73", "s74", "s75", "s76", "s77", "s78", "s79", "s80", "s81", "s82", "s83", "s84", "s85", "s86", "s87", "s88", "s89", "s90", "s91", "s92", "s93", "s94", "s95", "vcc", "scc", "memory")

#define HELP 5
#define PULL 0
#define CLOB20 "v20", "v21", "v22", "v23", "v24", "v25", "v26", "v27", "v28", "v29", "v30", "v31", "v32", "v33", "v34", "v35", "v36", "v37", "v38", "v39", "v40", "v41", "v42", "v43", "v44", "v45", "v46", "v47", "v48", "v49", "v50", "v51", "v52", "v53", "v54", "v55", "v56", "v57", "v58", "v59", "v60", "v61", "v62", "v63", "v64", "v65", "v66", "v67", "v68", "v69", "v70", "v71", "v72", "v73", "v74", "v75", "v76", "v77", "v78", "v79", "v80", "v81", "v82", "v83", "v84", "v85", "v86", "v87", "v88", "v89", "v90", "v91", "v92", "v93", "v94", "v95", "v96", "v97", "v98", "v99", "v100", "v101", "v102", "v103", "v104", "v105", "v106", "v107", "v108", "v109", "v110", "v111", "v112", "v113", "v114", "v115", "v116", "v117", "v118", "v119", "v120", "v121", "v122", "v123", "v124", "v125", "v126", "v127", "v128", "v129", "v130", "v131", "v132", "v133", "v134", "v135", "v136", "v137", "v138", "v139", "v140", "v141", "v142", "v143", "v144", "v145", "v146", "v147", "v148", "v149", "v150", "v151", "v152", "v153", "v154", "v155", "v156", "v157", "v158", "v159", "v160", "v161", "v162", "v163", "v164", "v165", "v166", "v167", "v168", "v169", "v170", "v171", "v172", "v173", "v174", "v175", "v176", "v177", "v178", "v179", "v180", "v181", "v182", "v183", "v184", "v185", "v186", "v187", "v188", "v189", "v190", "v191", "v192", "v193", "v194", "v195", "v196", "v197", "v198", "v199", "v200", "v201", "v202", "v203", "v204", "v205", "v206", "v207", "v208", "v209", "v210", "v211", "v212", "v213", "v214", "v215", "v216", "v217", "v218", "v219", "v220", "v221", "v222", "v223", "v224", "v225", "v226", "v227", "v228", "v229", "v230", "v231", "v232", "v233", "v234", "v235", "v236", "v237", "v238", "v239", "v240", "v241", "v242", "v243", "v244", "v245", "v246", "v247", "v248", "v249", "v250", "v251", "v252", "v253", "v254", "v255", "a0", "a1", "a2", "a3", "a4", "a5", "a6", "a7", "a8", "a9", "a10", "a11", "a12", "a13", "a14", "a15", "a16", "a17", "a18", "a19", "a20", "a21", "a22", "a23", "a24", "a25", "a26", "a27", "a28", "a29", "a30", "a31", "a32", "a33", "a34", "a35", "a36", "a37", "a38", "a39", "a40", "a41", "a42", "a43", "a44", "a45", "a46", "a47", "a48", "a49", "a50", "a51", "a52", "a53", "a54", "a55", "a56", "a57", "a58", "a59", "a60", "a61", "a62", "a63", "a64", "a65", "a66", "a67", "a68", "a69", "a70", "a71", "a72", "a73", "a74", "a75", "a76", "a77", "a78", "a79", "a80", "a81", "a82", "a83", "a84", "a85", "a86", "a87", "a88", "a89", "a90", "a91", "a92", "a93", "a94", "a95", "a96", "a97", "a98", "a99", "a100", "a101", "a102", "a103", "a104", "a105", "a106", "a107", "a108", "a109", "a110", "a111", "a112", "a113", "a114", "a115", "a116", "a117", "a118", "a119", "a120", "a121", "a122", "a123", "a124", "a125", "a126", "a127", "a128", "a129", "a130", "a131", "a132", "a133", "a134", "a135", "a136", "a137", "a138", "a139", "a140", "a141", "a142", "a143", "a144", "a145", "a146", "a147", "a148", "a149", "a150", "a151", "a152", "a153", "a154", "a155", "a156", "a157", "a158", "a159", "a160", "a161", "a162", "a163", "a164", "a165", "a166", "a167", "a168", "a169", "a170", "a171", "a172", "a173", "a174", "a175", "a176", "a177", "a178", "a179", "a180", "a181", "a182", "a183", "a184", "a185", "a186", "a187", "a188", "a189", "a190", "a191", "a192", "a193", "a194", "a195", "a196", "a197", "a198", "a199", "a200", "a201", "a202", "a203", "a204", "a205", "a206", "a207", "a208", "a209", "a210", "a211", "a212", "a213", "a214", "a215", "a216", "a217", "a218", "a219", "a220", "a221", "a222", "a223", "a224", "a225", "a226", "a227", "a228", "a229", "a230", "a231", "a232", "a233", "a234", "a235", "a236", "a237", "a238", "a239", "a240", "a241", "a242", "a243", "a244", "a245", "a246", "a247", "a248", "a249", "a250", "a251", "a252", "a253", "a254", "a255", "s64", "s65", "s66", "s67", "s68", "s69", "s70", "s71", "s72", "s73", "s74", "s75", "s76", "s77", "s78", "s79", "s80", "s81", "s82", "s83", "s84", "s85", "s86", "s87", "s88", "s89", "s90", "s91", "s92", "s93", "s94", "s95", "vcc", "scc", "memory"
#define CLOB32 "v32", "v33", "v34", "v35", "v36", "v37", "v38", "v39", "v40", "v41", "v42", "v43", "v44", "v45", "v46", "v47", "v48", "v49", "v50", "v51", "v52", "v53", "v54", "v55", "v56", "v57", "v58", "v59", "v60", "v61", "v62", "v63", "v64", "v65", "v66", "v67", "v68", "v69", "v70", "v71", "v72", "v73", "v74", "v75", "v76", "v77", "v78", "v79", "v80", "v81", "v82", "v83", "v84", "v85", "v86", "v87", "v88", "v89", "v90", "v91", "v92", "v93", "v94", "v95", "v96", "v97", "v98", "v99", "v100", "v101", "v102", "v103", "v104", "v105", "v106", "v107", "v108", "v109", "v110", "v111", "v112", "v113", "v114", "v115", "v116", "v117", "v118", "v119", "v120", "v121", "v122", "v123", "v124", "v125", "v126", "v127", "v128", "v129", "v130", "v131", "v132", "v133", "v134", "v135", "v136", "v137", "v138", "v139", "v140", "v141", "v142", "v143", "v144", "v145", "v146", "v147", "v148", "v149", "v150", "v151", "v152", "v153", "v154", "v155", "v156", "v157", "v158", "v159", "v160", "v161", "v162", "v163", "v164", "v165", "v166", "v167", "v168", "v169", "v170", "v171", "v172", "v173", "v174", "v175", "v176", "v177", "v178", "v179", "v180", "v181", "v182", "v183", "v184", "v185", "v186", "v187", "v188", "v189", "v190", "v191", "v192", "v193", "v194", "v195", "v196", "v197", "v198", "v199", "v200", "v201", "v202", "v203", "v204", "v205", "v206", "v207", "v208", "v209", "v210", "v211", "v212", "v213", "v214", "v215", "v216", "v217", "v218", "v219", "v220", "v221", "v222", "v223", "v224", "v225", "v226", "v227", "v228", "v229", "v230", "v231", "v232", "v233", "v234", "v235", "v236", "v237", "v238", "v239", "v240", "v241", "v242", "v243", "v244", "v245", "v246", "v247", "v248", "v249", "v250", "v251", "v252", "v253", "v254", "v255", "s64", "s65", "s66", "s67", "s68", "s69", "s70", "s71", "s72", "s73", "s74", "s75", "s76", "s77", "s78", "s79", "s80", "s81", "s82", "s83", "s84", "s85", "s86", "s87", "s88", "s89", "s90", "s91", "s92", "s93", "s94", "s95", "vcc", "scc", "memory"
#define K_ASM() asm volatile( \
"s_nop 4\n" \
"v_add_u32 v24, 0x10000, %[tr0]\n" \
"v_add_u32 v25, 0x10000, %[tr1]\n" \
"v_add_u32 v26, 0x10000, %[tr2]\n" \
"s_add_u32 m0, %[wbase], 0\n" \
"s_add_u32 s66, %[so0], 0\n" \
"buffer_load_dwordx4 %[wvo], %[rw], s66 offen lds\n" \
"s_add_u32 m0, %[wbase], 1024\n" \
"s_add_u32 s66, %[so0], 131072\n" \
"buffer_load_dwordx4 %[wvo], %[rw], s66 offen lds\n" \
"s_add_u32 m0, %[wbase], 2048\n" \
"s_add_u32 s66, %[so0], 128\n" \
"buffer_load_dwordx4 %[wvo], %[rw], s66 offen lds\n" \
"s_add_u32 m0, %[wbase], 3072\n" \
"s_add_u32 s66, %[so0], 131200\n" \
"buffer_load_dwordx4 %[wvo], %[rw], s66 offen lds\n" \
"s_add_u32 m0, %[wbase], 4096\n" \
"s_add_u32 s66, %[so0], 256\n" \
"buffer_load_dwordx4 %[wvo], %[rw], s66 offen lds\n" \
"s_add_u32 m0, %[wbase], 5120\n" \
"s_add_u32 s66, %[so0], 131328\n" \
"buffer_load_dwordx4 %[wvo], %[rw], s66 offen lds\n" \
"s_add_u32 m0, %[wbase], 6144\n" \
"s_add_u32 s66, %[so0], 524288\n" \
"buffer_load_dwordx4 %[wvo], %[rw], s66 offen lds\n" \
"s_add_u32 m0, %[wbase], 7168\n" \
"s_add_u32 s66, %[so0], 655360\n" \
"buffer_load_dwordx4 %[wvo], %[rw], s66 offen lds\n" \
"s_add_u32 m0, %[wbase], 8192\n" \
"s_add_u32 s66, %[so0], 524416\n" \
"buffer_load_dwordx4 %[wvo], %[rw], s66 offen lds\n" \
"s_waitcnt vmcnt(8)\n" \
"s_add_u32 m0, %[wbase], 9216\n" \
"s_add_u32 s66, %[so0], 655488\n" \
"buffer_load_dwordx4 %[wvo], %[rw], s66 offen lds\n" \
"s_waitcnt vmcnt(8)\n" \
"s_add_u32 m0, %[wbase], 10240\n" \
"s_add_u32 s66, %[so0], 524544\n" \
"buffer_load_dwordx4 %[wvo], %[rw], s66 offen lds\n" \
"s_waitcnt vmcnt(8)\n" \
"s_add_u32 m0, %[wbase], 11264\n" \
"s_add_u32 s66, %[so0], 655616\n" \
"buffer_load_dwordx4 %[wvo], %[rw], s66 offen lds\n" \
"s_waitcnt vmcnt(8)\n" \
"s_add_u32 m0, %[wbase], 12288\n" \
"s_add_u32 s66, %[so0], 1048576\n" \
"buffer_load_dwordx4 %[wvo], %[rw], s66 offen lds\n" \
"s_waitcnt vmcnt(8)\n" \
"s_add_u32 m0, %[wbase], 13312\n" \
"s_add_u32 s66, %[so0], 1179648\n" \
"buffer_load_dwordx4 %[wvo], %[rw], s66 offen lds\n" \
"s_waitcnt vmcnt(8)\n" \
"s_add_u32 m0, %[wbase], 14336\n" \
"s_add_u32 s66, %[so0], 1048704\n" \
"buffer_load_dwordx4 %[wvo], %[rw], s66 offen lds\n" \
"s_barrier\n" \
"ds_read_b64_tr_b16 a[0:1], %[tr0] offset:0\n" \
"ds_read_b64_tr_b16 a[2:3], %[tr0] offset:32768\n" \
"ds_read_b64_tr_b16 a[8:9], v24 offset:0\n" \
"ds_read_b64_tr_b16 a[10:11], v24 offset:32768\n" \
"ds_read_b64_tr_b16 a[4:5], %[tr1] offset:0\n" \
"ds_read_b64_tr_b16 a[6:7], %[tr1] offset:32768\n" \
"ds_read_b64_tr_b16 a[12:13], v25 offset:0\n" \
"ds_read_b64_tr_b16 a[14:15], v25 offset:32768\n" \
"ds_read_b64_tr_b16 v[128:129], %[tr2] offset:0\n" \
"ds_read_b64_tr_b16 v[130:131], %[tr2] offset:32768\n" \
"ds_read_b64_tr_b16 v[132:133], v26 offset:0\n" \
"ds_read_b64_tr_b16 v[134:135], v26 offset:32768\n" \
"s_waitcnt lgkmcnt(0)\n" \
"s_waitcnt vmcnt(8)\n" \
"s_add_u32 m0, %[wbase], 15360\n" \
"s_add_u32 s66, %[so0], 1179776\n" \
"buffer_load_dwordx4 %[wvo], %[rw], s66 offen lds\n" \
"s_waitcnt vmcnt(8)\n" \
"s_add_u32 m0, %[wbase], 16384\n" \
"s_add_u32 s66, %[so0], 1048832\n" \
"buffer_load_dwordx4 %[wvo], %[rw], s66 offen lds\n" \
"s_waitcnt vmcnt(8)\n" \
"s_add_u32 m0, %[wbase], 17408\n" \
"s_add_u32 s66, %[so0], 1179904\n" \
"buffer_load_dwordx4 %[wvo], %[rw], s66 offen lds\n" \
"s_waitcnt vmcnt(8)\n" \
"s_add_u32 m0, %[wbase], 18432\n" \
"s_add_u32 s66, %[so0], 1572864\n" \
"buffer_load_dwordx4 %[wvo], %[rw], s66 offen lds\n" \
"s_waitcnt vmcnt(8)\n" \
"s_add_u32 m0, %[wbase], 19456\n" \
"s_add_u32 s66, %[so0], 1703936\n" \
"buffer_load_dwordx4 %[wvo], %[rw], s66 offen lds\n" \
"s_waitcnt vmcnt(8)\n" \
"s_add_u32 m0, %[wbase], 20480\n" \
"s_add_u32 s66, %[so0], 1572992\n" \
"buffer_load_dwordx4 %[wvo], %[rw], s66 offen lds\n" \
"s_barrier\n" \
"ds_read_b64_tr_b16 a[16:17], %[tr0] offset:6144\n" \
"ds_read_b64_tr_b16 a[18:19], %[tr0] offset:38912\n" \
"ds_read_b64_tr_b16 a[24:25], v24 offset:6144\n" \
"ds_read_b64_tr_b16 a[26:27], v24 offset:38912\n" \
"ds_read_b64_tr_b16 a[20:21], %[tr1] offset:6144\n" \
"ds_read_b64_tr_b16 a[22:23], %[tr1] offset:38912\n" \
"ds_read_b64_tr_b16 a[28:29], v25 offset:6144\n" \
"ds_read_b64_tr_b16 a[30:31], v25 offset:38912\n" \
"ds_read_b64_tr_b16 v[136:137], %[tr2] offset:6144\n" \
"ds_read_b64_tr_b16 v[138:139], %[tr2] offset:38912\n" \
"ds_read_b64_tr_b16 v[140:141], v26 offset:6144\n" \
"ds_read_b64_tr_b16 v[142:143], v26 offset:38912\n" \
"s_waitcnt lgkmcnt(0)\n" \
"s_waitcnt vmcnt(8)\n" \
"s_add_u32 m0, %[wbase], 21504\n" \
"s_add_u32 s66, %[so0], 1704064\n" \
"buffer_load_dwordx4 %[wvo], %[rw], s66 offen lds\n" \
"s_waitcnt vmcnt(8)\n" \
"s_add_u32 m0, %[wbase], 22528\n" \
"s_add_u32 s66, %[so0], 1573120\n" \
"buffer_load_dwordx4 %[wvo], %[rw], s66 offen lds\n" \
"s_waitcnt vmcnt(8)\n" \
"s_add_u32 m0, %[wbase], 23552\n" \
"s_add_u32 s66, %[so0], 1704192\n" \
"buffer_load_dwordx4 %[wvo], %[rw], s66 offen lds\n" \
"s_waitcnt vmcnt(8)\n" \
"s_add_u32 m0, %[wbase], 24576\n" \
"s_add_u32 s66, %[so0], 2097152\n" \
"buffer_load_dwordx4 %[wvo], %[rw], s66 offen lds\n" \
"s_waitcnt vmcnt(8)\n" \
"s_add_u32 m0, %[wbase], 25600\n" \
"s_add_u32 s66, %[so0], 2228224\n" \
"buffer_load_dwordx4 %[wvo], %[rw], s66 offen lds\n" \
"s_waitcnt vmcnt(8)\n" \
"s_add_u32 m0, %[wbase], 26624\n" \
"s_add_u32 s66, %[so0], 2097280\n" \
"buffer_load_dwordx4 %[wvo], %[rw], s66 offen lds\n" \
"s_barrier\n" \
"ds_read_b64_tr_b16 a[32:33], %[tr0] offset:12288\n" \
"ds_read_b64_tr_b16 a[34:35], %[tr0] offset:45056\n" \
"ds_read_b64_tr_b16 a[40:41], v24 offset:12288\n" \
"ds_read_b64_tr_b16 a[42:43], v24 offset:45056\n" \
"ds_read_b64_tr_b16 a[36:37], %[tr1] offset:12288\n" \
"ds_read_b64_tr_b16 a[38:39], %[tr1] offset:45056\n" \
"ds_read_b64_tr_b16 a[44:45], v25 offset:12288\n" \
"ds_read_b64_tr_b16 a[46:47], v25 offset:45056\n" \
"ds_read_b64_tr_b16 v[144:145], %[tr2] offset:12288\n" \
"ds_read_b64_tr_b16 v[146:147], %[tr2] offset:45056\n" \
"ds_read_b64_tr_b16 v[148:149], v26 offset:12288\n" \
"ds_read_b64_tr_b16 v[150:151], v26 offset:45056\n" \
"s_waitcnt lgkmcnt(0)\n" \
"s_waitcnt vmcnt(8)\n" \
"s_add_u32 m0, %[wbase], 27648\n" \
"s_add_u32 s66, %[so0], 2228352\n" \
"buffer_load_dwordx4 %[wvo], %[rw], s66 offen lds\n" \
"s_waitcnt vmcnt(8)\n" \
"s_add_u32 m0, %[wbase], 28672\n" \
"s_add_u32 s66, %[so0], 2097408\n" \
"buffer_load_dwordx4 %[wvo], %[rw], s66 offen lds\n" \
"s_waitcnt vmcnt(8)\n" \
"s_add_u32 m0, %[wbase], 29696\n" \
"s_add_u32 s66, %[so0], 2228480\n" \
"buffer_load_dwordx4 %[wvo], %[rw], s66 offen lds\n" \
"s_waitcnt vmcnt(8)\n" \
"s_add_u32 m0, %[wbase], 0\n" \
"s_add_u32 s66, %[so0], 2621440\n" \
"buffer_load_dwordx4 %[wvo], %[rw], s66 offen lds\n" \
"s_waitcnt vmcnt(8)\n" \
"s_add_u32 m0, %[wbase], 1024\n" \
"s_add_u32 s66, %[so0], 2752512\n" \
"buffer_load_dwordx4 %[wvo], %[rw], s66 offen lds\n" \
"s_waitcnt vmcnt(8)\n" \
"s_add_u32 m0, %[wbase], 2048\n" \
"s_add_u32 s66, %[so0], 2621568\n" \
"buffer_load_dwordx4 %[wvo], %[rw], s66 offen lds\n" \
"s_barrier\n" \
"ds_read_b64_tr_b16 a[48:49], %[tr0] offset:18432\n" \
"ds_read_b64_tr_b16 a[50:51], %[tr0] offset:51200\n" \
"ds_read_b64_tr_b16 a[56:57], v24 offset:18432\n" \
"ds_read_b64_tr_b16 a[58:59], v24 offset:51200\n" \
"ds_read_b64_tr_b16 a[52:53], %[tr1] offset:18432\n" \
"ds_read_b64_tr_b16 a[54:55], %[tr1] offset:51200\n" \
"ds_read_b64_tr_b16 a[60:61], v25 offset:18432\n" \
"ds_read_b64_tr_b16 a[62:63], v25 offset:51200\n" \
"ds_read_b64_tr_b16 v[152:153], %[tr2] offset:18432\n" \
"ds_read_b64_tr_b16 v[154:155], %[tr2] offset:51200\n" \
"ds_read_b64_tr_b16 v[156:157], v26 offset:18432\n" \
"ds_read_b64_tr_b16 v[158:159], v26 offset:51200\n" \
"s_waitcnt lgkmcnt(0)\n" \
"s_waitcnt vmcnt(8)\n" \
"s_add_u32 m0, %[wbase], 3072\n" \
"s_add_u32 s66, %[so0], 2752640\n" \
"buffer_load_dwordx4 %[wvo], %[rw], s66 offen lds\n" \
"s_waitcnt vmcnt(8)\n" \
"s_add_u32 m0, %[wbase], 4096\n" \
"s_add_u32 s66, %[so0], 2621696\n" \
"buffer_load_dwordx4 %[wvo], %[rw], s66 offen lds\n" \
"s_waitcnt vmcnt(8)\n" \
"s_add_u32 m0, %[wbase], 5120\n" \
"s_add_u32 s66, %[so0], 2752768\n" \
"buffer_load_dwordx4 %[wvo], %[rw], s66 offen lds\n" \
"s_waitcnt vmcnt(8)\n" \
"s_add_u32 m0, %[wbase], 6144\n" \
"s_add_u32 s66, %[so0], 3145728\n" \
"buffer_load_dwordx4 %[wvo], %[rw], s66 offen lds\n" \
"s_waitcnt vmcnt(8)\n" \
"s_add_u32 m0, %[wbase], 7168\n" \
"s_add_u32 s66, %[so0], 3276800\n" \
"buffer_load_dwordx4 %[wvo], %[rw], s66 offen lds\n" \
"s_waitcnt vmcnt(8)\n" \
"s_add_u32 m0, %[wbase], 8192\n" \
"s_add_u32 s66, %[so0], 3145856\n" \
"buffer_load_dwordx4 %[wvo], %[rw], s66 offen lds\n" \
"s_barrier\n" \
"ds_read_b64_tr_b16 a[64:65], %[tr0] offset:24576\n" \
"ds_read_b64_tr_b16 a[66:67], %[tr0] offset:57344\n" \
"ds_read_b64_tr_b16 a[72:73], v24 offset:24576\n" \
"ds_read_b64_tr_b16 a[74:75], v24 offset:57344\n" \
"ds_read_b64_tr_b16 a[68:69], %[tr1] offset:24576\n" \
"ds_read_b64_tr_b16 a[70:71], %[tr1] offset:57344\n" \
"ds_read_b64_tr_b16 a[76:77], v25 offset:24576\n" \
"ds_read_b64_tr_b16 a[78:79], v25 offset:57344\n" \
"ds_read_b64_tr_b16 v[160:161], %[tr2] offset:24576\n" \
"ds_read_b64_tr_b16 v[162:163], %[tr2] offset:57344\n" \
"ds_read_b64_tr_b16 v[164:165], v26 offset:24576\n" \
"ds_read_b64_tr_b16 v[166:167], v26 offset:57344\n" \
"s_waitcnt lgkmcnt(0)\n" \
"s_waitcnt vmcnt(8)\n" \
"s_add_u32 m0, %[wbase], 9216\n" \
"s_add_u32 s66, %[so0], 3276928\n" \
"buffer_load_dwordx4 %[wvo], %[rw], s66 offen lds\n" \
"s_waitcnt vmcnt(8)\n" \
"s_add_u32 m0, %[wbase], 10240\n" \
"s_add_u32 s66, %[so0], 3145984\n" \
"buffer_load_dwordx4 %[wvo], %[rw], s66 offen lds\n" \
"s_waitcnt vmcnt(8)\n" \
"s_add_u32 m0, %[wbase], 11264\n" \
"s_add_u32 s66, %[so0], 3277056\n" \
"buffer_load_dwordx4 %[wvo], %[rw], s66 offen lds\n" \
"s_waitcnt vmcnt(8)\n" \
"s_add_u32 m0, %[wbase], 12288\n" \
"s_add_u32 s66, %[so0], 3670016\n" \
"buffer_load_dwordx4 %[wvo], %[rw], s66 offen lds\n" \
"s_waitcnt vmcnt(8)\n" \
"s_add_u32 m0, %[wbase], 13312\n" \
"s_add_u32 s66, %[so0], 3801088\n" \
"buffer_load_dwordx4 %[wvo], %[rw], s66 offen lds\n" \
"s_waitcnt vmcnt(8)\n" \
"s_add_u32 m0, %[wbase], 14336\n" \
"s_add_u32 s66, %[so0], 3670144\n" \
"buffer_load_dwordx4 %[wvo], %[rw], s66 offen lds\n" \
"s_barrier\n" \
"ds_read_b64_tr_b16 a[80:81], %[tr0] offset:0\n" \
"ds_read_b64_tr_b16 a[82:83], %[tr0] offset:32768\n" \
"ds_read_b64_tr_b16 a[88:89], v24 offset:0\n" \
"ds_read_b64_tr_b16 a[90:91], v24 offset:32768\n" \
"ds_read_b64_tr_b16 a[84:85], %[tr1] offset:0\n" \
"ds_read_b64_tr_b16 a[86:87], %[tr1] offset:32768\n" \
"ds_read_b64_tr_b16 a[92:93], v25 offset:0\n" \
"ds_read_b64_tr_b16 a[94:95], v25 offset:32768\n" \
"ds_read_b64_tr_b16 v[168:169], %[tr2] offset:0\n" \
"ds_read_b64_tr_b16 v[170:171], %[tr2] offset:32768\n" \
"ds_read_b64_tr_b16 v[172:173], v26 offset:0\n" \
"ds_read_b64_tr_b16 v[174:175], v26 offset:32768\n" \
"s_waitcnt lgkmcnt(0)\n" \
"s_waitcnt vmcnt(8)\n" \
"s_add_u32 m0, %[wbase], 15360\n" \
"s_add_u32 s66, %[so0], 3801216\n" \
"buffer_load_dwordx4 %[wvo], %[rw], s66 offen lds\n" \
"s_waitcnt vmcnt(8)\n" \
"s_add_u32 m0, %[wbase], 16384\n" \
"s_add_u32 s66, %[so0], 3670272\n" \
"buffer_load_dwordx4 %[wvo], %[rw], s66 offen lds\n" \
"s_waitcnt vmcnt(8)\n" \
"s_add_u32 m0, %[wbase], 17408\n" \
"s_add_u32 s66, %[so0], 3801344\n" \
"buffer_load_dwordx4 %[wvo], %[rw], s66 offen lds\n" \
"s_waitcnt vmcnt(8)\n" \
"s_add_u32 m0, %[wbase], 18432\n" \
"s_add_u32 s66, %[so0], 4194304\n" \
"buffer_load_dwordx4 %[wvo], %[rw], s66 offen lds\n" \
"s_waitcnt vmcnt(8)\n" \
"s_add_u32 m0, %[wbase], 19456\n" \
"s_add_u32 s66, %[so0], 4325376\n" \
"buffer_load_dwordx4 %[wvo], %[rw], s66 offen lds\n" \
"s_waitcnt vmcnt(8)\n" \
"s_add_u32 m0, %[wbase], 20480\n" \
"s_add_u32 s66, %[so0], 4194432\n" \
"buffer_load_dwordx4 %[wvo], %[rw], s66 offen lds\n" \
"s_barrier\n" \
"ds_read_b64_tr_b16 a[96:97], %[tr0] offset:6144\n" \
"ds_read_b64_tr_b16 a[98:99], %[tr0] offset:38912\n" \
"ds_read_b64_tr_b16 a[104:105], v24 offset:6144\n" \
"ds_read_b64_tr_b16 a[106:107], v24 offset:38912\n" \
"ds_read_b64_tr_b16 a[100:101], %[tr1] offset:6144\n" \
"ds_read_b64_tr_b16 a[102:103], %[tr1] offset:38912\n" \
"ds_read_b64_tr_b16 a[108:109], v25 offset:6144\n" \
"ds_read_b64_tr_b16 a[110:111], v25 offset:38912\n" \
"ds_read_b64_tr_b16 v[176:177], %[tr2] offset:6144\n" \
"ds_read_b64_tr_b16 v[178:179], %[tr2] offset:38912\n" \
"ds_read_b64_tr_b16 v[180:181], v26 offset:6144\n" \
"ds_read_b64_tr_b16 v[182:183], v26 offset:38912\n" \
"s_waitcnt lgkmcnt(0)\n" \
"s_waitcnt vmcnt(8)\n" \
"s_add_u32 m0, %[wbase], 21504\n" \
"s_add_u32 s66, %[so0], 4325504\n" \
"buffer_load_dwordx4 %[wvo], %[rw], s66 offen lds\n" \
"s_waitcnt vmcnt(8)\n" \
"s_add_u32 m0, %[wbase], 22528\n" \
"s_add_u32 s66, %[so0], 4194560\n" \
"buffer_load_dwordx4 %[wvo], %[rw], s66 offen lds\n" \
"s_waitcnt vmcnt(8)\n" \
"s_add_u32 m0, %[wbase], 23552\n" \
"s_add_u32 s66, %[so0], 4325632\n" \
"buffer_load_dwordx4 %[wvo], %[rw], s66 offen lds\n" \
"s_waitcnt vmcnt(8)\n" \
"s_add_u32 m0, %[wbase], 24576\n" \
"s_add_u32 s66, %[so0], 4718592\n" \
"buffer_load_dwordx4 %[wvo], %[rw], s66 offen lds\n" \
"s_waitcnt vmcnt(8)\n" \
"s_add_u32 m0, %[wbase], 25600\n" \
"s_add_u32 s66, %[so0], 4849664\n" \
"buffer_load_dwordx4 %[wvo], %[rw], s66 offen lds\n" \
"s_waitcnt vmcnt(8)\n" \
"s_add_u32 m0, %[wbase], 26624\n" \
"s_add_u32 s66, %[so0], 4718720\n" \
"buffer_load_dwordx4 %[wvo], %[rw], s66 offen lds\n" \
"s_barrier\n" \
"ds_read_b64_tr_b16 a[112:113], %[tr0] offset:12288\n" \
"ds_read_b64_tr_b16 a[114:115], %[tr0] offset:45056\n" \
"ds_read_b64_tr_b16 a[120:121], v24 offset:12288\n" \
"ds_read_b64_tr_b16 a[122:123], v24 offset:45056\n" \
"ds_read_b64_tr_b16 a[116:117], %[tr1] offset:12288\n" \
"ds_read_b64_tr_b16 a[118:119], %[tr1] offset:45056\n" \
"ds_read_b64_tr_b16 a[124:125], v25 offset:12288\n" \
"ds_read_b64_tr_b16 a[126:127], v25 offset:45056\n" \
"ds_read_b64_tr_b16 v[184:185], %[tr2] offset:12288\n" \
"ds_read_b64_tr_b16 v[186:187], %[tr2] offset:45056\n" \
"ds_read_b64_tr_b16 v[188:189], v26 offset:12288\n" \
"ds_read_b64_tr_b16 v[190:191], v26 offset:45056\n" \
"s_waitcnt lgkmcnt(0)\n" \
"s_waitcnt vmcnt(8)\n" \
"s_add_u32 m0, %[wbase], 27648\n" \
"s_add_u32 s66, %[so0], 4849792\n" \
"buffer_load_dwordx4 %[wvo], %[rw], s66 offen lds\n" \
"s_waitcnt vmcnt(8)\n" \
"s_add_u32 m0, %[wbase], 28672\n" \
"s_add_u32 s66, %[so0], 4718848\n" \
"buffer_load_dwordx4 %[wvo], %[rw], s66 offen lds\n" \
"s_waitcnt vmcnt(8)\n" \
"s_add_u32 m0, %[wbase], 29696\n" \
"s_add_u32 s66, %[so0], 4849920\n" \
"buffer_load_dwordx4 %[wvo], %[rw], s66 offen lds\n" \
"s_waitcnt vmcnt(8)\n" \
"s_add_u32 m0, %[wbase], 0\n" \
"s_add_u32 s66, %[so0], 5242880\n" \
"buffer_load_dwordx4 %[wvo], %[rw], s66 offen lds\n" \
"s_waitcnt vmcnt(8)\n" \
"s_add_u32 m0, %[wbase], 1024\n" \
"s_add_u32 s66, %[so0], 5373952\n" \
"buffer_load_dwordx4 %[wvo], %[rw], s66 offen lds\n" \
"s_waitcnt vmcnt(8)\n" \
"s_add_u32 m0, %[wbase], 2048\n" \
"s_add_u32 s66, %[so0], 5243008\n" \
"buffer_load_dwordx4 %[wvo], %[rw], s66 offen lds\n" \
"s_barrier\n" \
"ds_read_b64_tr_b16 a[128:129], %[tr0] offset:18432\n" \
"ds_read_b64_tr_b16 a[130:131], %[tr0] offset:51200\n" \
"ds_read_b64_tr_b16 a[136:137], v24 offset:18432\n" \
"ds_read_b64_tr_b16 a[138:139], v24 offset:51200\n" \
"ds_read_b64_tr_b16 a[132:133], %[tr1] offset:18432\n" \
"ds_read_b64_tr_b16 a[134:135], %[tr1] offset:51200\n" \
"ds_read_b64_tr_b16 a[140:141], v25 offset:18432\n" \
"ds_read_b64_tr_b16 a[142:143], v25 offset:51200\n" \
"ds_read_b64_tr_b16 v[192:193], %[tr2] offset:18432\n" \
"ds_read_b64_tr_b16 v[194:195], %[tr2] offset:51200\n" \
"ds_read_b64_tr_b16 v[196:197], v26 offset:18432\n" \
"ds_read_b64_tr_b16 v[198:199], v26 offset:51200\n" \
"s_waitcnt lgkmcnt(0)\n" \
"s_waitcnt vmcnt(8)\n" \
"s_add_u32 m0, %[wbase], 3072\n" \
"s_add_u32 s66, %[so0], 5374080\n" \
"buffer_load_dwordx4 %[wvo], %[rw], s66 offen lds\n" \
"s_waitcnt vmcnt(8)\n" \
"s_add_u32 m0, %[wbase], 4096\n" \
"s_add_u32 s66, %[so0], 5243136\n" \
"buffer_load_dwordx4 %[wvo], %[rw], s66 offen lds\n" \
"s_waitcnt vmcnt(8)\n" \
"s_add_u32 m0, %[wbase], 5120\n" \
"s_add_u32 s66, %[so0], 5374208\n" \
"buffer_load_dwordx4 %[wvo], %[rw], s66 offen lds\n" \
"s_waitcnt vmcnt(8)\n" \
"s_add_u32 m0, %[wbase], 6144\n" \
"s_add_u32 s66, %[so0], 5767168\n" \
"buffer_load_dwordx4 %[wvo], %[rw], s66 offen lds\n" \
"s_waitcnt vmcnt(8)\n" \
"s_add_u32 m0, %[wbase], 7168\n" \
"s_add_u32 s66, %[so0], 5898240\n" \
"buffer_load_dwordx4 %[wvo], %[rw], s66 offen lds\n" \
"s_waitcnt vmcnt(8)\n" \
"s_add_u32 m0, %[wbase], 8192\n" \
"s_add_u32 s66, %[so0], 5767296\n" \
"buffer_load_dwordx4 %[wvo], %[rw], s66 offen lds\n" \
"s_barrier\n" \
"ds_read_b64_tr_b16 a[144:145], %[tr0] offset:24576\n" \
"ds_read_b64_tr_b16 a[146:147], %[tr0] offset:57344\n" \
"ds_read_b64_tr_b16 a[152:153], v24 offset:24576\n" \
"ds_read_b64_tr_b16 a[154:155], v24 offset:57344\n" \
"ds_read_b64_tr_b16 a[148:149], %[tr1] offset:24576\n" \
"ds_read_b64_tr_b16 a[150:151], %[tr1] offset:57344\n" \
"ds_read_b64_tr_b16 a[156:157], v25 offset:24576\n" \
"ds_read_b64_tr_b16 a[158:159], v25 offset:57344\n" \
"ds_read_b64_tr_b16 v[200:201], %[tr2] offset:24576\n" \
"ds_read_b64_tr_b16 v[202:203], %[tr2] offset:57344\n" \
"ds_read_b64_tr_b16 v[204:205], v26 offset:24576\n" \
"ds_read_b64_tr_b16 v[206:207], v26 offset:57344\n" \
"s_waitcnt lgkmcnt(0)\n" \
"s_waitcnt vmcnt(8)\n" \
"s_add_u32 m0, %[wbase], 9216\n" \
"s_add_u32 s66, %[so0], 5898368\n" \
"buffer_load_dwordx4 %[wvo], %[rw], s66 offen lds\n" \
"s_waitcnt vmcnt(8)\n" \
"s_add_u32 m0, %[wbase], 10240\n" \
"s_add_u32 s66, %[so0], 5767424\n" \
"buffer_load_dwordx4 %[wvo], %[rw], s66 offen lds\n" \
"s_waitcnt vmcnt(8)\n" \
"s_add_u32 m0, %[wbase], 11264\n" \
"s_add_u32 s66, %[so0], 5898496\n" \
"buffer_load_dwordx4 %[wvo], %[rw], s66 offen lds\n" \
"s_waitcnt vmcnt(8)\n" \
"s_add_u32 m0, %[wbase], 12288\n" \
"s_add_u32 s66, %[so0], 6291456\n" \
"buffer_load_dwordx4 %[wvo], %[rw], s66 offen lds\n" \
"s_waitcnt vmcnt(8)\n" \
"s_add_u32 m0, %[wbase], 13312\n" \
"s_add_u32 s66, %[so0], 6422528\n" \
"buffer_load_dwordx4 %[wvo], %[rw], s66 offen lds\n" \
"s_waitcnt vmcnt(8)\n" \
"s_add_u32 m0, %[wbase], 14336\n" \
"s_add_u32 s66, %[so0], 6291584\n" \
"buffer_load_dwordx4 %[wvo], %[rw], s66 offen lds\n" \
"s_barrier\n" \
"ds_read_b64_tr_b16 a[160:161], %[tr0] offset:0\n" \
"ds_read_b64_tr_b16 a[162:163], %[tr0] offset:32768\n" \
"ds_read_b64_tr_b16 a[168:169], v24 offset:0\n" \
"ds_read_b64_tr_b16 a[170:171], v24 offset:32768\n" \
"ds_read_b64_tr_b16 a[164:165], %[tr1] offset:0\n" \
"ds_read_b64_tr_b16 a[166:167], %[tr1] offset:32768\n" \
"ds_read_b64_tr_b16 a[172:173], v25 offset:0\n" \
"ds_read_b64_tr_b16 a[174:175], v25 offset:32768\n" \
"ds_read_b64_tr_b16 v[208:209], %[tr2] offset:0\n" \
"ds_read_b64_tr_b16 v[210:211], %[tr2] offset:32768\n" \
"ds_read_b64_tr_b16 v[212:213], v26 offset:0\n" \
"ds_read_b64_tr_b16 v[214:215], v26 offset:32768\n" \
"s_waitcnt lgkmcnt(0)\n" \
"s_waitcnt vmcnt(8)\n" \
"s_add_u32 m0, %[wbase], 15360\n" \
"s_add_u32 s66, %[so0], 6422656\n" \
"buffer_load_dwordx4 %[wvo], %[rw], s66 offen lds\n" \
"s_waitcnt vmcnt(8)\n" \
"s_add_u32 m0, %[wbase], 16384\n" \
"s_add_u32 s66, %[so0], 6291712\n" \
"buffer_load_dwordx4 %[wvo], %[rw], s66 offen lds\n" \
"s_waitcnt vmcnt(8)\n" \
"s_add_u32 m0, %[wbase], 17408\n" \
"s_add_u32 s66, %[so0], 6422784\n" \
"buffer_load_dwordx4 %[wvo], %[rw], s66 offen lds\n" \
"s_waitcnt vmcnt(8)\n" \
"s_add_u32 m0, %[wbase], 18432\n" \
"s_add_u32 s66, %[so0], 6815744\n" \
"buffer_load_dwordx4 %[wvo], %[rw], s66 offen lds\n" \
"s_waitcnt vmcnt(8)\n" \
"s_add_u32 m0, %[wbase], 19456\n" \
"s_add_u32 s66, %[so0], 6946816\n" \
"buffer_load_dwordx4 %[wvo], %[rw], s66 offen lds\n" \
"s_waitcnt vmcnt(8)\n" \
"s_add_u32 m0, %[wbase], 20480\n" \
"s_add_u32 s66, %[so0], 6815872\n" \
"buffer_load_dwordx4 %[wvo], %[rw], s66 offen lds\n" \
"s_barrier\n" \
"ds_read_b64_tr_b16 a[176:177], %[tr0] offset:6144\n" \
"ds_read_b64_tr_b16 a[178:179], %[tr0] offset:38912\n" \
"ds_read_b64_tr_b16 a[184:185], v24 offset:6144\n" \
"ds_read_b64_tr_b16 a[186:187], v24 offset:38912\n" \
"ds_read_b64_tr_b16 a[180:181], %[tr1] offset:6144\n" \
"ds_read_b64_tr_b16 a[182:183], %[tr1] offset:38912\n" \
"ds_read_b64_tr_b16 a[188:189], v25 offset:6144\n" \
"ds_read_b64_tr_b16 a[190:191], v25 offset:38912\n" \
"ds_read_b64_tr_b16 v[216:217], %[tr2] offset:6144\n" \
"ds_read_b64_tr_b16 v[218:219], %[tr2] offset:38912\n" \
"ds_read_b64_tr_b16 v[220:221], v26 offset:6144\n" \
"ds_read_b64_tr_b16 v[222:223], v26 offset:38912\n" \
"s_waitcnt lgkmcnt(0)\n" \
"s_waitcnt vmcnt(8)\n" \
"s_add_u32 m0, %[wbase], 21504\n" \
"s_add_u32 s66, %[so0], 6946944\n" \
"buffer_load_dwordx4 %[wvo], %[rw], s66 offen lds\n" \
"s_waitcnt vmcnt(8)\n" \
"s_add_u32 m0, %[wbase], 22528\n" \
"s_add_u32 s66, %[so0], 6816000\n" \
"buffer_load_dwordx4 %[wvo], %[rw], s66 offen lds\n" \
"s_waitcnt vmcnt(8)\n" \
"s_add_u32 m0, %[wbase], 23552\n" \
"s_add_u32 s66, %[so0], 6947072\n" \
"buffer_load_dwordx4 %[wvo], %[rw], s66 offen lds\n" \
"s_waitcnt vmcnt(8)\n" \
"s_add_u32 m0, %[wbase], 24576\n" \
"s_add_u32 s66, %[so0], 7340032\n" \
"buffer_load_dwordx4 %[wvo], %[rw], s66 offen lds\n" \
"s_waitcnt vmcnt(8)\n" \
"s_add_u32 m0, %[wbase], 25600\n" \
"s_add_u32 s66, %[so0], 7471104\n" \
"buffer_load_dwordx4 %[wvo], %[rw], s66 offen lds\n" \
"s_waitcnt vmcnt(8)\n" \
"s_add_u32 m0, %[wbase], 26624\n" \
"s_add_u32 s66, %[so0], 7340160\n" \
"buffer_load_dwordx4 %[wvo], %[rw], s66 offen lds\n" \
"s_barrier\n" \
"ds_read_b64_tr_b16 a[192:193], %[tr0] offset:12288\n" \
"ds_read_b64_tr_b16 a[194:195], %[tr0] offset:45056\n" \
"ds_read_b64_tr_b16 a[200:201], v24 offset:12288\n" \
"ds_read_b64_tr_b16 a[202:203], v24 offset:45056\n" \
"ds_read_b64_tr_b16 a[196:197], %[tr1] offset:12288\n" \
"ds_read_b64_tr_b16 a[198:199], %[tr1] offset:45056\n" \
"ds_read_b64_tr_b16 a[204:205], v25 offset:12288\n" \
"ds_read_b64_tr_b16 a[206:207], v25 offset:45056\n" \
"ds_read_b64_tr_b16 v[224:225], %[tr2] offset:12288\n" \
"ds_read_b64_tr_b16 v[226:227], %[tr2] offset:45056\n" \
"ds_read_b64_tr_b16 v[228:229], v26 offset:12288\n" \
"ds_read_b64_tr_b16 v[230:231], v26 offset:45056\n" \
"s_waitcnt lgkmcnt(0)\n" \
"s_waitcnt vmcnt(8)\n" \
"s_add_u32 m0, %[wbase], 27648\n" \
"s_add_u32 s66, %[so0], 7471232\n" \
"buffer_load_dwordx4 %[wvo], %[rw], s66 offen lds\n" \
"s_waitcnt vmcnt(8)\n" \
"s_add_u32 m0, %[wbase], 28672\n" \
"s_add_u32 s66, %[so0], 7340288\n" \
"buffer_load_dwordx4 %[wvo], %[rw], s66 offen lds\n" \
"s_waitcnt vmcnt(8)\n" \
"s_add_u32 m0, %[wbase], 29696\n" \
"s_add_u32 s66, %[so0], 7471360\n" \
"buffer_load_dwordx4 %[wvo], %[rw], s66 offen lds\n" \
"s_waitcnt vmcnt(8)\n" \
"s_add_u32 m0, %[wbase], 0\n" \
"s_add_u32 s66, %[so0], 7864320\n" \
"buffer_load_dwordx4 %[wvo], %[rw], s66 offen lds\n" \
"s_waitcnt vmcnt(8)\n" \
"s_add_u32 m0, %[wbase], 1024\n" \
"s_add_u32 s66, %[so0], 7995392\n" \
"buffer_load_dwordx4 %[wvo], %[rw], s66 offen lds\n" \
"s_waitcnt vmcnt(8)\n" \
"s_add_u32 m0, %[wbase], 2048\n" \
"s_add_u32 s66, %[so0], 7864448\n" \
"buffer_load_dwordx4 %[wvo], %[rw], s66 offen lds\n" \
"s_barrier\n" \
"ds_read_b64_tr_b16 a[208:209], %[tr0] offset:18432\n" \
"ds_read_b64_tr_b16 a[210:211], %[tr0] offset:51200\n" \
"ds_read_b64_tr_b16 a[216:217], v24 offset:18432\n" \
"ds_read_b64_tr_b16 a[218:219], v24 offset:51200\n" \
"ds_read_b64_tr_b16 a[212:213], %[tr1] offset:18432\n" \
"ds_read_b64_tr_b16 a[214:215], %[tr1] offset:51200\n" \
"ds_read_b64_tr_b16 a[220:221], v25 offset:18432\n" \
"ds_read_b64_tr_b16 a[222:223], v25 offset:51200\n" \
"ds_read_b64_tr_b16 v[232:233], %[tr2] offset:18432\n" \
"ds_read_b64_tr_b16 v[234:235], %[tr2] offset:51200\n" \
"ds_read_b64_tr_b16 v[236:237], v26 offset:18432\n" \
"ds_read_b64_tr_b16 v[238:239], v26 offset:51200\n" \
"s_waitcnt lgkmcnt(0)\n" \
"s_waitcnt vmcnt(8)\n" \
"s_add_u32 m0, %[wbase], 3072\n" \
"s_add_u32 s66, %[so0], 7995520\n" \
"buffer_load_dwordx4 %[wvo], %[rw], s66 offen lds\n" \
"s_waitcnt vmcnt(8)\n" \
"s_add_u32 m0, %[wbase], 4096\n" \
"s_add_u32 s66, %[so0], 7864576\n" \
"buffer_load_dwordx4 %[wvo], %[rw], s66 offen lds\n" \
"s_waitcnt vmcnt(8)\n" \
"s_add_u32 m0, %[wbase], 5120\n" \
"s_add_u32 s66, %[so0], 7995648\n" \
"buffer_load_dwordx4 %[wvo], %[rw], s66 offen lds\n" \
"buffer_load_dword v91, off, %[ws], %[hs] sc1\n" \
"s_waitcnt vmcnt(7)\n" \
"s_barrier\n" \
"ds_read_b64_tr_b16 a[224:225], %[tr0] offset:24576\n" \
"ds_read_b64_tr_b16 a[226:227], %[tr0] offset:57344\n" \
"ds_read_b64_tr_b16 a[232:233], v24 offset:24576\n" \
"ds_read_b64_tr_b16 a[234:235], v24 offset:57344\n" \
"ds_read_b64_tr_b16 a[228:229], %[tr1] offset:24576\n" \
"ds_read_b64_tr_b16 a[230:231], %[tr1] offset:57344\n" \
"ds_read_b64_tr_b16 a[236:237], v25 offset:24576\n" \
"ds_read_b64_tr_b16 a[238:239], v25 offset:57344\n" \
"ds_read_b64_tr_b16 v[240:241], %[tr2] offset:24576\n" \
"ds_read_b64_tr_b16 v[242:243], %[tr2] offset:57344\n" \
"ds_read_b64_tr_b16 v[244:245], v26 offset:24576\n" \
"ds_read_b64_tr_b16 v[246:247], v26 offset:57344\n" \
"s_waitcnt lgkmcnt(0)\n" \
"s_waitcnt vmcnt(1)\n" \
"s_barrier\n" \
"ds_read_b64_tr_b16 a[240:241], %[tr0] offset:0\n" \
"ds_read_b64_tr_b16 a[242:243], %[tr0] offset:32768\n" \
"ds_read_b64_tr_b16 a[248:249], v24 offset:0\n" \
"ds_read_b64_tr_b16 a[250:251], v24 offset:32768\n" \
"ds_read_b64_tr_b16 a[244:245], %[tr1] offset:0\n" \
"ds_read_b64_tr_b16 a[246:247], %[tr1] offset:32768\n" \
"ds_read_b64_tr_b16 a[252:253], v25 offset:0\n" \
"ds_read_b64_tr_b16 a[254:255], v25 offset:32768\n" \
"ds_read_b64_tr_b16 v[248:249], %[tr2] offset:0\n" \
"ds_read_b64_tr_b16 v[250:251], %[tr2] offset:32768\n" \
"ds_read_b64_tr_b16 v[252:253], v26 offset:0\n" \
"ds_read_b64_tr_b16 v[254:255], v26 offset:32768\n" \
"s_waitcnt lgkmcnt(0)\n" \
"s_barrier\n" \
"s_mov_b32 s64, 262144\n" \
"s_waitcnt vmcnt(0)\n" \
"Lk_hs_%=:\n" \
"v_readfirstlane_b32 s66, v91\n" \
"s_cmp_eq_u32 s66, 0\n" \
"s_cbranch_scc1 Lk_shs_%=\n" \
"v_mov_b32 v91, 2\n" \
"s_mov_b64 exec, 1\n" \
"buffer_store_dword v91, off, %[ws], %[hs] offset:2048 sc1\n" \
"s_mov_b64 exec, -1\n" \
"s_and_b32 s66, s66, 8192\n" \
"v_xor_b32 %[fo], s66, %[fo]\n" \
"s_mov_b32 s64, 262144\n" \
"s_mov_b32 s65, 0\n" \
"s_mov_b32 s67, %[src]\n" \
"s_mov_b32 s68, %[dst]\n" \
"s_lshr_b32 s69, %[wbase], 1\n" \
"v_mov_b32 v90, %[l16]\n" \
"s_barrier\n" \
"s_add_u32 m0, s69, 0\n" \
"s_add_u32 s66, s67, 0\n" \
"buffer_load_dwordx4 %[l16], %[ws], s66 offen offset:0 sc1 lds\n" \
"buffer_load_dwordx4 %[l16], %[ws], s66 offen offset:1024 sc1 lds\n" \
"buffer_load_dwordx4 %[l16], %[ws], s66 offen offset:2048 sc1 lds\n" \
"buffer_load_dwordx4 %[l16], %[ws], s66 offen offset:3072 sc1 lds\n" \
"s_add_u32 m0, s69, 4096\n" \
"s_add_u32 s66, s67, 4096\n" \
"buffer_load_dwordx4 %[l16], %[ws], s66 offen offset:0 sc1 lds\n" \
"buffer_load_dwordx4 %[l16], %[ws], s66 offen offset:1024 sc1 lds\n" \
"buffer_load_dwordx4 %[l16], %[ws], s66 offen offset:2048 sc1 lds\n" \
"buffer_load_dwordx4 %[l16], %[ws], s66 offen offset:3072 sc1 lds\n" \
"s_add_u32 m0, s69, 8192\n" \
"s_add_u32 s66, s67, 8192\n" \
"buffer_load_dwordx4 %[l16], %[ws], s66 offen offset:0 sc1 lds\n" \
"buffer_load_dwordx4 %[l16], %[ws], s66 offen offset:1024 sc1 lds\n" \
"buffer_load_dwordx4 %[l16], %[ws], s66 offen offset:2048 sc1 lds\n" \
"buffer_load_dwordx4 %[l16], %[ws], s66 offen offset:3072 sc1 lds\n" \
"s_add_u32 m0, s69, 12288\n" \
"s_add_u32 s66, s67, 12288\n" \
"buffer_load_dwordx4 %[l16], %[ws], s66 offen offset:0 sc1 lds\n" \
"buffer_load_dwordx4 %[l16], %[ws], s66 offen offset:1024 sc1 lds\n" \
"buffer_load_dwordx4 %[l16], %[ws], s66 offen offset:2048 sc1 lds\n" \
"buffer_load_dwordx4 %[l16], %[ws], s66 offen offset:3072 sc1 lds\n" \
"s_add_u32 s67, s67, 65536\n" \
"s_xor_b32 s69, s69, 65536\n" \
"s_waitcnt vmcnt(0)\n" \
"v_mov_b32 v96, -1\n" \
"v_mov_b32 v97, -1\n" \
"v_mov_b32 v98, -1\n" \
"v_mov_b32 v99, -1\n" \
"s_mov_b32 s70, %[om]\n" \
"Lk_top_%=:\n" \
"s_barrier\n" \
"ds_read_b128 v[20:23], v90 offset:0\n" \
"ds_read_b128 v[24:27], v90 offset:1024\n" \
"ds_read_b128 v[28:31], v90 offset:2048\n" \
"ds_read_b128 v[32:35], v90 offset:3072\n" \
"ds_read_b128 v[36:39], v90 offset:4096\n" \
"ds_read_b128 v[40:43], v90 offset:5120\n" \
"s_waitcnt lgkmcnt(4)\n" \
"v_mfma_f32_16x16x32_bf16 v[52:55], a[0:3], v[20:23], 0\n" \
"ds_read_b128 v[44:47], v90 offset:6144\n" \
"v_mfma_f32_16x16x32_bf16 v[56:59], a[4:7], v[20:23], 0\n" \
"v_mfma_f32_16x16x32_bf16 v[60:63], v[128:131], v[20:23], 0\n" \
"v_mfma_f32_16x16x32_bf16 v[64:67], a[0:3], v[24:27], 0\n" \
"ds_read_b128 v[48:51], v90 offset:7168\n" \
"v_mfma_f32_16x16x32_bf16 v[68:71], a[4:7], v[24:27], 0\n" \
"v_mfma_f32_16x16x32_bf16 v[72:75], v[128:131], v[24:27], 0\n" \
"s_waitcnt lgkmcnt(4)\n" \
"v_mfma_f32_16x16x32_bf16 v[52:55], a[8:11], v[28:31], v[52:55]\n" \
"ds_read_b128 v[20:23], v90 offset:8192\n" \
"v_mfma_f32_16x16x32_bf16 v[56:59], a[12:15], v[28:31], v[56:59]\n" \
"s_add_u32 m0, s69, 0\n" \
"s_add_u32 s66, s67, 0\n" \
"buffer_load_dwordx4 %[l16], %[ws], s66 offen offset:0 sc1 lds\n" \
"v_mfma_f32_16x16x32_bf16 v[60:63], v[132:135], v[28:31], v[60:63]\n" \
"v_mfma_f32_16x16x32_bf16 v[64:67], a[8:11], v[32:35], v[64:67]\n" \
"ds_read_b128 v[24:27], v90 offset:9216\n" \
"v_mfma_f32_16x16x32_bf16 v[68:71], a[12:15], v[32:35], v[68:71]\n" \
"v_mfma_f32_16x16x32_bf16 v[72:75], v[132:135], v[32:35], v[72:75]\n" \
"s_waitcnt lgkmcnt(4)\n" \
"v_mfma_f32_16x16x32_bf16 v[52:55], a[16:19], v[36:39], v[52:55]\n" \
"ds_read_b128 v[28:31], v90 offset:10240\n" \
"v_mfma_f32_16x16x32_bf16 v[56:59], a[20:23], v[36:39], v[56:59]\n" \
"buffer_load_dwordx4 %[l16], %[ws], s66 offen offset:1024 sc1 lds\n" \
"v_mfma_f32_16x16x32_bf16 v[60:63], v[136:139], v[36:39], v[60:63]\n" \
"v_mfma_f32_16x16x32_bf16 v[64:67], a[16:19], v[40:43], v[64:67]\n" \
"ds_read_b128 v[32:35], v90 offset:11264\n" \
"v_mfma_f32_16x16x32_bf16 v[68:71], a[20:23], v[40:43], v[68:71]\n" \
"v_mfma_f32_16x16x32_bf16 v[72:75], v[136:139], v[40:43], v[72:75]\n" \
"s_waitcnt lgkmcnt(4)\n" \
"v_mfma_f32_16x16x32_bf16 v[52:55], a[24:27], v[44:47], v[52:55]\n" \
"ds_read_b128 v[36:39], v90 offset:12288\n" \
"v_mfma_f32_16x16x32_bf16 v[56:59], a[28:31], v[44:47], v[56:59]\n" \
"buffer_load_dwordx4 %[l16], %[ws], s66 offen offset:2048 sc1 lds\n" \
"v_mfma_f32_16x16x32_bf16 v[60:63], v[140:143], v[44:47], v[60:63]\n" \
"v_mfma_f32_16x16x32_bf16 v[64:67], a[24:27], v[48:51], v[64:67]\n" \
"ds_read_b128 v[40:43], v90 offset:13312\n" \
"v_mfma_f32_16x16x32_bf16 v[68:71], a[28:31], v[48:51], v[68:71]\n" \
"v_mfma_f32_16x16x32_bf16 v[72:75], v[140:143], v[48:51], v[72:75]\n" \
"s_waitcnt lgkmcnt(4)\n" \
"v_mfma_f32_16x16x32_bf16 v[52:55], a[32:35], v[20:23], v[52:55]\n" \
"ds_read_b128 v[44:47], v90 offset:14336\n" \
"v_mfma_f32_16x16x32_bf16 v[56:59], a[36:39], v[20:23], v[56:59]\n" \
"buffer_load_dwordx4 %[l16], %[ws], s66 offen offset:3072 sc1 lds\n" \
"v_mfma_f32_16x16x32_bf16 v[60:63], v[144:147], v[20:23], v[60:63]\n" \
"v_mfma_f32_16x16x32_bf16 v[64:67], a[32:35], v[24:27], v[64:67]\n" \
"ds_read_b128 v[48:51], v90 offset:15360\n" \
"v_mfma_f32_16x16x32_bf16 v[68:71], a[36:39], v[24:27], v[68:71]\n" \
"v_mfma_f32_16x16x32_bf16 v[72:75], v[144:147], v[24:27], v[72:75]\n" \
"s_waitcnt lgkmcnt(4)\n" \
"v_mfma_f32_16x16x32_bf16 v[52:55], a[40:43], v[28:31], v[52:55]\n" \
"ds_read_b128 v[20:23], v90 offset:16384\n" \
"v_mfma_f32_16x16x32_bf16 v[56:59], a[44:47], v[28:31], v[56:59]\n" \
"s_add_u32 m0, s69, 4096\n" \
"s_add_u32 s66, s67, 4096\n" \
"buffer_load_dwordx4 %[l16], %[ws], s66 offen offset:0 sc1 lds\n" \
"v_mfma_f32_16x16x32_bf16 v[60:63], v[148:151], v[28:31], v[60:63]\n" \
"s_waitcnt vmcnt(5)\n" \
"v_mov_b32 v91, s65\n" \
"buffer_store_dword v91, %[fo], %[ws], 0 offen sc1\n" \
"v_mfma_f32_16x16x32_bf16 v[64:67], a[40:43], v[32:35], v[64:67]\n" \
"ds_read_b128 v[24:27], v90 offset:17408\n" \
"v_mfma_f32_16x16x32_bf16 v[68:71], a[44:47], v[32:35], v[68:71]\n" \
"v_mfma_f32_16x16x32_bf16 v[72:75], v[148:151], v[32:35], v[72:75]\n" \
"s_waitcnt lgkmcnt(4)\n" \
"v_mfma_f32_16x16x32_bf16 v[52:55], a[48:51], v[36:39], v[52:55]\n" \
"ds_read_b128 v[28:31], v90 offset:18432\n" \
"v_mfma_f32_16x16x32_bf16 v[56:59], a[52:55], v[36:39], v[56:59]\n" \
"buffer_load_dwordx4 %[l16], %[ws], s66 offen offset:1024 sc1 lds\n" \
"v_mfma_f32_16x16x32_bf16 v[60:63], v[152:155], v[36:39], v[60:63]\n" \
"v_mfma_f32_16x16x32_bf16 v[64:67], a[48:51], v[40:43], v[64:67]\n" \
"ds_read_b128 v[32:35], v90 offset:19456\n" \
"v_mfma_f32_16x16x32_bf16 v[68:71], a[52:55], v[40:43], v[68:71]\n" \
"v_mfma_f32_16x16x32_bf16 v[72:75], v[152:155], v[40:43], v[72:75]\n" \
"s_waitcnt lgkmcnt(4)\n" \
"v_mfma_f32_16x16x32_bf16 v[52:55], a[56:59], v[44:47], v[52:55]\n" \
"ds_read_b128 v[36:39], v90 offset:20480\n" \
"v_mfma_f32_16x16x32_bf16 v[56:59], a[60:63], v[44:47], v[56:59]\n" \
"buffer_load_dwordx4 %[l16], %[ws], s66 offen offset:2048 sc1 lds\n" \
"v_mfma_f32_16x16x32_bf16 v[60:63], v[156:159], v[44:47], v[60:63]\n" \
"v_mfma_f32_16x16x32_bf16 v[64:67], a[56:59], v[48:51], v[64:67]\n" \
"ds_read_b128 v[40:43], v90 offset:21504\n" \
"v_mfma_f32_16x16x32_bf16 v[68:71], a[60:63], v[48:51], v[68:71]\n" \
"v_mfma_f32_16x16x32_bf16 v[72:75], v[156:159], v[48:51], v[72:75]\n" \
"s_waitcnt lgkmcnt(4)\n" \
"v_mfma_f32_16x16x32_bf16 v[52:55], a[64:67], v[20:23], v[52:55]\n" \
"ds_read_b128 v[44:47], v90 offset:22528\n" \
"v_mfma_f32_16x16x32_bf16 v[56:59], a[68:71], v[20:23], v[56:59]\n" \
"buffer_load_dwordx4 %[l16], %[ws], s66 offen offset:3072 sc1 lds\n" \
"v_mfma_f32_16x16x32_bf16 v[60:63], v[160:163], v[20:23], v[60:63]\n" \
"v_mfma_f32_16x16x32_bf16 v[64:67], a[64:67], v[24:27], v[64:67]\n" \
"ds_read_b128 v[48:51], v90 offset:23552\n" \
"v_mfma_f32_16x16x32_bf16 v[68:71], a[68:71], v[24:27], v[68:71]\n" \
"v_mfma_f32_16x16x32_bf16 v[72:75], v[160:163], v[24:27], v[72:75]\n" \
"s_waitcnt lgkmcnt(4)\n" \
"v_mfma_f32_16x16x32_bf16 v[52:55], a[72:75], v[28:31], v[52:55]\n" \
"ds_read_b128 v[20:23], v90 offset:24576\n" \
"v_mfma_f32_16x16x32_bf16 v[56:59], a[76:79], v[28:31], v[56:59]\n" \
"s_add_u32 m0, s69, 8192\n" \
"s_add_u32 s66, s67, 8192\n" \
"buffer_load_dwordx4 %[l16], %[ws], s66 offen offset:0 sc1 lds\n" \
"v_mfma_f32_16x16x32_bf16 v[60:63], v[164:167], v[28:31], v[60:63]\n" \
"v_mfma_f32_16x16x32_bf16 v[64:67], a[72:75], v[32:35], v[64:67]\n" \
"ds_read_b128 v[24:27], v90 offset:25600\n" \
"v_mfma_f32_16x16x32_bf16 v[68:71], a[76:79], v[32:35], v[68:71]\n" \
"v_mfma_f32_16x16x32_bf16 v[72:75], v[164:167], v[32:35], v[72:75]\n" \
"s_waitcnt lgkmcnt(4)\n" \
"v_mfma_f32_16x16x32_bf16 v[52:55], a[80:83], v[36:39], v[52:55]\n" \
"ds_read_b128 v[28:31], v90 offset:26624\n" \
"v_mfma_f32_16x16x32_bf16 v[56:59], a[84:87], v[36:39], v[56:59]\n" \
"buffer_load_dwordx4 %[l16], %[ws], s66 offen offset:1024 sc1 lds\n" \
"v_mfma_f32_16x16x32_bf16 v[60:63], v[168:171], v[36:39], v[60:63]\n" \
"v_mfma_f32_16x16x32_bf16 v[64:67], a[80:83], v[40:43], v[64:67]\n" \
"ds_read_b128 v[32:35], v90 offset:27648\n" \
"v_mfma_f32_16x16x32_bf16 v[68:71], a[84:87], v[40:43], v[68:71]\n" \
"v_mfma_f32_16x16x32_bf16 v[72:75], v[168:171], v[40:43], v[72:75]\n" \
"s_waitcnt lgkmcnt(4)\n" \
"v_mfma_f32_16x16x32_bf16 v[52:55], a[88:91], v[44:47], v[52:55]\n" \
"ds_read_b128 v[36:39], v90 offset:28672\n" \
"v_mfma_f32_16x16x32_bf16 v[56:59], a[92:95], v[44:47], v[56:59]\n" \
"buffer_load_dwordx4 %[l16], %[ws], s66 offen offset:2048 sc1 lds\n" \
"v_mfma_f32_16x16x32_bf16 v[60:63], v[172:175], v[44:47], v[60:63]\n" \
"v_mfma_f32_16x16x32_bf16 v[64:67], a[88:91], v[48:51], v[64:67]\n" \
"ds_read_b128 v[40:43], v90 offset:29696\n" \
"v_mfma_f32_16x16x32_bf16 v[68:71], a[92:95], v[48:51], v[68:71]\n" \
"v_mfma_f32_16x16x32_bf16 v[72:75], v[172:175], v[48:51], v[72:75]\n" \
"s_waitcnt lgkmcnt(4)\n" \
"v_mfma_f32_16x16x32_bf16 v[52:55], a[96:99], v[20:23], v[52:55]\n" \
"ds_read_b128 v[44:47], v90 offset:30720\n" \
"v_mfma_f32_16x16x32_bf16 v[56:59], a[100:103], v[20:23], v[56:59]\n" \
"buffer_load_dwordx4 %[l16], %[ws], s66 offen offset:3072 sc1 lds\n" \
"v_mfma_f32_16x16x32_bf16 v[60:63], v[176:179], v[20:23], v[60:63]\n" \
"v_mfma_f32_16x16x32_bf16 v[64:67], a[96:99], v[24:27], v[64:67]\n" \
"ds_read_b128 v[48:51], v90 offset:31744\n" \
"v_mfma_f32_16x16x32_bf16 v[68:71], a[100:103], v[24:27], v[68:71]\n" \
"v_mfma_f32_16x16x32_bf16 v[72:75], v[176:179], v[24:27], v[72:75]\n" \
"s_waitcnt lgkmcnt(4)\n" \
"v_mfma_f32_16x16x32_bf16 v[52:55], a[104:107], v[28:31], v[52:55]\n" \
"ds_read_b128 v[20:23], v90 offset:32768\n" \
"v_mfma_f32_16x16x32_bf16 v[56:59], a[108:111], v[28:31], v[56:59]\n" \
"s_add_u32 m0, s69, 12288\n" \
"s_add_u32 s66, s67, 12288\n" \
"buffer_load_dwordx4 %[l16], %[ws], s66 offen offset:0 sc1 lds\n" \
"v_mfma_f32_16x16x32_bf16 v[60:63], v[180:183], v[28:31], v[60:63]\n" \
"v_mfma_f32_16x16x32_bf16 v[64:67], a[104:107], v[32:35], v[64:67]\n" \
"ds_read_b128 v[24:27], v90 offset:33792\n" \
"v_mfma_f32_16x16x32_bf16 v[68:71], a[108:111], v[32:35], v[68:71]\n" \
"v_mfma_f32_16x16x32_bf16 v[72:75], v[180:183], v[32:35], v[72:75]\n" \
"s_waitcnt lgkmcnt(4)\n" \
"v_mfma_f32_16x16x32_bf16 v[52:55], a[112:115], v[36:39], v[52:55]\n" \
"ds_read_b128 v[28:31], v90 offset:34816\n" \
"v_mfma_f32_16x16x32_bf16 v[56:59], a[116:119], v[36:39], v[56:59]\n" \
"buffer_load_dwordx4 %[l16], %[ws], s66 offen offset:1024 sc1 lds\n" \
"v_mfma_f32_16x16x32_bf16 v[60:63], v[184:187], v[36:39], v[60:63]\n" \
"v_mfma_f32_16x16x32_bf16 v[64:67], a[112:115], v[40:43], v[64:67]\n" \
"ds_read_b128 v[32:35], v90 offset:35840\n" \
"v_mfma_f32_16x16x32_bf16 v[68:71], a[116:119], v[40:43], v[68:71]\n" \
"v_mfma_f32_16x16x32_bf16 v[72:75], v[184:187], v[40:43], v[72:75]\n" \
"s_waitcnt lgkmcnt(4)\n" \
"v_mfma_f32_16x16x32_bf16 v[52:55], a[120:123], v[44:47], v[52:55]\n" \
"ds_read_b128 v[36:39], v90 offset:36864\n" \
"v_mfma_f32_16x16x32_bf16 v[56:59], a[124:127], v[44:47], v[56:59]\n" \
"buffer_load_dwordx4 %[l16], %[ws], s66 offen offset:2048 sc1 lds\n" \
"v_mfma_f32_16x16x32_bf16 v[60:63], v[188:191], v[44:47], v[60:63]\n" \
"v_mfma_f32_16x16x32_bf16 v[64:67], a[120:123], v[48:51], v[64:67]\n" \
"ds_read_b128 v[40:43], v90 offset:37888\n" \
"v_mfma_f32_16x16x32_bf16 v[68:71], a[124:127], v[48:51], v[68:71]\n" \
"v_mfma_f32_16x16x32_bf16 v[72:75], v[188:191], v[48:51], v[72:75]\n" \
"s_waitcnt lgkmcnt(4)\n" \
"v_mfma_f32_16x16x32_bf16 v[52:55], a[128:131], v[20:23], v[52:55]\n" \
"ds_read_b128 v[44:47], v90 offset:38912\n" \
"v_mfma_f32_16x16x32_bf16 v[56:59], a[132:135], v[20:23], v[56:59]\n" \
"buffer_load_dwordx4 %[l16], %[ws], s66 offen offset:3072 sc1 lds\n" \
"v_mfma_f32_16x16x32_bf16 v[60:63], v[192:195], v[20:23], v[60:63]\n" \
"v_mfma_f32_16x16x32_bf16 v[64:67], a[128:131], v[24:27], v[64:67]\n" \
"ds_read_b128 v[48:51], v90 offset:39936\n" \
"v_mfma_f32_16x16x32_bf16 v[68:71], a[132:135], v[24:27], v[68:71]\n" \
"v_mfma_f32_16x16x32_bf16 v[72:75], v[192:195], v[24:27], v[72:75]\n" \
"s_waitcnt lgkmcnt(4)\n" \
"v_mfma_f32_16x16x32_bf16 v[52:55], a[136:139], v[28:31], v[52:55]\n" \
"ds_read_b128 v[20:23], v90 offset:40960\n" \
"v_mfma_f32_16x16x32_bf16 v[56:59], a[140:143], v[28:31], v[56:59]\n" \
"v_mfma_f32_16x16x32_bf16 v[60:63], v[196:199], v[28:31], v[60:63]\n" \
"v_mfma_f32_16x16x32_bf16 v[64:67], a[136:139], v[32:35], v[64:67]\n" \
"ds_read_b128 v[24:27], v90 offset:41984\n" \
"v_mfma_f32_16x16x32_bf16 v[68:71], a[140:143], v[32:35], v[68:71]\n" \
"v_mfma_f32_16x16x32_bf16 v[72:75], v[196:199], v[32:35], v[72:75]\n" \
"s_waitcnt lgkmcnt(4)\n" \
"v_mfma_f32_16x16x32_bf16 v[52:55], a[144:147], v[36:39], v[52:55]\n" \
"ds_read_b128 v[28:31], v90 offset:43008\n" \
"v_mfma_f32_16x16x32_bf16 v[56:59], a[148:151], v[36:39], v[56:59]\n" \
"buffer_store_dwordx4 v[96:99], %[po], %[ws], s70 offen sc1\n" \
"s_add_u32 s70, s70, 65536\n" \
"v_mfma_f32_16x16x32_bf16 v[60:63], v[200:203], v[36:39], v[60:63]\n" \
"v_mfma_f32_16x16x32_bf16 v[64:67], a[144:147], v[40:43], v[64:67]\n" \
"ds_read_b128 v[32:35], v90 offset:44032\n" \
"v_mfma_f32_16x16x32_bf16 v[68:71], a[148:151], v[40:43], v[68:71]\n" \
"v_mfma_f32_16x16x32_bf16 v[72:75], v[200:203], v[40:43], v[72:75]\n" \
"s_waitcnt lgkmcnt(4)\n" \
"v_mfma_f32_16x16x32_bf16 v[52:55], a[152:155], v[44:47], v[52:55]\n" \
"ds_read_b128 v[36:39], v90 offset:45056\n" \
"v_mfma_f32_16x16x32_bf16 v[56:59], a[156:159], v[44:47], v[56:59]\n" \
"v_mfma_f32_16x16x32_bf16 v[60:63], v[204:207], v[44:47], v[60:63]\n" \
"v_mfma_f32_16x16x32_bf16 v[64:67], a[152:155], v[48:51], v[64:67]\n" \
"ds_read_b128 v[40:43], v90 offset:46080\n" \
"v_mfma_f32_16x16x32_bf16 v[68:71], a[156:159], v[48:51], v[68:71]\n" \
"v_mfma_f32_16x16x32_bf16 v[72:75], v[204:207], v[48:51], v[72:75]\n" \
"s_waitcnt lgkmcnt(4)\n" \
"v_mfma_f32_16x16x32_bf16 v[52:55], a[160:163], v[20:23], v[52:55]\n" \
"ds_read_b128 v[44:47], v90 offset:47104\n" \
"v_mfma_f32_16x16x32_bf16 v[56:59], a[164:167], v[20:23], v[56:59]\n" \
"s_add_u32 s67, s67, 65536\n" \
"s_xor_b32 s69, s69, 65536\n" \
"v_mfma_f32_16x16x32_bf16 v[60:63], v[208:211], v[20:23], v[60:63]\n" \
"v_mfma_f32_16x16x32_bf16 v[64:67], a[160:163], v[24:27], v[64:67]\n" \
"ds_read_b128 v[48:51], v90 offset:48128\n" \
"v_mfma_f32_16x16x32_bf16 v[68:71], a[164:167], v[24:27], v[68:71]\n" \
"v_mfma_f32_16x16x32_bf16 v[72:75], v[208:211], v[24:27], v[72:75]\n" \
"s_waitcnt lgkmcnt(4)\n" \
"v_mfma_f32_16x16x32_bf16 v[52:55], a[168:171], v[28:31], v[52:55]\n" \
"ds_read_b128 v[20:23], v90 offset:49152\n" \
"v_mfma_f32_16x16x32_bf16 v[56:59], a[172:175], v[28:31], v[56:59]\n" \
"v_mfma_f32_16x16x32_bf16 v[60:63], v[212:215], v[28:31], v[60:63]\n" \
"v_mfma_f32_16x16x32_bf16 v[64:67], a[168:171], v[32:35], v[64:67]\n" \
"ds_read_b128 v[24:27], v90 offset:50176\n" \
"v_mfma_f32_16x16x32_bf16 v[68:71], a[172:175], v[32:35], v[68:71]\n" \
"v_mfma_f32_16x16x32_bf16 v[72:75], v[212:215], v[32:35], v[72:75]\n" \
"s_waitcnt lgkmcnt(4)\n" \
"v_mfma_f32_16x16x32_bf16 v[52:55], a[176:179], v[36:39], v[52:55]\n" \
"ds_read_b128 v[28:31], v90 offset:51200\n" \
"v_mfma_f32_16x16x32_bf16 v[56:59], a[180:183], v[36:39], v[56:59]\n" \
"v_mfma_f32_16x16x32_bf16 v[60:63], v[216:219], v[36:39], v[60:63]\n" \
"v_mfma_f32_16x16x32_bf16 v[64:67], a[176:179], v[40:43], v[64:67]\n" \
"ds_read_b128 v[32:35], v90 offset:52224\n" \
"v_mfma_f32_16x16x32_bf16 v[68:71], a[180:183], v[40:43], v[68:71]\n" \
"v_mfma_f32_16x16x32_bf16 v[72:75], v[216:219], v[40:43], v[72:75]\n" \
"s_waitcnt lgkmcnt(4)\n" \
"v_mfma_f32_16x16x32_bf16 v[52:55], a[184:187], v[44:47], v[52:55]\n" \
"ds_read_b128 v[36:39], v90 offset:53248\n" \
"v_mfma_f32_16x16x32_bf16 v[56:59], a[188:191], v[44:47], v[56:59]\n" \
"v_mfma_f32_16x16x32_bf16 v[60:63], v[220:223], v[44:47], v[60:63]\n" \
"v_mfma_f32_16x16x32_bf16 v[64:67], a[184:187], v[48:51], v[64:67]\n" \
"ds_read_b128 v[40:43], v90 offset:54272\n" \
"v_mfma_f32_16x16x32_bf16 v[68:71], a[188:191], v[48:51], v[68:71]\n" \
"v_mfma_f32_16x16x32_bf16 v[72:75], v[220:223], v[48:51], v[72:75]\n" \
"s_waitcnt lgkmcnt(4)\n" \
"v_mfma_f32_16x16x32_bf16 v[52:55], a[192:195], v[20:23], v[52:55]\n" \
"ds_read_b128 v[44:47], v90 offset:55296\n" \
"v_mfma_f32_16x16x32_bf16 v[56:59], a[196:199], v[20:23], v[56:59]\n" \
"v_mfma_f32_16x16x32_bf16 v[60:63], v[224:227], v[20:23], v[60:63]\n" \
"v_mfma_f32_16x16x32_bf16 v[64:67], a[192:195], v[24:27], v[64:67]\n" \
"ds_read_b128 v[48:51], v90 offset:56320\n" \
"v_mfma_f32_16x16x32_bf16 v[68:71], a[196:199], v[24:27], v[68:71]\n" \
"v_mfma_f32_16x16x32_bf16 v[72:75], v[224:227], v[24:27], v[72:75]\n" \
"s_waitcnt lgkmcnt(4)\n" \
"v_mfma_f32_16x16x32_bf16 v[52:55], a[200:203], v[28:31], v[52:55]\n" \
"ds_read_b128 v[20:23], v90 offset:57344\n" \
"v_mfma_f32_16x16x32_bf16 v[56:59], a[204:207], v[28:31], v[56:59]\n" \
"v_mfma_f32_16x16x32_bf16 v[60:63], v[228:231], v[28:31], v[60:63]\n" \
"v_mfma_f32_16x16x32_bf16 v[64:67], a[200:203], v[32:35], v[64:67]\n" \
"ds_read_b128 v[24:27], v90 offset:58368\n" \
"v_mfma_f32_16x16x32_bf16 v[68:71], a[204:207], v[32:35], v[68:71]\n" \
"v_mfma_f32_16x16x32_bf16 v[72:75], v[228:231], v[32:35], v[72:75]\n" \
"s_waitcnt lgkmcnt(4)\n" \
"v_mfma_f32_16x16x32_bf16 v[52:55], a[208:211], v[36:39], v[52:55]\n" \
"ds_read_b128 v[28:31], v90 offset:59392\n" \
"v_mfma_f32_16x16x32_bf16 v[56:59], a[212:215], v[36:39], v[56:59]\n" \
"v_mfma_f32_16x16x32_bf16 v[60:63], v[232:235], v[36:39], v[60:63]\n" \
"v_mfma_f32_16x16x32_bf16 v[64:67], a[208:211], v[40:43], v[64:67]\n" \
"ds_read_b128 v[32:35], v90 offset:60416\n" \
"v_mfma_f32_16x16x32_bf16 v[68:71], a[212:215], v[40:43], v[68:71]\n" \
"v_mfma_f32_16x16x32_bf16 v[72:75], v[232:235], v[40:43], v[72:75]\n" \
"s_waitcnt lgkmcnt(4)\n" \
"v_mfma_f32_16x16x32_bf16 v[52:55], a[216:219], v[44:47], v[52:55]\n" \
"ds_read_b128 v[36:39], v90 offset:61440\n" \
"v_mfma_f32_16x16x32_bf16 v[56:59], a[220:223], v[44:47], v[56:59]\n" \
"v_mfma_f32_16x16x32_bf16 v[60:63], v[236:239], v[44:47], v[60:63]\n" \
"v_mfma_f32_16x16x32_bf16 v[64:67], a[216:219], v[48:51], v[64:67]\n" \
"ds_read_b128 v[40:43], v90 offset:62464\n" \
"v_mfma_f32_16x16x32_bf16 v[68:71], a[220:223], v[48:51], v[68:71]\n" \
"v_mfma_f32_16x16x32_bf16 v[72:75], v[236:239], v[48:51], v[72:75]\n" \
"s_waitcnt lgkmcnt(4)\n" \
"v_mfma_f32_16x16x32_bf16 v[52:55], a[224:227], v[20:23], v[52:55]\n" \
"ds_read_b128 v[44:47], v90 offset:63488\n" \
"v_mfma_f32_16x16x32_bf16 v[56:59], a[228:231], v[20:23], v[56:59]\n" \
"v_mfma_f32_16x16x32_bf16 v[60:63], v[240:243], v[20:23], v[60:63]\n" \
"v_mfma_f32_16x16x32_bf16 v[64:67], a[224:227], v[24:27], v[64:67]\n" \
"ds_read_b128 v[48:51], v90 offset:64512\n" \
"v_mfma_f32_16x16x32_bf16 v[68:71], a[228:231], v[24:27], v[68:71]\n" \
"v_mfma_f32_16x16x32_bf16 v[72:75], v[240:243], v[24:27], v[72:75]\n" \
"s_waitcnt lgkmcnt(4)\n" \
"v_mfma_f32_16x16x32_bf16 v[52:55], a[232:235], v[28:31], v[52:55]\n" \
"v_mfma_f32_16x16x32_bf16 v[56:59], a[236:239], v[28:31], v[56:59]\n" \
"v_mfma_f32_16x16x32_bf16 v[60:63], v[244:247], v[28:31], v[60:63]\n" \
"v_mfma_f32_16x16x32_bf16 v[64:67], a[232:235], v[32:35], v[64:67]\n" \
"v_mfma_f32_16x16x32_bf16 v[68:71], a[236:239], v[32:35], v[68:71]\n" \
"v_mfma_f32_16x16x32_bf16 v[72:75], v[244:247], v[32:35], v[72:75]\n" \
"s_waitcnt lgkmcnt(2)\n" \
"v_mfma_f32_16x16x32_bf16 v[52:55], a[240:243], v[36:39], v[52:55]\n" \
"v_mfma_f32_16x16x32_bf16 v[56:59], a[244:247], v[36:39], v[56:59]\n" \
"v_xor_b32 v90, 0x10000, v90\n" \
"s_add_u32 s65, s65, 1\n" \
"s_waitcnt vmcnt(0)\n" \
"v_mfma_f32_16x16x32_bf16 v[60:63], v[248:251], v[36:39], v[60:63]\n" \
"v_mfma_f32_16x16x32_bf16 v[64:67], a[240:243], v[40:43], v[64:67]\n" \
"v_mfma_f32_16x16x32_bf16 v[68:71], a[244:247], v[40:43], v[68:71]\n" \
"v_mfma_f32_16x16x32_bf16 v[72:75], v[248:251], v[40:43], v[72:75]\n" \
"s_waitcnt lgkmcnt(0)\n" \
"v_mfma_f32_16x16x32_bf16 v[52:55], a[248:251], v[44:47], v[52:55]\n" \
"v_mfma_f32_16x16x32_bf16 v[56:59], a[252:255], v[44:47], v[56:59]\n" \
"v_mfma_f32_16x16x32_bf16 v[60:63], v[252:255], v[44:47], v[60:63]\n" \
"v_mfma_f32_16x16x32_bf16 v[64:67], a[248:251], v[48:51], v[64:67]\n" \
"v_mfma_f32_16x16x32_bf16 v[68:71], a[252:255], v[48:51], v[68:71]\n" \
"v_mfma_f32_16x16x32_bf16 v[72:75], v[252:255], v[48:51], v[72:75]\n" \
"s_nop 2\n" \
"v_cvt_pk_bf16_f32 v76, v52, v53\n" \
"v_cvt_pk_bf16_f32 v77, v54, v55\n" \
"v_cvt_pk_bf16_f32 v80, v56, v57\n" \
"v_cvt_pk_bf16_f32 v81, v58, v59\n" \
"v_cvt_pk_bf16_f32 v84, v60, v61\n" \
"v_cvt_pk_bf16_f32 v85, v62, v63\n" \
"v_cvt_pk_bf16_f32 v78, v64, v65\n" \
"v_cvt_pk_bf16_f32 v79, v66, v67\n" \
"v_cvt_pk_bf16_f32 v82, v68, v69\n" \
"v_cvt_pk_bf16_f32 v83, v70, v71\n" \
"v_cvt_pk_bf16_f32 v86, v72, v73\n" \
"v_cvt_pk_bf16_f32 v87, v74, v75\n" \
"v_permlane16_swap_b32 v76, v78\n" \
"v_permlane16_swap_b32 v77, v79\n" \
"buffer_store_dwordx4 v[76:79], %[so1], %[ws], s68 offen sc1\n" \
"v_permlane16_swap_b32 v80, v82\n" \
"v_permlane16_swap_b32 v81, v83\n" \
"buffer_store_dwordx4 v[80:83], %[so2], %[ws], s68 offen sc1\n" \
"v_permlane16_swap_b32 v84, v86\n" \
"v_permlane16_swap_b32 v85, v87\n" \
"buffer_store_dwordx4 v[84:87], %[so3], %[ws], s68 offen sc1\n" \
"s_add_u32 s68, s68, 262144\n" \
"s_cmp_lg_u32 s65, 64\n" \
"s_cbranch_scc1 Lk_top_%=\n" \
"s_waitcnt vmcnt(0)\n" \
"v_mov_b32 v91, s65\n" \
"buffer_store_dword v91, %[fo], %[ws], 0 offen sc1\n" \
"s_endpgm\n" \
"Lk_shs_%=:\n" \
"s_sub_u32 s64, s64, 1\n" \
"s_cbranch_scc1 Lk_dead_%=\n" \
"s_sleep 8\n" \
"buffer_load_dword v91, off, %[ws], %[hs] sc1\n" \
"s_waitcnt vmcnt(0)\n" \
"s_branch Lk_hs_%=\n" \
"Lk_dead_%=:\n" \
"v_mov_b32 v91, 28929\n" \
"buffer_store_dword v91, off, %[ws], 0 sc1\n" \
"s_endpgm\n" \
    : [fo] "+v"(fo) : [l16] "v"(l16), [wvo] "v"(wvo), [tr0] "v"(tr0), [tr1] "v"(tr1), [tr2] "v"(tr2), [so1] "v"(so1), [so2] "v"(so2), [so3] "v"(so3), [po] "v"(po), [hv] "v"(hv), [ws] "s"(ws), [rw] "s"(rw), [so0] "s"(so0), [wbase] "s"(wbase), [src] "s"(src), [dst] "s"(dst), [hs] "s"(hs), [om] "s"(om) \
    : CLOB20)

#define V_ASM() asm volatile( \
"s_nop 4\n" \
"v_add_u32 v24, 0x10000, %[tr0]\n" \
"v_add_u32 v25, 0x10000, %[tr1]\n" \
"v_add_u32 v26, 0x10000, %[tr2]\n" \
"s_add_u32 m0, %[wbase], 0\n" \
"s_add_u32 s66, %[so0], 0\n" \
"buffer_load_dwordx4 %[wvo], %[rw], s66 offen lds\n" \
"s_add_u32 m0, %[wbase], 1024\n" \
"s_add_u32 s66, %[so0], 131072\n" \
"buffer_load_dwordx4 %[wvo], %[rw], s66 offen lds\n" \
"s_add_u32 m0, %[wbase], 2048\n" \
"s_add_u32 s66, %[so0], 128\n" \
"buffer_load_dwordx4 %[wvo], %[rw], s66 offen lds\n" \
"s_add_u32 m0, %[wbase], 3072\n" \
"s_add_u32 s66, %[so0], 131200\n" \
"buffer_load_dwordx4 %[wvo], %[rw], s66 offen lds\n" \
"s_add_u32 m0, %[wbase], 4096\n" \
"s_add_u32 s66, %[so0], 256\n" \
"buffer_load_dwordx4 %[wvo], %[rw], s66 offen lds\n" \
"s_add_u32 m0, %[wbase], 5120\n" \
"s_add_u32 s66, %[so0], 131328\n" \
"buffer_load_dwordx4 %[wvo], %[rw], s66 offen lds\n" \
"s_add_u32 m0, %[wbase], 6144\n" \
"s_add_u32 s66, %[so0], 524288\n" \
"buffer_load_dwordx4 %[wvo], %[rw], s66 offen lds\n" \
"s_add_u32 m0, %[wbase], 7168\n" \
"s_add_u32 s66, %[so0], 655360\n" \
"buffer_load_dwordx4 %[wvo], %[rw], s66 offen lds\n" \
"s_add_u32 m0, %[wbase], 8192\n" \
"s_add_u32 s66, %[so0], 524416\n" \
"buffer_load_dwordx4 %[wvo], %[rw], s66 offen lds\n" \
"s_waitcnt vmcnt(8)\n" \
"s_add_u32 m0, %[wbase], 9216\n" \
"s_add_u32 s66, %[so0], 655488\n" \
"buffer_load_dwordx4 %[wvo], %[rw], s66 offen lds\n" \
"s_waitcnt vmcnt(8)\n" \
"s_add_u32 m0, %[wbase], 10240\n" \
"s_add_u32 s66, %[so0], 524544\n" \
"buffer_load_dwordx4 %[wvo], %[rw], s66 offen lds\n" \
"s_waitcnt vmcnt(8)\n" \
"s_add_u32 m0, %[wbase], 11264\n" \
"s_add_u32 s66, %[so0], 655616\n" \
"buffer_load_dwordx4 %[wvo], %[rw], s66 offen lds\n" \
"s_waitcnt vmcnt(8)\n" \
"s_add_u32 m0, %[wbase], 12288\n" \
"s_add_u32 s66, %[so0], 1048576\n" \
"buffer_load_dwordx4 %[wvo], %[rw], s66 offen lds\n" \
"s_waitcnt vmcnt(8)\n" \
"s_add_u32 m0, %[wbase], 13312\n" \
"s_add_u32 s66, %[so0], 1179648\n" \
"buffer_load_dwordx4 %[wvo], %[rw], s66 offen lds\n" \
"s_waitcnt vmcnt(8)\n" \
"s_add_u32 m0, %[wbase], 14336\n" \
"s_add_u32 s66, %[so0], 1048704\n" \
"buffer_load_dwordx4 %[wvo], %[rw], s66 offen lds\n" \
"s_barrier\n" \
"ds_read_b64_tr_b16 a[0:1], %[tr0] offset:0\n" \
"ds_read_b64_tr_b16 a[2:3], %[tr0] offset:32768\n" \
"ds_read_b64_tr_b16 a[8:9], v24 offset:0\n" \
"ds_read_b64_tr_b16 a[10:11], v24 offset:32768\n" \
"ds_read_b64_tr_b16 a[4:5], %[tr1] offset:0\n" \
"ds_read_b64_tr_b16 a[6:7], %[tr1] offset:32768\n" \
"ds_read_b64_tr_b16 a[12:13], v25 offset:0\n" \
"ds_read_b64_tr_b16 a[14:15], v25 offset:32768\n" \
"ds_read_b64_tr_b16 v[128:129], %[tr2] offset:0\n" \
"ds_read_b64_tr_b16 v[130:131], %[tr2] offset:32768\n" \
"ds_read_b64_tr_b16 v[132:133], v26 offset:0\n" \
"ds_read_b64_tr_b16 v[134:135], v26 offset:32768\n" \
"s_waitcnt lgkmcnt(0)\n" \
"s_waitcnt vmcnt(8)\n" \
"s_add_u32 m0, %[wbase], 15360\n" \
"s_add_u32 s66, %[so0], 1179776\n" \
"buffer_load_dwordx4 %[wvo], %[rw], s66 offen lds\n" \
"s_waitcnt vmcnt(8)\n" \
"s_add_u32 m0, %[wbase], 16384\n" \
"s_add_u32 s66, %[so0], 1048832\n" \
"buffer_load_dwordx4 %[wvo], %[rw], s66 offen lds\n" \
"s_waitcnt vmcnt(8)\n" \
"s_add_u32 m0, %[wbase], 17408\n" \
"s_add_u32 s66, %[so0], 1179904\n" \
"buffer_load_dwordx4 %[wvo], %[rw], s66 offen lds\n" \
"s_waitcnt vmcnt(8)\n" \
"s_add_u32 m0, %[wbase], 18432\n" \
"s_add_u32 s66, %[so0], 1572864\n" \
"buffer_load_dwordx4 %[wvo], %[rw], s66 offen lds\n" \
"s_waitcnt vmcnt(8)\n" \
"s_add_u32 m0, %[wbase], 19456\n" \
"s_add_u32 s66, %[so0], 1703936\n" \
"buffer_load_dwordx4 %[wvo], %[rw], s66 offen lds\n" \
"s_waitcnt vmcnt(8)\n" \
"s_add_u32 m0, %[wbase], 20480\n" \
"s_add_u32 s66, %[so0], 1572992\n" \
"buffer_load_dwordx4 %[wvo], %[rw], s66 offen lds\n" \
"s_barrier\n" \
"ds_read_b64_tr_b16 a[16:17], %[tr0] offset:6144\n" \
"ds_read_b64_tr_b16 a[18:19], %[tr0] offset:38912\n" \
"ds_read_b64_tr_b16 a[24:25], v24 offset:6144\n" \
"ds_read_b64_tr_b16 a[26:27], v24 offset:38912\n" \
"ds_read_b64_tr_b16 a[20:21], %[tr1] offset:6144\n" \
"ds_read_b64_tr_b16 a[22:23], %[tr1] offset:38912\n" \
"ds_read_b64_tr_b16 a[28:29], v25 offset:6144\n" \
"ds_read_b64_tr_b16 a[30:31], v25 offset:38912\n" \
"ds_read_b64_tr_b16 v[136:137], %[tr2] offset:6144\n" \
"ds_read_b64_tr_b16 v[138:139], %[tr2] offset:38912\n" \
"ds_read_b64_tr_b16 v[140:141], v26 offset:6144\n" \
"ds_read_b64_tr_b16 v[142:143], v26 offset:38912\n" \
"s_waitcnt lgkmcnt(0)\n" \
"s_waitcnt vmcnt(8)\n" \
"s_add_u32 m0, %[wbase], 21504\n" \
"s_add_u32 s66, %[so0], 1704064\n" \
"buffer_load_dwordx4 %[wvo], %[rw], s66 offen lds\n" \
"s_waitcnt vmcnt(8)\n" \
"s_add_u32 m0, %[wbase], 22528\n" \
"s_add_u32 s66, %[so0], 1573120\n" \
"buffer_load_dwordx4 %[wvo], %[rw], s66 offen lds\n" \
"s_waitcnt vmcnt(8)\n" \
"s_add_u32 m0, %[wbase], 23552\n" \
"s_add_u32 s66, %[so0], 1704192\n" \
"buffer_load_dwordx4 %[wvo], %[rw], s66 offen lds\n" \
"s_waitcnt vmcnt(8)\n" \
"s_add_u32 m0, %[wbase], 24576\n" \
"s_add_u32 s66, %[so0], 2097152\n" \
"buffer_load_dwordx4 %[wvo], %[rw], s66 offen lds\n" \
"s_waitcnt vmcnt(8)\n" \
"s_add_u32 m0, %[wbase], 25600\n" \
"s_add_u32 s66, %[so0], 2228224\n" \
"buffer_load_dwordx4 %[wvo], %[rw], s66 offen lds\n" \
"s_waitcnt vmcnt(8)\n" \
"s_add_u32 m0, %[wbase], 26624\n" \
"s_add_u32 s66, %[so0], 2097280\n" \
"buffer_load_dwordx4 %[wvo], %[rw], s66 offen lds\n" \
"s_barrier\n" \
"ds_read_b64_tr_b16 a[32:33], %[tr0] offset:12288\n" \
"ds_read_b64_tr_b16 a[34:35], %[tr0] offset:45056\n" \
"ds_read_b64_tr_b16 a[40:41], v24 offset:12288\n" \
"ds_read_b64_tr_b16 a[42:43], v24 offset:45056\n" \
"ds_read_b64_tr_b16 a[36:37], %[tr1] offset:12288\n" \
"ds_read_b64_tr_b16 a[38:39], %[tr1] offset:45056\n" \
"ds_read_b64_tr_b16 a[44:45], v25 offset:12288\n" \
"ds_read_b64_tr_b16 a[46:47], v25 offset:45056\n" \
"ds_read_b64_tr_b16 v[144:145], %[tr2] offset:12288\n" \
"ds_read_b64_tr_b16 v[146:147], %[tr2] offset:45056\n" \
"ds_read_b64_tr_b16 v[148:149], v26 offset:12288\n" \
"ds_read_b64_tr_b16 v[150:151], v26 offset:45056\n" \
"s_waitcnt lgkmcnt(0)\n" \
"s_waitcnt vmcnt(8)\n" \
"s_add_u32 m0, %[wbase], 27648\n" \
"s_add_u32 s66, %[so0], 2228352\n" \
"buffer_load_dwordx4 %[wvo], %[rw], s66 offen lds\n" \
"s_waitcnt vmcnt(8)\n" \
"s_add_u32 m0, %[wbase], 28672\n" \
"s_add_u32 s66, %[so0], 2097408\n" \
"buffer_load_dwordx4 %[wvo], %[rw], s66 offen lds\n" \
"s_waitcnt vmcnt(8)\n" \
"s_add_u32 m0, %[wbase], 29696\n" \
"s_add_u32 s66, %[so0], 2228480\n" \
"buffer_load_dwordx4 %[wvo], %[rw], s66 offen lds\n" \
"s_waitcnt vmcnt(8)\n" \
"s_add_u32 m0, %[wbase], 0\n" \
"s_add_u32 s66, %[so0], 2621440\n" \
"buffer_load_dwordx4 %[wvo], %[rw], s66 offen lds\n" \
"s_waitcnt vmcnt(8)\n" \
"s_add_u32 m0, %[wbase], 1024\n" \
"s_add_u32 s66, %[so0], 2752512\n" \
"buffer_load_dwordx4 %[wvo], %[rw], s66 offen lds\n" \
"s_waitcnt vmcnt(8)\n" \
"s_add_u32 m0, %[wbase], 2048\n" \
"s_add_u32 s66, %[so0], 2621568\n" \
"buffer_load_dwordx4 %[wvo], %[rw], s66 offen lds\n" \
"s_barrier\n" \
"ds_read_b64_tr_b16 a[48:49], %[tr0] offset:18432\n" \
"ds_read_b64_tr_b16 a[50:51], %[tr0] offset:51200\n" \
"ds_read_b64_tr_b16 a[56:57], v24 offset:18432\n" \
"ds_read_b64_tr_b16 a[58:59], v24 offset:51200\n" \
"ds_read_b64_tr_b16 a[52:53], %[tr1] offset:18432\n" \
"ds_read_b64_tr_b16 a[54:55], %[tr1] offset:51200\n" \
"ds_read_b64_tr_b16 a[60:61], v25 offset:18432\n" \
"ds_read_b64_tr_b16 a[62:63], v25 offset:51200\n" \
"ds_read_b64_tr_b16 v[152:153], %[tr2] offset:18432\n" \
"ds_read_b64_tr_b16 v[154:155], %[tr2] offset:51200\n" \
"ds_read_b64_tr_b16 v[156:157], v26 offset:18432\n" \
"ds_read_b64_tr_b16 v[158:159], v26 offset:51200\n" \
"s_waitcnt lgkmcnt(0)\n" \
"s_waitcnt vmcnt(8)\n" \
"s_add_u32 m0, %[wbase], 3072\n" \
"s_add_u32 s66, %[so0], 2752640\n" \
"buffer_load_dwordx4 %[wvo], %[rw], s66 offen lds\n" \
"s_waitcnt vmcnt(8)\n" \
"s_add_u32 m0, %[wbase], 4096\n" \
"s_add_u32 s66, %[so0], 2621696\n" \
"buffer_load_dwordx4 %[wvo], %[rw], s66 offen lds\n" \
"s_waitcnt vmcnt(8)\n" \
"s_add_u32 m0, %[wbase], 5120\n" \
"s_add_u32 s66, %[so0], 2752768\n" \
"buffer_load_dwordx4 %[wvo], %[rw], s66 offen lds\n" \
"s_waitcnt vmcnt(8)\n" \
"s_add_u32 m0, %[wbase], 6144\n" \
"s_add_u32 s66, %[so0], 3145728\n" \
"buffer_load_dwordx4 %[wvo], %[rw], s66 offen lds\n" \
"s_waitcnt vmcnt(8)\n" \
"s_add_u32 m0, %[wbase], 7168\n" \
"s_add_u32 s66, %[so0], 3276800\n" \
"buffer_load_dwordx4 %[wvo], %[rw], s66 offen lds\n" \
"s_waitcnt vmcnt(8)\n" \
"s_add_u32 m0, %[wbase], 8192\n" \
"s_add_u32 s66, %[so0], 3145856\n" \
"buffer_load_dwordx4 %[wvo], %[rw], s66 offen lds\n" \
"s_barrier\n" \
"ds_read_b64_tr_b16 a[64:65], %[tr0] offset:24576\n" \
"ds_read_b64_tr_b16 a[66:67], %[tr0] offset:57344\n" \
"ds_read_b64_tr_b16 a[72:73], v24 offset:24576\n" \
"ds_read_b64_tr_b16 a[74:75], v24 offset:57344\n" \
"ds_read_b64_tr_b16 a[68:69], %[tr1] offset:24576\n" \
"ds_read_b64_tr_b16 a[70:71], %[tr1] offset:57344\n" \
"ds_read_b64_tr_b16 a[76:77], v25 offset:24576\n" \
"ds_read_b64_tr_b16 a[78:79], v25 offset:57344\n" \
"ds_read_b64_tr_b16 v[160:161], %[tr2] offset:24576\n" \
"ds_read_b64_tr_b16 v[162:163], %[tr2] offset:57344\n" \
"ds_read_b64_tr_b16 v[164:165], v26 offset:24576\n" \
"ds_read_b64_tr_b16 v[166:167], v26 offset:57344\n" \
"s_waitcnt lgkmcnt(0)\n" \
"s_waitcnt vmcnt(8)\n" \
"s_add_u32 m0, %[wbase], 9216\n" \
"s_add_u32 s66, %[so0], 3276928\n" \
"buffer_load_dwordx4 %[wvo], %[rw], s66 offen lds\n" \
"s_waitcnt vmcnt(8)\n" \
"s_add_u32 m0, %[wbase], 10240\n" \
"s_add_u32 s66, %[so0], 3145984\n" \
"buffer_load_dwordx4 %[wvo], %[rw], s66 offen lds\n" \
"s_waitcnt vmcnt(8)\n" \
"s_add_u32 m0, %[wbase], 11264\n" \
"s_add_u32 s66, %[so0], 3277056\n" \
"buffer_load_dwordx4 %[wvo], %[rw], s66 offen lds\n" \
"s_waitcnt vmcnt(8)\n" \
"s_add_u32 m0, %[wbase], 12288\n" \
"s_add_u32 s66, %[so0], 3670016\n" \
"buffer_load_dwordx4 %[wvo], %[rw], s66 offen lds\n" \
"s_waitcnt vmcnt(8)\n" \
"s_add_u32 m0, %[wbase], 13312\n" \
"s_add_u32 s66, %[so0], 3801088\n" \
"buffer_load_dwordx4 %[wvo], %[rw], s66 offen lds\n" \
"s_waitcnt vmcnt(8)\n" \
"s_add_u32 m0, %[wbase], 14336\n" \
"s_add_u32 s66, %[so0], 3670144\n" \
"buffer_load_dwordx4 %[wvo], %[rw], s66 offen lds\n" \
"s_barrier\n" \
"ds_read_b64_tr_b16 a[80:81], %[tr0] offset:0\n" \
"ds_read_b64_tr_b16 a[82:83], %[tr0] offset:32768\n" \
"ds_read_b64_tr_b16 a[88:89], v24 offset:0\n" \
"ds_read_b64_tr_b16 a[90:91], v24 offset:32768\n" \
"ds_read_b64_tr_b16 a[84:85], %[tr1] offset:0\n" \
"ds_read_b64_tr_b16 a[86:87], %[tr1] offset:32768\n" \
"ds_read_b64_tr_b16 a[92:93], v25 offset:0\n" \
"ds_read_b64_tr_b16 a[94:95], v25 offset:32768\n" \
"ds_read_b64_tr_b16 v[168:169], %[tr2] offset:0\n" \
"ds_read_b64_tr_b16 v[170:171], %[tr2] offset:32768\n" \
"ds_read_b64_tr_b16 v[172:173], v26 offset:0\n" \
"ds_read_b64_tr_b16 v[174:175], v26 offset:32768\n" \
"s_waitcnt lgkmcnt(0)\n" \
"s_waitcnt vmcnt(8)\n" \
"s_add_u32 m0, %[wbase], 15360\n" \
"s_add_u32 s66, %[so0], 3801216\n" \
"buffer_load_dwordx4 %[wvo], %[rw], s66 offen lds\n" \
"s_waitcnt vmcnt(8)\n" \
"s_add_u32 m0, %[wbase], 16384\n" \
"s_add_u32 s66, %[so0], 3670272\n" \
"buffer_load_dwordx4 %[wvo], %[rw], s66 offen lds\n" \
"s_waitcnt vmcnt(8)\n" \
"s_add_u32 m0, %[wbase], 17408\n" \
"s_add_u32 s66, %[so0], 3801344\n" \
"buffer_load_dwordx4 %[wvo], %[rw], s66 offen lds\n" \
"s_waitcnt vmcnt(8)\n" \
"s_add_u32 m0, %[wbase], 18432\n" \
"s_add_u32 s66, %[so0], 4194304\n" \
"buffer_load_dwordx4 %[wvo], %[rw], s66 offen lds\n" \
"s_waitcnt vmcnt(8)\n" \
"s_add_u32 m0, %[wbase], 19456\n" \
"s_add_u32 s66, %[so0], 4325376\n" \
"buffer_load_dwordx4 %[wvo], %[rw], s66 offen lds\n" \
"s_waitcnt vmcnt(8)\n" \
"s_add_u32 m0, %[wbase], 20480\n" \
"s_add_u32 s66, %[so0], 4194432\n" \
"buffer_load_dwordx4 %[wvo], %[rw], s66 offen lds\n" \
"s_barrier\n" \
"ds_read_b64_tr_b16 a[96:97], %[tr0] offset:6144\n" \
"ds_read_b64_tr_b16 a[98:99], %[tr0] offset:38912\n" \
"ds_read_b64_tr_b16 a[104:105], v24 offset:6144\n" \
"ds_read_b64_tr_b16 a[106:107], v24 offset:38912\n" \
"ds_read_b64_tr_b16 a[100:101], %[tr1] offset:6144\n" \
"ds_read_b64_tr_b16 a[102:103], %[tr1] offset:38912\n" \
"ds_read_b64_tr_b16 a[108:109], v25 offset:6144\n" \
"ds_read_b64_tr_b16 a[110:111], v25 offset:38912\n" \
"ds_read_b64_tr_b16 v[176:177], %[tr2] offset:6144\n" \
"ds_read_b64_tr_b16 v[178:179], %[tr2] offset:38912\n" \
"ds_read_b64_tr_b16 v[180:181], v26 offset:6144\n" \
"ds_read_b64_tr_b16 v[182:183], v26 offset:38912\n" \
"s_waitcnt lgkmcnt(0)\n" \
"s_waitcnt vmcnt(8)\n" \
"s_add_u32 m0, %[wbase], 21504\n" \
"s_add_u32 s66, %[so0], 4325504\n" \
"buffer_load_dwordx4 %[wvo], %[rw], s66 offen lds\n" \
"s_waitcnt vmcnt(8)\n" \
"s_add_u32 m0, %[wbase], 22528\n" \
"s_add_u32 s66, %[so0], 4194560\n" \
"buffer_load_dwordx4 %[wvo], %[rw], s66 offen lds\n" \
"s_waitcnt vmcnt(8)\n" \
"s_add_u32 m0, %[wbase], 23552\n" \
"s_add_u32 s66, %[so0], 4325632\n" \
"buffer_load_dwordx4 %[wvo], %[rw], s66 offen lds\n" \
"s_waitcnt vmcnt(8)\n" \
"s_add_u32 m0, %[wbase], 24576\n" \
"s_add_u32 s66, %[so0], 4718592\n" \
"buffer_load_dwordx4 %[wvo], %[rw], s66 offen lds\n" \
"s_waitcnt vmcnt(8)\n" \
"s_add_u32 m0, %[wbase], 25600\n" \
"s_add_u32 s66, %[so0], 4849664\n" \
"buffer_load_dwordx4 %[wvo], %[rw], s66 offen lds\n" \
"s_waitcnt vmcnt(8)\n" \
"s_add_u32 m0, %[wbase], 26624\n" \
"s_add_u32 s66, %[so0], 4718720\n" \
"buffer_load_dwordx4 %[wvo], %[rw], s66 offen lds\n" \
"s_barrier\n" \
"ds_read_b64_tr_b16 a[112:113], %[tr0] offset:12288\n" \
"ds_read_b64_tr_b16 a[114:115], %[tr0] offset:45056\n" \
"ds_read_b64_tr_b16 a[120:121], v24 offset:12288\n" \
"ds_read_b64_tr_b16 a[122:123], v24 offset:45056\n" \
"ds_read_b64_tr_b16 a[116:117], %[tr1] offset:12288\n" \
"ds_read_b64_tr_b16 a[118:119], %[tr1] offset:45056\n" \
"ds_read_b64_tr_b16 a[124:125], v25 offset:12288\n" \
"ds_read_b64_tr_b16 a[126:127], v25 offset:45056\n" \
"ds_read_b64_tr_b16 v[184:185], %[tr2] offset:12288\n" \
"ds_read_b64_tr_b16 v[186:187], %[tr2] offset:45056\n" \
"ds_read_b64_tr_b16 v[188:189], v26 offset:12288\n" \
"ds_read_b64_tr_b16 v[190:191], v26 offset:45056\n" \
"s_waitcnt lgkmcnt(0)\n" \
"s_waitcnt vmcnt(8)\n" \
"s_add_u32 m0, %[wbase], 27648\n" \
"s_add_u32 s66, %[so0], 4849792\n" \
"buffer_load_dwordx4 %[wvo], %[rw], s66 offen lds\n" \
"s_waitcnt vmcnt(8)\n" \
"s_add_u32 m0, %[wbase], 28672\n" \
"s_add_u32 s66, %[so0], 4718848\n" \
"buffer_load_dwordx4 %[wvo], %[rw], s66 offen lds\n" \
"s_waitcnt vmcnt(8)\n" \
"s_add_u32 m0, %[wbase], 29696\n" \
"s_add_u32 s66, %[so0], 4849920\n" \
"buffer_load_dwordx4 %[wvo], %[rw], s66 offen lds\n" \
"s_waitcnt vmcnt(8)\n" \
"s_add_u32 m0, %[wbase], 0\n" \
"s_add_u32 s66, %[so0], 5242880\n" \
"buffer_load_dwordx4 %[wvo], %[rw], s66 offen lds\n" \
"s_waitcnt vmcnt(8)\n" \
"s_add_u32 m0, %[wbase], 1024\n" \
"s_add_u32 s66, %[so0], 5373952\n" \
"buffer_load_dwordx4 %[wvo], %[rw], s66 offen lds\n" \
"s_waitcnt vmcnt(8)\n" \
"s_add_u32 m0, %[wbase], 2048\n" \
"s_add_u32 s66, %[so0], 5243008\n" \
"buffer_load_dwordx4 %[wvo], %[rw], s66 offen lds\n" \
"s_barrier\n" \
"ds_read_b64_tr_b16 a[128:129], %[tr0] offset:18432\n" \
"ds_read_b64_tr_b16 a[130:131], %[tr0] offset:51200\n" \
"ds_read_b64_tr_b16 a[136:137], v24 offset:18432\n" \
"ds_read_b64_tr_b16 a[138:139], v24 offset:51200\n" \
"ds_read_b64_tr_b16 a[132:133], %[tr1] offset:18432\n" \
"ds_read_b64_tr_b16 a[134:135], %[tr1] offset:51200\n" \
"ds_read_b64_tr_b16 a[140:141], v25 offset:18432\n" \
"ds_read_b64_tr_b16 a[142:143], v25 offset:51200\n" \
"ds_read_b64_tr_b16 v[192:193], %[tr2] offset:18432\n" \
"ds_read_b64_tr_b16 v[194:195], %[tr2] offset:51200\n" \
"ds_read_b64_tr_b16 v[196:197], v26 offset:18432\n" \
"ds_read_b64_tr_b16 v[198:199], v26 offset:51200\n" \
"s_waitcnt lgkmcnt(0)\n" \
"s_waitcnt vmcnt(8)\n" \
"s_add_u32 m0, %[wbase], 3072\n" \
"s_add_u32 s66, %[so0], 5374080\n" \
"buffer_load_dwordx4 %[wvo], %[rw], s66 offen lds\n" \
"s_waitcnt vmcnt(8)\n" \
"s_add_u32 m0, %[wbase], 4096\n" \
"s_add_u32 s66, %[so0], 5243136\n" \
"buffer_load_dwordx4 %[wvo], %[rw], s66 offen lds\n" \
"s_waitcnt vmcnt(8)\n" \
"s_add_u32 m0, %[wbase], 5120\n" \
"s_add_u32 s66, %[so0], 5374208\n" \
"buffer_load_dwordx4 %[wvo], %[rw], s66 offen lds\n" \
"s_waitcnt vmcnt(8)\n" \
"s_add_u32 m0, %[wbase], 6144\n" \
"s_add_u32 s66, %[so0], 5767168\n" \
"buffer_load_dwordx4 %[wvo], %[rw], s66 offen lds\n" \
"s_waitcnt vmcnt(8)\n" \
"s_add_u32 m0, %[wbase], 7168\n" \
"s_add_u32 s66, %[so0], 5898240\n" \
"buffer_load_dwordx4 %[wvo], %[rw], s66 offen lds\n" \
"s_waitcnt vmcnt(8)\n" \
"s_add_u32 m0, %[wbase], 8192\n" \
"s_add_u32 s66, %[so0], 5767296\n" \
"buffer_load_dwordx4 %[wvo], %[rw], s66 offen lds\n" \
"s_barrier\n" \
"ds_read_b64_tr_b16 a[144:145], %[tr0] offset:24576\n" \
"ds_read_b64_tr_b16 a[146:147], %[tr0] offset:57344\n" \
"ds_read_b64_tr_b16 a[152:153], v24 offset:24576\n" \
"ds_read_b64_tr_b16 a[154:155], v24 offset:57344\n" \
"ds_read_b64_tr_b16 a[148:149], %[tr1] offset:24576\n" \
"ds_read_b64_tr_b16 a[150:151], %[tr1] offset:57344\n" \
"ds_read_b64_tr_b16 a[156:157], v25 offset:24576\n" \
"ds_read_b64_tr_b16 a[158:159], v25 offset:57344\n" \
"ds_read_b64_tr_b16 v[200:201], %[tr2] offset:24576\n" \
"ds_read_b64_tr_b16 v[202:203], %[tr2] offset:57344\n" \
"ds_read_b64_tr_b16 v[204:205], v26 offset:24576\n" \
"ds_read_b64_tr_b16 v[206:207], v26 offset:57344\n" \
"s_waitcnt lgkmcnt(0)\n" \
"s_waitcnt vmcnt(8)\n" \
"s_add_u32 m0, %[wbase], 9216\n" \
"s_add_u32 s66, %[so0], 5898368\n" \
"buffer_load_dwordx4 %[wvo], %[rw], s66 offen lds\n" \
"s_waitcnt vmcnt(8)\n" \
"s_add_u32 m0, %[wbase], 10240\n" \
"s_add_u32 s66, %[so0], 5767424\n" \
"buffer_load_dwordx4 %[wvo], %[rw], s66 offen lds\n" \
"s_waitcnt vmcnt(8)\n" \
"s_add_u32 m0, %[wbase], 11264\n" \
"s_add_u32 s66, %[so0], 5898496\n" \
"buffer_load_dwordx4 %[wvo], %[rw], s66 offen lds\n" \
"s_waitcnt vmcnt(8)\n" \
"s_add_u32 m0, %[wbase], 12288\n" \
"s_add_u32 s66, %[so0], 6291456\n" \
"buffer_load_dwordx4 %[wvo], %[rw], s66 offen lds\n" \
"s_waitcnt vmcnt(8)\n" \
"s_add_u32 m0, %[wbase], 13312\n" \
"s_add_u32 s66, %[so0], 6422528\n" \
"buffer_load_dwordx4 %[wvo], %[rw], s66 offen lds\n" \
"s_waitcnt vmcnt(8)\n" \
"s_add_u32 m0, %[wbase], 14336\n" \
"s_add_u32 s66, %[so0], 6291584\n" \
"buffer_load_dwordx4 %[wvo], %[rw], s66 offen lds\n" \
"s_barrier\n" \
"ds_read_b64_tr_b16 a[160:161], %[tr0] offset:0\n" \
"ds_read_b64_tr_b16 a[162:163], %[tr0] offset:32768\n" \
"ds_read_b64_tr_b16 a[168:169], v24 offset:0\n" \
"ds_read_b64_tr_b16 a[170:171], v24 offset:32768\n" \
"ds_read_b64_tr_b16 a[164:165], %[tr1] offset:0\n" \
"ds_read_b64_tr_b16 a[166:167], %[tr1] offset:32768\n" \
"ds_read_b64_tr_b16 a[172:173], v25 offset:0\n" \
"ds_read_b64_tr_b16 a[174:175], v25 offset:32768\n" \
"ds_read_b64_tr_b16 v[208:209], %[tr2] offset:0\n" \
"ds_read_b64_tr_b16 v[210:211], %[tr2] offset:32768\n" \
"ds_read_b64_tr_b16 v[212:213], v26 offset:0\n" \
"ds_read_b64_tr_b16 v[214:215], v26 offset:32768\n" \
"s_waitcnt lgkmcnt(0)\n" \
"s_waitcnt vmcnt(8)\n" \
"s_add_u32 m0, %[wbase], 15360\n" \
"s_add_u32 s66, %[so0], 6422656\n" \
"buffer_load_dwordx4 %[wvo], %[rw], s66 offen lds\n" \
"s_waitcnt vmcnt(8)\n" \
"s_add_u32 m0, %[wbase], 16384\n" \
"s_add_u32 s66, %[so0], 6291712\n" \
"buffer_load_dwordx4 %[wvo], %[rw], s66 offen lds\n" \
"s_waitcnt vmcnt(8)\n" \
"s_add_u32 m0, %[wbase], 17408\n" \
"s_add_u32 s66, %[so0], 6422784\n" \
"buffer_load_dwordx4 %[wvo], %[rw], s66 offen lds\n" \
"s_waitcnt vmcnt(8)\n" \
"s_add_u32 m0, %[wbase], 18432\n" \
"s_add_u32 s66, %[so0], 6815744\n" \
"buffer_load_dwordx4 %[wvo], %[rw], s66 offen lds\n" \
"s_waitcnt vmcnt(8)\n" \
"s_add_u32 m0, %[wbase], 19456\n" \
"s_add_u32 s66, %[so0], 6946816\n" \
"buffer_load_dwordx4 %[wvo], %[rw], s66 offen lds\n" \
"s_waitcnt vmcnt(8)\n" \
"s_add_u32 m0, %[wbase], 20480\n" \
"s_add_u32 s66, %[so0], 6815872\n" \
"buffer_load_dwordx4 %[wvo], %[rw], s66 offen lds\n" \
"s_barrier\n" \
"ds_read_b64_tr_b16 a[176:177], %[tr0] offset:6144\n" \
"ds_read_b64_tr_b16 a[178:179], %[tr0] offset:38912\n" \
"ds_read_b64_tr_b16 a[184:185], v24 offset:6144\n" \
"ds_read_b64_tr_b16 a[186:187], v24 offset:38912\n" \
"ds_read_b64_tr_b16 a[180:181], %[tr1] offset:6144\n" \
"ds_read_b64_tr_b16 a[182:183], %[tr1] offset:38912\n" \
"ds_read_b64_tr_b16 a[188:189], v25 offset:6144\n" \
"ds_read_b64_tr_b16 a[190:191], v25 offset:38912\n" \
"ds_read_b64_tr_b16 v[216:217], %[tr2] offset:6144\n" \
"ds_read_b64_tr_b16 v[218:219], %[tr2] offset:38912\n" \
"ds_read_b64_tr_b16 v[220:221], v26 offset:6144\n" \
"ds_read_b64_tr_b16 v[222:223], v26 offset:38912\n" \
"s_waitcnt lgkmcnt(0)\n" \
"s_waitcnt vmcnt(8)\n" \
"s_add_u32 m0, %[wbase], 21504\n" \
"s_add_u32 s66, %[so0], 6946944\n" \
"buffer_load_dwordx4 %[wvo], %[rw], s66 offen lds\n" \
"s_waitcnt vmcnt(8)\n" \
"s_add_u32 m0, %[wbase], 22528\n" \
"s_add_u32 s66, %[so0], 6816000\n" \
"buffer_load_dwordx4 %[wvo], %[rw], s66 offen lds\n" \
"s_waitcnt vmcnt(8)\n" \
"s_add_u32 m0, %[wbase], 23552\n" \
"s_add_u32 s66, %[so0], 6947072\n" \
"buffer_load_dwordx4 %[wvo], %[rw], s66 offen lds\n" \
"s_waitcnt vmcnt(8)\n" \
"s_add_u32 m0, %[wbase], 24576\n" \
"s_add_u32 s66, %[so0], 7340032\n" \
"buffer_load_dwordx4 %[wvo], %[rw], s66 offen lds\n" \
"s_waitcnt vmcnt(8)\n" \
"s_add_u32 m0, %[wbase], 25600\n" \
"s_add_u32 s66, %[so0], 7471104\n" \
"buffer_load_dwordx4 %[wvo], %[rw], s66 offen lds\n" \
"s_waitcnt vmcnt(8)\n" \
"s_add_u32 m0, %[wbase], 26624\n" \
"s_add_u32 s66, %[so0], 7340160\n" \
"buffer_load_dwordx4 %[wvo], %[rw], s66 offen lds\n" \
"s_barrier\n" \
"ds_read_b64_tr_b16 a[192:193], %[tr0] offset:12288\n" \
"ds_read_b64_tr_b16 a[194:195], %[tr0] offset:45056\n" \
"ds_read_b64_tr_b16 a[200:201], v24 offset:12288\n" \
"ds_read_b64_tr_b16 a[202:203], v24 offset:45056\n" \
"ds_read_b64_tr_b16 a[196:197], %[tr1] offset:12288\n" \
"ds_read_b64_tr_b16 a[198:199], %[tr1] offset:45056\n" \
"ds_read_b64_tr_b16 a[204:205], v25 offset:12288\n" \
"ds_read_b64_tr_b16 a[206:207], v25 offset:45056\n" \
"ds_read_b64_tr_b16 v[224:225], %[tr2] offset:12288\n" \
"ds_read_b64_tr_b16 v[226:227], %[tr2] offset:45056\n" \
"ds_read_b64_tr_b16 v[228:229], v26 offset:12288\n" \
"ds_read_b64_tr_b16 v[230:231], v26 offset:45056\n" \
"s_waitcnt lgkmcnt(0)\n" \
"s_waitcnt vmcnt(8)\n" \
"s_add_u32 m0, %[wbase], 27648\n" \
"s_add_u32 s66, %[so0], 7471232\n" \
"buffer_load_dwordx4 %[wvo], %[rw], s66 offen lds\n" \
"s_waitcnt vmcnt(8)\n" \
"s_add_u32 m0, %[wbase], 28672\n" \
"s_add_u32 s66, %[so0], 7340288\n" \
"buffer_load_dwordx4 %[wvo], %[rw], s66 offen lds\n" \
"s_waitcnt vmcnt(8)\n" \
"s_add_u32 m0, %[wbase], 29696\n" \
"s_add_u32 s66, %[so0], 7471360\n" \
"buffer_load_dwordx4 %[wvo], %[rw], s66 offen lds\n" \
"s_waitcnt vmcnt(8)\n" \
"s_add_u32 m0, %[wbase], 0\n" \
"s_add_u32 s66, %[so0], 7864320\n" \
"buffer_load_dwordx4 %[wvo], %[rw], s66 offen lds\n" \
"s_waitcnt vmcnt(8)\n" \
"s_add_u32 m0, %[wbase], 1024\n" \
"s_add_u32 s66, %[so0], 7995392\n" \
"buffer_load_dwordx4 %[wvo], %[rw], s66 offen lds\n" \
"s_waitcnt vmcnt(8)\n" \
"s_add_u32 m0, %[wbase], 2048\n" \
"s_add_u32 s66, %[so0], 7864448\n" \
"buffer_load_dwordx4 %[wvo], %[rw], s66 offen lds\n" \
"s_barrier\n" \
"ds_read_b64_tr_b16 a[208:209], %[tr0] offset:18432\n" \
"ds_read_b64_tr_b16 a[210:211], %[tr0] offset:51200\n" \
"ds_read_b64_tr_b16 a[216:217], v24 offset:18432\n" \
"ds_read_b64_tr_b16 a[218:219], v24 offset:51200\n" \
"ds_read_b64_tr_b16 a[212:213], %[tr1] offset:18432\n" \
"ds_read_b64_tr_b16 a[214:215], %[tr1] offset:51200\n" \
"ds_read_b64_tr_b16 a[220:221], v25 offset:18432\n" \
"ds_read_b64_tr_b16 a[222:223], v25 offset:51200\n" \
"ds_read_b64_tr_b16 v[232:233], %[tr2] offset:18432\n" \
"ds_read_b64_tr_b16 v[234:235], %[tr2] offset:51200\n" \
"ds_read_b64_tr_b16 v[236:237], v26 offset:18432\n" \
"ds_read_b64_tr_b16 v[238:239], v26 offset:51200\n" \
"s_waitcnt lgkmcnt(0)\n" \
"s_waitcnt vmcnt(8)\n" \
"s_add_u32 m0, %[wbase], 3072\n" \
"s_add_u32 s66, %[so0], 7995520\n" \
"buffer_load_dwordx4 %[wvo], %[rw], s66 offen lds\n" \
"s_waitcnt vmcnt(8)\n" \
"s_add_u32 m0, %[wbase], 4096\n" \
"s_add_u32 s66, %[so0], 7864576\n" \
"buffer_load_dwordx4 %[wvo], %[rw], s66 offen lds\n" \
"s_waitcnt vmcnt(8)\n" \
"s_add_u32 m0, %[wbase], 5120\n" \
"s_add_u32 s66, %[so0], 7995648\n" \
"buffer_load_dwordx4 %[wvo], %[rw], s66 offen lds\n" \
"buffer_load_dword v91, off, %[ws], %[hs] sc1\n" \
"s_waitcnt vmcnt(7)\n" \
"s_barrier\n" \
"ds_read_b64_tr_b16 a[224:225], %[tr0] offset:24576\n" \
"ds_read_b64_tr_b16 a[226:227], %[tr0] offset:57344\n" \
"ds_read_b64_tr_b16 a[232:233], v24 offset:24576\n" \
"ds_read_b64_tr_b16 a[234:235], v24 offset:57344\n" \
"ds_read_b64_tr_b16 a[228:229], %[tr1] offset:24576\n" \
"ds_read_b64_tr_b16 a[230:231], %[tr1] offset:57344\n" \
"ds_read_b64_tr_b16 a[236:237], v25 offset:24576\n" \
"ds_read_b64_tr_b16 a[238:239], v25 offset:57344\n" \
"ds_read_b64_tr_b16 v[240:241], %[tr2] offset:24576\n" \
"ds_read_b64_tr_b16 v[242:243], %[tr2] offset:57344\n" \
"ds_read_b64_tr_b16 v[244:245], v26 offset:24576\n" \
"ds_read_b64_tr_b16 v[246:247], v26 offset:57344\n" \
"s_waitcnt lgkmcnt(0)\n" \
"s_waitcnt vmcnt(1)\n" \
"s_barrier\n" \
"ds_read_b64_tr_b16 a[240:241], %[tr0] offset:0\n" \
"ds_read_b64_tr_b16 a[242:243], %[tr0] offset:32768\n" \
"ds_read_b64_tr_b16 a[248:249], v24 offset:0\n" \
"ds_read_b64_tr_b16 a[250:251], v24 offset:32768\n" \
"ds_read_b64_tr_b16 a[244:245], %[tr1] offset:0\n" \
"ds_read_b64_tr_b16 a[246:247], %[tr1] offset:32768\n" \
"ds_read_b64_tr_b16 a[252:253], v25 offset:0\n" \
"ds_read_b64_tr_b16 a[254:255], v25 offset:32768\n" \
"ds_read_b64_tr_b16 v[248:249], %[tr2] offset:0\n" \
"ds_read_b64_tr_b16 v[250:251], %[tr2] offset:32768\n" \
"ds_read_b64_tr_b16 v[252:253], v26 offset:0\n" \
"ds_read_b64_tr_b16 v[254:255], v26 offset:32768\n" \
"s_waitcnt lgkmcnt(0)\n" \
"s_barrier\n" \
"s_mov_b32 s64, 262144\n" \
"s_waitcnt vmcnt(0)\n" \
"Lv_hs_%=:\n" \
"v_readfirstlane_b32 s66, v91\n" \
"s_cmp_eq_u32 s66, 0\n" \
"s_cbranch_scc1 Lv_shs_%=\n" \
"v_mov_b32 v91, 2\n" \
"s_mov_b64 exec, 1\n" \
"buffer_store_dword v91, off, %[ws], %[hs] offset:2048 sc1\n" \
"s_mov_b64 exec, -1\n" \
"s_and_b32 s66, s66, 8192\n" \
"v_xor_b32 %[fo], s66, %[fo]\n" \
"s_mov_b32 s64, 262144\n" \
"s_mov_b32 s65, 0\n" \
"s_mov_b32 s67, %[src]\n" \
"s_mov_b32 s68, %[dst]\n" \
"s_lshr_b32 s69, %[wbase], 1\n" \
"v_mov_b32 v90, %[l16]\n" \
"s_barrier\n" \
"s_add_u32 m0, s69, 0\n" \
"s_add_u32 s66, s67, 0\n" \
"buffer_load_dwordx4 %[l16], %[ws], s66 offen offset:0 sc1 lds\n" \
"buffer_load_dwordx4 %[l16], %[ws], s66 offen offset:1024 sc1 lds\n" \
"buffer_load_dwordx4 %[l16], %[ws], s66 offen offset:2048 sc1 lds\n" \
"buffer_load_dwordx4 %[l16], %[ws], s66 offen offset:3072 sc1 lds\n" \
"s_add_u32 m0, s69, 4096\n" \
"s_add_u32 s66, s67, 4096\n" \
"buffer_load_dwordx4 %[l16], %[ws], s66 offen offset:0 sc1 lds\n" \
"buffer_load_dwordx4 %[l16], %[ws], s66 offen offset:1024 sc1 lds\n" \
"buffer_load_dwordx4 %[l16], %[ws], s66 offen offset:2048 sc1 lds\n" \
"buffer_load_dwordx4 %[l16], %[ws], s66 offen offset:3072 sc1 lds\n" \
"s_add_u32 m0, s69, 8192\n" \
"s_add_u32 s66, s67, 8192\n" \
"buffer_load_dwordx4 %[l16], %[ws], s66 offen offset:0 sc1 lds\n" \
"buffer_load_dwordx4 %[l16], %[ws], s66 offen offset:1024 sc1 lds\n" \
"buffer_load_dwordx4 %[l16], %[ws], s66 offen offset:2048 sc1 lds\n" \
"buffer_load_dwordx4 %[l16], %[ws], s66 offen offset:3072 sc1 lds\n" \
"s_add_u32 m0, s69, 12288\n" \
"s_add_u32 s66, s67, 12288\n" \
"buffer_load_dwordx4 %[l16], %[ws], s66 offen offset:0 sc1 lds\n" \
"buffer_load_dwordx4 %[l16], %[ws], s66 offen offset:1024 sc1 lds\n" \
"buffer_load_dwordx4 %[l16], %[ws], s66 offen offset:2048 sc1 lds\n" \
"buffer_load_dwordx4 %[l16], %[ws], s66 offen offset:3072 sc1 lds\n" \
"s_add_u32 s67, s67, 65536\n" \
"s_xor_b32 s69, s69, 65536\n" \
"s_waitcnt vmcnt(0)\n" \
"v_mov_b32 v96, -1\n" \
"v_mov_b32 v97, -1\n" \
"v_mov_b32 v98, -1\n" \
"v_mov_b32 v99, -1\n" \
"s_mov_b32 s70, %[om]\n" \
"Lv_top_%=:\n" \
"s_barrier\n" \
"ds_read_b128 v[20:23], v90 offset:0\n" \
"ds_read_b128 v[24:27], v90 offset:1024\n" \
"ds_read_b128 v[28:31], v90 offset:2048\n" \
"ds_read_b128 v[32:35], v90 offset:3072\n" \
"ds_read_b128 v[36:39], v90 offset:4096\n" \
"ds_read_b128 v[40:43], v90 offset:5120\n" \
"s_waitcnt lgkmcnt(4)\n" \
"v_mfma_f32_16x16x32_bf16 v[52:55], v[20:23], a[0:3], 0\n" \
"ds_read_b128 v[44:47], v90 offset:6144\n" \
"v_mfma_f32_16x16x32_bf16 v[56:59], v[20:23], a[4:7], 0\n" \
"v_mfma_f32_16x16x32_bf16 v[60:63], v[20:23], v[128:131], 0\n" \
"v_mfma_f32_16x16x32_bf16 v[64:67], v[24:27], a[0:3], 0\n" \
"ds_read_b128 v[48:51], v90 offset:7168\n" \
"v_mfma_f32_16x16x32_bf16 v[68:71], v[24:27], a[4:7], 0\n" \
"v_mfma_f32_16x16x32_bf16 v[72:75], v[24:27], v[128:131], 0\n" \
"s_waitcnt lgkmcnt(4)\n" \
"v_mfma_f32_16x16x32_bf16 v[52:55], v[28:31], a[8:11], v[52:55]\n" \
"ds_read_b128 v[20:23], v90 offset:8192\n" \
"v_mfma_f32_16x16x32_bf16 v[56:59], v[28:31], a[12:15], v[56:59]\n" \
"s_add_u32 m0, s69, 0\n" \
"s_add_u32 s66, s67, 0\n" \
"buffer_load_dwordx4 %[l16], %[ws], s66 offen offset:0 sc1 lds\n" \
"v_mfma_f32_16x16x32_bf16 v[60:63], v[28:31], v[132:135], v[60:63]\n" \
"v_mfma_f32_16x16x32_bf16 v[64:67], v[32:35], a[8:11], v[64:67]\n" \
"ds_read_b128 v[24:27], v90 offset:9216\n" \
"v_mfma_f32_16x16x32_bf16 v[68:71], v[32:35], a[12:15], v[68:71]\n" \
"v_mfma_f32_16x16x32_bf16 v[72:75], v[32:35], v[132:135], v[72:75]\n" \
"s_waitcnt lgkmcnt(4)\n" \
"v_mfma_f32_16x16x32_bf16 v[52:55], v[36:39], a[16:19], v[52:55]\n" \
"ds_read_b128 v[28:31], v90 offset:10240\n" \
"v_mfma_f32_16x16x32_bf16 v[56:59], v[36:39], a[20:23], v[56:59]\n" \
"buffer_load_dwordx4 %[l16], %[ws], s66 offen offset:1024 sc1 lds\n" \
"v_mfma_f32_16x16x32_bf16 v[60:63], v[36:39], v[136:139], v[60:63]\n" \
"v_mfma_f32_16x16x32_bf16 v[64:67], v[40:43], a[16:19], v[64:67]\n" \
"ds_read_b128 v[32:35], v90 offset:11264\n" \
"v_mfma_f32_16x16x32_bf16 v[68:71], v[40:43], a[20:23], v[68:71]\n" \
"v_mfma_f32_16x16x32_bf16 v[72:75], v[40:43], v[136:139], v[72:75]\n" \
"s_waitcnt lgkmcnt(4)\n" \
"v_mfma_f32_16x16x32_bf16 v[52:55], v[44:47], a[24:27], v[52:55]\n" \
"ds_read_b128 v[36:39], v90 offset:12288\n" \
"v_mfma_f32_16x16x32_bf16 v[56:59], v[44:47], a[28:31], v[56:59]\n" \
"buffer_load_dwordx4 %[l16], %[ws], s66 offen offset:2048 sc1 lds\n" \
"v_mfma_f32_16x16x32_bf16 v[60:63], v[44:47], v[140:143], v[60:63]\n" \
"v_mfma_f32_16x16x32_bf16 v[64:67], v[48:51], a[24:27], v[64:67]\n" \
"ds_read_b128 v[40:43], v90 offset:13312\n" \
"v_mfma_f32_16x16x32_bf16 v[68:71], v[48:51], a[28:31], v[68:71]\n" \
"v_mfma_f32_16x16x32_bf16 v[72:75], v[48:51], v[140:143], v[72:75]\n" \
"s_waitcnt lgkmcnt(4)\n" \
"v_mfma_f32_16x16x32_bf16 v[52:55], v[20:23], a[32:35], v[52:55]\n" \
"ds_read_b128 v[44:47], v90 offset:14336\n" \
"v_mfma_f32_16x16x32_bf16 v[56:59], v[20:23], a[36:39], v[56:59]\n" \
"buffer_load_dwordx4 %[l16], %[ws], s66 offen offset:3072 sc1 lds\n" \
"v_mfma_f32_16x16x32_bf16 v[60:63], v[20:23], v[144:147], v[60:63]\n" \
"v_mfma_f32_16x16x32_bf16 v[64:67], v[24:27], a[32:35], v[64:67]\n" \
"ds_read_b128 v[48:51], v90 offset:15360\n" \
"v_mfma_f32_16x16x32_bf16 v[68:71], v[24:27], a[36:39], v[68:71]\n" \
"v_mfma_f32_16x16x32_bf16 v[72:75], v[24:27], v[144:147], v[72:75]\n" \
"s_waitcnt lgkmcnt(4)\n" \
"v_mfma_f32_16x16x32_bf16 v[52:55], v[28:31], a[40:43], v[52:55]\n" \
"ds_read_b128 v[20:23], v90 offset:16384\n" \
"v_mfma_f32_16x16x32_bf16 v[56:59], v[28:31], a[44:47], v[56:59]\n" \
"s_add_u32 m0, s69, 4096\n" \
"s_add_u32 s66, s67, 4096\n" \
"buffer_load_dwordx4 %[l16], %[ws], s66 offen offset:0 sc1 lds\n" \
"v_mfma_f32_16x16x32_bf16 v[60:63], v[28:31], v[148:151], v[60:63]\n" \
"s_waitcnt vmcnt(5)\n" \
"v_mov_b32 v91, s65\n" \
"buffer_store_dword v91, %[fo], %[ws], 0 offen sc1\n" \
"v_mfma_f32_16x16x32_bf16 v[64:67], v[32:35], a[40:43], v[64:67]\n" \
"ds_read_b128 v[24:27], v90 offset:17408\n" \
"v_mfma_f32_16x16x32_bf16 v[68:71], v[32:35], a[44:47], v[68:71]\n" \
"v_mfma_f32_16x16x32_bf16 v[72:75], v[32:35], v[148:151], v[72:75]\n" \
"s_waitcnt lgkmcnt(4)\n" \
"v_mfma_f32_16x16x32_bf16 v[52:55], v[36:39], a[48:51], v[52:55]\n" \
"ds_read_b128 v[28:31], v90 offset:18432\n" \
"v_mfma_f32_16x16x32_bf16 v[56:59], v[36:39], a[52:55], v[56:59]\n" \
"buffer_load_dwordx4 %[l16], %[ws], s66 offen offset:1024 sc1 lds\n" \
"v_mfma_f32_16x16x32_bf16 v[60:63], v[36:39], v[152:155], v[60:63]\n" \
"v_mfma_f32_16x16x32_bf16 v[64:67], v[40:43], a[48:51], v[64:67]\n" \
"ds_read_b128 v[32:35], v90 offset:19456\n" \
"v_mfma_f32_16x16x32_bf16 v[68:71], v[40:43], a[52:55], v[68:71]\n" \
"v_mfma_f32_16x16x32_bf16 v[72:75], v[40:43], v[152:155], v[72:75]\n" \
"s_waitcnt lgkmcnt(4)\n" \
"v_mfma_f32_16x16x32_bf16 v[52:55], v[44:47], a[56:59], v[52:55]\n" \
"ds_read_b128 v[36:39], v90 offset:20480\n" \
"v_mfma_f32_16x16x32_bf16 v[56:59], v[44:47], a[60:63], v[56:59]\n" \
"buffer_load_dwordx4 %[l16], %[ws], s66 offen offset:2048 sc1 lds\n" \
"v_mfma_f32_16x16x32_bf16 v[60:63], v[44:47], v[156:159], v[60:63]\n" \
"v_mfma_f32_16x16x32_bf16 v[64:67], v[48:51], a[56:59], v[64:67]\n" \
"ds_read_b128 v[40:43], v90 offset:21504\n" \
"v_mfma_f32_16x16x32_bf16 v[68:71], v[48:51], a[60:63], v[68:71]\n" \
"v_mfma_f32_16x16x32_bf16 v[72:75], v[48:51], v[156:159], v[72:75]\n" \
"s_waitcnt lgkmcnt(4)\n" \
"v_mfma_f32_16x16x32_bf16 v[52:55], v[20:23], a[64:67], v[52:55]\n" \
"ds_read_b128 v[44:47], v90 offset:22528\n" \
"v_mfma_f32_16x16x32_bf16 v[56:59], v[20:23], a[68:71], v[56:59]\n" \
"buffer_load_dwordx4 %[l16], %[ws], s66 offen offset:3072 sc1 lds\n" \
"v_mfma_f32_16x16x32_bf16 v[60:63], v[20:23], v[160:163], v[60:63]\n" \
"v_mfma_f32_16x16x32_bf16 v[64:67], v[24:27], a[64:67], v[64:67]\n" \
"ds_read_b128 v[48:51], v90 offset:23552\n" \
"v_mfma_f32_16x16x32_bf16 v[68:71], v[24:27], a[68:71], v[68:71]\n" \
"v_mfma_f32_16x16x32_bf16 v[72:75], v[24:27], v[160:163], v[72:75]\n" \
"s_waitcnt lgkmcnt(4)\n" \
"v_mfma_f32_16x16x32_bf16 v[52:55], v[28:31], a[72:75], v[52:55]\n" \
"ds_read_b128 v[20:23], v90 offset:24576\n" \
"v_mfma_f32_16x16x32_bf16 v[56:59], v[28:31], a[76:79], v[56:59]\n" \
"s_add_u32 m0, s69, 8192\n" \
"s_add_u32 s66, s67, 8192\n" \
"buffer_load_dwordx4 %[l16], %[ws], s66 offen offset:0 sc1 lds\n" \
"v_mfma_f32_16x16x32_bf16 v[60:63], v[28:31], v[164:167], v[60:63]\n" \
"v_mfma_f32_16x16x32_bf16 v[64:67], v[32:35], a[72:75], v[64:67]\n" \
"ds_read_b128 v[24:27], v90 offset:25600\n" \
"v_mfma_f32_16x16x32_bf16 v[68:71], v[32:35], a[76:79], v[68:71]\n" \
"v_mfma_f32_16x16x32_bf16 v[72:75], v[32:35], v[164:167], v[72:75]\n" \
"s_waitcnt lgkmcnt(4)\n" \
"v_mfma_f32_16x16x32_bf16 v[52:55], v[36:39], a[80:83], v[52:55]\n" \
"ds_read_b128 v[28:31], v90 offset:26624\n" \
"v_mfma_f32_16x16x32_bf16 v[56:59], v[36:39], a[84:87], v[56:59]\n" \
"buffer_load_dwordx4 %[l16], %[ws], s66 offen offset:1024 sc1 lds\n" \
"v_mfma_f32_16x16x32_bf16 v[60:63], v[36:39], v[168:171], v[60:63]\n" \
"v_mfma_f32_16x16x32_bf16 v[64:67], v[40:43], a[80:83], v[64:67]\n" \
"ds_read_b128 v[32:35], v90 offset:27648\n" \
"v_mfma_f32_16x16x32_bf16 v[68:71], v[40:43], a[84:87], v[68:71]\n" \
"v_mfma_f32_16x16x32_bf16 v[72:75], v[40:43], v[168:171], v[72:75]\n" \
"s_waitcnt lgkmcnt(4)\n" \
"v_mfma_f32_16x16x32_bf16 v[52:55], v[44:47], a[88:91], v[52:55]\n" \
"ds_read_b128 v[36:39], v90 offset:28672\n" \
"v_mfma_f32_16x16x32_bf16 v[56:59], v[44:47], a[92:95], v[56:59]\n" \
"buffer_load_dwordx4 %[l16], %[ws], s66 offen offset:2048 sc1 lds\n" \
"v_mfma_f32_16x16x32_bf16 v[60:63], v[44:47], v[172:175], v[60:63]\n" \
"v_mfma_f32_16x16x32_bf16 v[64:67], v[48:51], a[88:91], v[64:67]\n" \
"ds_read_b128 v[40:43], v90 offset:29696\n" \
"v_mfma_f32_16x16x32_bf16 v[68:71], v[48:51], a[92:95], v[68:71]\n" \
"v_mfma_f32_16x16x32_bf16 v[72:75], v[48:51], v[172:175], v[72:75]\n" \
"s_waitcnt lgkmcnt(4)\n" \
"v_mfma_f32_16x16x32_bf16 v[52:55], v[20:23], a[96:99], v[52:55]\n" \
"ds_read_b128 v[44:47], v90 offset:30720\n" \
"v_mfma_f32_16x16x32_bf16 v[56:59], v[20:23], a[100:103], v[56:59]\n" \
"buffer_load_dwordx4 %[l16], %[ws], s66 offen offset:3072 sc1 lds\n" \
"v_mfma_f32_16x16x32_bf16 v[60:63], v[20:23], v[176:179], v[60:63]\n" \
"v_mfma_f32_16x16x32_bf16 v[64:67], v[24:27], a[96:99], v[64:67]\n" \
"ds_read_b128 v[48:51], v90 offset:31744\n" \
"v_mfma_f32_16x16x32_bf16 v[68:71], v[24:27], a[100:103], v[68:71]\n" \
"v_mfma_f32_16x16x32_bf16 v[72:75], v[24:27], v[176:179], v[72:75]\n" \
"s_waitcnt lgkmcnt(4)\n" \
"v_mfma_f32_16x16x32_bf16 v[52:55], v[28:31], a[104:107], v[52:55]\n" \
"ds_read_b128 v[20:23], v90 offset:32768\n" \
"v_mfma_f32_16x16x32_bf16 v[56:59], v[28:31], a[108:111], v[56:59]\n" \
"s_add_u32 m0, s69, 12288\n" \
"s_add_u32 s66, s67, 12288\n" \
"buffer_load_dwordx4 %[l16], %[ws], s66 offen offset:0 sc1 lds\n" \
"v_mfma_f32_16x16x32_bf16 v[60:63], v[28:31], v[180:183], v[60:63]\n" \
"v_mfma_f32_16x16x32_bf16 v[64:67], v[32:35], a[104:107], v[64:67]\n" \
"ds_read_b128 v[24:27], v90 offset:33792\n" \
"v_mfma_f32_16x16x32_bf16 v[68:71], v[32:35], a[108:111], v[68:71]\n" \
"v_mfma_f32_16x16x32_bf16 v[72:75], v[32:35], v[180:183], v[72:75]\n" \
"s_waitcnt lgkmcnt(4)\n" \
"v_mfma_f32_16x16x32_bf16 v[52:55], v[36:39], a[112:115], v[52:55]\n" \
"ds_read_b128 v[28:31], v90 offset:34816\n" \
"v_mfma_f32_16x16x32_bf16 v[56:59], v[36:39], a[116:119], v[56:59]\n" \
"buffer_load_dwordx4 %[l16], %[ws], s66 offen offset:1024 sc1 lds\n" \
"v_mfma_f32_16x16x32_bf16 v[60:63], v[36:39], v[184:187], v[60:63]\n" \
"v_mfma_f32_16x16x32_bf16 v[64:67], v[40:43], a[112:115], v[64:67]\n" \
"ds_read_b128 v[32:35], v90 offset:35840\n" \
"v_mfma_f32_16x16x32_bf16 v[68:71], v[40:43], a[116:119], v[68:71]\n" \
"v_mfma_f32_16x16x32_bf16 v[72:75], v[40:43], v[184:187], v[72:75]\n" \
"s_waitcnt lgkmcnt(4)\n" \
"v_mfma_f32_16x16x32_bf16 v[52:55], v[44:47], a[120:123], v[52:55]\n" \
"ds_read_b128 v[36:39], v90 offset:36864\n" \
"v_mfma_f32_16x16x32_bf16 v[56:59], v[44:47], a[124:127], v[56:59]\n" \
"buffer_load_dwordx4 %[l16], %[ws], s66 offen offset:2048 sc1 lds\n" \
"v_mfma_f32_16x16x32_bf16 v[60:63], v[44:47], v[188:191], v[60:63]\n" \
"v_mfma_f32_16x16x32_bf16 v[64:67], v[48:51], a[120:123], v[64:67]\n" \
"ds_read_b128 v[40:43], v90 offset:37888\n" \
"v_mfma_f32_16x16x32_bf16 v[68:71], v[48:51], a[124:127], v[68:71]\n" \
"v_mfma_f32_16x16x32_bf16 v[72:75], v[48:51], v[188:191], v[72:75]\n" \
"s_waitcnt lgkmcnt(4)\n" \
"v_mfma_f32_16x16x32_bf16 v[52:55], v[20:23], a[128:131], v[52:55]\n" \
"ds_read_b128 v[44:47], v90 offset:38912\n" \
"v_mfma_f32_16x16x32_bf16 v[56:59], v[20:23], a[132:135], v[56:59]\n" \
"buffer_load_dwordx4 %[l16], %[ws], s66 offen offset:3072 sc1 lds\n" \
"v_mfma_f32_16x16x32_bf16 v[60:63], v[20:23], v[192:195], v[60:63]\n" \
"v_mfma_f32_16x16x32_bf16 v[64:67], v[24:27], a[128:131], v[64:67]\n" \
"ds_read_b128 v[48:51], v90 offset:39936\n" \
"v_mfma_f32_16x16x32_bf16 v[68:71], v[24:27], a[132:135], v[68:71]\n" \
"v_mfma_f32_16x16x32_bf16 v[72:75], v[24:27], v[192:195], v[72:75]\n" \
"s_waitcnt lgkmcnt(4)\n" \
"v_mfma_f32_16x16x32_bf16 v[52:55], v[28:31], a[136:139], v[52:55]\n" \
"ds_read_b128 v[20:23], v90 offset:40960\n" \
"v_mfma_f32_16x16x32_bf16 v[56:59], v[28:31], a[140:143], v[56:59]\n" \
"v_mfma_f32_16x16x32_bf16 v[60:63], v[28:31], v[196:199], v[60:63]\n" \
"v_mfma_f32_16x16x32_bf16 v[64:67], v[32:35], a[136:139], v[64:67]\n" \
"ds_read_b128 v[24:27], v90 offset:41984\n" \
"v_mfma_f32_16x16x32_bf16 v[68:71], v[32:35], a[140:143], v[68:71]\n" \
"v_mfma_f32_16x16x32_bf16 v[72:75], v[32:35], v[196:199], v[72:75]\n" \
"s_waitcnt lgkmcnt(4)\n" \
"v_mfma_f32_16x16x32_bf16 v[52:55], v[36:39], a[144:147], v[52:55]\n" \
"ds_read_b128 v[28:31], v90 offset:43008\n" \
"v_mfma_f32_16x16x32_bf16 v[56:59], v[36:39], a[148:151], v[56:59]\n" \
"buffer_store_dwordx4 v[96:99], %[po], %[ws], s70 offen sc1\n" \
"s_add_u32 s70, s70, 65536\n" \
"v_mfma_f32_16x16x32_bf16 v[60:63], v[36:39], v[200:203], v[60:63]\n" \
"v_mfma_f32_16x16x32_bf16 v[64:67], v[40:43], a[144:147], v[64:67]\n" \
"ds_read_b128 v[32:35], v90 offset:44032\n" \
"v_mfma_f32_16x16x32_bf16 v[68:71], v[40:43], a[148:151], v[68:71]\n" \
"v_mfma_f32_16x16x32_bf16 v[72:75], v[40:43], v[200:203], v[72:75]\n" \
"s_waitcnt lgkmcnt(4)\n" \
"v_mfma_f32_16x16x32_bf16 v[52:55], v[44:47], a[152:155], v[52:55]\n" \
"ds_read_b128 v[36:39], v90 offset:45056\n" \
"v_mfma_f32_16x16x32_bf16 v[56:59], v[44:47], a[156:159], v[56:59]\n" \
"v_mfma_f32_16x16x32_bf16 v[60:63], v[44:47], v[204:207], v[60:63]\n" \
"v_mfma_f32_16x16x32_bf16 v[64:67], v[48:51], a[152:155], v[64:67]\n" \
"ds_read_b128 v[40:43], v90 offset:46080\n" \
"v_mfma_f32_16x16x32_bf16 v[68:71], v[48:51], a[156:159], v[68:71]\n" \
"v_mfma_f32_16x16x32_bf16 v[72:75], v[48:51], v[204:207], v[72:75]\n" \
"s_waitcnt lgkmcnt(4)\n" \
"v_mfma_f32_16x16x32_bf16 v[52:55], v[20:23], a[160:163], v[52:55]\n" \
"ds_read_b128 v[44:47], v90 offset:47104\n" \
"v_mfma_f32_16x16x32_bf16 v[56:59], v[20:23], a[164:167], v[56:59]\n" \
"s_add_u32 s67, s67, 65536\n" \
"s_xor_b32 s69, s69, 65536\n" \
"v_mfma_f32_16x16x32_bf16 v[60:63], v[20:23], v[208:211], v[60:63]\n" \
"v_mfma_f32_16x16x32_bf16 v[64:67], v[24:27], a[160:163], v[64:67]\n" \
"ds_read_b128 v[48:51], v90 offset:48128\n" \
"v_mfma_f32_16x16x32_bf16 v[68:71], v[24:27], a[164:167], v[68:71]\n" \
"v_mfma_f32_16x16x32_bf16 v[72:75], v[24:27], v[208:211], v[72:75]\n" \
"s_waitcnt lgkmcnt(4)\n" \
"v_mfma_f32_16x16x32_bf16 v[52:55], v[28:31], a[168:171], v[52:55]\n" \
"ds_read_b128 v[20:23], v90 offset:49152\n" \
"v_mfma_f32_16x16x32_bf16 v[56:59], v[28:31], a[172:175], v[56:59]\n" \
"v_mfma_f32_16x16x32_bf16 v[60:63], v[28:31], v[212:215], v[60:63]\n" \
"v_mfma_f32_16x16x32_bf16 v[64:67], v[32:35], a[168:171], v[64:67]\n" \
"ds_read_b128 v[24:27], v90 offset:50176\n" \
"v_mfma_f32_16x16x32_bf16 v[68:71], v[32:35], a[172:175], v[68:71]\n" \
"v_mfma_f32_16x16x32_bf16 v[72:75], v[32:35], v[212:215], v[72:75]\n" \
"s_waitcnt lgkmcnt(4)\n" \
"v_mfma_f32_16x16x32_bf16 v[52:55], v[36:39], a[176:179], v[52:55]\n" \
"ds_read_b128 v[28:31], v90 offset:51200\n" \
"v_mfma_f32_16x16x32_bf16 v[56:59], v[36:39], a[180:183], v[56:59]\n" \
"v_mfma_f32_16x16x32_bf16 v[60:63], v[36:39], v[216:219], v[60:63]\n" \
"v_mfma_f32_16x16x32_bf16 v[64:67], v[40:43], a[176:179], v[64:67]\n" \
"ds_read_b128 v[32:35], v90 offset:52224\n" \
"v_mfma_f32_16x16x32_bf16 v[68:71], v[40:43], a[180:183], v[68:71]\n" \
"v_mfma_f32_16x16x32_bf16 v[72:75], v[40:43], v[216:219], v[72:75]\n" \
"s_waitcnt lgkmcnt(4)\n" \
"v_mfma_f32_16x16x32_bf16 v[52:55], v[44:47], a[184:187], v[52:55]\n" \
"ds_read_b128 v[36:39], v90 offset:53248\n" \
"v_mfma_f32_16x16x32_bf16 v[56:59], v[44:47], a[188:191], v[56:59]\n" \
"v_mfma_f32_16x16x32_bf16 v[60:63], v[44:47], v[220:223], v[60:63]\n" \
"v_mfma_f32_16x16x32_bf16 v[64:67], v[48:51], a[184:187], v[64:67]\n" \
"ds_read_b128 v[40:43], v90 offset:54272\n" \
"v_mfma_f32_16x16x32_bf16 v[68:71], v[48:51], a[188:191], v[68:71]\n" \
"v_mfma_f32_16x16x32_bf16 v[72:75], v[48:51], v[220:223], v[72:75]\n" \
"s_waitcnt lgkmcnt(4)\n" \
"v_mfma_f32_16x16x32_bf16 v[52:55], v[20:23], a[192:195], v[52:55]\n" \
"ds_read_b128 v[44:47], v90 offset:55296\n" \
"v_mfma_f32_16x16x32_bf16 v[56:59], v[20:23], a[196:199], v[56:59]\n" \
"v_mfma_f32_16x16x32_bf16 v[60:63], v[20:23], v[224:227], v[60:63]\n" \
"v_mfma_f32_16x16x32_bf16 v[64:67], v[24:27], a[192:195], v[64:67]\n" \
"ds_read_b128 v[48:51], v90 offset:56320\n" \
"v_mfma_f32_16x16x32_bf16 v[68:71], v[24:27], a[196:199], v[68:71]\n" \
"v_mfma_f32_16x16x32_bf16 v[72:75], v[24:27], v[224:227], v[72:75]\n" \
"s_waitcnt lgkmcnt(4)\n" \
"v_mfma_f32_16x16x32_bf16 v[52:55], v[28:31], a[200:203], v[52:55]\n" \
"ds_read_b128 v[20:23], v90 offset:57344\n" \
"v_mfma_f32_16x16x32_bf16 v[56:59], v[28:31], a[204:207], v[56:59]\n" \
"v_mfma_f32_16x16x32_bf16 v[60:63], v[28:31], v[228:231], v[60:63]\n" \
"v_mfma_f32_16x16x32_bf16 v[64:67], v[32:35], a[200:203], v[64:67]\n" \
"ds_read_b128 v[24:27], v90 offset:58368\n" \
"v_mfma_f32_16x16x32_bf16 v[68:71], v[32:35], a[204:207], v[68:71]\n" \
"v_mfma_f32_16x16x32_bf16 v[72:75], v[32:35], v[228:231], v[72:75]\n" \
"s_waitcnt lgkmcnt(4)\n" \
"v_mfma_f32_16x16x32_bf16 v[52:55], v[36:39], a[208:211], v[52:55]\n" \
"ds_read_b128 v[28:31], v90 offset:59392\n" \
"v_mfma_f32_16x16x32_bf16 v[56:59], v[36:39], a[212:215], v[56:59]\n" \
"v_mfma_f32_16x16x32_bf16 v[60:63], v[36:39], v[232:235], v[60:63]\n" \
"v_mfma_f32_16x16x32_bf16 v[64:67], v[40:43], a[208:211], v[64:67]\n" \
"ds_read_b128 v[32:35], v90 offset:60416\n" \
"v_mfma_f32_16x16x32_bf16 v[68:71], v[40:43], a[212:215], v[68:71]\n" \
"v_mfma_f32_16x16x32_bf16 v[72:75], v[40:43], v[232:235], v[72:75]\n" \
"s_waitcnt lgkmcnt(4)\n" \
"v_mfma_f32_16x16x32_bf16 v[52:55], v[44:47], a[216:219], v[52:55]\n" \
"ds_read_b128 v[36:39], v90 offset:61440\n" \
"v_mfma_f32_16x16x32_bf16 v[56:59], v[44:47], a[220:223], v[56:59]\n" \
"v_mfma_f32_16x16x32_bf16 v[60:63], v[44:47], v[236:239], v[60:63]\n" \
"v_mfma_f32_16x16x32_bf16 v[64:67], v[48:51], a[216:219], v[64:67]\n" \
"ds_read_b128 v[40:43], v90 offset:62464\n" \
"v_mfma_f32_16x16x32_bf16 v[68:71], v[48:51], a[220:223], v[68:71]\n" \
"v_mfma_f32_16x16x32_bf16 v[72:75], v[48:51], v[236:239], v[72:75]\n" \
"s_waitcnt lgkmcnt(4)\n" \
"v_mfma_f32_16x16x32_bf16 v[52:55], v[20:23], a[224:227], v[52:55]\n" \
"ds_read_b128 v[44:47], v90 offset:63488\n" \
"v_mfma_f32_16x16x32_bf16 v[56:59], v[20:23], a[228:231], v[56:59]\n" \
"v_mfma_f32_16x16x32_bf16 v[60:63], v[20:23], v[240:243], v[60:63]\n" \
"v_mfma_f32_16x16x32_bf16 v[64:67], v[24:27], a[224:227], v[64:67]\n" \
"ds_read_b128 v[48:51], v90 offset:64512\n" \
"v_mfma_f32_16x16x32_bf16 v[68:71], v[24:27], a[228:231], v[68:71]\n" \
"v_mfma_f32_16x16x32_bf16 v[72:75], v[24:27], v[240:243], v[72:75]\n" \
"s_waitcnt lgkmcnt(4)\n" \
"v_mfma_f32_16x16x32_bf16 v[52:55], v[28:31], a[232:235], v[52:55]\n" \
"v_mfma_f32_16x16x32_bf16 v[56:59], v[28:31], a[236:239], v[56:59]\n" \
"v_mfma_f32_16x16x32_bf16 v[60:63], v[28:31], v[244:247], v[60:63]\n" \
"v_mfma_f32_16x16x32_bf16 v[64:67], v[32:35], a[232:235], v[64:67]\n" \
"v_mfma_f32_16x16x32_bf16 v[68:71], v[32:35], a[236:239], v[68:71]\n" \
"v_mfma_f32_16x16x32_bf16 v[72:75], v[32:35], v[244:247], v[72:75]\n" \
"s_waitcnt lgkmcnt(2)\n" \
"v_mfma_f32_16x16x32_bf16 v[52:55], v[36:39], a[240:243], v[52:55]\n" \
"v_mfma_f32_16x16x32_bf16 v[56:59], v[36:39], a[244:247], v[56:59]\n" \
"v_xor_b32 v90, 0x10000, v90\n" \
"s_add_u32 s65, s65, 1\n" \
"s_waitcnt vmcnt(0)\n" \
"v_mfma_f32_16x16x32_bf16 v[60:63], v[36:39], v[248:251], v[60:63]\n" \
"v_mfma_f32_16x16x32_bf16 v[64:67], v[40:43], a[240:243], v[64:67]\n" \
"v_mfma_f32_16x16x32_bf16 v[68:71], v[40:43], a[244:247], v[68:71]\n" \
"v_mfma_f32_16x16x32_bf16 v[72:75], v[40:43], v[248:251], v[72:75]\n" \
"s_waitcnt lgkmcnt(0)\n" \
"v_mfma_f32_16x16x32_bf16 v[52:55], v[44:47], a[248:251], v[52:55]\n" \
"v_mfma_f32_16x16x32_bf16 v[56:59], v[44:47], a[252:255], v[56:59]\n" \
"v_mfma_f32_16x16x32_bf16 v[60:63], v[44:47], v[252:255], v[60:63]\n" \
"v_mfma_f32_16x16x32_bf16 v[64:67], v[48:51], a[248:251], v[64:67]\n" \
"v_mfma_f32_16x16x32_bf16 v[68:71], v[48:51], a[252:255], v[68:71]\n" \
"v_mfma_f32_16x16x32_bf16 v[72:75], v[48:51], v[252:255], v[72:75]\n" \
"s_nop 2\n" \
"v_cvt_pk_bf16_f32 v76, v52, v53\n" \
"v_cvt_pk_bf16_f32 v77, v54, v55\n" \
"v_cvt_pk_bf16_f32 v80, v56, v57\n" \
"v_cvt_pk_bf16_f32 v81, v58, v59\n" \
"v_cvt_pk_bf16_f32 v84, v60, v61\n" \
"v_cvt_pk_bf16_f32 v85, v62, v63\n" \
"v_cvt_pk_bf16_f32 v78, v64, v65\n" \
"v_cvt_pk_bf16_f32 v79, v66, v67\n" \
"v_cvt_pk_bf16_f32 v82, v68, v69\n" \
"v_cvt_pk_bf16_f32 v83, v70, v71\n" \
"v_cvt_pk_bf16_f32 v86, v72, v73\n" \
"v_cvt_pk_bf16_f32 v87, v74, v75\n" \
"buffer_store_dwordx4 v[76:79], %[so1], %[ws], s68 offen sc1\n" \
"buffer_store_dwordx4 v[80:83], %[so2], %[ws], s68 offen sc1\n" \
"buffer_store_dwordx4 v[84:87], %[so3], %[ws], s68 offen sc1\n" \
"s_add_u32 s68, s68, 262144\n" \
"s_cmp_lg_u32 s65, 64\n" \
"s_cbranch_scc1 Lv_top_%=\n" \
"s_waitcnt vmcnt(0)\n" \
"v_mov_b32 v91, s65\n" \
"buffer_store_dword v91, %[fo], %[ws], 0 offen sc1\n" \
"s_endpgm\n" \
"Lv_shs_%=:\n" \
"s_sub_u32 s64, s64, 1\n" \
"s_cbranch_scc1 Lv_dead_%=\n" \
"s_sleep 8\n" \
"buffer_load_dword v91, off, %[ws], %[hs] sc1\n" \
"s_waitcnt vmcnt(0)\n" \
"s_branch Lv_hs_%=\n" \
"Lv_dead_%=:\n" \
"v_mov_b32 v91, 28929\n" \
"buffer_store_dword v91, off, %[ws], 0 sc1\n" \
"s_endpgm\n" \
    : [fo] "+v"(fo) : [l16] "v"(l16), [wvo] "v"(wvo), [tr0] "v"(tr0), [tr1] "v"(tr1), [tr2] "v"(tr2), [so1] "v"(so1), [so2] "v"(so2), [so3] "v"(so3), [po] "v"(po), [hv] "v"(hv), [ws] "s"(ws), [rw] "s"(rw), [so0] "s"(so0), [wbase] "s"(wbase), [src] "s"(src), [dst] "s"(dst), [hs] "s"(hs), [om] "s"(om) \
    : CLOB20)

#define QA_ASM() asm volatile( \
"s_nop 4\n" \
"s_mov_b32 s66, %[d0]\n" \
"Lqa_dly_%=:\n" \
"s_sleep 8\n" \
"s_sub_u32 s66, s66, 1\n" \
"s_cbranch_scc0 Lqa_dly_%=\n" \
"v_lshrrev_b32 v24, 9, %[tr0]\n" \
"v_xor_b32 v24, v24, %[tr0]\n" \
"v_and_b32 v24, 0xc0, v24\n" \
"v_and_b32 v25, 0x3c0, %[tr0]\n" \
"v_lshl_or_b32 v24, v25, 2, v24\n" \
"v_and_or_b32 v24, %[tr0], 48, v24\n" \
"v_add_u32 v25, 0x10000, v24\n" \
"s_add_u32 m0, %[wbase], 0\n" \
"s_add_u32 s66, %[so0], 0\n" \
"buffer_load_dwordx4 %[wvo], %[rw], s66 offen lds\n" \
"s_add_u32 m0, %[wbase], 1024\n" \
"s_add_u32 s66, %[so0], 65536\n" \
"buffer_load_dwordx4 %[wvo], %[rw], s66 offen lds\n" \
"s_waitcnt vmcnt(1)\n" \
"s_add_u32 m0, %[wbase], 2048\n" \
"s_add_u32 s66, %[so0], 131072\n" \
"buffer_load_dwordx4 %[wvo], %[rw], s66 offen lds\n" \
"s_waitcnt vmcnt(1)\n" \
"s_add_u32 m0, %[wbase], 3072\n" \
"s_add_u32 s66, %[so0], 196608\n" \
"buffer_load_dwordx4 %[wvo], %[rw], s66 offen lds\n" \
"s_waitcnt vmcnt(1)\n" \
"s_add_u32 m0, %[wbase], 4096\n" \
"s_add_u32 s66, %[so0], 524288\n" \
"buffer_load_dwordx4 %[wvo], %[rw], s66 offen lds\n" \
"s_waitcnt vmcnt(1)\n" \
"s_add_u32 m0, %[wbase], 5120\n" \
"s_add_u32 s66, %[so0], 589824\n" \
"buffer_load_dwordx4 %[wvo], %[rw], s66 offen lds\n" \
"s_barrier\n" \
"ds_read_b64_tr_b16 a[0:1], v24 offset:0\n" \
"ds_read_b64_tr_b16 a[2:3], v24 offset:32768\n" \
"ds_read_b64_tr_b16 a[8:9], v25 offset:0\n" \
"ds_read_b64_tr_b16 a[10:11], v25 offset:32768\n" \
"ds_read_b64_tr_b16 a[4:5], v24 offset:8\n" \
"ds_read_b64_tr_b16 a[6:7], v24 offset:32776\n" \
"ds_read_b64_tr_b16 a[12:13], v25 offset:8\n" \
"ds_read_b64_tr_b16 a[14:15], v25 offset:32776\n" \
"s_waitcnt lgkmcnt(0)\n" \
"s_waitcnt vmcnt(1)\n" \
"s_add_u32 m0, %[wbase], 6144\n" \
"s_add_u32 s66, %[so0], 655360\n" \
"buffer_load_dwordx4 %[wvo], %[rw], s66 offen lds\n" \
"s_waitcnt vmcnt(1)\n" \
"s_add_u32 m0, %[wbase], 7168\n" \
"s_add_u32 s66, %[so0], 720896\n" \
"buffer_load_dwordx4 %[wvo], %[rw], s66 offen lds\n" \
"s_waitcnt vmcnt(1)\n" \
"s_add_u32 m0, %[wbase], 8192\n" \
"s_add_u32 s66, %[so0], 1048576\n" \
"buffer_load_dwordx4 %[wvo], %[rw], s66 offen lds\n" \
"s_waitcnt vmcnt(1)\n" \
"s_add_u32 m0, %[wbase], 9216\n" \
"s_add_u32 s66, %[so0], 1114112\n" \
"buffer_load_dwordx4 %[wvo], %[rw], s66 offen lds\n" \
"s_barrier\n" \
"ds_read_b64_tr_b16 a[16:17], v24 offset:4096\n" \
"ds_read_b64_tr_b16 a[18:19], v24 offset:36864\n" \
"ds_read_b64_tr_b16 a[24:25], v25 offset:4096\n" \
"ds_read_b64_tr_b16 a[26:27], v25 offset:36864\n" \
"ds_read_b64_tr_b16 a[20:21], v24 offset:4104\n" \
"ds_read_b64_tr_b16 a[22:23], v24 offset:36872\n" \
"ds_read_b64_tr_b16 a[28:29], v25 offset:4104\n" \
"ds_read_b64_tr_b16 a[30:31], v25 offset:36872\n" \
"s_waitcnt lgkmcnt(0)\n" \
"s_waitcnt vmcnt(1)\n" \
"s_add_u32 m0, %[wbase], 10240\n" \
"s_add_u32 s66, %[so0], 1179648\n" \
"buffer_load_dwordx4 %[wvo], %[rw], s66 offen lds\n" \
"s_waitcnt vmcnt(1)\n" \
"s_add_u32 m0, %[wbase], 11264\n" \
"s_add_u32 s66, %[so0], 1245184\n" \
"buffer_load_dwordx4 %[wvo], %[rw], s66 offen lds\n" \
"s_waitcnt vmcnt(1)\n" \
"s_add_u32 m0, %[wbase], 12288\n" \
"s_add_u32 s66, %[so0], 1572864\n" \
"buffer_load_dwordx4 %[wvo], %[rw], s66 offen lds\n" \
"s_waitcnt vmcnt(1)\n" \
"s_add_u32 m0, %[wbase], 13312\n" \
"s_add_u32 s66, %[so0], 1638400\n" \
"buffer_load_dwordx4 %[wvo], %[rw], s66 offen lds\n" \
"s_barrier\n" \
"ds_read_b64_tr_b16 a[32:33], v24 offset:8192\n" \
"ds_read_b64_tr_b16 a[34:35], v24 offset:40960\n" \
"ds_read_b64_tr_b16 a[40:41], v25 offset:8192\n" \
"ds_read_b64_tr_b16 a[42:43], v25 offset:40960\n" \
"ds_read_b64_tr_b16 a[36:37], v24 offset:8200\n" \
"ds_read_b64_tr_b16 a[38:39], v24 offset:40968\n" \
"ds_read_b64_tr_b16 a[44:45], v25 offset:8200\n" \
"ds_read_b64_tr_b16 a[46:47], v25 offset:40968\n" \
"s_waitcnt lgkmcnt(0)\n" \
"s_waitcnt vmcnt(1)\n" \
"s_add_u32 m0, %[wbase], 14336\n" \
"s_add_u32 s66, %[so0], 1703936\n" \
"buffer_load_dwordx4 %[wvo], %[rw], s66 offen lds\n" \
"s_waitcnt vmcnt(1)\n" \
"s_add_u32 m0, %[wbase], 15360\n" \
"s_add_u32 s66, %[so0], 1769472\n" \
"buffer_load_dwordx4 %[wvo], %[rw], s66 offen lds\n" \
"s_waitcnt vmcnt(1)\n" \
"s_add_u32 m0, %[wbase], 16384\n" \
"s_add_u32 s66, %[so0], 2097152\n" \
"buffer_load_dwordx4 %[wvo], %[rw], s66 offen lds\n" \
"s_waitcnt vmcnt(1)\n" \
"s_add_u32 m0, %[wbase], 17408\n" \
"s_add_u32 s66, %[so0], 2162688\n" \
"buffer_load_dwordx4 %[wvo], %[rw], s66 offen lds\n" \
"s_barrier\n" \
"ds_read_b64_tr_b16 a[48:49], v24 offset:12288\n" \
"ds_read_b64_tr_b16 a[50:51], v24 offset:45056\n" \
"ds_read_b64_tr_b16 a[56:57], v25 offset:12288\n" \
"ds_read_b64_tr_b16 a[58:59], v25 offset:45056\n" \
"ds_read_b64_tr_b16 a[52:53], v24 offset:12296\n" \
"ds_read_b64_tr_b16 a[54:55], v24 offset:45064\n" \
"ds_read_b64_tr_b16 a[60:61], v25 offset:12296\n" \
"ds_read_b64_tr_b16 a[62:63], v25 offset:45064\n" \
"s_waitcnt lgkmcnt(0)\n" \
"s_waitcnt vmcnt(1)\n" \
"s_add_u32 m0, %[wbase], 18432\n" \
"s_add_u32 s66, %[so0], 2228224\n" \
"buffer_load_dwordx4 %[wvo], %[rw], s66 offen lds\n" \
"s_waitcnt vmcnt(1)\n" \
"s_add_u32 m0, %[wbase], 19456\n" \
"s_add_u32 s66, %[so0], 2293760\n" \
"buffer_load_dwordx4 %[wvo], %[rw], s66 offen lds\n" \
"s_waitcnt vmcnt(1)\n" \
"s_add_u32 m0, %[wbase], 20480\n" \
"s_add_u32 s66, %[so0], 2621440\n" \
"buffer_load_dwordx4 %[wvo], %[rw], s66 offen lds\n" \
"s_waitcnt vmcnt(1)\n" \
"s_add_u32 m0, %[wbase], 21504\n" \
"s_add_u32 s66, %[so0], 2686976\n" \
"buffer_load_dwordx4 %[wvo], %[rw], s66 offen lds\n" \
"s_barrier\n" \
"ds_read_b64_tr_b16 a[64:65], v24 offset:16384\n" \
"ds_read_b64_tr_b16 a[66:67], v24 offset:49152\n" \
"ds_read_b64_tr_b16 a[72:73], v25 offset:16384\n" \
"ds_read_b64_tr_b16 a[74:75], v25 offset:49152\n" \
"ds_read_b64_tr_b16 a[68:69], v24 offset:16392\n" \
"ds_read_b64_tr_b16 a[70:71], v24 offset:49160\n" \
"ds_read_b64_tr_b16 a[76:77], v25 offset:16392\n" \
"ds_read_b64_tr_b16 a[78:79], v25 offset:49160\n" \
"s_waitcnt lgkmcnt(0)\n" \
"s_waitcnt vmcnt(1)\n" \
"s_add_u32 m0, %[wbase], 22528\n" \
"s_add_u32 s66, %[so0], 2752512\n" \
"buffer_load_dwordx4 %[wvo], %[rw], s66 offen lds\n" \
"s_waitcnt vmcnt(1)\n" \
"s_add_u32 m0, %[wbase], 23552\n" \
"s_add_u32 s66, %[so0], 2818048\n" \
"buffer_load_dwordx4 %[wvo], %[rw], s66 offen lds\n" \
"s_waitcnt vmcnt(1)\n" \
"s_add_u32 m0, %[wbase], 24576\n" \
"s_add_u32 s66, %[so0], 3145728\n" \
"buffer_load_dwordx4 %[wvo], %[rw], s66 offen lds\n" \
"s_waitcnt vmcnt(1)\n" \
"s_add_u32 m0, %[wbase], 25600\n" \
"s_add_u32 s66, %[so0], 3211264\n" \
"buffer_load_dwordx4 %[wvo], %[rw], s66 offen lds\n" \
"s_barrier\n" \
"ds_read_b64_tr_b16 a[80:81], v24 offset:20480\n" \
"ds_read_b64_tr_b16 a[82:83], v24 offset:53248\n" \
"ds_read_b64_tr_b16 a[88:89], v25 offset:20480\n" \
"ds_read_b64_tr_b16 a[90:91], v25 offset:53248\n" \
"ds_read_b64_tr_b16 a[84:85], v24 offset:20488\n" \
"ds_read_b64_tr_b16 a[86:87], v24 offset:53256\n" \
"ds_read_b64_tr_b16 a[92:93], v25 offset:20488\n" \
"ds_read_b64_tr_b16 a[94:95], v25 offset:53256\n" \
"s_waitcnt lgkmcnt(0)\n" \
"s_waitcnt vmcnt(1)\n" \
"s_add_u32 m0, %[wbase], 26624\n" \
"s_add_u32 s66, %[so0], 3276800\n" \
"buffer_load_dwordx4 %[wvo], %[rw], s66 offen lds\n" \
"s_waitcnt vmcnt(1)\n" \
"s_add_u32 m0, %[wbase], 27648\n" \
"s_add_u32 s66, %[so0], 3342336\n" \
"buffer_load_dwordx4 %[wvo], %[rw], s66 offen lds\n" \
"s_waitcnt vmcnt(1)\n" \
"s_add_u32 m0, %[wbase], 28672\n" \
"s_add_u32 s66, %[so0], 3670016\n" \
"buffer_load_dwordx4 %[wvo], %[rw], s66 offen lds\n" \
"s_waitcnt vmcnt(1)\n" \
"s_add_u32 m0, %[wbase], 29696\n" \
"s_add_u32 s66, %[so0], 3735552\n" \
"buffer_load_dwordx4 %[wvo], %[rw], s66 offen lds\n" \
"s_barrier\n" \
"ds_read_b64_tr_b16 a[96:97], v24 offset:24576\n" \
"ds_read_b64_tr_b16 a[98:99], v24 offset:57344\n" \
"ds_read_b64_tr_b16 a[104:105], v25 offset:24576\n" \
"ds_read_b64_tr_b16 a[106:107], v25 offset:57344\n" \
"ds_read_b64_tr_b16 a[100:101], v24 offset:24584\n" \
"ds_read_b64_tr_b16 a[102:103], v24 offset:57352\n" \
"ds_read_b64_tr_b16 a[108:109], v25 offset:24584\n" \
"ds_read_b64_tr_b16 a[110:111], v25 offset:57352\n" \
"s_waitcnt lgkmcnt(0)\n" \
"s_waitcnt vmcnt(1)\n" \
"s_add_u32 m0, %[wbase], 30720\n" \
"s_add_u32 s66, %[so0], 3801088\n" \
"buffer_load_dwordx4 %[wvo], %[rw], s66 offen lds\n" \
"s_waitcnt vmcnt(1)\n" \
"s_add_u32 m0, %[wbase], 31744\n" \
"s_add_u32 s66, %[so0], 3866624\n" \
"buffer_load_dwordx4 %[wvo], %[rw], s66 offen lds\n" \
"s_waitcnt vmcnt(1)\n" \
"s_add_u32 m0, %[wbase], 0\n" \
"s_add_u32 s66, %[so0], 4194304\n" \
"buffer_load_dwordx4 %[wvo], %[rw], s66 offen lds\n" \
"s_waitcnt vmcnt(1)\n" \
"s_add_u32 m0, %[wbase], 1024\n" \
"s_add_u32 s66, %[so0], 4259840\n" \
"buffer_load_dwordx4 %[wvo], %[rw], s66 offen lds\n" \
"s_barrier\n" \
"ds_read_b64_tr_b16 a[112:113], v24 offset:28672\n" \
"ds_read_b64_tr_b16 a[114:115], v24 offset:61440\n" \
"ds_read_b64_tr_b16 a[120:121], v25 offset:28672\n" \
"ds_read_b64_tr_b16 a[122:123], v25 offset:61440\n" \
"ds_read_b64_tr_b16 a[116:117], v24 offset:28680\n" \
"ds_read_b64_tr_b16 a[118:119], v24 offset:61448\n" \
"ds_read_b64_tr_b16 a[124:125], v25 offset:28680\n" \
"ds_read_b64_tr_b16 a[126:127], v25 offset:61448\n" \
"s_waitcnt lgkmcnt(0)\n" \
"s_waitcnt vmcnt(1)\n" \
"s_add_u32 m0, %[wbase], 2048\n" \
"s_add_u32 s66, %[so0], 4325376\n" \
"buffer_load_dwordx4 %[wvo], %[rw], s66 offen lds\n" \
"s_waitcnt vmcnt(1)\n" \
"s_add_u32 m0, %[wbase], 3072\n" \
"s_add_u32 s66, %[so0], 4390912\n" \
"buffer_load_dwordx4 %[wvo], %[rw], s66 offen lds\n" \
"s_waitcnt vmcnt(1)\n" \
"s_add_u32 m0, %[wbase], 4096\n" \
"s_add_u32 s66, %[so0], 4718592\n" \
"buffer_load_dwordx4 %[wvo], %[rw], s66 offen lds\n" \
"s_waitcnt vmcnt(1)\n" \
"s_add_u32 m0, %[wbase], 5120\n" \
"s_add_u32 s66, %[so0], 4784128\n" \
"buffer_load_dwordx4 %[wvo], %[rw], s66 offen lds\n" \
"s_barrier\n" \
"ds_read_b64_tr_b16 a[128:129], v24 offset:0\n" \
"ds_read_b64_tr_b16 a[130:131], v24 offset:32768\n" \
"ds_read_b64_tr_b16 a[136:137], v25 offset:0\n" \
"ds_read_b64_tr_b16 a[138:139], v25 offset:32768\n" \
"ds_read_b64_tr_b16 a[132:133], v24 offset:8\n" \
"ds_read_b64_tr_b16 a[134:135], v24 offset:32776\n" \
"ds_read_b64_tr_b16 a[140:141], v25 offset:8\n" \
"ds_read_b64_tr_b16 a[142:143], v25 offset:32776\n" \
"s_waitcnt lgkmcnt(0)\n" \
"s_waitcnt vmcnt(1)\n" \
"s_add_u32 m0, %[wbase], 6144\n" \
"s_add_u32 s66, %[so0], 4849664\n" \
"buffer_load_dwordx4 %[wvo], %[rw], s66 offen lds\n" \
"s_waitcnt vmcnt(1)\n" \
"s_add_u32 m0, %[wbase], 7168\n" \
"s_add_u32 s66, %[so0], 4915200\n" \
"buffer_load_dwordx4 %[wvo], %[rw], s66 offen lds\n" \
"s_waitcnt vmcnt(1)\n" \
"s_add_u32 m0, %[wbase], 8192\n" \
"s_add_u32 s66, %[so0], 5242880\n" \
"buffer_load_dwordx4 %[wvo], %[rw], s66 offen lds\n" \
"s_waitcnt vmcnt(1)\n" \
"s_add_u32 m0, %[wbase], 9216\n" \
"s_add_u32 s66, %[so0], 5308416\n" \
"buffer_load_dwordx4 %[wvo], %[rw], s66 offen lds\n" \
"s_barrier\n" \
"ds_read_b64_tr_b16 a[144:145], v24 offset:4096\n" \
"ds_read_b64_tr_b16 a[146:147], v24 offset:36864\n" \
"ds_read_b64_tr_b16 a[152:153], v25 offset:4096\n" \
"ds_read_b64_tr_b16 a[154:155], v25 offset:36864\n" \
"ds_read_b64_tr_b16 a[148:149], v24 offset:4104\n" \
"ds_read_b64_tr_b16 a[150:151], v24 offset:36872\n" \
"ds_read_b64_tr_b16 a[156:157], v25 offset:4104\n" \
"ds_read_b64_tr_b16 a[158:159], v25 offset:36872\n" \
"s_waitcnt lgkmcnt(0)\n" \
"s_waitcnt vmcnt(1)\n" \
"s_add_u32 m0, %[wbase], 10240\n" \
"s_add_u32 s66, %[so0], 5373952\n" \
"buffer_load_dwordx4 %[wvo], %[rw], s66 offen lds\n" \
"s_waitcnt vmcnt(1)\n" \
"s_add_u32 m0, %[wbase], 11264\n" \
"s_add_u32 s66, %[so0], 5439488\n" \
"buffer_load_dwordx4 %[wvo], %[rw], s66 offen lds\n" \
"s_waitcnt vmcnt(1)\n" \
"s_add_u32 m0, %[wbase], 12288\n" \
"s_add_u32 s66, %[so0], 5767168\n" \
"buffer_load_dwordx4 %[wvo], %[rw], s66 offen lds\n" \
"s_waitcnt vmcnt(1)\n" \
"s_add_u32 m0, %[wbase], 13312\n" \
"s_add_u32 s66, %[so0], 5832704\n" \
"buffer_load_dwordx4 %[wvo], %[rw], s66 offen lds\n" \
"s_barrier\n" \
"ds_read_b64_tr_b16 a[160:161], v24 offset:8192\n" \
"ds_read_b64_tr_b16 a[162:163], v24 offset:40960\n" \
"ds_read_b64_tr_b16 a[168:169], v25 offset:8192\n" \
"ds_read_b64_tr_b16 a[170:171], v25 offset:40960\n" \
"ds_read_b64_tr_b16 a[164:165], v24 offset:8200\n" \
"ds_read_b64_tr_b16 a[166:167], v24 offset:40968\n" \
"ds_read_b64_tr_b16 a[172:173], v25 offset:8200\n" \
"ds_read_b64_tr_b16 a[174:175], v25 offset:40968\n" \
"s_waitcnt lgkmcnt(0)\n" \
"s_waitcnt vmcnt(1)\n" \
"s_add_u32 m0, %[wbase], 14336\n" \
"s_add_u32 s66, %[so0], 5898240\n" \
"buffer_load_dwordx4 %[wvo], %[rw], s66 offen lds\n" \
"s_waitcnt vmcnt(1)\n" \
"s_add_u32 m0, %[wbase], 15360\n" \
"s_add_u32 s66, %[so0], 5963776\n" \
"buffer_load_dwordx4 %[wvo], %[rw], s66 offen lds\n" \
"s_waitcnt vmcnt(1)\n" \
"s_add_u32 m0, %[wbase], 16384\n" \
"s_add_u32 s66, %[so0], 6291456\n" \
"buffer_load_dwordx4 %[wvo], %[rw], s66 offen lds\n" \
"s_waitcnt vmcnt(1)\n" \
"s_add_u32 m0, %[wbase], 17408\n" \
"s_add_u32 s66, %[so0], 6356992\n" \
"buffer_load_dwordx4 %[wvo], %[rw], s66 offen lds\n" \
"s_barrier\n" \
"ds_read_b64_tr_b16 a[176:177], v24 offset:12288\n" \
"ds_read_b64_tr_b16 a[178:179], v24 offset:45056\n" \
"ds_read_b64_tr_b16 a[184:185], v25 offset:12288\n" \
"ds_read_b64_tr_b16 a[186:187], v25 offset:45056\n" \
"ds_read_b64_tr_b16 a[180:181], v24 offset:12296\n" \
"ds_read_b64_tr_b16 a[182:183], v24 offset:45064\n" \
"ds_read_b64_tr_b16 a[188:189], v25 offset:12296\n" \
"ds_read_b64_tr_b16 a[190:191], v25 offset:45064\n" \
"s_waitcnt lgkmcnt(0)\n" \
"s_waitcnt vmcnt(1)\n" \
"s_add_u32 m0, %[wbase], 18432\n" \
"s_add_u32 s66, %[so0], 6422528\n" \
"buffer_load_dwordx4 %[wvo], %[rw], s66 offen lds\n" \
"s_waitcnt vmcnt(1)\n" \
"s_add_u32 m0, %[wbase], 19456\n" \
"s_add_u32 s66, %[so0], 6488064\n" \
"buffer_load_dwordx4 %[wvo], %[rw], s66 offen lds\n" \
"s_waitcnt vmcnt(1)\n" \
"s_add_u32 m0, %[wbase], 20480\n" \
"s_add_u32 s66, %[so0], 6815744\n" \
"buffer_load_dwordx4 %[wvo], %[rw], s66 offen lds\n" \
"s_waitcnt vmcnt(1)\n" \
"s_add_u32 m0, %[wbase], 21504\n" \
"s_add_u32 s66, %[so0], 6881280\n" \
"buffer_load_dwordx4 %[wvo], %[rw], s66 offen lds\n" \
"s_barrier\n" \
"ds_read_b64_tr_b16 a[192:193], v24 offset:16384\n" \
"ds_read_b64_tr_b16 a[194:195], v24 offset:49152\n" \
"ds_read_b64_tr_b16 a[200:201], v25 offset:16384\n" \
"ds_read_b64_tr_b16 a[202:203], v25 offset:49152\n" \
"ds_read_b64_tr_b16 a[196:197], v24 offset:16392\n" \
"ds_read_b64_tr_b16 a[198:199], v24 offset:49160\n" \
"ds_read_b64_tr_b16 a[204:205], v25 offset:16392\n" \
"ds_read_b64_tr_b16 a[206:207], v25 offset:49160\n" \
"s_waitcnt lgkmcnt(0)\n" \
"s_waitcnt vmcnt(1)\n" \
"s_add_u32 m0, %[wbase], 22528\n" \
"s_add_u32 s66, %[so0], 6946816\n" \
"buffer_load_dwordx4 %[wvo], %[rw], s66 offen lds\n" \
"s_waitcnt vmcnt(1)\n" \
"s_add_u32 m0, %[wbase], 23552\n" \
"s_add_u32 s66, %[so0], 7012352\n" \
"buffer_load_dwordx4 %[wvo], %[rw], s66 offen lds\n" \
"s_waitcnt vmcnt(1)\n" \
"s_add_u32 m0, %[wbase], 24576\n" \
"s_add_u32 s66, %[so0], 7340032\n" \
"buffer_load_dwordx4 %[wvo], %[rw], s66 offen lds\n" \
"s_waitcnt vmcnt(1)\n" \
"s_add_u32 m0, %[wbase], 25600\n" \
"s_add_u32 s66, %[so0], 7405568\n" \
"buffer_load_dwordx4 %[wvo], %[rw], s66 offen lds\n" \
"s_barrier\n" \
"ds_read_b64_tr_b16 a[208:209], v24 offset:20480\n" \
"ds_read_b64_tr_b16 a[210:211], v24 offset:53248\n" \
"ds_read_b64_tr_b16 a[216:217], v25 offset:20480\n" \
"ds_read_b64_tr_b16 a[218:219], v25 offset:53248\n" \
"ds_read_b64_tr_b16 a[212:213], v24 offset:20488\n" \
"ds_read_b64_tr_b16 a[214:215], v24 offset:53256\n" \
"ds_read_b64_tr_b16 a[220:221], v25 offset:20488\n" \
"ds_read_b64_tr_b16 a[222:223], v25 offset:53256\n" \
"s_waitcnt lgkmcnt(0)\n" \
"s_waitcnt vmcnt(1)\n" \
"s_add_u32 m0, %[wbase], 26624\n" \
"s_add_u32 s66, %[so0], 7471104\n" \
"buffer_load_dwordx4 %[wvo], %[rw], s66 offen lds\n" \
"s_waitcnt vmcnt(1)\n" \
"s_add_u32 m0, %[wbase], 27648\n" \
"s_add_u32 s66, %[so0], 7536640\n" \
"buffer_load_dwordx4 %[wvo], %[rw], s66 offen lds\n" \
"s_waitcnt vmcnt(1)\n" \
"s_add_u32 m0, %[wbase], 28672\n" \
"s_add_u32 s66, %[so0], 7864320\n" \
"buffer_load_dwordx4 %[wvo], %[rw], s66 offen lds\n" \
"s_waitcnt vmcnt(1)\n" \
"s_add_u32 m0, %[wbase], 29696\n" \
"s_add_u32 s66, %[so0], 7929856\n" \
"buffer_load_dwordx4 %[wvo], %[rw], s66 offen lds\n" \
"s_barrier\n" \
"ds_read_b64_tr_b16 a[224:225], v24 offset:24576\n" \
"ds_read_b64_tr_b16 a[226:227], v24 offset:57344\n" \
"ds_read_b64_tr_b16 a[232:233], v25 offset:24576\n" \
"ds_read_b64_tr_b16 a[234:235], v25 offset:57344\n" \
"ds_read_b64_tr_b16 a[228:229], v24 offset:24584\n" \
"ds_read_b64_tr_b16 a[230:231], v24 offset:57352\n" \
"ds_read_b64_tr_b16 a[236:237], v25 offset:24584\n" \
"ds_read_b64_tr_b16 a[238:239], v25 offset:57352\n" \
"s_waitcnt lgkmcnt(0)\n" \
"s_waitcnt vmcnt(1)\n" \
"s_add_u32 m0, %[wbase], 30720\n" \
"s_add_u32 s66, %[so0], 7995392\n" \
"buffer_load_dwordx4 %[wvo], %[rw], s66 offen lds\n" \
"s_waitcnt vmcnt(1)\n" \
"s_add_u32 m0, %[wbase], 31744\n" \
"s_add_u32 s66, %[so0], 8060928\n" \
"buffer_load_dwordx4 %[wvo], %[rw], s66 offen lds\n" \
"s_waitcnt vmcnt(0)\n" \
"s_barrier\n" \
"ds_read_b64_tr_b16 a[240:241], v24 offset:28672\n" \
"ds_read_b64_tr_b16 a[242:243], v24 offset:61440\n" \
"ds_read_b64_tr_b16 a[248:249], v25 offset:28672\n" \
"ds_read_b64_tr_b16 a[250:251], v25 offset:61440\n" \
"ds_read_b64_tr_b16 a[244:245], v24 offset:28680\n" \
"ds_read_b64_tr_b16 a[246:247], v24 offset:61448\n" \
"ds_read_b64_tr_b16 a[252:253], v25 offset:28680\n" \
"ds_read_b64_tr_b16 a[254:255], v25 offset:61448\n" \
"s_waitcnt lgkmcnt(0)\n" \
"s_barrier\n" \
"s_add_u32 s81, %[dst], 13631488\n" \
"v_mov_b32 v244, -1\n" \
"v_mov_b32 v245, -1\n" \
"v_mov_b32 v246, -1\n" \
"v_mov_b32 v247, -1\n" \
"s_mov_b32 s80, 48\n" \
"v_mov_b32 v204, 0xf149f2ca\n" \
"v_cmp_gt_i32 vcc, 0, %[dd]\n" \
"s_nop 1\n" \
"v_cndmask_b32 v192, 0, v204, vcc\n" \
"v_cmp_gt_i32 vcc, 1, %[dd]\n" \
"s_nop 1\n" \
"v_cndmask_b32 v193, 0, v204, vcc\n" \
"v_cmp_gt_i32 vcc, 2, %[dd]\n" \
"s_nop 1\n" \
"v_cndmask_b32 v194, 0, v204, vcc\n" \
"v_cmp_gt_i32 vcc, 3, %[dd]\n" \
"s_nop 1\n" \
"v_cndmask_b32 v195, 0, v204, vcc\n" \
"v_cmp_gt_i32 vcc, 16, %[dd]\n" \
"s_nop 1\n" \
"v_cndmask_b32 v196, 0, v204, vcc\n" \
"v_cmp_gt_i32 vcc, 17, %[dd]\n" \
"s_nop 1\n" \
"v_cndmask_b32 v197, 0, v204, vcc\n" \
"v_cmp_gt_i32 vcc, 18, %[dd]\n" \
"s_nop 1\n" \
"v_cndmask_b32 v198, 0, v204, vcc\n" \
"v_cmp_gt_i32 vcc, 19, %[dd]\n" \
"s_nop 1\n" \
"v_cndmask_b32 v199, 0, v204, vcc\n" \
"v_mov_b32 v201, 0\n" \
"v_mov_b32 v202, 0\n" \
"v_mov_b32 v220, 0x3f803f80\n" \
"v_mov_b32 v221, 0x3f803f80\n" \
"v_mov_b32 v222, 0x3f803f80\n" \
"v_mov_b32 v223, 0x3f803f80\n" \
"s_mov_b32 s70, %[okx]\n" \
"s_mov_b32 s71, %[okm]\n" \
"s_mov_b32 s72, %[ovm]\n" \
"s_mov_b32 s73, 0x3e0293ee\n" \
"s_mov_b32 s74, 1\n" \
"s_lshr_b32 s75, %[wbase], 4\n" \
"s_add_u32 s75, s75, 139264\n" \
"s_mov_b32 s64, 262144\n" \
"s_mov_b32 s65, 0\n" \
"s_mov_b32 s67, %[src]\n" \
"s_mov_b32 s68, %[dst]\n" \
"s_lshr_b32 s69, %[wbase], 1\n" \
"v_mov_b32 v68, %[l16]\n" \
"s_barrier\n" \
"s_add_u32 m0, s69, 0\n" \
"s_add_u32 s66, s67, 0\n" \
"buffer_load_dwordx4 %[l16], %[ws], s66 offen offset:0 sc1 lds\n" \
"buffer_load_dwordx4 %[l16], %[ws], s66 offen offset:1024 sc1 lds\n" \
"buffer_load_dwordx4 %[l16], %[ws], s66 offen offset:2048 sc1 lds\n" \
"buffer_load_dwordx4 %[l16], %[ws], s66 offen offset:3072 sc1 lds\n" \
"s_add_u32 m0, s69, 4096\n" \
"s_add_u32 s66, s67, 4096\n" \
"buffer_load_dwordx4 %[l16], %[ws], s66 offen offset:0 sc1 lds\n" \
"buffer_load_dwordx4 %[l16], %[ws], s66 offen offset:1024 sc1 lds\n" \
"buffer_load_dwordx4 %[l16], %[ws], s66 offen offset:2048 sc1 lds\n" \
"buffer_load_dwordx4 %[l16], %[ws], s66 offen offset:3072 sc1 lds\n" \
"s_add_u32 m0, s69, 8192\n" \
"s_add_u32 s66, s67, 8192\n" \
"buffer_load_dwordx4 %[l16], %[ws], s66 offen offset:0 sc1 lds\n" \
"buffer_load_dwordx4 %[l16], %[ws], s66 offen offset:1024 sc1 lds\n" \
"buffer_load_dwordx4 %[l16], %[ws], s66 offen offset:2048 sc1 lds\n" \
"buffer_load_dwordx4 %[l16], %[ws], s66 offen offset:3072 sc1 lds\n" \
"s_add_u32 m0, s69, 12288\n" \
"s_add_u32 s66, s67, 12288\n" \
"buffer_load_dwordx4 %[l16], %[ws], s66 offen offset:0 sc1 lds\n" \
"buffer_load_dwordx4 %[l16], %[ws], s66 offen offset:1024 sc1 lds\n" \
"buffer_load_dwordx4 %[l16], %[ws], s66 offen offset:2048 sc1 lds\n" \
"buffer_load_dwordx4 %[l16], %[ws], s66 offen offset:3072 sc1 lds\n" \
"s_add_u32 s67, s67, 65536\n" \
"s_xor_b32 s69, s69, 65536\n" \
"s_waitcnt vmcnt(0)\n" \
"Lqa_top_%=:\n" \
"s_barrier\n" \
"ds_read_b128 v[20:23], v68 offset:0\n" \
"ds_read_b128 v[24:27], v68 offset:1024\n" \
"ds_read_b128 v[28:31], v68 offset:2048\n" \
"ds_read_b128 v[32:35], v68 offset:3072\n" \
"ds_read_b128 v[36:39], v68 offset:4096\n" \
"ds_read_b128 v[40:43], v68 offset:5120\n" \
"buffer_load_dword v70, %[fkv], %[ws], 0 offen sc1\n" \
"v_mov_b32 v69, s65\n" \
"s_waitcnt lgkmcnt(5)\n" \
"v_mfma_f32_16x16x32_bf16 v[52:55], a[0:3], v[20:23], 0\n" \
"s_nop 0\n" \
"ds_read_b128 v[44:47], v68 offset:6144\n" \
"v_mfma_f32_16x16x32_bf16 v[56:59], a[4:7], v[20:23], 0\n" \
"s_waitcnt lgkmcnt(5)\n" \
"s_add_u32 m0, s69, 0\n" \
"s_add_u32 s66, s67, 0\n" \
"v_mfma_f32_16x16x32_bf16 v[60:63], a[0:3], v[24:27], 0\n" \
"s_nop 0\n" \
"ds_read_b128 v[48:51], v68 offset:7168\n" \
"v_mfma_f32_16x16x32_bf16 v[64:67], a[4:7], v[24:27], 0\n" \
"s_waitcnt lgkmcnt(5)\n" \
"buffer_load_dwordx4 %[l16], %[ws], s66 offen offset:0 sc1 lds\n" \
"v_mfma_f32_16x16x32_bf16 v[52:55], a[8:11], v[28:31], v[52:55]\n" \
"s_nop 0\n" \
"ds_read_b128 v[20:23], v68 offset:8192\n" \
"v_mfma_f32_16x16x32_bf16 v[56:59], a[12:15], v[28:31], v[56:59]\n" \
"s_waitcnt lgkmcnt(5)\n" \
"s_nop 1\n" \
"v_mfma_f32_16x16x32_bf16 v[60:63], a[8:11], v[32:35], v[60:63]\n" \
"s_nop 0\n" \
"ds_read_b128 v[24:27], v68 offset:9216\n" \
"v_mfma_f32_16x16x32_bf16 v[64:67], a[12:15], v[32:35], v[64:67]\n" \
"s_waitcnt lgkmcnt(5)\n" \
"buffer_load_dwordx4 %[l16], %[ws], s66 offen offset:1024 sc1 lds\n" \
"v_mfma_f32_16x16x32_bf16 v[52:55], a[16:19], v[36:39], v[52:55]\n" \
"s_nop 0\n" \
"ds_read_b128 v[28:31], v68 offset:10240\n" \
"v_mfma_f32_16x16x32_bf16 v[56:59], a[20:23], v[36:39], v[56:59]\n" \
"s_waitcnt lgkmcnt(5)\n" \
"s_nop 1\n" \
"v_mfma_f32_16x16x32_bf16 v[60:63], a[16:19], v[40:43], v[60:63]\n" \
"s_nop 0\n" \
"ds_read_b128 v[32:35], v68 offset:11264\n" \
"v_mfma_f32_16x16x32_bf16 v[64:67], a[20:23], v[40:43], v[64:67]\n" \
"s_waitcnt lgkmcnt(5)\n" \
"buffer_load_dwordx4 %[l16], %[ws], s66 offen offset:2048 sc1 lds\n" \
"v_mfma_f32_16x16x32_bf16 v[52:55], a[24:27], v[44:47], v[52:55]\n" \
"s_nop 0\n" \
"ds_read_b128 v[36:39], v68 offset:12288\n" \
"v_mfma_f32_16x16x32_bf16 v[56:59], a[28:31], v[44:47], v[56:59]\n" \
"s_waitcnt lgkmcnt(5)\n" \
"s_nop 1\n" \
"v_mfma_f32_16x16x32_bf16 v[60:63], a[24:27], v[48:51], v[60:63]\n" \
"s_nop 0\n" \
"ds_read_b128 v[40:43], v68 offset:13312\n" \
"v_mfma_f32_16x16x32_bf16 v[64:67], a[28:31], v[48:51], v[64:67]\n" \
"s_waitcnt lgkmcnt(5)\n" \
"buffer_load_dwordx4 %[l16], %[ws], s66 offen offset:3072 sc1 lds\n" \
"v_mfma_f32_16x16x32_bf16 v[52:55], a[32:35], v[20:23], v[52:55]\n" \
"s_nop 0\n" \
"ds_read_b128 v[44:47], v68 offset:14336\n" \
"v_mfma_f32_16x16x32_bf16 v[56:59], a[36:39], v[20:23], v[56:59]\n" \
"s_waitcnt lgkmcnt(5)\n" \
"s_add_u32 m0, s69, 4096\n" \
"s_add_u32 s66, s67, 4096\n" \
"v_mfma_f32_16x16x32_bf16 v[60:63], a[32:35], v[24:27], v[60:63]\n" \
"s_nop 0\n" \
"ds_read_b128 v[48:51], v68 offset:15360\n" \
"v_mfma_f32_16x16x32_bf16 v[64:67], a[36:39], v[24:27], v[64:67]\n" \
"s_waitcnt lgkmcnt(5)\n" \
"buffer_load_dwordx4 %[l16], %[ws], s66 offen offset:0 sc1 lds\n" \
"v_mfma_f32_16x16x32_bf16 v[52:55], a[40:43], v[28:31], v[52:55]\n" \
"s_nop 0\n" \
"ds_read_b128 v[20:23], v68 offset:16384\n" \
"v_mfma_f32_16x16x32_bf16 v[56:59], a[44:47], v[28:31], v[56:59]\n" \
"s_waitcnt lgkmcnt(5)\n" \
"s_nop 1\n" \
"v_mfma_f32_16x16x32_bf16 v[60:63], a[40:43], v[32:35], v[60:63]\n" \
"s_nop 0\n" \
"ds_read_b128 v[24:27], v68 offset:17408\n" \
"v_mfma_f32_16x16x32_bf16 v[64:67], a[44:47], v[32:35], v[64:67]\n" \
"s_waitcnt lgkmcnt(5)\n" \
"buffer_load_dwordx4 %[l16], %[ws], s66 offen offset:1024 sc1 lds\n" \
"v_mfma_f32_16x16x32_bf16 v[52:55], a[48:51], v[36:39], v[52:55]\n" \
"s_nop 0\n" \
"ds_read_b128 v[28:31], v68 offset:18432\n" \
"v_mfma_f32_16x16x32_bf16 v[56:59], a[52:55], v[36:39], v[56:59]\n" \
"s_waitcnt lgkmcnt(5)\n" \
"s_nop 1\n" \
"v_mfma_f32_16x16x32_bf16 v[60:63], a[48:51], v[40:43], v[60:63]\n" \
"s_nop 0\n" \
"ds_read_b128 v[32:35], v68 offset:19456\n" \
"v_mfma_f32_16x16x32_bf16 v[64:67], a[52:55], v[40:43], v[64:67]\n" \
"s_waitcnt lgkmcnt(5)\n" \
"buffer_load_dwordx4 %[l16], %[ws], s66 offen offset:2048 sc1 lds\n" \
"v_mfma_f32_16x16x32_bf16 v[52:55], a[56:59], v[44:47], v[52:55]\n" \
"s_nop 0\n" \
"ds_read_b128 v[36:39], v68 offset:20480\n" \
"v_mfma_f32_16x16x32_bf16 v[56:59], a[60:63], v[44:47], v[56:59]\n" \
"s_waitcnt lgkmcnt(5)\n" \
"s_nop 1\n" \
"v_mfma_f32_16x16x32_bf16 v[60:63], a[56:59], v[48:51], v[60:63]\n" \
"s_nop 0\n" \
"ds_read_b128 v[40:43], v68 offset:21504\n" \
"v_mfma_f32_16x16x32_bf16 v[64:67], a[60:63], v[48:51], v[64:67]\n" \
"s_waitcnt lgkmcnt(5)\n" \
"buffer_load_dwordx4 %[l16], %[ws], s66 offen offset:3072 sc1 lds\n" \
"v_mfma_f32_16x16x32_bf16 v[52:55], a[64:67], v[20:23], v[52:55]\n" \
"s_nop 0\n" \
"ds_read_b128 v[44:47], v68 offset:22528\n" \
"v_mfma_f32_16x16x32_bf16 v[56:59], a[68:71], v[20:23], v[56:59]\n" \
"s_waitcnt lgkmcnt(5)\n" \
"s_nop 1\n" \
"v_mfma_f32_16x16x32_bf16 v[60:63], a[64:67], v[24:27], v[60:63]\n" \
"s_nop 0\n" \
"ds_read_b128 v[48:51], v68 offset:23552\n" \
"v_mfma_f32_16x16x32_bf16 v[64:67], a[68:71], v[24:27], v[64:67]\n" \
"s_waitcnt lgkmcnt(5)\n" \
"s_nop 1\n" \
"v_mfma_f32_16x16x32_bf16 v[52:55], a[72:75], v[28:31], v[52:55]\n" \
"s_nop 0\n" \
"ds_read_b128 v[20:23], v68 offset:24576\n" \
"v_mfma_f32_16x16x32_bf16 v[56:59], a[76:79], v[28:31], v[56:59]\n" \
"s_waitcnt lgkmcnt(5)\n" \
"s_nop 1\n" \
"v_mfma_f32_16x16x32_bf16 v[60:63], a[72:75], v[32:35], v[60:63]\n" \
"s_nop 0\n" \
"ds_read_b128 v[24:27], v68 offset:25600\n" \
"v_mfma_f32_16x16x32_bf16 v[64:67], a[76:79], v[32:35], v[64:67]\n" \
"s_waitcnt vmcnt(8) lgkmcnt(5)\n" \
"buffer_store_dword v69, %[fo], %[ws], 0 offen sc1\n" \
"v_cmp_gt_u32 vcc, s74, v70\n" \
"v_mfma_f32_16x16x32_bf16 v[52:55], a[80:83], v[36:39], v[52:55]\n" \
"s_nop 0\n" \
"ds_read_b128 v[28:31], v68 offset:26624\n" \
"v_mfma_f32_16x16x32_bf16 v[56:59], a[84:87], v[36:39], v[56:59]\n" \
"s_waitcnt lgkmcnt(5)\n" \
"s_cbranch_vccnz Lqa_skv_%=\n" \
"Lqa_kv_%=:\n" \
"v_mfma_f32_16x16x32_bf16 v[60:63], a[80:83], v[40:43], v[60:63]\n" \
"s_nop 0\n" \
"ds_read_b128 v[32:35], v68 offset:27648\n" \
"v_mfma_f32_16x16x32_bf16 v[64:67], a[84:87], v[40:43], v[64:67]\n" \
"s_waitcnt lgkmcnt(5)\n" \
"s_add_u32 m0, s75, 0\n" \
"s_add_u32 s66, s70, 0\n" \
"v_mfma_f32_16x16x32_bf16 v[52:55], a[88:91], v[44:47], v[52:55]\n" \
"s_nop 0\n" \
"ds_read_b128 v[36:39], v68 offset:28672\n" \
"v_mfma_f32_16x16x32_bf16 v[56:59], a[92:95], v[44:47], v[56:59]\n" \
"s_waitcnt lgkmcnt(5)\n" \
"buffer_load_dwordx4 %[l16], %[ws], s66 offen offset:0 sc1 lds\n" \
"v_mfma_f32_16x16x32_bf16 v[60:63], a[88:91], v[48:51], v[60:63]\n" \
"s_nop 0\n" \
"ds_read_b128 v[40:43], v68 offset:29696\n" \
"v_mfma_f32_16x16x32_bf16 v[64:67], a[92:95], v[48:51], v[64:67]\n" \
"s_waitcnt lgkmcnt(5)\n" \
"s_nop 1\n" \
"v_mfma_f32_16x16x32_bf16 v[52:55], a[96:99], v[20:23], v[52:55]\n" \
"s_nop 0\n" \
"ds_read_b128 v[44:47], v68 offset:30720\n" \
"v_mfma_f32_16x16x32_bf16 v[56:59], a[100:103], v[20:23], v[56:59]\n" \
"s_waitcnt lgkmcnt(5)\n" \
"buffer_load_dwordx4 %[l16], %[ws], s66 offen offset:1024 sc1 lds\n" \
"v_mfma_f32_16x16x32_bf16 v[60:63], a[96:99], v[24:27], v[60:63]\n" \
"s_nop 0\n" \
"ds_read_b128 v[48:51], v68 offset:31744\n" \
"v_mfma_f32_16x16x32_bf16 v[64:67], a[100:103], v[24:27], v[64:67]\n" \
"s_waitcnt lgkmcnt(5)\n" \
"s_add_u32 m0, s75, 8192\n" \
"s_add_u32 s66, s70, %[dvx]\n" \
"v_mfma_f32_16x16x32_bf16 v[52:55], a[104:107], v[28:31], v[52:55]\n" \
"s_nop 0\n" \
"ds_read_b128 v[20:23], v68 offset:32768\n" \
"v_mfma_f32_16x16x32_bf16 v[56:59], a[108:111], v[28:31], v[56:59]\n" \
"s_waitcnt lgkmcnt(5)\n" \
"buffer_load_dwordx4 %[l16], %[ws], s66 offen offset:0 sc1 lds\n" \
"v_mfma_f32_16x16x32_bf16 v[60:63], a[104:107], v[32:35], v[60:63]\n" \
"s_nop 0\n" \
"ds_read_b128 v[24:27], v68 offset:33792\n" \
"v_mfma_f32_16x16x32_bf16 v[64:67], a[108:111], v[32:35], v[64:67]\n" \
"s_waitcnt lgkmcnt(5)\n" \
"s_nop 1\n" \
"v_mfma_f32_16x16x32_bf16 v[52:55], a[112:115], v[36:39], v[52:55]\n" \
"s_nop 0\n" \
"ds_read_b128 v[28:31], v68 offset:34816\n" \
"v_mfma_f32_16x16x32_bf16 v[56:59], a[116:119], v[36:39], v[56:59]\n" \
"s_waitcnt lgkmcnt(5)\n" \
"buffer_load_dwordx4 %[l16], %[ws], s66 offen offset:1024 sc1 lds\n" \
"v_mfma_f32_16x16x32_bf16 v[60:63], a[112:115], v[40:43], v[60:63]\n" \
"s_nop 0\n" \
"ds_read_b128 v[32:35], v68 offset:35840\n" \
"v_mfma_f32_16x16x32_bf16 v[64:67], a[116:119], v[40:43], v[64:67]\n" \
"s_waitcnt lgkmcnt(5)\n" \
"s_add_u32 m0, s69, 8192\n" \
"s_add_u32 s66, s67, 8192\n" \
"v_mfma_f32_16x16x32_bf16 v[52:55], a[120:123], v[44:47], v[52:55]\n" \
"s_nop 0\n" \
"ds_read_b128 v[36:39], v68 offset:36864\n" \
"v_mfma_f32_16x16x32_bf16 v[56:59], a[124:127], v[44:47], v[56:59]\n" \
"s_waitcnt lgkmcnt(5)\n" \
"buffer_load_dwordx4 %[l16], %[ws], s66 offen offset:0 sc1 lds\n" \
"v_mfma_f32_16x16x32_bf16 v[60:63], a[120:123], v[48:51], v[60:63]\n" \
"s_nop 0\n" \
"ds_read_b128 v[40:43], v68 offset:37888\n" \
"v_mfma_f32_16x16x32_bf16 v[64:67], a[124:127], v[48:51], v[64:67]\n" \
"s_waitcnt lgkmcnt(5)\n" \
"s_nop 1\n" \
"v_mfma_f32_16x16x32_bf16 v[52:55], a[128:131], v[20:23], v[52:55]\n" \
"s_nop 0\n" \
"ds_read_b128 v[44:47], v68 offset:38912\n" \
"v_mfma_f32_16x16x32_bf16 v[56:59], a[132:135], v[20:23], v[56:59]\n" \
"s_waitcnt lgkmcnt(5)\n" \
"buffer_load_dwordx4 %[l16], %[ws], s66 offen offset:1024 sc1 lds\n" \
"v_mfma_f32_16x16x32_bf16 v[60:63], a[128:131], v[24:27], v[60:63]\n" \
"s_nop 0\n" \
"ds_read_b128 v[48:51], v68 offset:39936\n" \
"v_mfma_f32_16x16x32_bf16 v[64:67], a[132:135], v[24:27], v[64:67]\n" \
"s_waitcnt lgkmcnt(5)\n" \
"s_nop 1\n" \
"v_mfma_f32_16x16x32_bf16 v[52:55], a[136:139], v[28:31], v[52:55]\n" \
"s_nop 0\n" \
"ds_read_b128 v[20:23], v68 offset:40960\n" \
"v_mfma_f32_16x16x32_bf16 v[56:59], a[140:143], v[28:31], v[56:59]\n" \
"s_waitcnt lgkmcnt(5)\n" \
"buffer_load_dwordx4 %[l16], %[ws], s66 offen offset:2048 sc1 lds\n" \
"v_mfma_f32_16x16x32_bf16 v[60:63], a[136:139], v[32:35], v[60:63]\n" \
"s_nop 0\n" \
"ds_read_b128 v[24:27], v68 offset:41984\n" \
"v_mfma_f32_16x16x32_bf16 v[64:67], a[140:143], v[32:35], v[64:67]\n" \
"s_waitcnt lgkmcnt(5)\n" \
"s_nop 1\n" \
"v_mfma_f32_16x16x32_bf16 v[52:55], a[144:147], v[36:39], v[52:55]\n" \
"s_nop 0\n" \
"ds_read_b128 v[28:31], v68 offset:43008\n" \
"v_mfma_f32_16x16x32_bf16 v[56:59], a[148:151], v[36:39], v[56:59]\n" \
"s_waitcnt lgkmcnt(5)\n" \
"buffer_load_dwordx4 %[l16], %[ws], s66 offen offset:3072 sc1 lds\n" \
"v_mfma_f32_16x16x32_bf16 v[60:63], a[144:147], v[40:43], v[60:63]\n" \
"s_nop 0\n" \
"ds_read_b128 v[32:35], v68 offset:44032\n" \
"v_mfma_f32_16x16x32_bf16 v[64:67], a[148:151], v[40:43], v[64:67]\n" \
"s_waitcnt lgkmcnt(5)\n" \
"s_add_u32 m0, s69, 12288\n" \
"s_add_u32 s66, s67, 12288\n" \
"v_mfma_f32_16x16x32_bf16 v[52:55], a[152:155], v[44:47], v[52:55]\n" \
"s_nop 0\n" \
"ds_read_b128 v[36:39], v68 offset:45056\n" \
"v_mfma_f32_16x16x32_bf16 v[56:59], a[156:159], v[44:47], v[56:59]\n" \
"s_waitcnt lgkmcnt(5)\n" \
"buffer_load_dwordx4 %[l16], %[ws], s66 offen offset:0 sc1 lds\n" \
"v_mfma_f32_16x16x32_bf16 v[60:63], a[152:155], v[48:51], v[60:63]\n" \
"s_nop 0\n" \
"ds_read_b128 v[40:43], v68 offset:46080\n" \
"v_mfma_f32_16x16x32_bf16 v[64:67], a[156:159], v[48:51], v[64:67]\n" \
"s_waitcnt lgkmcnt(5)\n" \
"s_nop 1\n" \
"v_mfma_f32_16x16x32_bf16 v[52:55], a[160:163], v[20:23], v[52:55]\n" \
"s_nop 0\n" \
"ds_read_b128 v[44:47], v68 offset:47104\n" \
"v_mfma_f32_16x16x32_bf16 v[56:59], a[164:167], v[20:23], v[56:59]\n" \
"s_waitcnt lgkmcnt(5)\n" \
"buffer_load_dwordx4 %[l16], %[ws], s66 offen offset:1024 sc1 lds\n" \
"v_mfma_f32_16x16x32_bf16 v[60:63], a[160:163], v[24:27], v[60:63]\n" \
"s_nop 0\n" \
"ds_read_b128 v[48:51], v68 offset:48128\n" \
"v_mfma_f32_16x16x32_bf16 v[64:67], a[164:167], v[24:27], v[64:67]\n" \
"s_waitcnt lgkmcnt(5)\n" \
"s_nop 1\n" \
"v_mfma_f32_16x16x32_bf16 v[52:55], a[168:171], v[28:31], v[52:55]\n" \
"s_nop 0\n" \
"ds_read_b128 v[20:23], v68 offset:49152\n" \
"v_mfma_f32_16x16x32_bf16 v[56:59], a[172:175], v[28:31], v[56:59]\n" \
"s_waitcnt lgkmcnt(5)\n" \
"buffer_load_dwordx4 %[l16], %[ws], s66 offen offset:2048 sc1 lds\n" \
"v_mfma_f32_16x16x32_bf16 v[60:63], a[168:171], v[32:35], v[60:63]\n" \
"s_nop 0\n" \
"ds_read_b128 v[24:27], v68 offset:50176\n" \
"v_mfma_f32_16x16x32_bf16 v[64:67], a[172:175], v[32:35], v[64:67]\n" \
"s_waitcnt lgkmcnt(5)\n" \
"s_nop 1\n" \
"v_mfma_f32_16x16x32_bf16 v[52:55], a[176:179], v[36:39], v[52:55]\n" \
"s_nop 0\n" \
"ds_read_b128 v[28:31], v68 offset:51200\n" \
"v_mfma_f32_16x16x32_bf16 v[56:59], a[180:183], v[36:39], v[56:59]\n" \
"s_waitcnt lgkmcnt(5)\n" \
"buffer_load_dwordx4 %[l16], %[ws], s66 offen offset:3072 sc1 lds\n" \
"v_mfma_f32_16x16x32_bf16 v[60:63], a[176:179], v[40:43], v[60:63]\n" \
"s_nop 0\n" \
"ds_read_b128 v[32:35], v68 offset:52224\n" \
"v_mfma_f32_16x16x32_bf16 v[64:67], a[180:183], v[40:43], v[64:67]\n" \
"s_waitcnt lgkmcnt(5)\n" \
"buffer_load_dword v71, %[fm], %[ws], 0 offen sc1\n" \
"v_mfma_f32_16x16x32_bf16 v[52:55], a[184:187], v[44:47], v[52:55]\n" \
"s_nop 0\n" \
"ds_read_b128 v[36:39], v68 offset:53248\n" \
"v_mfma_f32_16x16x32_bf16 v[56:59], a[188:191], v[44:47], v[56:59]\n" \
"s_waitcnt lgkmcnt(5)\n" \
"s_nop 1\n" \
"v_mfma_f32_16x16x32_bf16 v[60:63], a[184:187], v[48:51], v[60:63]\n" \
"s_nop 0\n" \
"ds_read_b128 v[40:43], v68 offset:54272\n" \
"v_mfma_f32_16x16x32_bf16 v[64:67], a[188:191], v[48:51], v[64:67]\n" \
"s_waitcnt lgkmcnt(5)\n" \
"s_nop 1\n" \
"v_mfma_f32_16x16x32_bf16 v[52:55], a[192:195], v[20:23], v[52:55]\n" \
"s_nop 0\n" \
"ds_read_b128 v[44:47], v68 offset:55296\n" \
"v_mfma_f32_16x16x32_bf16 v[56:59], a[196:199], v[20:23], v[56:59]\n" \
"s_waitcnt lgkmcnt(5)\n" \
"s_add_u32 s67, s67, 65536\n" \
"s_xor_b32 s69, s69, 65536\n" \
"v_mfma_f32_16x16x32_bf16 v[60:63], a[192:195], v[24:27], v[60:63]\n" \
"s_nop 0\n" \
"ds_read_b128 v[48:51], v68 offset:56320\n" \
"v_mfma_f32_16x16x32_bf16 v[64:67], a[196:199], v[24:27], v[64:67]\n" \
"s_waitcnt lgkmcnt(5)\n" \
"s_add_u32 s70, s70, 262144\n" \
"s_add_u32 s65, s65, 2\n" \
"v_mfma_f32_16x16x32_bf16 v[52:55], a[200:203], v[28:31], v[52:55]\n" \
"s_nop 0\n" \
"ds_read_b128 v[20:23], v68 offset:57344\n" \
"v_mfma_f32_16x16x32_bf16 v[56:59], a[204:207], v[28:31], v[56:59]\n" \
"s_waitcnt vmcnt(9) lgkmcnt(5)\n" \
"s_barrier\n" \
"v_mfma_f32_16x16x32_bf16 v[60:63], a[200:203], v[32:35], v[60:63]\n" \
"s_nop 0\n" \
"ds_read_b128 v[24:27], v68 offset:58368\n" \
"v_mfma_f32_16x16x32_bf16 v[64:67], a[204:207], v[32:35], v[64:67]\n" \
"s_waitcnt lgkmcnt(5)\n" \
"ds_read_b128 v[72:75], %[kr] offset:0\n" \
"v_mfma_f32_16x16x32_bf16 v[52:55], a[208:211], v[36:39], v[52:55]\n" \
"s_nop 0\n" \
"ds_read_b128 v[28:31], v68 offset:59392\n" \
"v_mfma_f32_16x16x32_bf16 v[56:59], a[212:215], v[36:39], v[56:59]\n" \
"s_waitcnt lgkmcnt(6)\n" \
"ds_read_b128 v[76:79], %[kr] offset:1024\n" \
"v_mfma_f32_16x16x32_bf16 v[60:63], a[208:211], v[40:43], v[60:63]\n" \
"s_nop 0\n" \
"ds_read_b128 v[32:35], v68 offset:61440\n" \
"v_mfma_f32_16x16x32_bf16 v[64:67], a[212:215], v[40:43], v[64:67]\n" \
"s_waitcnt lgkmcnt(7)\n" \
"ds_read_b128 v[80:83], %[kr] offset:2048\n" \
"v_mfma_f32_16x16x32_bf16 v[52:55], a[216:219], v[44:47], v[52:55]\n" \
"s_nop 0\n" \
"ds_read_b128 v[36:39], v68 offset:63488\n" \
"v_mfma_f32_16x16x32_bf16 v[56:59], a[220:223], v[44:47], v[56:59]\n" \
"s_waitcnt lgkmcnt(8)\n" \
"ds_read_b128 v[84:87], %[kr] offset:3072\n" \
"v_mfma_f32_16x16x32_bf16 v[60:63], a[216:219], v[48:51], v[60:63]\n" \
"s_nop 0\n" \
"ds_read_b128 v[40:43], v68 offset:60416\n" \
"v_mfma_f32_16x16x32_bf16 v[64:67], a[220:223], v[48:51], v[64:67]\n" \
"s_waitcnt lgkmcnt(9)\n" \
"ds_read_b128 v[88:91], %[kr] offset:4096\n" \
"v_mfma_f32_16x16x32_bf16 v[52:55], a[224:227], v[20:23], v[52:55]\n" \
"s_nop 0\n" \
"ds_read_b128 v[44:47], v68 offset:62464\n" \
"v_mfma_f32_16x16x32_bf16 v[56:59], a[228:231], v[20:23], v[56:59]\n" \
"s_waitcnt lgkmcnt(10)\n" \
"ds_read_b128 v[92:95], %[kr] offset:5120\n" \
"v_mfma_f32_16x16x32_bf16 v[60:63], a[224:227], v[24:27], v[60:63]\n" \
"s_nop 0\n" \
"ds_read_b128 v[48:51], v68 offset:64512\n" \
"v_mfma_f32_16x16x32_bf16 v[64:67], a[228:231], v[24:27], v[64:67]\n" \
"s_waitcnt lgkmcnt(10)\n" \
"ds_read_b128 v[96:99], %[kr] offset:6144\n" \
"v_mfma_f32_16x16x32_bf16 v[52:55], a[232:235], v[28:31], v[52:55]\n" \
"s_nop 0\n" \
"ds_read_b128 v[100:103], %[kr] offset:7168\n" \
"v_mfma_f32_16x16x32_bf16 v[56:59], a[236:239], v[28:31], v[56:59]\n" \
"s_waitcnt lgkmcnt(10)\n" \
"ds_read_b128 v[104:107], %[vr] offset:0\n" \
"v_mfma_f32_16x16x32_bf16 v[52:55], a[240:243], v[32:35], v[52:55]\n" \
"s_nop 0\n" \
"ds_read_b128 v[108:111], %[vr] offset:1024\n" \
"v_mfma_f32_16x16x32_bf16 v[56:59], a[244:247], v[32:35], v[56:59]\n" \
"s_waitcnt lgkmcnt(10)\n" \
"ds_read_b128 v[112:115], %[vr] offset:2048\n" \
"v_mfma_f32_16x16x32_bf16 v[52:55], a[248:251], v[36:39], v[52:55]\n" \
"s_nop 0\n" \
"ds_read_b128 v[116:119], %[vr] offset:3072\n" \
"v_mfma_f32_16x16x32_bf16 v[56:59], a[252:255], v[36:39], v[56:59]\n" \
"s_waitcnt lgkmcnt(10)\n" \
"v_xor_b32 v68, 0x10000, v68\n" \
"v_mfma_f32_16x16x32_bf16 v[60:63], a[232:235], v[40:43], v[60:63]\n" \
"s_nop 2\n" \
"v_mfma_f32_16x16x32_bf16 v[64:67], a[236:239], v[40:43], v[64:67]\n" \
"s_waitcnt lgkmcnt(8)\n" \
"v_cvt_pk_bf16_f32 v232, v52, v53\n" \
"v_cvt_pk_bf16_f32 v233, v54, v55\n" \
"v_mfma_f32_16x16x32_bf16 v[60:63], a[240:243], v[44:47], v[60:63]\n" \
"s_nop 0\n" \
"v_cvt_pk_bf16_f32 v234, v56, v57\n" \
"v_cvt_pk_bf16_f32 v235, v58, v59\n" \
"v_mfma_f32_16x16x32_bf16 v[64:67], a[244:247], v[44:47], v[64:67]\n" \
"s_waitcnt lgkmcnt(6)\n" \
"ds_write_b128 %[qw], v[232:235] offset:0\n" \
"v_mfma_f32_16x16x32_bf16 v[60:63], a[248:251], v[48:51], v[60:63]\n" \
"s_waitcnt vmcnt(0)\n" \
"v_cmp_gt_u32 vcc, s74, v71\n" \
"v_mfma_f32_16x16x32_bf16 v[64:67], a[252:255], v[48:51], v[64:67]\n" \
"s_nop 2\n" \
"s_cbranch_vccnz Lqa_nokm_%=\n" \
"buffer_load_dwordx4 v[120:123], %[kmo], %[ws], s71 offen offset:0 sc1\n" \
"buffer_load_dwordx4 v[124:127], %[kmo], %[ws], s71 offen offset:512 sc1\n" \
"buffer_load_dwordx4 v[128:131], %[kmo], %[ws], s71 offen offset:1024 sc1\n" \
"buffer_load_dwordx4 v[132:135], %[kmo], %[ws], s71 offen offset:1536 sc1\n" \
"buffer_load_dwordx2 v[136:137], %[vmo], %[ws], s72 offen offset:0 sc1\n" \
"buffer_load_dwordx2 v[138:139], %[vmo], %[ws], s72 offen offset:512 sc1\n" \
"Lqa_tok_%=:\n" \
"s_nop 0\n" \
"v_cvt_pk_bf16_f32 v236, v60, v61\n" \
"v_cvt_pk_bf16_f32 v237, v62, v63\n" \
"s_nop 0\n" \
"v_cvt_pk_bf16_f32 v238, v64, v65\n" \
"v_cvt_pk_bf16_f32 v239, v66, v67\n" \
"ds_write_b128 %[qw], v[236:239] offset:4096\n" \
"s_waitcnt lgkmcnt(0)\n" \
"s_barrier\n" \
"ds_read_b128 v[144:147], %[qr] offset:0\n" \
"ds_read_b128 v[148:151], %[qr] offset:1024\n" \
"ds_read_b128 v[152:155], %[qr] offset:2048\n" \
"ds_read_b128 v[156:159], %[qr] offset:3072\n" \
"s_waitcnt lgkmcnt(3)\n" \
"v_mfma_f32_16x16x32_bf16 v[160:163], v[72:75], v[144:147], v[192:195]\n" \
"v_mfma_f32_16x16x32_bf16 v[164:167], v[88:91], v[144:147], v[196:199]\n" \
"s_waitcnt lgkmcnt(2)\n" \
"v_mfma_f32_16x16x32_bf16 v[160:163], v[76:79], v[148:151], v[160:163]\n" \
"v_mfma_f32_16x16x32_bf16 v[164:167], v[92:95], v[148:151], v[164:167]\n" \
"s_waitcnt lgkmcnt(1)\n" \
"v_mfma_f32_16x16x32_bf16 v[160:163], v[80:83], v[152:155], v[160:163]\n" \
"v_mfma_f32_16x16x32_bf16 v[164:167], v[96:99], v[152:155], v[164:167]\n" \
"s_waitcnt lgkmcnt(0)\n" \
"v_mfma_f32_16x16x32_bf16 v[160:163], v[84:87], v[156:159], v[160:163]\n" \
"v_mfma_f32_16x16x32_bf16 v[164:167], v[100:103], v[156:159], v[164:167]\n" \
"s_nop 6\n" \
"v_max3_f32 v216, v160, v161, v162\n" \
"v_max3_f32 v216, v216, v163, v164\n" \
"v_max3_f32 v216, v216, v165, v166\n" \
"v_max_f32 v216, v216, v167\n" \
"v_mov_b32 v217, v216\n" \
"s_nop 1\n" \
"v_permlane16_swap_b32 v216, v217\n" \
"v_max_f32 v216, v216, v217\n" \
"v_mov_b32 v217, v216\n" \
"s_nop 1\n" \
"v_permlane32_swap_b32 v216, v217\n" \
"v_max_f32 v216, v216, v217\n" \
"v_mul_f32 v217, s73, v216\n" \
"v_fma_f32 v204, v160, s73, -v217\n" \
"v_fma_f32 v205, v161, s73, -v217\n" \
"v_fma_f32 v206, v162, s73, -v217\n" \
"v_fma_f32 v207, v163, s73, -v217\n" \
"v_fma_f32 v208, v164, s73, -v217\n" \
"v_fma_f32 v209, v165, s73, -v217\n" \
"v_fma_f32 v210, v166, s73, -v217\n" \
"v_fma_f32 v211, v167, s73, -v217\n" \
"v_exp_f32 v204, v204\n" \
"v_exp_f32 v205, v205\n" \
"v_exp_f32 v206, v206\n" \
"v_exp_f32 v207, v207\n" \
"v_exp_f32 v208, v208\n" \
"v_exp_f32 v209, v209\n" \
"v_exp_f32 v210, v210\n" \
"v_exp_f32 v211, v211\n" \
"v_cvt_pk_bf16_f32 v228, v204, v205\n" \
"v_cvt_pk_bf16_f32 v229, v206, v207\n" \
"v_cvt_pk_bf16_f32 v230, v208, v209\n" \
"v_cvt_pk_bf16_f32 v231, v210, v211\n" \
"s_nop 1\n" \
"v_mfma_f32_16x16x32_bf16 v[224:227], v[220:223], v[228:231], 0\n" \
"v_mfma_f32_16x16x32_bf16 v[176:179], v[104:107], v[228:231], 0\n" \
"v_mfma_f32_16x16x32_bf16 v[180:183], v[108:111], v[228:231], 0\n" \
"v_mfma_f32_16x16x32_bf16 v[184:187], v[112:115], v[228:231], 0\n" \
"v_mfma_f32_16x16x32_bf16 v[188:191], v[116:119], v[228:231], 0\n" \
"s_cbranch_vccnz Lqa_late_%=\n" \
"Lqa_mem_%=:\n" \
"s_cmp_gt_u32 s65, 104\n" \
"s_cselect_b32 s79, 0, 1073741824\n" \
"v_mov_b32 v69, s65\n" \
"s_add_u32 s74, s74, 1\n" \
"buffer_store_dword v69, %[fo], %[ws], s79 offen sc1\n" \
"Lqa_mem2_%=:\n" \
"s_waitcnt vmcnt(6)\n" \
"v_mfma_f32_16x16x32_bf16 v[168:171], v[120:123], v[144:147], 0\n" \
"v_max3_u32 v240, v120, v121, v122\n" \
"s_waitcnt vmcnt(5)\n" \
"v_mfma_f32_16x16x32_bf16 v[168:171], v[124:127], v[148:151], v[168:171]\n" \
"v_max3_u32 v240, v240, v123, v124\n" \
"v_max3_u32 v240, v240, v125, v126\n" \
"s_waitcnt vmcnt(4)\n" \
"v_mfma_f32_16x16x32_bf16 v[168:171], v[128:131], v[152:155], v[168:171]\n" \
"v_max3_u32 v240, v240, v127, v128\n" \
"v_max3_u32 v240, v240, v129, v130\n" \
"s_waitcnt vmcnt(3)\n" \
"v_mfma_f32_16x16x32_bf16 v[168:171], v[132:135], v[156:159], v[168:171]\n" \
"v_max3_u32 v240, v240, v131, v132\n" \
"v_max3_u32 v240, v240, v133, v134\n" \
"v_cmp_u_f32 vcc, v240, v135\n" \
"s_cbranch_vccnz Lqa_skk_%=\n" \
"Lqa_kk_%=:\n" \
"s_nop 3\n" \
"v_fma_f32 v212, v168, s73, -v217\n" \
"v_fma_f32 v213, v169, s73, -v217\n" \
"v_max_f32 v214, v212, v213\n" \
"v_exp_f32 v212, v212\n" \
"v_exp_f32 v213, v213\n" \
"v_cmp_nge_f32 vcc, 0x41f00000, v214\n" \
"v_cvt_pk_bf16_f32 v200, v212, v213\n" \
"v_cvt_pk_bf16_f32 v203, v212, v213\n" \
"s_cbranch_vccnz Lqa_smax_%=\n" \
"Lqa_cmax_%=:\n" \
"s_waitcnt vmcnt(1)\n" \
"v_max3_u32 v240, v136, v137, v138\n" \
"v_cmp_u_f32 vcc, v240, v139\n" \
"v_mfma_f32_16x16x16_bf16 v[224:227], v[220:221], v[200:201], v[224:227]\n" \
"s_cbranch_vccnz Lqa_skw_%=\n" \
"Lqa_kw_%=:\n" \
"v_mfma_f32_16x16x16_bf16 v[176:179], v[136:137], v[200:201], v[176:179]\n" \
"v_mfma_f32_16x16x16_bf16 v[180:183], v[136:137], v[202:203], v[180:183]\n" \
"v_mfma_f32_16x16x16_bf16 v[184:187], v[138:139], v[200:201], v[184:187]\n" \
"s_nop 0\n" \
"s_nop 2\n" \
"v_rcp_f32 v224, v224\n" \
"v_mfma_f32_16x16x16_bf16 v[188:191], v[138:139], v[202:203], v[188:191]\n" \
"v_pk_mul_f32 v[176:177], v[176:177], v[224:225] op_sel_hi:[1,0]\n" \
"v_pk_mul_f32 v[178:179], v[178:179], v[224:225] op_sel_hi:[1,0]\n" \
"v_pk_mul_f32 v[180:181], v[180:181], v[224:225] op_sel_hi:[1,0]\n" \
"v_pk_mul_f32 v[182:183], v[182:183], v[224:225] op_sel_hi:[1,0]\n" \
"v_cvt_pk_bf16_f32 v176, v176, v177\n" \
"v_cvt_pk_bf16_f32 v177, v178, v179\n" \
"v_cvt_pk_bf16_f32 v178, v180, v181\n" \
"v_cvt_pk_bf16_f32 v179, v182, v183\n" \
"buffer_store_dwordx4 v[176:179], %[lo], %[ws], s68 offen offset:0 sc1\n" \
"v_pk_mul_f32 v[184:185], v[184:185], v[224:225] op_sel_hi:[1,0]\n" \
"v_pk_mul_f32 v[186:187], v[186:187], v[224:225] op_sel_hi:[1,0]\n" \
"v_pk_mul_f32 v[188:189], v[188:189], v[224:225] op_sel_hi:[1,0]\n" \
"v_pk_mul_f32 v[190:191], v[190:191], v[224:225] op_sel_hi:[1,0]\n" \
"v_cvt_pk_bf16_f32 v184, v184, v185\n" \
"v_cvt_pk_bf16_f32 v185, v186, v187\n" \
"v_cvt_pk_bf16_f32 v186, v188, v189\n" \
"v_cvt_pk_bf16_f32 v187, v190, v191\n" \
"buffer_store_dwordx4 v[184:187], %[lo], %[ws], s68 offen offset:1024 sc1\n" \
"s_add_u32 s68, s68, 262144\n" \
"s_add_u32 s71, s71, %[stm]\n" \
"s_add_u32 s72, s72, %[stm]\n" \
"s_cmp_lg_u32 s65, s80\n" \
"s_cbranch_scc1 Lqa_top_%=\n" \
"s_branch Lqa_lim_%=\n" \
"Lqa_end_%=:\n" \
"s_endpgm\n" \
"Lqa_lim_%=:\n" \
"s_cmp_eq_u32 s65, 128\n" \
"s_cbranch_scc1 Lqa_end_%=\n" \
"buffer_store_dwordx4 v[244:247], %[lo], %[ws], s81 offen offset:0 sc1\n" \
"buffer_store_dwordx4 v[244:247], %[lo], %[ws], s81 offen offset:1024 sc1\n" \
"s_add_u32 s81, s81, 262144\n" \
"s_add_u32 s80, s80, 2\n" \
"s_cmp_eq_u32 s80, 72\n" \
"s_cselect_b32 s80, 128, s80\n" \
"s_branch Lqa_top_%=\n" \
"Lqa_skv_%=:\n" \
"Lqa_pkv_%=:\n" \
"s_sub_u32 s64, s64, 1\n" \
"s_cbranch_scc1 Lqa_dead_%=\n" \
"buffer_load_dword v70, %[fkv], %[ws], 0 offen sc1\n" \
"s_waitcnt vmcnt(0)\n" \
"Lqa_ckv_%=:\n" \
"v_cmp_gt_u32 vcc, s74, v70\n" \
"s_cbranch_vccz Lqa_kv_%=\n" \
"s_sleep 1\n" \
"s_branch Lqa_pkv_%=\n" \
"Lqa_nokm_%=:\n" \
"buffer_load_dword v71, %[fm], %[ws], 0 offen sc1\n" \
"s_branch Lqa_tok_%=\n" \
"Lqa_late_%=:\n" \
"s_waitcnt vmcnt(0)\n" \
"s_branch Lqa_ckm_%=\n" \
"Lqa_km_%=:\n" \
"buffer_load_dwordx4 v[120:123], %[kmo], %[ws], s71 offen offset:0 sc1\n" \
"buffer_load_dwordx4 v[124:127], %[kmo], %[ws], s71 offen offset:512 sc1\n" \
"buffer_load_dwordx4 v[128:131], %[kmo], %[ws], s71 offen offset:1024 sc1\n" \
"buffer_load_dwordx4 v[132:135], %[kmo], %[ws], s71 offen offset:1536 sc1\n" \
"buffer_load_dwordx2 v[136:137], %[vmo], %[ws], s72 offen offset:0 sc1\n" \
"buffer_load_dwordx2 v[138:139], %[vmo], %[ws], s72 offen offset:512 sc1\n" \
"s_branch Lqa_mem_%=\n" \
"Lqa_skm_%=:\n" \
"Lqa_pkm_%=:\n" \
"s_sub_u32 s64, s64, 1\n" \
"s_cbranch_scc1 Lqa_dead_%=\n" \
"buffer_load_dword v71, %[fm], %[ws], 0 offen sc1\n" \
"s_waitcnt vmcnt(0)\n" \
"Lqa_ckm_%=:\n" \
"v_cmp_gt_u32 vcc, s74, v71\n" \
"s_cbranch_vccz Lqa_km_%=\n" \
"s_sleep 1\n" \
"s_branch Lqa_pkm_%=\n" \
"Lqa_skk_%=:\n" \
"v_max3_u32 v240, v120, v121, v122\n" \
"v_max3_u32 v240, v240, v123, v124\n" \
"v_max3_u32 v240, v240, v125, v126\n" \
"v_max3_u32 v240, v240, v127, v128\n" \
"v_max3_u32 v240, v240, v129, v130\n" \
"v_max3_u32 v240, v240, v131, v132\n" \
"v_max3_u32 v240, v240, v133, v134\n" \
"v_max3_u32 v240, v240, v135, v135\n" \
"v_cmp_eq_u32 vcc, -1, v240\n" \
"s_nop 1\n" \
"s_cbranch_vccz Lqa_kk_%=\n" \
"Lqa_rkk_%=:\n" \
"s_sub_u32 s64, s64, 1\n" \
"s_cbranch_scc1 Lqa_dead_%=\n" \
"buffer_load_dwordx4 v[120:123], %[kmo], %[ws], s71 offen offset:0 sc1\n" \
"buffer_load_dwordx4 v[124:127], %[kmo], %[ws], s71 offen offset:512 sc1\n" \
"buffer_load_dwordx4 v[128:131], %[kmo], %[ws], s71 offen offset:1024 sc1\n" \
"buffer_load_dwordx4 v[132:135], %[kmo], %[ws], s71 offen offset:1536 sc1\n" \
"buffer_load_dwordx2 v[136:137], %[vmo], %[ws], s72 offen offset:0 sc1\n" \
"buffer_load_dwordx2 v[138:139], %[vmo], %[ws], s72 offen offset:512 sc1\n" \
"s_waitcnt vmcnt(0)\n" \
"v_max3_u32 v240, v120, v121, v122\n" \
"v_max3_u32 v240, v240, v123, v124\n" \
"v_max3_u32 v240, v240, v125, v126\n" \
"v_max3_u32 v240, v240, v127, v128\n" \
"v_max3_u32 v240, v240, v129, v130\n" \
"v_max3_u32 v240, v240, v131, v132\n" \
"v_max3_u32 v240, v240, v133, v134\n" \
"v_max3_u32 v240, v240, v135, v135\n" \
"v_cmp_eq_u32 vcc, -1, v240\n" \
"s_nop 1\n" \
"s_cbranch_vccnz Lqa_rkk_%=\n" \
"s_branch Lqa_mem2_%=\n" \
"Lqa_smax_%=:\n" \
"s_nop 7\n" \
"v_max3_f32 v216, v216, v168, v169\n" \
"v_mov_b32 v217, v216\n" \
"s_nop 1\n" \
"v_permlane16_swap_b32 v216, v217\n" \
"v_max_f32 v216, v216, v217\n" \
"v_mov_b32 v217, v216\n" \
"s_nop 1\n" \
"v_permlane32_swap_b32 v216, v217\n" \
"v_max_f32 v216, v216, v217\n" \
"v_mul_f32 v217, s73, v216\n" \
"v_fma_f32 v204, v160, s73, -v217\n" \
"v_fma_f32 v205, v161, s73, -v217\n" \
"v_fma_f32 v206, v162, s73, -v217\n" \
"v_fma_f32 v207, v163, s73, -v217\n" \
"v_fma_f32 v208, v164, s73, -v217\n" \
"v_fma_f32 v209, v165, s73, -v217\n" \
"v_fma_f32 v210, v166, s73, -v217\n" \
"v_fma_f32 v211, v167, s73, -v217\n" \
"v_exp_f32 v204, v204\n" \
"v_exp_f32 v205, v205\n" \
"v_exp_f32 v206, v206\n" \
"v_exp_f32 v207, v207\n" \
"v_exp_f32 v208, v208\n" \
"v_exp_f32 v209, v209\n" \
"v_exp_f32 v210, v210\n" \
"v_exp_f32 v211, v211\n" \
"v_cvt_pk_bf16_f32 v228, v204, v205\n" \
"v_cvt_pk_bf16_f32 v229, v206, v207\n" \
"v_cvt_pk_bf16_f32 v230, v208, v209\n" \
"v_cvt_pk_bf16_f32 v231, v210, v211\n" \
"s_nop 1\n" \
"v_mfma_f32_16x16x32_bf16 v[224:227], v[220:223], v[228:231], 0\n" \
"v_mfma_f32_16x16x32_bf16 v[176:179], v[104:107], v[228:231], 0\n" \
"v_mfma_f32_16x16x32_bf16 v[180:183], v[108:111], v[228:231], 0\n" \
"v_mfma_f32_16x16x32_bf16 v[184:187], v[112:115], v[228:231], 0\n" \
"v_mfma_f32_16x16x32_bf16 v[188:191], v[116:119], v[228:231], 0\n" \
"v_fma_f32 v212, v168, s73, -v217\n" \
"v_fma_f32 v213, v169, s73, -v217\n" \
"v_max_f32 v214, v212, v213\n" \
"v_exp_f32 v212, v212\n" \
"v_exp_f32 v213, v213\n" \
"v_cmp_nge_f32 vcc, 0x41f00000, v214\n" \
"v_cvt_pk_bf16_f32 v200, v212, v213\n" \
"v_cvt_pk_bf16_f32 v203, v212, v213\n" \
"s_nop 7\n" \
"s_branch Lqa_cmax_%=\n" \
"Lqa_skw_%=:\n" \
"v_max3_u32 v240, v136, v137, v138\n" \
"v_max3_u32 v240, v240, v139, v139\n" \
"v_cmp_eq_u32 vcc, -1, v240\n" \
"s_nop 1\n" \
"s_cbranch_vccz Lqa_kw_%=\n" \
"Lqa_rkw_%=:\n" \
"s_sub_u32 s64, s64, 1\n" \
"s_cbranch_scc1 Lqa_dead_%=\n" \
"buffer_load_dwordx2 v[136:137], %[vmo], %[ws], s72 offen offset:0 sc1\n" \
"buffer_load_dwordx2 v[138:139], %[vmo], %[ws], s72 offen offset:512 sc1\n" \
"s_waitcnt vmcnt(0)\n" \
"v_max3_u32 v240, v136, v137, v138\n" \
"v_max3_u32 v240, v240, v139, v139\n" \
"v_cmp_eq_u32 vcc, -1, v240\n" \
"s_nop 1\n" \
"s_cbranch_vccnz Lqa_rkw_%=\n" \
"s_branch Lqa_kw_%=\n" \
"Lqa_dead_%=:\n" \
"v_mov_b32 v70, 28931\n" \
"buffer_store_dword v70, off, %[ws], 0 sc1\n" \
"s_endpgm\n" \
    :  : [l16] "v"(l16), [wvo] "v"(wvo), [tr0] "v"(tr0), [fo] "v"(fo), [lo] "v"(lo), [dd] "v"(dd), [fkv] "v"(fkv), [fm] "v"(fm), [qw] "v"(qw), [qr] "v"(qr), [kr] "v"(kr), [vr] "v"(vr), [kmo] "v"(kmo), [vmo] "v"(vmo), [ws] "s"(ws), [rw] "s"(rw), [so0] "s"(so0), [wbase] "s"(wbase), [src] "s"(src), [dst] "s"(dst), [d0] "s"(d0), [okx] "s"(okx), [okm] "s"(okm), [ovm] "s"(ovm), [dvx] "s"(dvx), [stm] "s"(stm) \
    : CLOB20)

#define WO_ASM() asm volatile( \
"s_nop 4\n" \
"s_mov_b32 s66, %[d0]\n" \
"Lwo_dly_%=:\n" \
"s_sleep 8\n" \
"s_sub_u32 s66, s66, 1\n" \
"s_cbranch_scc0 Lwo_dly_%=\n" \
"v_xor_b32 v20, 0, %[tr0]\n" \
"v_xor_b32 v21, 64, %[tr0]\n" \
"v_xor_b32 v22, 8, %[tr0]\n" \
"v_xor_b32 v23, 72, %[tr0]\n" \
"v_xor_b32 v24, 128, %[tr0]\n" \
"v_xor_b32 v25, 192, %[tr0]\n" \
"v_xor_b32 v26, 136, %[tr0]\n" \
"v_xor_b32 v27, 200, %[tr0]\n" \
"s_add_u32 m0, %[wbase], 0\n" \
"s_add_u32 s66, %[so0], 0\n" \
"buffer_load_dwordx4 %[wvo], %[rw], s66 offen lds\n" \
"s_add_u32 m0, %[wbase], 1024\n" \
"s_add_u32 s66, %[so0], 8192\n" \
"buffer_load_dwordx4 %[wvo], %[rw], s66 offen lds\n" \
"s_waitcnt vmcnt(1)\n" \
"s_add_u32 m0, %[wbase], 2048\n" \
"s_add_u32 s66, %[so0], 16384\n" \
"buffer_load_dwordx4 %[wvo], %[rw], s66 offen lds\n" \
"s_waitcnt vmcnt(1)\n" \
"s_add_u32 m0, %[wbase], 3072\n" \
"s_add_u32 s66, %[so0], 24576\n" \
"buffer_load_dwordx4 %[wvo], %[rw], s66 offen lds\n" \
"s_waitcnt vmcnt(1)\n" \
"s_add_u32 m0, %[wbase], 4096\n" \
"s_add_u32 s66, %[so0], 32768\n" \
"buffer_load_dwordx4 %[wvo], %[rw], s66 offen lds\n" \
"s_waitcnt vmcnt(1)\n" \
"s_add_u32 m0, %[wbase], 5120\n" \
"s_add_u32 s66, %[so0], 40960\n" \
"buffer_load_dwordx4 %[wvo], %[rw], s66 offen lds\n" \
"s_waitcnt vmcnt(1)\n" \
"s_add_u32 m0, %[wbase], 6144\n" \
"s_add_u32 s66, %[so0], 49152\n" \
"buffer_load_dwordx4 %[wvo], %[rw], s66 offen lds\n" \
"s_waitcnt vmcnt(1)\n" \
"s_add_u32 m0, %[wbase], 7168\n" \
"s_add_u32 s66, %[so0], 57344\n" \
"buffer_load_dwordx4 %[wvo], %[rw], s66 offen lds\n" \
"s_waitcnt vmcnt(1)\n" \
"s_add_u32 m0, %[wbase], 8192\n" \
"s_add_u32 s66, %[so0], 65536\n" \
"buffer_load_dwordx4 %[wvo], %[rw], s66 offen lds\n" \
"s_waitcnt vmcnt(1)\n" \
"s_add_u32 m0, %[wbase], 9216\n" \
"s_add_u32 s66, %[so0], 73728\n" \
"buffer_load_dwordx4 %[wvo], %[rw], s66 offen lds\n" \
"ds_read_b64_tr_b16 a[0:1], v20 offset:0\n" \
"ds_read_b64_tr_b16 a[2:3], v20 offset:1024\n" \
"ds_read_b64_tr_b16 a[4:5], v21 offset:0\n" \
"ds_read_b64_tr_b16 a[6:7], v21 offset:1024\n" \
"ds_read_b64_tr_b16 a[8:9], v22 offset:0\n" \
"ds_read_b64_tr_b16 a[10:11], v22 offset:1024\n" \
"ds_read_b64_tr_b16 a[12:13], v23 offset:0\n" \
"ds_read_b64_tr_b16 a[14:15], v23 offset:1024\n" \
"ds_read_b64_tr_b16 a[16:17], v24 offset:0\n" \
"ds_read_b64_tr_b16 a[18:19], v24 offset:1024\n" \
"ds_read_b64_tr_b16 a[20:21], v25 offset:0\n" \
"ds_read_b64_tr_b16 a[22:23], v25 offset:1024\n" \
"ds_read_b64_tr_b16 a[24:25], v26 offset:0\n" \
"ds_read_b64_tr_b16 a[26:27], v26 offset:1024\n" \
"ds_read_b64_tr_b16 a[28:29], v27 offset:0\n" \
"ds_read_b64_tr_b16 a[30:31], v27 offset:1024\n" \
"s_waitcnt lgkmcnt(0)\n" \
"s_waitcnt vmcnt(1)\n" \
"s_add_u32 m0, %[wbase], 10240\n" \
"s_add_u32 s66, %[so0], 81920\n" \
"buffer_load_dwordx4 %[wvo], %[rw], s66 offen lds\n" \
"s_waitcnt vmcnt(1)\n" \
"s_add_u32 m0, %[wbase], 11264\n" \
"s_add_u32 s66, %[so0], 90112\n" \
"buffer_load_dwordx4 %[wvo], %[rw], s66 offen lds\n" \
"s_waitcnt vmcnt(1)\n" \
"s_add_u32 m0, %[wbase], 12288\n" \
"s_add_u32 s66, %[so0], 98304\n" \
"buffer_load_dwordx4 %[wvo], %[rw], s66 offen lds\n" \
"s_waitcnt vmcnt(1)\n" \
"s_add_u32 m0, %[wbase], 13312\n" \
"s_add_u32 s66, %[so0], 106496\n" \
"buffer_load_dwordx4 %[wvo], %[rw], s66 offen lds\n" \
"s_waitcnt vmcnt(1)\n" \
"s_add_u32 m0, %[wbase], 14336\n" \
"s_add_u32 s66, %[so0], 114688\n" \
"buffer_load_dwordx4 %[wvo], %[rw], s66 offen lds\n" \
"s_waitcnt vmcnt(1)\n" \
"s_add_u32 m0, %[wbase], 15360\n" \
"s_add_u32 s66, %[so0], 122880\n" \
"buffer_load_dwordx4 %[wvo], %[rw], s66 offen lds\n" \
"s_waitcnt vmcnt(1)\n" \
"s_add_u32 m0, %[wbase], 16384\n" \
"s_add_u32 s66, %[so0], 131072\n" \
"buffer_load_dwordx4 %[wvo], %[rw], s66 offen lds\n" \
"s_waitcnt vmcnt(1)\n" \
"s_add_u32 m0, %[wbase], 17408\n" \
"s_add_u32 s66, %[so0], 139264\n" \
"buffer_load_dwordx4 %[wvo], %[rw], s66 offen lds\n" \
"ds_read_b64_tr_b16 a[32:33], v20 offset:8192\n" \
"ds_read_b64_tr_b16 a[34:35], v20 offset:9216\n" \
"ds_read_b64_tr_b16 a[36:37], v21 offset:8192\n" \
"ds_read_b64_tr_b16 a[38:39], v21 offset:9216\n" \
"ds_read_b64_tr_b16 a[40:41], v22 offset:8192\n" \
"ds_read_b64_tr_b16 a[42:43], v22 offset:9216\n" \
"ds_read_b64_tr_b16 a[44:45], v23 offset:8192\n" \
"ds_read_b64_tr_b16 a[46:47], v23 offset:9216\n" \
"ds_read_b64_tr_b16 a[48:49], v24 offset:8192\n" \
"ds_read_b64_tr_b16 a[50:51], v24 offset:9216\n" \
"ds_read_b64_tr_b16 a[52:53], v25 offset:8192\n" \
"ds_read_b64_tr_b16 a[54:55], v25 offset:9216\n" \
"ds_read_b64_tr_b16 a[56:57], v26 offset:8192\n" \
"ds_read_b64_tr_b16 a[58:59], v26 offset:9216\n" \
"ds_read_b64_tr_b16 a[60:61], v27 offset:8192\n" \
"ds_read_b64_tr_b16 a[62:63], v27 offset:9216\n" \
"s_waitcnt lgkmcnt(0)\n" \
"s_waitcnt vmcnt(1)\n" \
"s_add_u32 m0, %[wbase], 18432\n" \
"s_add_u32 s66, %[so0], 147456\n" \
"buffer_load_dwordx4 %[wvo], %[rw], s66 offen lds\n" \
"s_waitcnt vmcnt(1)\n" \
"s_add_u32 m0, %[wbase], 19456\n" \
"s_add_u32 s66, %[so0], 155648\n" \
"buffer_load_dwordx4 %[wvo], %[rw], s66 offen lds\n" \
"s_waitcnt vmcnt(1)\n" \
"s_add_u32 m0, %[wbase], 20480\n" \
"s_add_u32 s66, %[so0], 163840\n" \
"buffer_load_dwordx4 %[wvo], %[rw], s66 offen lds\n" \
"s_waitcnt vmcnt(1)\n" \
"s_add_u32 m0, %[wbase], 21504\n" \
"s_add_u32 s66, %[so0], 172032\n" \
"buffer_load_dwordx4 %[wvo], %[rw], s66 offen lds\n" \
"s_waitcnt vmcnt(1)\n" \
"s_add_u32 m0, %[wbase], 22528\n" \
"s_add_u32 s66, %[so0], 180224\n" \
"buffer_load_dwordx4 %[wvo], %[rw], s66 offen lds\n" \
"s_waitcnt vmcnt(1)\n" \
"s_add_u32 m0, %[wbase], 23552\n" \
"s_add_u32 s66, %[so0], 188416\n" \
"buffer_load_dwordx4 %[wvo], %[rw], s66 offen lds\n" \
"s_waitcnt vmcnt(1)\n" \
"s_add_u32 m0, %[wbase], 24576\n" \
"s_add_u32 s66, %[so0], 196608\n" \
"buffer_load_dwordx4 %[wvo], %[rw], s66 offen lds\n" \
"s_waitcnt vmcnt(1)\n" \
"s_add_u32 m0, %[wbase], 25600\n" \
"s_add_u32 s66, %[so0], 204800\n" \
"buffer_load_dwordx4 %[wvo], %[rw], s66 offen lds\n" \
"ds_read_b64_tr_b16 a[64:65], v20 offset:16384\n" \
"ds_read_b64_tr_b16 a[66:67], v20 offset:17408\n" \
"ds_read_b64_tr_b16 a[68:69], v21 offset:16384\n" \
"ds_read_b64_tr_b16 a[70:71], v21 offset:17408\n" \
"ds_read_b64_tr_b16 a[72:73], v22 offset:16384\n" \
"ds_read_b64_tr_b16 a[74:75], v22 offset:17408\n" \
"ds_read_b64_tr_b16 a[76:77], v23 offset:16384\n" \
"ds_read_b64_tr_b16 a[78:79], v23 offset:17408\n" \
"ds_read_b64_tr_b16 a[80:81], v24 offset:16384\n" \
"ds_read_b64_tr_b16 a[82:83], v24 offset:17408\n" \
"ds_read_b64_tr_b16 a[84:85], v25 offset:16384\n" \
"ds_read_b64_tr_b16 a[86:87], v25 offset:17408\n" \
"ds_read_b64_tr_b16 a[88:89], v26 offset:16384\n" \
"ds_read_b64_tr_b16 a[90:91], v26 offset:17408\n" \
"ds_read_b64_tr_b16 a[92:93], v27 offset:16384\n" \
"ds_read_b64_tr_b16 a[94:95], v27 offset:17408\n" \
"s_waitcnt lgkmcnt(0)\n" \
"s_waitcnt vmcnt(1)\n" \
"s_add_u32 m0, %[wbase], 26624\n" \
"s_add_u32 s66, %[so0], 212992\n" \
"buffer_load_dwordx4 %[wvo], %[rw], s66 offen lds\n" \
"s_waitcnt vmcnt(1)\n" \
"s_add_u32 m0, %[wbase], 27648\n" \
"s_add_u32 s66, %[so0], 221184\n" \
"buffer_load_dwordx4 %[wvo], %[rw], s66 offen lds\n" \
"s_waitcnt vmcnt(1)\n" \
"s_add_u32 m0, %[wbase], 28672\n" \
"s_add_u32 s66, %[so0], 229376\n" \
"buffer_load_dwordx4 %[wvo], %[rw], s66 offen lds\n" \
"s_waitcnt vmcnt(1)\n" \
"s_add_u32 m0, %[wbase], 29696\n" \
"s_add_u32 s66, %[so0], 237568\n" \
"buffer_load_dwordx4 %[wvo], %[rw], s66 offen lds\n" \
"s_waitcnt vmcnt(1)\n" \
"s_add_u32 m0, %[wbase], 30720\n" \
"s_add_u32 s66, %[so0], 245760\n" \
"buffer_load_dwordx4 %[wvo], %[rw], s66 offen lds\n" \
"s_waitcnt vmcnt(1)\n" \
"s_add_u32 m0, %[wbase], 31744\n" \
"s_add_u32 s66, %[so0], 253952\n" \
"buffer_load_dwordx4 %[wvo], %[rw], s66 offen lds\n" \
"s_waitcnt vmcnt(1)\n" \
"s_add_u32 m0, %[wbase], 0\n" \
"s_add_u32 s66, %[so0], 262144\n" \
"buffer_load_dwordx4 %[wvo], %[rw], s66 offen lds\n" \
"s_waitcnt vmcnt(1)\n" \
"s_add_u32 m0, %[wbase], 1024\n" \
"s_add_u32 s66, %[so0], 270336\n" \
"buffer_load_dwordx4 %[wvo], %[rw], s66 offen lds\n" \
"ds_read_b64_tr_b16 a[96:97], v20 offset:24576\n" \
"ds_read_b64_tr_b16 a[98:99], v20 offset:25600\n" \
"ds_read_b64_tr_b16 a[100:101], v21 offset:24576\n" \
"ds_read_b64_tr_b16 a[102:103], v21 offset:25600\n" \
"ds_read_b64_tr_b16 a[104:105], v22 offset:24576\n" \
"ds_read_b64_tr_b16 a[106:107], v22 offset:25600\n" \
"ds_read_b64_tr_b16 a[108:109], v23 offset:24576\n" \
"ds_read_b64_tr_b16 a[110:111], v23 offset:25600\n" \
"ds_read_b64_tr_b16 a[112:113], v24 offset:24576\n" \
"ds_read_b64_tr_b16 a[114:115], v24 offset:25600\n" \
"ds_read_b64_tr_b16 a[116:117], v25 offset:24576\n" \
"ds_read_b64_tr_b16 a[118:119], v25 offset:25600\n" \
"ds_read_b64_tr_b16 a[120:121], v26 offset:24576\n" \
"ds_read_b64_tr_b16 a[122:123], v26 offset:25600\n" \
"ds_read_b64_tr_b16 a[124:125], v27 offset:24576\n" \
"ds_read_b64_tr_b16 a[126:127], v27 offset:25600\n" \
"s_waitcnt lgkmcnt(0)\n" \
"s_waitcnt vmcnt(1)\n" \
"s_add_u32 m0, %[wbase], 2048\n" \
"s_add_u32 s66, %[so0], 278528\n" \
"buffer_load_dwordx4 %[wvo], %[rw], s66 offen lds\n" \
"s_waitcnt vmcnt(1)\n" \
"s_add_u32 m0, %[wbase], 3072\n" \
"s_add_u32 s66, %[so0], 286720\n" \
"buffer_load_dwordx4 %[wvo], %[rw], s66 offen lds\n" \
"s_waitcnt vmcnt(1)\n" \
"s_add_u32 m0, %[wbase], 4096\n" \
"s_add_u32 s66, %[so0], 294912\n" \
"buffer_load_dwordx4 %[wvo], %[rw], s66 offen lds\n" \
"s_waitcnt vmcnt(1)\n" \
"s_add_u32 m0, %[wbase], 5120\n" \
"s_add_u32 s66, %[so0], 303104\n" \
"buffer_load_dwordx4 %[wvo], %[rw], s66 offen lds\n" \
"s_waitcnt vmcnt(1)\n" \
"s_add_u32 m0, %[wbase], 6144\n" \
"s_add_u32 s66, %[so0], 311296\n" \
"buffer_load_dwordx4 %[wvo], %[rw], s66 offen lds\n" \
"s_waitcnt vmcnt(1)\n" \
"s_add_u32 m0, %[wbase], 7168\n" \
"s_add_u32 s66, %[so0], 319488\n" \
"buffer_load_dwordx4 %[wvo], %[rw], s66 offen lds\n" \
"s_waitcnt vmcnt(1)\n" \
"s_add_u32 m0, %[wbase], 8192\n" \
"s_add_u32 s66, %[so0], 327680\n" \
"buffer_load_dwordx4 %[wvo], %[rw], s66 offen lds\n" \
"s_waitcnt vmcnt(1)\n" \
"s_add_u32 m0, %[wbase], 9216\n" \
"s_add_u32 s66, %[so0], 335872\n" \
"buffer_load_dwordx4 %[wvo], %[rw], s66 offen lds\n" \
"ds_read_b64_tr_b16 a[128:129], v20 offset:0\n" \
"ds_read_b64_tr_b16 a[130:131], v20 offset:1024\n" \
"ds_read_b64_tr_b16 a[132:133], v21 offset:0\n" \
"ds_read_b64_tr_b16 a[134:135], v21 offset:1024\n" \
"ds_read_b64_tr_b16 a[136:137], v22 offset:0\n" \
"ds_read_b64_tr_b16 a[138:139], v22 offset:1024\n" \
"ds_read_b64_tr_b16 a[140:141], v23 offset:0\n" \
"ds_read_b64_tr_b16 a[142:143], v23 offset:1024\n" \
"ds_read_b64_tr_b16 a[144:145], v24 offset:0\n" \
"ds_read_b64_tr_b16 a[146:147], v24 offset:1024\n" \
"ds_read_b64_tr_b16 a[148:149], v25 offset:0\n" \
"ds_read_b64_tr_b16 a[150:151], v25 offset:1024\n" \
"ds_read_b64_tr_b16 a[152:153], v26 offset:0\n" \
"ds_read_b64_tr_b16 a[154:155], v26 offset:1024\n" \
"ds_read_b64_tr_b16 a[156:157], v27 offset:0\n" \
"ds_read_b64_tr_b16 a[158:159], v27 offset:1024\n" \
"s_waitcnt lgkmcnt(0)\n" \
"s_waitcnt vmcnt(1)\n" \
"s_add_u32 m0, %[wbase], 10240\n" \
"s_add_u32 s66, %[so0], 344064\n" \
"buffer_load_dwordx4 %[wvo], %[rw], s66 offen lds\n" \
"s_waitcnt vmcnt(1)\n" \
"s_add_u32 m0, %[wbase], 11264\n" \
"s_add_u32 s66, %[so0], 352256\n" \
"buffer_load_dwordx4 %[wvo], %[rw], s66 offen lds\n" \
"s_waitcnt vmcnt(1)\n" \
"s_add_u32 m0, %[wbase], 12288\n" \
"s_add_u32 s66, %[so0], 360448\n" \
"buffer_load_dwordx4 %[wvo], %[rw], s66 offen lds\n" \
"s_waitcnt vmcnt(1)\n" \
"s_add_u32 m0, %[wbase], 13312\n" \
"s_add_u32 s66, %[so0], 368640\n" \
"buffer_load_dwordx4 %[wvo], %[rw], s66 offen lds\n" \
"s_waitcnt vmcnt(1)\n" \
"s_add_u32 m0, %[wbase], 14336\n" \
"s_add_u32 s66, %[so0], 376832\n" \
"buffer_load_dwordx4 %[wvo], %[rw], s66 offen lds\n" \
"s_waitcnt vmcnt(1)\n" \
"s_add_u32 m0, %[wbase], 15360\n" \
"s_add_u32 s66, %[so0], 385024\n" \
"buffer_load_dwordx4 %[wvo], %[rw], s66 offen lds\n" \
"s_waitcnt vmcnt(1)\n" \
"s_add_u32 m0, %[wbase], 16384\n" \
"s_add_u32 s66, %[so0], 393216\n" \
"buffer_load_dwordx4 %[wvo], %[rw], s66 offen lds\n" \
"s_waitcnt vmcnt(1)\n" \
"s_add_u32 m0, %[wbase], 17408\n" \
"s_add_u32 s66, %[so0], 401408\n" \
"buffer_load_dwordx4 %[wvo], %[rw], s66 offen lds\n" \
"ds_read_b64_tr_b16 a[160:161], v20 offset:8192\n" \
"ds_read_b64_tr_b16 a[162:163], v20 offset:9216\n" \
"ds_read_b64_tr_b16 a[164:165], v21 offset:8192\n" \
"ds_read_b64_tr_b16 a[166:167], v21 offset:9216\n" \
"ds_read_b64_tr_b16 a[168:169], v22 offset:8192\n" \
"ds_read_b64_tr_b16 a[170:171], v22 offset:9216\n" \
"ds_read_b64_tr_b16 a[172:173], v23 offset:8192\n" \
"ds_read_b64_tr_b16 a[174:175], v23 offset:9216\n" \
"ds_read_b64_tr_b16 a[176:177], v24 offset:8192\n" \
"ds_read_b64_tr_b16 a[178:179], v24 offset:9216\n" \
"ds_read_b64_tr_b16 a[180:181], v25 offset:8192\n" \
"ds_read_b64_tr_b16 a[182:183], v25 offset:9216\n" \
"ds_read_b64_tr_b16 a[184:185], v26 offset:8192\n" \
"ds_read_b64_tr_b16 a[186:187], v26 offset:9216\n" \
"ds_read_b64_tr_b16 a[188:189], v27 offset:8192\n" \
"ds_read_b64_tr_b16 a[190:191], v27 offset:9216\n" \
"s_waitcnt lgkmcnt(0)\n" \
"s_waitcnt vmcnt(1)\n" \
"s_add_u32 m0, %[wbase], 18432\n" \
"s_add_u32 s66, %[so0], 409600\n" \
"buffer_load_dwordx4 %[wvo], %[rw], s66 offen lds\n" \
"s_waitcnt vmcnt(1)\n" \
"s_add_u32 m0, %[wbase], 19456\n" \
"s_add_u32 s66, %[so0], 417792\n" \
"buffer_load_dwordx4 %[wvo], %[rw], s66 offen lds\n" \
"s_waitcnt vmcnt(1)\n" \
"s_add_u32 m0, %[wbase], 20480\n" \
"s_add_u32 s66, %[so0], 425984\n" \
"buffer_load_dwordx4 %[wvo], %[rw], s66 offen lds\n" \
"s_waitcnt vmcnt(1)\n" \
"s_add_u32 m0, %[wbase], 21504\n" \
"s_add_u32 s66, %[so0], 434176\n" \
"buffer_load_dwordx4 %[wvo], %[rw], s66 offen lds\n" \
"s_waitcnt vmcnt(1)\n" \
"s_add_u32 m0, %[wbase], 22528\n" \
"s_add_u32 s66, %[so0], 442368\n" \
"buffer_load_dwordx4 %[wvo], %[rw], s66 offen lds\n" \
"s_waitcnt vmcnt(1)\n" \
"s_add_u32 m0, %[wbase], 23552\n" \
"s_add_u32 s66, %[so0], 450560\n" \
"buffer_load_dwordx4 %[wvo], %[rw], s66 offen lds\n" \
"s_waitcnt vmcnt(1)\n" \
"s_add_u32 m0, %[wbase], 24576\n" \
"s_add_u32 s66, %[so0], 458752\n" \
"buffer_load_dwordx4 %[wvo], %[rw], s66 offen lds\n" \
"s_waitcnt vmcnt(1)\n" \
"s_add_u32 m0, %[wbase], 25600\n" \
"s_add_u32 s66, %[so0], 466944\n" \
"buffer_load_dwordx4 %[wvo], %[rw], s66 offen lds\n" \
"ds_read_b64_tr_b16 a[192:193], v20 offset:16384\n" \
"ds_read_b64_tr_b16 a[194:195], v20 offset:17408\n" \
"ds_read_b64_tr_b16 a[196:197], v21 offset:16384\n" \
"ds_read_b64_tr_b16 a[198:199], v21 offset:17408\n" \
"ds_read_b64_tr_b16 a[200:201], v22 offset:16384\n" \
"ds_read_b64_tr_b16 a[202:203], v22 offset:17408\n" \
"ds_read_b64_tr_b16 a[204:205], v23 offset:16384\n" \
"ds_read_b64_tr_b16 a[206:207], v23 offset:17408\n" \
"ds_read_b64_tr_b16 a[208:209], v24 offset:16384\n" \
"ds_read_b64_tr_b16 a[210:211], v24 offset:17408\n" \
"ds_read_b64_tr_b16 a[212:213], v25 offset:16384\n" \
"ds_read_b64_tr_b16 a[214:215], v25 offset:17408\n" \
"ds_read_b64_tr_b16 a[216:217], v26 offset:16384\n" \
"ds_read_b64_tr_b16 a[218:219], v26 offset:17408\n" \
"ds_read_b64_tr_b16 a[220:221], v27 offset:16384\n" \
"ds_read_b64_tr_b16 a[222:223], v27 offset:17408\n" \
"s_waitcnt lgkmcnt(0)\n" \
"s_waitcnt vmcnt(1)\n" \
"s_add_u32 m0, %[wbase], 26624\n" \
"s_add_u32 s66, %[so0], 475136\n" \
"buffer_load_dwordx4 %[wvo], %[rw], s66 offen lds\n" \
"s_waitcnt vmcnt(1)\n" \
"s_add_u32 m0, %[wbase], 27648\n" \
"s_add_u32 s66, %[so0], 483328\n" \
"buffer_load_dwordx4 %[wvo], %[rw], s66 offen lds\n" \
"s_waitcnt vmcnt(1)\n" \
"s_add_u32 m0, %[wbase], 28672\n" \
"s_add_u32 s66, %[so0], 491520\n" \
"buffer_load_dwordx4 %[wvo], %[rw], s66 offen lds\n" \
"s_waitcnt vmcnt(1)\n" \
"s_add_u32 m0, %[wbase], 29696\n" \
"s_add_u32 s66, %[so0], 499712\n" \
"buffer_load_dwordx4 %[wvo], %[rw], s66 offen lds\n" \
"s_waitcnt vmcnt(1)\n" \
"s_add_u32 m0, %[wbase], 30720\n" \
"s_add_u32 s66, %[so0], 507904\n" \
"buffer_load_dwordx4 %[wvo], %[rw], s66 offen lds\n" \
"s_waitcnt vmcnt(1)\n" \
"s_add_u32 m0, %[wbase], 31744\n" \
"s_add_u32 s66, %[so0], 516096\n" \
"buffer_load_dwordx4 %[wvo], %[rw], s66 offen lds\n" \
"s_waitcnt vmcnt(0)\n" \
"ds_read_b64_tr_b16 a[224:225], v20 offset:24576\n" \
"ds_read_b64_tr_b16 a[226:227], v20 offset:25600\n" \
"ds_read_b64_tr_b16 a[228:229], v21 offset:24576\n" \
"ds_read_b64_tr_b16 a[230:231], v21 offset:25600\n" \
"ds_read_b64_tr_b16 a[232:233], v22 offset:24576\n" \
"ds_read_b64_tr_b16 a[234:235], v22 offset:25600\n" \
"ds_read_b64_tr_b16 a[236:237], v23 offset:24576\n" \
"ds_read_b64_tr_b16 a[238:239], v23 offset:25600\n" \
"ds_read_b64_tr_b16 a[240:241], v24 offset:24576\n" \
"ds_read_b64_tr_b16 a[242:243], v24 offset:25600\n" \
"ds_read_b64_tr_b16 a[244:245], v25 offset:24576\n" \
"ds_read_b64_tr_b16 a[246:247], v25 offset:25600\n" \
"ds_read_b64_tr_b16 a[248:249], v26 offset:24576\n" \
"ds_read_b64_tr_b16 a[250:251], v26 offset:25600\n" \
"ds_read_b64_tr_b16 a[252:253], v27 offset:24576\n" \
"ds_read_b64_tr_b16 a[254:255], v27 offset:25600\n" \
"s_waitcnt lgkmcnt(0)\n" \
"v_xor_b32 v212, 8192, %[xr]\n" \
"v_xor_b32 v213, 16384, %[xr]\n" \
"v_xor_b32 v214, 24576, %[xr]\n" \
"s_barrier\n" \
"s_add_u32 s81, %[dst], 13369344\n" \
"v_mov_b32 v220, -1\n" \
"v_mov_b32 v221, -1\n" \
"v_mov_b32 v222, -1\n" \
"v_mov_b32 v223, -1\n" \
"s_mov_b32 s80, 48\n" \
"s_mov_b32 s64, 262144\n" \
"s_mov_b32 s65, 0\n" \
"s_mov_b32 s74, 1\n" \
"s_mov_b32 s69, 1073741824\n" \
"s_mov_b32 s67, %[src]\n" \
"s_mov_b32 s68, %[dst]\n" \
"v_mov_b32 v202, 0\n" \
"v_mov_b32 v203, 0\n" \
"Lwo_top0_%=:\n" \
"v_cmp_gt_u32 vcc, s74, v202\n" \
"s_mov_b32 s72, s67\n" \
"s_add_u32 s73, s67, 4096\n" \
"s_cbranch_vccnz Lwo_sgo0_%=\n" \
"Lwo_go0_%=:\n" \
"buffer_load_dwordx4 v[20:23], %[lo], %[ws], s72 offen offset:0 sc1\n" \
"buffer_load_dwordx4 v[24:27], %[lo], %[ws], s72 offen offset:1024 sc1\n" \
"buffer_load_dwordx4 v[28:31], %[lo], %[ws], s72 offen offset:2048 sc1\n" \
"buffer_load_dwordx4 v[32:35], %[lo], %[ws], s72 offen offset:3072 sc1\n" \
"buffer_load_dwordx4 v[36:39], %[lo], %[ws], s73 offen offset:0 sc1\n" \
"buffer_load_dwordx4 v[40:43], %[lo], %[ws], s73 offen offset:1024 sc1\n" \
"buffer_load_dwordx4 v[44:47], %[lo], %[ws], s73 offen offset:2048 sc1\n" \
"buffer_load_dwordx4 v[48:51], %[lo], %[ws], s73 offen offset:3072 sc1\n" \
"s_waitcnt vmcnt(8)\n" \
"buffer_store_dword v203, %[fo], %[ws], s69 offen sc1\n" \
"s_mov_b32 s69, 1073741824\n" \
"Lwo_body0_%=:\n" \
"s_waitcnt vmcnt(8)\n" \
"v_mfma_f32_16x16x32_bf16 v[108:111], a[8:11], v[20:23], 0\n" \
"s_nop 2\n" \
"v_mfma_f32_16x16x32_bf16 v[112:115], a[12:15], v[20:23], 0\n" \
"s_nop 2\n" \
"v_mfma_f32_16x16x32_bf16 v[116:119], a[16:19], v[20:23], 0\n" \
"s_add_u32 s74, s74, 1\n" \
"s_add_u32 s67, s67, 131072\n" \
"s_nop 0\n" \
"v_mfma_f32_16x16x32_bf16 v[120:123], a[20:23], v[20:23], 0\n" \
"s_add_u32 s65, s65, 1\n" \
"s_nop 1\n" \
"v_mfma_f32_16x16x32_bf16 v[124:127], a[24:27], v[20:23], 0\n" \
"s_cmp_gt_u32 s65, 104\n" \
"s_cselect_b32 s79, 1073741824, 0\n" \
"s_cselect_b32 s69, 0, s69\n" \
"v_mfma_f32_16x16x32_bf16 v[128:131], a[28:31], v[20:23], 0\n" \
"s_waitcnt vmcnt(7)\n" \
"s_addc_u32 s66, s65, -1\n" \
"s_nop 0\n" \
"v_mfma_f32_16x16x32_bf16 v[108:111], a[40:43], v[24:27], v[108:111]\n" \
"s_nop 0\n" \
"buffer_load_dword v202, %[fvo], %[ws], 0 offen sc1\n" \
"v_mov_b32 v203, s66\n" \
"v_mfma_f32_16x16x32_bf16 v[112:115], a[44:47], v[24:27], v[112:115]\n" \
"s_nop 2\n" \
"v_mfma_f32_16x16x32_bf16 v[116:119], a[48:51], v[24:27], v[116:119]\n" \
"s_nop 2\n" \
"v_mfma_f32_16x16x32_bf16 v[120:123], a[52:55], v[24:27], v[120:123]\n" \
"s_nop 2\n" \
"v_mfma_f32_16x16x32_bf16 v[124:127], a[56:59], v[24:27], v[124:127]\n" \
"s_nop 2\n" \
"v_mfma_f32_16x16x32_bf16 v[128:131], a[60:63], v[24:27], v[128:131]\n" \
"s_waitcnt vmcnt(7)\n" \
"s_nop 1\n" \
"v_mfma_f32_16x16x32_bf16 v[108:111], a[72:75], v[28:31], v[108:111]\n" \
"s_nop 2\n" \
"v_mfma_f32_16x16x32_bf16 v[112:115], a[76:79], v[28:31], v[112:115]\n" \
"s_nop 2\n" \
"v_mfma_f32_16x16x32_bf16 v[116:119], a[80:83], v[28:31], v[116:119]\n" \
"s_nop 2\n" \
"v_mfma_f32_16x16x32_bf16 v[120:123], a[84:87], v[28:31], v[120:123]\n" \
"s_nop 2\n" \
"v_mfma_f32_16x16x32_bf16 v[124:127], a[88:91], v[28:31], v[124:127]\n" \
"s_nop 2\n" \
"v_mfma_f32_16x16x32_bf16 v[128:131], a[92:95], v[28:31], v[128:131]\n" \
"s_waitcnt vmcnt(6)\n" \
"s_nop 1\n" \
"v_mfma_f32_16x16x32_bf16 v[108:111], a[104:107], v[32:35], v[108:111]\n" \
"s_nop 2\n" \
"v_mfma_f32_16x16x32_bf16 v[112:115], a[108:111], v[32:35], v[112:115]\n" \
"s_nop 2\n" \
"v_mfma_f32_16x16x32_bf16 v[116:119], a[112:115], v[32:35], v[116:119]\n" \
"s_nop 2\n" \
"v_mfma_f32_16x16x32_bf16 v[120:123], a[116:119], v[32:35], v[120:123]\n" \
"s_nop 2\n" \
"v_mfma_f32_16x16x32_bf16 v[124:127], a[120:123], v[32:35], v[124:127]\n" \
"s_nop 2\n" \
"v_mfma_f32_16x16x32_bf16 v[128:131], a[124:127], v[32:35], v[128:131]\n" \
"s_waitcnt vmcnt(5)\n" \
"s_nop 1\n" \
"v_mfma_f32_16x16x32_bf16 v[108:111], a[136:139], v[36:39], v[108:111]\n" \
"s_nop 2\n" \
"v_mfma_f32_16x16x32_bf16 v[112:115], a[140:143], v[36:39], v[112:115]\n" \
"s_nop 2\n" \
"v_mfma_f32_16x16x32_bf16 v[116:119], a[144:147], v[36:39], v[116:119]\n" \
"s_nop 2\n" \
"v_mfma_f32_16x16x32_bf16 v[120:123], a[148:151], v[36:39], v[120:123]\n" \
"s_nop 2\n" \
"v_mfma_f32_16x16x32_bf16 v[124:127], a[152:155], v[36:39], v[124:127]\n" \
"s_nop 2\n" \
"v_mfma_f32_16x16x32_bf16 v[128:131], a[156:159], v[36:39], v[128:131]\n" \
"s_waitcnt vmcnt(4)\n" \
"s_nop 1\n" \
"v_mfma_f32_16x16x32_bf16 v[108:111], a[168:171], v[40:43], v[108:111]\n" \
"s_waitcnt vmcnt(3)\n" \
"s_nop 1\n" \
"v_mfma_f32_16x16x32_bf16 v[108:111], a[200:203], v[44:47], v[108:111]\n" \
"s_waitcnt vmcnt(2)\n" \
"s_nop 1\n" \
"v_mfma_f32_16x16x32_bf16 v[108:111], a[232:235], v[48:51], v[108:111]\n" \
"s_nop 2\n" \
"v_mfma_f32_16x16x32_bf16 v[112:115], a[172:175], v[40:43], v[112:115]\n" \
"s_nop 2\n" \
"v_mfma_f32_16x16x32_bf16 v[112:115], a[204:207], v[44:47], v[112:115]\n" \
"s_nop 0\n" \
"ds_write_b128 v212, v[108:111] offset:2048\n" \
"v_mfma_f32_16x16x32_bf16 v[112:115], a[236:239], v[48:51], v[112:115]\n" \
"s_nop 0\n" \
"v_cmp_u_f32 vcc, v108, v108\n" \
"v_mfma_f32_16x16x32_bf16 v[116:119], a[176:179], v[40:43], v[116:119]\n" \
"s_cbranch_vccnz Lwo_sn0_%=\n" \
"Lwo_n0_%=:\n" \
"v_mfma_f32_16x16x32_bf16 v[116:119], a[208:211], v[44:47], v[116:119]\n" \
"s_nop 0\n" \
"s_nop 1\n" \
"ds_write_b128 v212, v[112:115] offset:3072\n" \
"v_mfma_f32_16x16x32_bf16 v[116:119], a[240:243], v[48:51], v[116:119]\n" \
"s_nop 2\n" \
"v_mfma_f32_16x16x32_bf16 v[120:123], a[180:183], v[40:43], v[120:123]\n" \
"s_nop 2\n" \
"v_mfma_f32_16x16x32_bf16 v[120:123], a[212:215], v[44:47], v[120:123]\n" \
"s_nop 0\n" \
"ds_write_b128 v213, v[116:119] offset:4096\n" \
"v_mfma_f32_16x16x32_bf16 v[120:123], a[244:247], v[48:51], v[120:123]\n" \
"s_waitcnt vmcnt(0)\n" \
"v_cmp_gt_u32 vcc, s74, v202\n" \
"v_mfma_f32_16x16x32_bf16 v[124:127], a[184:187], v[40:43], v[124:127]\n" \
"s_nop 0\n" \
"buffer_load_dword v202, %[fvo], %[ws], 0 offen sc1\n" \
"buffer_store_dword v203, %[fo], %[ws], s69 offen sc1\n" \
"v_mfma_f32_16x16x32_bf16 v[124:127], a[216:219], v[44:47], v[124:127]\n" \
"s_cmp_eq_u64 vcc, 0\n" \
"ds_write_b128 v213, v[120:123] offset:5120\n" \
"v_mfma_f32_16x16x32_bf16 v[124:127], a[248:251], v[48:51], v[124:127]\n" \
"s_cselect_b32 s72, s67, 1073741824\n" \
"buffer_load_dwordx4 v[52:55], %[lo], %[ws], s72 offen offset:0 sc1\n" \
"v_mfma_f32_16x16x32_bf16 v[128:131], a[188:191], v[40:43], v[128:131]\n" \
"s_add_u32 s73, s72, 4096\n" \
"s_nop 1\n" \
"v_mfma_f32_16x16x32_bf16 v[128:131], a[220:223], v[44:47], v[128:131]\n" \
"s_nop 0\n" \
"ds_write_b128 v214, v[124:127] offset:6144\n" \
"v_mfma_f32_16x16x32_bf16 v[128:131], a[252:255], v[48:51], v[128:131]\n" \
"s_waitcnt lgkmcnt(1)\n" \
"s_barrier\n" \
"v_mfma_f32_16x16x32_bf16 v[100:103], a[0:3], v[20:23], 0\n" \
"s_nop 0\n" \
"ds_read_b128 v[132:135], %[xr] offset:2048\n" \
"buffer_load_dwordx4 v[56:59], %[lo], %[ws], s72 offen offset:1024 sc1\n" \
"v_mfma_f32_16x16x32_bf16 v[104:107], a[4:7], v[20:23], 0\n" \
"s_nop 0\n" \
"ds_write_b128 v214, v[128:131] offset:7168\n" \
"v_mfma_f32_16x16x32_bf16 v[100:103], a[32:35], v[24:27], v[100:103]\n" \
"s_nop 0\n" \
"ds_read_b128 v[136:139], %[xr] offset:3072\n" \
"v_mfma_f32_16x16x32_bf16 v[104:107], a[36:39], v[24:27], v[104:107]\n" \
"s_nop 0\n" \
"ds_read_b128 v[140:143], %[xr] offset:4096\n" \
"v_mfma_f32_16x16x32_bf16 v[100:103], a[64:67], v[28:31], v[100:103]\n" \
"s_nop 0\n" \
"ds_read_b128 v[144:147], %[xr] offset:5120\n" \
"buffer_load_dwordx4 v[60:63], %[lo], %[ws], s72 offen offset:2048 sc1\n" \
"v_mfma_f32_16x16x32_bf16 v[104:107], a[68:71], v[28:31], v[104:107]\n" \
"s_waitcnt lgkmcnt(3)\n" \
"s_barrier\n" \
"v_mfma_f32_16x16x32_bf16 v[100:103], a[96:99], v[32:35], v[100:103]\n" \
"s_nop 0\n" \
"ds_read_b128 v[148:151], %[xr] offset:6144\n" \
"v_mfma_f32_16x16x32_bf16 v[104:107], a[100:103], v[32:35], v[104:107]\n" \
"s_nop 0\n" \
"ds_read_b128 v[152:155], %[xr] offset:7168\n" \
"v_mfma_f32_16x16x32_bf16 v[100:103], a[128:131], v[36:39], v[100:103]\n" \
"s_waitcnt lgkmcnt(3)\n" \
"buffer_load_dwordx4 v[64:67], %[lo], %[ws], s72 offen offset:3072 sc1\n" \
"v_add_f32 v132, v132, v140\n" \
"v_add_f32 v133, v133, v141\n" \
"v_mfma_f32_16x16x32_bf16 v[104:107], a[132:135], v[36:39], v[104:107]\n" \
"s_nop 0\n" \
"v_add_f32 v134, v134, v142\n" \
"v_add_f32 v135, v135, v143\n" \
"v_mfma_f32_16x16x32_bf16 v[100:103], a[160:163], v[40:43], v[100:103]\n" \
"s_waitcnt lgkmcnt(2)\n" \
"v_add_f32 v136, v136, v144\n" \
"v_add_f32 v137, v137, v145\n" \
"v_mfma_f32_16x16x32_bf16 v[104:107], a[164:167], v[40:43], v[104:107]\n" \
"s_nop 0\n" \
"v_add_f32 v138, v138, v146\n" \
"v_add_f32 v139, v139, v147\n" \
"v_mfma_f32_16x16x32_bf16 v[100:103], a[192:195], v[44:47], v[100:103]\n" \
"s_waitcnt lgkmcnt(1)\n" \
"buffer_load_dwordx4 v[68:71], %[lo], %[ws], s73 offen offset:0 sc1\n" \
"v_add_f32 v132, v132, v148\n" \
"v_add_f32 v133, v133, v149\n" \
"v_mfma_f32_16x16x32_bf16 v[104:107], a[196:199], v[44:47], v[104:107]\n" \
"s_nop 0\n" \
"v_add_f32 v134, v134, v150\n" \
"v_add_f32 v135, v135, v151\n" \
"v_mfma_f32_16x16x32_bf16 v[100:103], a[224:227], v[48:51], v[100:103]\n" \
"s_waitcnt lgkmcnt(0)\n" \
"v_add_f32 v136, v136, v152\n" \
"v_add_f32 v137, v137, v153\n" \
"v_mfma_f32_16x16x32_bf16 v[104:107], a[228:231], v[48:51], v[104:107]\n" \
"s_nop 0\n" \
"v_add_f32 v138, v138, v154\n" \
"v_add_f32 v139, v139, v155\n" \
"v_mov_b32 v203, s65\n" \
"s_mov_b32 s69, s79\n" \
"buffer_load_dwordx4 v[72:75], %[lo], %[ws], s73 offen offset:1024 sc1\n" \
"v_pk_add_f32 v[100:101], v[100:101], v[132:133]\n" \
"v_pk_add_f32 v[102:103], v[102:103], v[134:135]\n" \
"v_pk_add_f32 v[104:105], v[104:105], v[136:137]\n" \
"v_pk_add_f32 v[106:107], v[106:107], v[138:139]\n" \
"buffer_load_dwordx4 v[76:79], %[lo], %[ws], s73 offen offset:2048 sc1\n" \
"buffer_load_dwordx4 v[80:83], %[lo], %[ws], s73 offen offset:3072 sc1\n" \
"v_cvt_pk_bf16_f32 v204, v100, v101\n" \
"v_cvt_pk_bf16_f32 v205, v102, v103\n" \
"v_cvt_pk_bf16_f32 v206, v104, v105\n" \
"v_cvt_pk_bf16_f32 v207, v106, v107\n" \
"buffer_store_dwordx4 v[204:207], %[so], %[ws], s68 offen sc1\n" \
"s_add_u32 s68, s68, 131072\n" \
"s_cmp_eq_u32 s72, s67\n" \
"s_cbranch_scc1 Lwo_body1_%=\n" \
"s_waitcnt vmcnt(10)\n" \
"Lwo_top1_%=:\n" \
"v_cmp_gt_u32 vcc, s74, v202\n" \
"s_mov_b32 s72, s67\n" \
"s_add_u32 s73, s67, 4096\n" \
"s_cbranch_vccnz Lwo_sgo1_%=\n" \
"Lwo_go1_%=:\n" \
"buffer_load_dwordx4 v[52:55], %[lo], %[ws], s72 offen offset:0 sc1\n" \
"buffer_load_dwordx4 v[56:59], %[lo], %[ws], s72 offen offset:1024 sc1\n" \
"buffer_load_dwordx4 v[60:63], %[lo], %[ws], s72 offen offset:2048 sc1\n" \
"buffer_load_dwordx4 v[64:67], %[lo], %[ws], s72 offen offset:3072 sc1\n" \
"buffer_load_dwordx4 v[68:71], %[lo], %[ws], s73 offen offset:0 sc1\n" \
"buffer_load_dwordx4 v[72:75], %[lo], %[ws], s73 offen offset:1024 sc1\n" \
"buffer_load_dwordx4 v[76:79], %[lo], %[ws], s73 offen offset:2048 sc1\n" \
"buffer_load_dwordx4 v[80:83], %[lo], %[ws], s73 offen offset:3072 sc1\n" \
"s_waitcnt vmcnt(8)\n" \
"buffer_store_dword v203, %[fo], %[ws], s69 offen sc1\n" \
"s_mov_b32 s69, 1073741824\n" \
"Lwo_body1_%=:\n" \
"s_waitcnt vmcnt(8)\n" \
"v_mfma_f32_16x16x32_bf16 v[108:111], a[8:11], v[52:55], 0\n" \
"s_nop 2\n" \
"v_mfma_f32_16x16x32_bf16 v[112:115], a[12:15], v[52:55], 0\n" \
"s_nop 2\n" \
"v_mfma_f32_16x16x32_bf16 v[116:119], a[16:19], v[52:55], 0\n" \
"s_add_u32 s74, s74, 1\n" \
"s_add_u32 s67, s67, 131072\n" \
"s_nop 0\n" \
"v_mfma_f32_16x16x32_bf16 v[120:123], a[20:23], v[52:55], 0\n" \
"s_add_u32 s65, s65, 1\n" \
"s_nop 1\n" \
"v_mfma_f32_16x16x32_bf16 v[124:127], a[24:27], v[52:55], 0\n" \
"s_cmp_gt_u32 s65, 104\n" \
"s_cselect_b32 s79, 1073741824, 0\n" \
"s_cselect_b32 s69, 0, s69\n" \
"v_mfma_f32_16x16x32_bf16 v[128:131], a[28:31], v[52:55], 0\n" \
"s_waitcnt vmcnt(7)\n" \
"s_addc_u32 s66, s65, -1\n" \
"s_nop 0\n" \
"v_mfma_f32_16x16x32_bf16 v[108:111], a[40:43], v[56:59], v[108:111]\n" \
"s_nop 0\n" \
"buffer_load_dword v202, %[fvo], %[ws], 0 offen sc1\n" \
"v_mov_b32 v203, s66\n" \
"v_mfma_f32_16x16x32_bf16 v[112:115], a[44:47], v[56:59], v[112:115]\n" \
"s_nop 2\n" \
"v_mfma_f32_16x16x32_bf16 v[116:119], a[48:51], v[56:59], v[116:119]\n" \
"s_nop 2\n" \
"v_mfma_f32_16x16x32_bf16 v[120:123], a[52:55], v[56:59], v[120:123]\n" \
"s_nop 2\n" \
"v_mfma_f32_16x16x32_bf16 v[124:127], a[56:59], v[56:59], v[124:127]\n" \
"s_nop 2\n" \
"v_mfma_f32_16x16x32_bf16 v[128:131], a[60:63], v[56:59], v[128:131]\n" \
"s_waitcnt vmcnt(7)\n" \
"s_nop 1\n" \
"v_mfma_f32_16x16x32_bf16 v[108:111], a[72:75], v[60:63], v[108:111]\n" \
"s_nop 2\n" \
"v_mfma_f32_16x16x32_bf16 v[112:115], a[76:79], v[60:63], v[112:115]\n" \
"s_nop 2\n" \
"v_mfma_f32_16x16x32_bf16 v[116:119], a[80:83], v[60:63], v[116:119]\n" \
"s_nop 2\n" \
"v_mfma_f32_16x16x32_bf16 v[120:123], a[84:87], v[60:63], v[120:123]\n" \
"s_nop 2\n" \
"v_mfma_f32_16x16x32_bf16 v[124:127], a[88:91], v[60:63], v[124:127]\n" \
"s_nop 2\n" \
"v_mfma_f32_16x16x32_bf16 v[128:131], a[92:95], v[60:63], v[128:131]\n" \
"s_waitcnt vmcnt(6)\n" \
"s_nop 1\n" \
"v_mfma_f32_16x16x32_bf16 v[108:111], a[104:107], v[64:67], v[108:111]\n" \
"s_nop 2\n" \
"v_mfma_f32_16x16x32_bf16 v[112:115], a[108:111], v[64:67], v[112:115]\n" \
"s_nop 2\n" \
"v_mfma_f32_16x16x32_bf16 v[116:119], a[112:115], v[64:67], v[116:119]\n" \
"s_nop 2\n" \
"v_mfma_f32_16x16x32_bf16 v[120:123], a[116:119], v[64:67], v[120:123]\n" \
"s_nop 2\n" \
"v_mfma_f32_16x16x32_bf16 v[124:127], a[120:123], v[64:67], v[124:127]\n" \
"s_nop 2\n" \
"v_mfma_f32_16x16x32_bf16 v[128:131], a[124:127], v[64:67], v[128:131]\n" \
"s_waitcnt vmcnt(5)\n" \
"s_nop 1\n" \
"v_mfma_f32_16x16x32_bf16 v[108:111], a[136:139], v[68:71], v[108:111]\n" \
"s_nop 2\n" \
"v_mfma_f32_16x16x32_bf16 v[112:115], a[140:143], v[68:71], v[112:115]\n" \
"s_nop 2\n" \
"v_mfma_f32_16x16x32_bf16 v[116:119], a[144:147], v[68:71], v[116:119]\n" \
"s_nop 2\n" \
"v_mfma_f32_16x16x32_bf16 v[120:123], a[148:151], v[68:71], v[120:123]\n" \
"s_nop 2\n" \
"v_mfma_f32_16x16x32_bf16 v[124:127], a[152:155], v[68:71], v[124:127]\n" \
"s_nop 2\n" \
"v_mfma_f32_16x16x32_bf16 v[128:131], a[156:159], v[68:71], v[128:131]\n" \
"s_waitcnt vmcnt(4)\n" \
"s_nop 1\n" \
"v_mfma_f32_16x16x32_bf16 v[108:111], a[168:171], v[72:75], v[108:111]\n" \
"s_waitcnt vmcnt(3)\n" \
"s_nop 1\n" \
"v_mfma_f32_16x16x32_bf16 v[108:111], a[200:203], v[76:79], v[108:111]\n" \
"s_waitcnt vmcnt(2)\n" \
"s_nop 1\n" \
"v_mfma_f32_16x16x32_bf16 v[108:111], a[232:235], v[80:83], v[108:111]\n" \
"s_nop 2\n" \
"v_mfma_f32_16x16x32_bf16 v[112:115], a[172:175], v[72:75], v[112:115]\n" \
"s_nop 2\n" \
"v_mfma_f32_16x16x32_bf16 v[112:115], a[204:207], v[76:79], v[112:115]\n" \
"s_nop 0\n" \
"ds_write_b128 v212, v[108:111] offset:34816\n" \
"v_mfma_f32_16x16x32_bf16 v[112:115], a[236:239], v[80:83], v[112:115]\n" \
"s_nop 0\n" \
"v_cmp_u_f32 vcc, v108, v108\n" \
"v_mfma_f32_16x16x32_bf16 v[116:119], a[176:179], v[72:75], v[116:119]\n" \
"s_cbranch_vccnz Lwo_sn1_%=\n" \
"Lwo_n1_%=:\n" \
"v_mfma_f32_16x16x32_bf16 v[116:119], a[208:211], v[76:79], v[116:119]\n" \
"s_nop 0\n" \
"s_nop 1\n" \
"ds_write_b128 v212, v[112:115] offset:35840\n" \
"v_mfma_f32_16x16x32_bf16 v[116:119], a[240:243], v[80:83], v[116:119]\n" \
"s_nop 2\n" \
"v_mfma_f32_16x16x32_bf16 v[120:123], a[180:183], v[72:75], v[120:123]\n" \
"s_nop 2\n" \
"v_mfma_f32_16x16x32_bf16 v[120:123], a[212:215], v[76:79], v[120:123]\n" \
"s_nop 0\n" \
"ds_write_b128 v213, v[116:119] offset:36864\n" \
"v_mfma_f32_16x16x32_bf16 v[120:123], a[244:247], v[80:83], v[120:123]\n" \
"s_waitcnt vmcnt(0)\n" \
"v_cmp_gt_u32 vcc, s74, v202\n" \
"v_mfma_f32_16x16x32_bf16 v[124:127], a[184:187], v[72:75], v[124:127]\n" \
"s_nop 0\n" \
"buffer_load_dword v202, %[fvo], %[ws], 0 offen sc1\n" \
"buffer_store_dword v203, %[fo], %[ws], s69 offen sc1\n" \
"v_mfma_f32_16x16x32_bf16 v[124:127], a[216:219], v[76:79], v[124:127]\n" \
"s_cmp_eq_u64 vcc, 0\n" \
"ds_write_b128 v213, v[120:123] offset:37888\n" \
"v_mfma_f32_16x16x32_bf16 v[124:127], a[248:251], v[80:83], v[124:127]\n" \
"s_cselect_b32 s72, s67, 1073741824\n" \
"buffer_load_dwordx4 v[20:23], %[lo], %[ws], s72 offen offset:0 sc1\n" \
"v_mfma_f32_16x16x32_bf16 v[128:131], a[188:191], v[72:75], v[128:131]\n" \
"s_add_u32 s73, s72, 4096\n" \
"s_nop 1\n" \
"v_mfma_f32_16x16x32_bf16 v[128:131], a[220:223], v[76:79], v[128:131]\n" \
"s_nop 0\n" \
"ds_write_b128 v214, v[124:127] offset:38912\n" \
"v_mfma_f32_16x16x32_bf16 v[128:131], a[252:255], v[80:83], v[128:131]\n" \
"s_waitcnt lgkmcnt(1)\n" \
"s_barrier\n" \
"v_mfma_f32_16x16x32_bf16 v[100:103], a[0:3], v[52:55], 0\n" \
"s_nop 0\n" \
"ds_read_b128 v[132:135], %[xr] offset:34816\n" \
"buffer_load_dwordx4 v[24:27], %[lo], %[ws], s72 offen offset:1024 sc1\n" \
"v_mfma_f32_16x16x32_bf16 v[104:107], a[4:7], v[52:55], 0\n" \
"s_nop 0\n" \
"ds_write_b128 v214, v[128:131] offset:39936\n" \
"v_mfma_f32_16x16x32_bf16 v[100:103], a[32:35], v[56:59], v[100:103]\n" \
"s_nop 0\n" \
"ds_read_b128 v[136:139], %[xr] offset:35840\n" \
"v_mfma_f32_16x16x32_bf16 v[104:107], a[36:39], v[56:59], v[104:107]\n" \
"s_nop 0\n" \
"ds_read_b128 v[140:143], %[xr] offset:36864\n" \
"v_mfma_f32_16x16x32_bf16 v[100:103], a[64:67], v[60:63], v[100:103]\n" \
"s_nop 0\n" \
"ds_read_b128 v[144:147], %[xr] offset:37888\n" \
"buffer_load_dwordx4 v[28:31], %[lo], %[ws], s72 offen offset:2048 sc1\n" \
"v_mfma_f32_16x16x32_bf16 v[104:107], a[68:71], v[60:63], v[104:107]\n" \
"s_waitcnt lgkmcnt(3)\n" \
"s_barrier\n" \
"v_mfma_f32_16x16x32_bf16 v[100:103], a[96:99], v[64:67], v[100:103]\n" \
"s_nop 0\n" \
"ds_read_b128 v[148:151], %[xr] offset:38912\n" \
"v_mfma_f32_16x16x32_bf16 v[104:107], a[100:103], v[64:67], v[104:107]\n" \
"s_nop 0\n" \
"ds_read_b128 v[152:155], %[xr] offset:39936\n" \
"v_mfma_f32_16x16x32_bf16 v[100:103], a[128:131], v[68:71], v[100:103]\n" \
"s_waitcnt lgkmcnt(3)\n" \
"buffer_load_dwordx4 v[32:35], %[lo], %[ws], s72 offen offset:3072 sc1\n" \
"v_add_f32 v132, v132, v140\n" \
"v_add_f32 v133, v133, v141\n" \
"v_mfma_f32_16x16x32_bf16 v[104:107], a[132:135], v[68:71], v[104:107]\n" \
"s_nop 0\n" \
"v_add_f32 v134, v134, v142\n" \
"v_add_f32 v135, v135, v143\n" \
"v_mfma_f32_16x16x32_bf16 v[100:103], a[160:163], v[72:75], v[100:103]\n" \
"s_waitcnt lgkmcnt(2)\n" \
"v_add_f32 v136, v136, v144\n" \
"v_add_f32 v137, v137, v145\n" \
"v_mfma_f32_16x16x32_bf16 v[104:107], a[164:167], v[72:75], v[104:107]\n" \
"s_nop 0\n" \
"v_add_f32 v138, v138, v146\n" \
"v_add_f32 v139, v139, v147\n" \
"v_mfma_f32_16x16x32_bf16 v[100:103], a[192:195], v[76:79], v[100:103]\n" \
"s_waitcnt lgkmcnt(1)\n" \
"buffer_load_dwordx4 v[36:39], %[lo], %[ws], s73 offen offset:0 sc1\n" \
"v_add_f32 v132, v132, v148\n" \
"v_add_f32 v133, v133, v149\n" \
"v_mfma_f32_16x16x32_bf16 v[104:107], a[196:199], v[76:79], v[104:107]\n" \
"s_nop 0\n" \
"v_add_f32 v134, v134, v150\n" \
"v_add_f32 v135, v135, v151\n" \
"v_mfma_f32_16x16x32_bf16 v[100:103], a[224:227], v[80:83], v[100:103]\n" \
"s_waitcnt lgkmcnt(0)\n" \
"v_add_f32 v136, v136, v152\n" \
"v_add_f32 v137, v137, v153\n" \
"v_mfma_f32_16x16x32_bf16 v[104:107], a[228:231], v[80:83], v[104:107]\n" \
"s_nop 0\n" \
"v_add_f32 v138, v138, v154\n" \
"v_add_f32 v139, v139, v155\n" \
"v_mov_b32 v203, s65\n" \
"s_mov_b32 s69, s79\n" \
"buffer_load_dwordx4 v[40:43], %[lo], %[ws], s73 offen offset:1024 sc1\n" \
"v_pk_add_f32 v[100:101], v[100:101], v[132:133]\n" \
"v_pk_add_f32 v[102:103], v[102:103], v[134:135]\n" \
"v_pk_add_f32 v[104:105], v[104:105], v[136:137]\n" \
"v_pk_add_f32 v[106:107], v[106:107], v[138:139]\n" \
"buffer_load_dwordx4 v[44:47], %[lo], %[ws], s73 offen offset:2048 sc1\n" \
"buffer_load_dwordx4 v[48:51], %[lo], %[ws], s73 offen offset:3072 sc1\n" \
"v_cvt_pk_bf16_f32 v204, v100, v101\n" \
"v_cvt_pk_bf16_f32 v205, v102, v103\n" \
"v_cvt_pk_bf16_f32 v206, v104, v105\n" \
"v_cvt_pk_bf16_f32 v207, v106, v107\n" \
"buffer_store_dwordx4 v[204:207], %[so], %[ws], s68 offen sc1\n" \
"s_add_u32 s68, s68, 131072\n" \
"s_cmp_eq_u32 s65, s80\n" \
"s_cbranch_scc1 Lwo_lim_%=\n" \
"Lwo_on_%=:\n" \
"s_cmp_eq_u32 s72, s67\n" \
"s_cbranch_scc1 Lwo_body0_%=\n" \
"s_waitcnt vmcnt(10)\n" \
"s_branch Lwo_top0_%=\n" \
"Lwo_end_%=:\n" \
"s_endpgm\n" \
"Lwo_lim_%=:\n" \
"s_cmp_eq_u32 s65, 128\n" \
"s_cbranch_scc1 Lwo_end_%=\n" \
"buffer_store_dwordx4 v[220:223], %[so], %[ws], s81 offen sc1\n" \
"s_add_u32 s81, s81, 131072\n" \
"buffer_store_dwordx4 v[220:223], %[so], %[ws], s81 offen sc1\n" \
"s_add_u32 s81, s81, 131072\n" \
"s_add_u32 s80, s80, 2\n" \
"s_cmp_eq_u32 s80, 74\n" \
"s_cselect_b32 s80, 128, s80\n" \
"s_branch Lwo_on_%=\n" \
"Lwo_sgo0_%=:\n" \
"s_waitcnt vmcnt(0)\n" \
"buffer_load_dword v202, %[fvo], %[ws], 0 offen sc1\n" \
"buffer_store_dword v203, %[fo], %[ws], s69 offen sc1\n" \
"s_mov_b32 s69, 1073741824\n" \
"s_waitcnt vmcnt(1)\n" \
"s_branch Lwo_cgo0_%=\n" \
"Lwo_pgo0_%=:\n" \
"s_sub_u32 s64, s64, 1\n" \
"s_cbranch_scc1 Lwo_dead_%=\n" \
"buffer_load_dword v202, %[fvo], %[ws], 0 offen sc1\n" \
"s_waitcnt vmcnt(0)\n" \
"Lwo_cgo0_%=:\n" \
"v_cmp_gt_u32 vcc, s74, v202\n" \
"s_cbranch_vccz Lwo_go0_%=\n" \
"s_sleep 1\n" \
"s_branch Lwo_pgo0_%=\n" \
"Lwo_sn0_%=:\n" \
"v_max3_u32 v215, v20, v21, v22\n" \
"v_max3_u32 v215, v215, v23, v24\n" \
"v_max3_u32 v215, v215, v25, v26\n" \
"v_max3_u32 v215, v215, v27, v28\n" \
"v_max3_u32 v215, v215, v29, v30\n" \
"v_max3_u32 v215, v215, v31, v32\n" \
"v_max3_u32 v215, v215, v33, v34\n" \
"v_max3_u32 v215, v215, v35, v36\n" \
"v_max3_u32 v215, v215, v37, v38\n" \
"v_max3_u32 v215, v215, v39, v40\n" \
"v_max3_u32 v215, v215, v41, v42\n" \
"v_max3_u32 v215, v215, v43, v44\n" \
"v_max3_u32 v215, v215, v45, v46\n" \
"v_max3_u32 v215, v215, v47, v48\n" \
"v_max3_u32 v215, v215, v49, v50\n" \
"v_max3_u32 v215, v215, v51, v51\n" \
"v_cmp_eq_u32 vcc, -1, v215\n" \
"s_nop 1\n" \
"s_cbranch_vccz Lwo_n0_%=\n" \
"Lwo_rn0_%=:\n" \
"s_sub_u32 s64, s64, 1\n" \
"s_cbranch_scc1 Lwo_dead_%=\n" \
"buffer_load_dwordx4 v[20:23], %[lo], %[ws], s72 offen offset:0 sc1\n" \
"buffer_load_dwordx4 v[24:27], %[lo], %[ws], s72 offen offset:1024 sc1\n" \
"buffer_load_dwordx4 v[28:31], %[lo], %[ws], s72 offen offset:2048 sc1\n" \
"buffer_load_dwordx4 v[32:35], %[lo], %[ws], s72 offen offset:3072 sc1\n" \
"buffer_load_dwordx4 v[36:39], %[lo], %[ws], s73 offen offset:0 sc1\n" \
"buffer_load_dwordx4 v[40:43], %[lo], %[ws], s73 offen offset:1024 sc1\n" \
"buffer_load_dwordx4 v[44:47], %[lo], %[ws], s73 offen offset:2048 sc1\n" \
"buffer_load_dwordx4 v[48:51], %[lo], %[ws], s73 offen offset:3072 sc1\n" \
"s_waitcnt vmcnt(0)\n" \
"v_max3_u32 v215, v20, v21, v22\n" \
"v_max3_u32 v215, v215, v23, v24\n" \
"v_max3_u32 v215, v215, v25, v26\n" \
"v_max3_u32 v215, v215, v27, v28\n" \
"v_max3_u32 v215, v215, v29, v30\n" \
"v_max3_u32 v215, v215, v31, v32\n" \
"v_max3_u32 v215, v215, v33, v34\n" \
"v_max3_u32 v215, v215, v35, v36\n" \
"v_max3_u32 v215, v215, v37, v38\n" \
"v_max3_u32 v215, v215, v39, v40\n" \
"v_max3_u32 v215, v215, v41, v42\n" \
"v_max3_u32 v215, v215, v43, v44\n" \
"v_max3_u32 v215, v215, v45, v46\n" \
"v_max3_u32 v215, v215, v47, v48\n" \
"v_max3_u32 v215, v215, v49, v50\n" \
"v_max3_u32 v215, v215, v51, v51\n" \
"v_cmp_eq_u32 vcc, -1, v215\n" \
"s_nop 1\n" \
"s_cbranch_vccnz Lwo_rn0_%=\n" \
"v_mfma_f32_16x16x32_bf16 v[108:111], a[8:11], v[20:23], 0\n" \
"v_mfma_f32_16x16x32_bf16 v[112:115], a[12:15], v[20:23], 0\n" \
"v_mfma_f32_16x16x32_bf16 v[116:119], a[16:19], v[20:23], 0\n" \
"v_mfma_f32_16x16x32_bf16 v[120:123], a[20:23], v[20:23], 0\n" \
"v_mfma_f32_16x16x32_bf16 v[124:127], a[24:27], v[20:23], 0\n" \
"v_mfma_f32_16x16x32_bf16 v[128:131], a[28:31], v[20:23], 0\n" \
"v_mfma_f32_16x16x32_bf16 v[108:111], a[40:43], v[24:27], v[108:111]\n" \
"v_mfma_f32_16x16x32_bf16 v[112:115], a[44:47], v[24:27], v[112:115]\n" \
"v_mfma_f32_16x16x32_bf16 v[116:119], a[48:51], v[24:27], v[116:119]\n" \
"v_mfma_f32_16x16x32_bf16 v[120:123], a[52:55], v[24:27], v[120:123]\n" \
"v_mfma_f32_16x16x32_bf16 v[124:127], a[56:59], v[24:27], v[124:127]\n" \
"v_mfma_f32_16x16x32_bf16 v[128:131], a[60:63], v[24:27], v[128:131]\n" \
"v_mfma_f32_16x16x32_bf16 v[108:111], a[72:75], v[28:31], v[108:111]\n" \
"v_mfma_f32_16x16x32_bf16 v[112:115], a[76:79], v[28:31], v[112:115]\n" \
"v_mfma_f32_16x16x32_bf16 v[116:119], a[80:83], v[28:31], v[116:119]\n" \
"v_mfma_f32_16x16x32_bf16 v[120:123], a[84:87], v[28:31], v[120:123]\n" \
"v_mfma_f32_16x16x32_bf16 v[124:127], a[88:91], v[28:31], v[124:127]\n" \
"v_mfma_f32_16x16x32_bf16 v[128:131], a[92:95], v[28:31], v[128:131]\n" \
"v_mfma_f32_16x16x32_bf16 v[108:111], a[104:107], v[32:35], v[108:111]\n" \
"v_mfma_f32_16x16x32_bf16 v[112:115], a[108:111], v[32:35], v[112:115]\n" \
"v_mfma_f32_16x16x32_bf16 v[116:119], a[112:115], v[32:35], v[116:119]\n" \
"v_mfma_f32_16x16x32_bf16 v[120:123], a[116:119], v[32:35], v[120:123]\n" \
"v_mfma_f32_16x16x32_bf16 v[124:127], a[120:123], v[32:35], v[124:127]\n" \
"v_mfma_f32_16x16x32_bf16 v[128:131], a[124:127], v[32:35], v[128:131]\n" \
"v_mfma_f32_16x16x32_bf16 v[108:111], a[136:139], v[36:39], v[108:111]\n" \
"v_mfma_f32_16x16x32_bf16 v[112:115], a[140:143], v[36:39], v[112:115]\n" \
"v_mfma_f32_16x16x32_bf16 v[116:119], a[144:147], v[36:39], v[116:119]\n" \
"v_mfma_f32_16x16x32_bf16 v[120:123], a[148:151], v[36:39], v[120:123]\n" \
"v_mfma_f32_16x16x32_bf16 v[124:127], a[152:155], v[36:39], v[124:127]\n" \
"v_mfma_f32_16x16x32_bf16 v[128:131], a[156:159], v[36:39], v[128:131]\n" \
"v_mfma_f32_16x16x32_bf16 v[108:111], a[168:171], v[40:43], v[108:111]\n" \
"v_mfma_f32_16x16x32_bf16 v[108:111], a[200:203], v[44:47], v[108:111]\n" \
"v_mfma_f32_16x16x32_bf16 v[108:111], a[232:235], v[48:51], v[108:111]\n" \
"v_mfma_f32_16x16x32_bf16 v[112:115], a[172:175], v[40:43], v[112:115]\n" \
"v_mfma_f32_16x16x32_bf16 v[112:115], a[204:207], v[44:47], v[112:115]\n" \
"v_mfma_f32_16x16x32_bf16 v[112:115], a[236:239], v[48:51], v[112:115]\n" \
"v_mfma_f32_16x16x32_bf16 v[116:119], a[176:179], v[40:43], v[116:119]\n" \
"s_nop 7\n" \
"ds_write_b128 v212, v[108:111] offset:2048\n" \
"s_branch Lwo_n0_%=\n" \
"Lwo_sgo1_%=:\n" \
"s_waitcnt vmcnt(0)\n" \
"buffer_load_dword v202, %[fvo], %[ws], 0 offen sc1\n" \
"buffer_store_dword v203, %[fo], %[ws], s69 offen sc1\n" \
"s_mov_b32 s69, 1073741824\n" \
"s_waitcnt vmcnt(1)\n" \
"s_branch Lwo_cgo1_%=\n" \
"Lwo_pgo1_%=:\n" \
"s_sub_u32 s64, s64, 1\n" \
"s_cbranch_scc1 Lwo_dead_%=\n" \
"buffer_load_dword v202, %[fvo], %[ws], 0 offen sc1\n" \
"s_waitcnt vmcnt(0)\n" \
"Lwo_cgo1_%=:\n" \
"v_cmp_gt_u32 vcc, s74, v202\n" \
"s_cbranch_vccz Lwo_go1_%=\n" \
"s_sleep 1\n" \
"s_branch Lwo_pgo1_%=\n" \
"Lwo_sn1_%=:\n" \
"v_max3_u32 v215, v52, v53, v54\n" \
"v_max3_u32 v215, v215, v55, v56\n" \
"v_max3_u32 v215, v215, v57, v58\n" \
"v_max3_u32 v215, v215, v59, v60\n" \
"v_max3_u32 v215, v215, v61, v62\n" \
"v_max3_u32 v215, v215, v63, v64\n" \
"v_max3_u32 v215, v215, v65, v66\n" \
"v_max3_u32 v215, v215, v67, v68\n" \
"v_max3_u32 v215, v215, v69, v70\n" \
"v_max3_u32 v215, v215, v71, v72\n" \
"v_max3_u32 v215, v215, v73, v74\n" \
"v_max3_u32 v215, v215, v75, v76\n" \
"v_max3_u32 v215, v215, v77, v78\n" \
"v_max3_u32 v215, v215, v79, v80\n" \
"v_max3_u32 v215, v215, v81, v82\n" \
"v_max3_u32 v215, v215, v83, v83\n" \
"v_cmp_eq_u32 vcc, -1, v215\n" \
"s_nop 1\n" \
"s_cbranch_vccz Lwo_n1_%=\n" \
"Lwo_rn1_%=:\n" \
"s_sub_u32 s64, s64, 1\n" \
"s_cbranch_scc1 Lwo_dead_%=\n" \
"buffer_load_dwordx4 v[52:55], %[lo], %[ws], s72 offen offset:0 sc1\n" \
"buffer_load_dwordx4 v[56:59], %[lo], %[ws], s72 offen offset:1024 sc1\n" \
"buffer_load_dwordx4 v[60:63], %[lo], %[ws], s72 offen offset:2048 sc1\n" \
"buffer_load_dwordx4 v[64:67], %[lo], %[ws], s72 offen offset:3072 sc1\n" \
"buffer_load_dwordx4 v[68:71], %[lo], %[ws], s73 offen offset:0 sc1\n" \
"buffer_load_dwordx4 v[72:75], %[lo], %[ws], s73 offen offset:1024 sc1\n" \
"buffer_load_dwordx4 v[76:79], %[lo], %[ws], s73 offen offset:2048 sc1\n" \
"buffer_load_dwordx4 v[80:83], %[lo], %[ws], s73 offen offset:3072 sc1\n" \
"s_waitcnt vmcnt(0)\n" \
"v_max3_u32 v215, v52, v53, v54\n" \
"v_max3_u32 v215, v215, v55, v56\n" \
"v_max3_u32 v215, v215, v57, v58\n" \
"v_max3_u32 v215, v215, v59, v60\n" \
"v_max3_u32 v215, v215, v61, v62\n" \
"v_max3_u32 v215, v215, v63, v64\n" \
"v_max3_u32 v215, v215, v65, v66\n" \
"v_max3_u32 v215, v215, v67, v68\n" \
"v_max3_u32 v215, v215, v69, v70\n" \
"v_max3_u32 v215, v215, v71, v72\n" \
"v_max3_u32 v215, v215, v73, v74\n" \
"v_max3_u32 v215, v215, v75, v76\n" \
"v_max3_u32 v215, v215, v77, v78\n" \
"v_max3_u32 v215, v215, v79, v80\n" \
"v_max3_u32 v215, v215, v81, v82\n" \
"v_max3_u32 v215, v215, v83, v83\n" \
"v_cmp_eq_u32 vcc, -1, v215\n" \
"s_nop 1\n" \
"s_cbranch_vccnz Lwo_rn1_%=\n" \
"v_mfma_f32_16x16x32_bf16 v[108:111], a[8:11], v[52:55], 0\n" \
"v_mfma_f32_16x16x32_bf16 v[112:115], a[12:15], v[52:55], 0\n" \
"v_mfma_f32_16x16x32_bf16 v[116:119], a[16:19], v[52:55], 0\n" \
"v_mfma_f32_16x16x32_bf16 v[120:123], a[20:23], v[52:55], 0\n" \
"v_mfma_f32_16x16x32_bf16 v[124:127], a[24:27], v[52:55], 0\n" \
"v_mfma_f32_16x16x32_bf16 v[128:131], a[28:31], v[52:55], 0\n" \
"v_mfma_f32_16x16x32_bf16 v[108:111], a[40:43], v[56:59], v[108:111]\n" \
"v_mfma_f32_16x16x32_bf16 v[112:115], a[44:47], v[56:59], v[112:115]\n" \
"v_mfma_f32_16x16x32_bf16 v[116:119], a[48:51], v[56:59], v[116:119]\n" \
"v_mfma_f32_16x16x32_bf16 v[120:123], a[52:55], v[56:59], v[120:123]\n" \
"v_mfma_f32_16x16x32_bf16 v[124:127], a[56:59], v[56:59], v[124:127]\n" \
"v_mfma_f32_16x16x32_bf16 v[128:131], a[60:63], v[56:59], v[128:131]\n" \
"v_mfma_f32_16x16x32_bf16 v[108:111], a[72:75], v[60:63], v[108:111]\n" \
"v_mfma_f32_16x16x32_bf16 v[112:115], a[76:79], v[60:63], v[112:115]\n" \
"v_mfma_f32_16x16x32_bf16 v[116:119], a[80:83], v[60:63], v[116:119]\n" \
"v_mfma_f32_16x16x32_bf16 v[120:123], a[84:87], v[60:63], v[120:123]\n" \
"v_mfma_f32_16x16x32_bf16 v[124:127], a[88:91], v[60:63], v[124:127]\n" \
"v_mfma_f32_16x16x32_bf16 v[128:131], a[92:95], v[60:63], v[128:131]\n" \
"v_mfma_f32_16x16x32_bf16 v[108:111], a[104:107], v[64:67], v[108:111]\n" \
"v_mfma_f32_16x16x32_bf16 v[112:115], a[108:111], v[64:67], v[112:115]\n" \
"v_mfma_f32_16x16x32_bf16 v[116:119], a[112:115], v[64:67], v[116:119]\n" \
"v_mfma_f32_16x16x32_bf16 v[120:123], a[116:119], v[64:67], v[120:123]\n" \
"v_mfma_f32_16x16x32_bf16 v[124:127], a[120:123], v[64:67], v[124:127]\n" \
"v_mfma_f32_16x16x32_bf16 v[128:131], a[124:127], v[64:67], v[128:131]\n" \
"v_mfma_f32_16x16x32_bf16 v[108:111], a[136:139], v[68:71], v[108:111]\n" \
"v_mfma_f32_16x16x32_bf16 v[112:115], a[140:143], v[68:71], v[112:115]\n" \
"v_mfma_f32_16x16x32_bf16 v[116:119], a[144:147], v[68:71], v[116:119]\n" \
"v_mfma_f32_16x16x32_bf16 v[120:123], a[148:151], v[68:71], v[120:123]\n" \
"v_mfma_f32_16x16x32_bf16 v[124:127], a[152:155], v[68:71], v[124:127]\n" \
"v_mfma_f32_16x16x32_bf16 v[128:131], a[156:159], v[68:71], v[128:131]\n" \
"v_mfma_f32_16x16x32_bf16 v[108:111], a[168:171], v[72:75], v[108:111]\n" \
"v_mfma_f32_16x16x32_bf16 v[108:111], a[200:203], v[76:79], v[108:111]\n" \
"v_mfma_f32_16x16x32_bf16 v[108:111], a[232:235], v[80:83], v[108:111]\n" \
"v_mfma_f32_16x16x32_bf16 v[112:115], a[172:175], v[72:75], v[112:115]\n" \
"v_mfma_f32_16x16x32_bf16 v[112:115], a[204:207], v[76:79], v[112:115]\n" \
"v_mfma_f32_16x16x32_bf16 v[112:115], a[236:239], v[80:83], v[112:115]\n" \
"v_mfma_f32_16x16x32_bf16 v[116:119], a[176:179], v[72:75], v[116:119]\n" \
"s_nop 7\n" \
"ds_write_b128 v212, v[108:111] offset:34816\n" \
"s_branch Lwo_n1_%=\n" \
"Lwo_dead_%=:\n" \
"v_mov_b32 v202, 29184\n" \
"buffer_store_dword v202, off, %[ws], 0 sc1\n" \
"s_endpgm\n" \
    :  : [wvo] "v"(wvo), [tr0] "v"(tr0), [fo] "v"(fo), [xr] "v"(xr), [fvo] "v"(fvo), [lo] "v"(lo), [so] "v"(so), [ws] "s"(ws), [rw] "s"(rw), [so0] "s"(so0), [wbase] "s"(wbase), [src] "s"(src), [dst] "s"(dst), [d0] "s"(d0) \
    : CLOB20)

#define W1_ASM() asm volatile( \
"s_nop 4\n" \
"s_mov_b32 s66, %[d0]\n" \
"Lw1_dly_%=:\n" \
"s_sleep 8\n" \
"s_sub_u32 s66, s66, 1\n" \
"s_cbranch_scc0 Lw1_dly_%=\n" \
"v_xor_b32 v20, 0, %[tr0]\n" \
"v_xor_b32 v21, 8, %[tr0]\n" \
"v_xor_b32 v22, 64, %[tr0]\n" \
"v_xor_b32 v23, 72, %[tr0]\n" \
"v_xor_b32 v24, 128, %[tr0]\n" \
"v_xor_b32 v25, 136, %[tr0]\n" \
"v_xor_b32 v26, 192, %[tr0]\n" \
"v_xor_b32 v27, 200, %[tr0]\n" \
"s_add_u32 m0, %[wbase], 0\n" \
"s_add_u32 s66, %[so0], 0\n" \
"buffer_load_dwordx4 %[wvo], %[rw], s66 offen lds\n" \
"s_add_u32 m0, %[wbase], 1024\n" \
"s_add_u32 s66, %[so0], 8192\n" \
"buffer_load_dwordx4 %[wvo], %[rw], s66 offen lds\n" \
"s_add_u32 m0, %[wbase], 2048\n" \
"s_add_u32 s66, %[so0], 16384\n" \
"buffer_load_dwordx4 %[wvo], %[rw], s66 offen lds\n" \
"s_waitcnt vmcnt(2)\n" \
"s_add_u32 m0, %[wbase], 3072\n" \
"s_add_u32 s66, %[so0], 24576\n" \
"buffer_load_dwordx4 %[wvo], %[rw], s66 offen lds\n" \
"s_waitcnt vmcnt(2)\n" \
"s_add_u32 m0, %[wbase], 4096\n" \
"s_add_u32 s66, %[so0], 32768\n" \
"buffer_load_dwordx4 %[wvo], %[rw], s66 offen lds\n" \
"s_waitcnt vmcnt(2)\n" \
"s_add_u32 m0, %[wbase], 5120\n" \
"s_add_u32 s66, %[so0], 40960\n" \
"buffer_load_dwordx4 %[wvo], %[rw], s66 offen lds\n" \
"s_waitcnt vmcnt(2)\n" \
"s_add_u32 m0, %[wbase], 6144\n" \
"s_add_u32 s66, %[so0], 49152\n" \
"buffer_load_dwordx4 %[wvo], %[rw], s66 offen lds\n" \
"s_waitcnt vmcnt(2)\n" \
"s_add_u32 m0, %[wbase], 7168\n" \
"s_add_u32 s66, %[so0], 57344\n" \
"buffer_load_dwordx4 %[wvo], %[rw], s66 offen lds\n" \
"s_waitcnt vmcnt(2)\n" \
"s_add_u32 m0, %[wbase], 8192\n" \
"s_add_u32 s66, %[so0], 65536\n" \
"buffer_load_dwordx4 %[wvo], %[rw], s66 offen lds\n" \
"s_waitcnt vmcnt(2)\n" \
"s_add_u32 m0, %[wbase], 9216\n" \
"s_add_u32 s66, %[so0], 73728\n" \
"buffer_load_dwordx4 %[wvo], %[rw], s66 offen lds\n" \
"s_waitcnt vmcnt(2)\n" \
"s_add_u32 m0, %[wbase], 10240\n" \
"s_add_u32 s66, %[so0], 81920\n" \
"buffer_load_dwordx4 %[wvo], %[rw], s66 offen lds\n" \
"ds_read_b64_tr_b16 a[0:1], v20 offset:0\n" \
"ds_read_b64_tr_b16 a[2:3], v20 offset:1024\n" \
"ds_read_b64_tr_b16 a[4:5], v21 offset:0\n" \
"ds_read_b64_tr_b16 a[6:7], v21 offset:1024\n" \
"ds_read_b64_tr_b16 a[8:9], v22 offset:0\n" \
"ds_read_b64_tr_b16 a[10:11], v22 offset:1024\n" \
"ds_read_b64_tr_b16 a[12:13], v23 offset:0\n" \
"ds_read_b64_tr_b16 a[14:15], v23 offset:1024\n" \
"ds_read_b64_tr_b16 a[16:17], v24 offset:0\n" \
"ds_read_b64_tr_b16 a[18:19], v24 offset:1024\n" \
"ds_read_b64_tr_b16 a[20:21], v25 offset:0\n" \
"ds_read_b64_tr_b16 a[22:23], v25 offset:1024\n" \
"ds_read_b64_tr_b16 a[24:25], v26 offset:0\n" \
"ds_read_b64_tr_b16 a[26:27], v26 offset:1024\n" \
"ds_read_b64_tr_b16 a[28:29], v27 offset:0\n" \
"ds_read_b64_tr_b16 a[30:31], v27 offset:1024\n" \
"s_waitcnt lgkmcnt(0)\n" \
"s_waitcnt vmcnt(2)\n" \
"s_add_u32 m0, %[wbase], 11264\n" \
"s_add_u32 s66, %[so0], 90112\n" \
"buffer_load_dwordx4 %[wvo], %[rw], s66 offen lds\n" \
"s_waitcnt vmcnt(2)\n" \
"s_add_u32 m0, %[wbase], 12288\n" \
"s_add_u32 s66, %[so0], 98304\n" \
"buffer_load_dwordx4 %[wvo], %[rw], s66 offen lds\n" \
"s_waitcnt vmcnt(2)\n" \
"s_add_u32 m0, %[wbase], 13312\n" \
"s_add_u32 s66, %[so0], 106496\n" \
"buffer_load_dwordx4 %[wvo], %[rw], s66 offen lds\n" \
"s_waitcnt vmcnt(2)\n" \
"s_add_u32 m0, %[wbase], 14336\n" \
"s_add_u32 s66, %[so0], 114688\n" \
"buffer_load_dwordx4 %[wvo], %[rw], s66 offen lds\n" \
"s_waitcnt vmcnt(2)\n" \
"s_add_u32 m0, %[wbase], 15360\n" \
"s_add_u32 s66, %[so0], 122880\n" \
"buffer_load_dwordx4 %[wvo], %[rw], s66 offen lds\n" \
"s_waitcnt vmcnt(2)\n" \
"s_add_u32 m0, %[wbase], 16384\n" \
"s_add_u32 s66, %[so0], 131072\n" \
"buffer_load_dwordx4 %[wvo], %[rw], s66 offen lds\n" \
"s_waitcnt vmcnt(2)\n" \
"s_add_u32 m0, %[wbase], 17408\n" \
"s_add_u32 s66, %[so0], 139264\n" \
"buffer_load_dwordx4 %[wvo], %[rw], s66 offen lds\n" \
"s_waitcnt vmcnt(2)\n" \
"s_add_u32 m0, %[wbase], 18432\n" \
"s_add_u32 s66, %[so0], 147456\n" \
"buffer_load_dwordx4 %[wvo], %[rw], s66 offen lds\n" \
"ds_read_b64_tr_b16 a[32:33], v20 offset:8192\n" \
"ds_read_b64_tr_b16 a[34:35], v20 offset:9216\n" \
"ds_read_b64_tr_b16 a[36:37], v21 offset:8192\n" \
"ds_read_b64_tr_b16 a[38:39], v21 offset:9216\n" \
"ds_read_b64_tr_b16 a[40:41], v22 offset:8192\n" \
"ds_read_b64_tr_b16 a[42:43], v22 offset:9216\n" \
"ds_read_b64_tr_b16 a[44:45], v23 offset:8192\n" \
"ds_read_b64_tr_b16 a[46:47], v23 offset:9216\n" \
"ds_read_b64_tr_b16 a[48:49], v24 offset:8192\n" \
"ds_read_b64_tr_b16 a[50:51], v24 offset:9216\n" \
"ds_read_b64_tr_b16 a[52:53], v25 offset:8192\n" \
"ds_read_b64_tr_b16 a[54:55], v25 offset:9216\n" \
"ds_read_b64_tr_b16 a[56:57], v26 offset:8192\n" \
"ds_read_b64_tr_b16 a[58:59], v26 offset:9216\n" \
"ds_read_b64_tr_b16 a[60:61], v27 offset:8192\n" \
"ds_read_b64_tr_b16 a[62:63], v27 offset:9216\n" \
"s_waitcnt lgkmcnt(0)\n" \
"s_waitcnt vmcnt(2)\n" \
"s_add_u32 m0, %[wbase], 19456\n" \
"s_add_u32 s66, %[so0], 155648\n" \
"buffer_load_dwordx4 %[wvo], %[rw], s66 offen lds\n" \
"s_waitcnt vmcnt(2)\n" \
"s_add_u32 m0, %[wbase], 20480\n" \
"s_add_u32 s66, %[so0], 163840\n" \
"buffer_load_dwordx4 %[wvo], %[rw], s66 offen lds\n" \
"s_waitcnt vmcnt(2)\n" \
"s_add_u32 m0, %[wbase], 21504\n" \
"s_add_u32 s66, %[so0], 172032\n" \
"buffer_load_dwordx4 %[wvo], %[rw], s66 offen lds\n" \
"s_waitcnt vmcnt(2)\n" \
"s_add_u32 m0, %[wbase], 22528\n" \
"s_add_u32 s66, %[so0], 180224\n" \
"buffer_load_dwordx4 %[wvo], %[rw], s66 offen lds\n" \
"s_waitcnt vmcnt(2)\n" \
"s_add_u32 m0, %[wbase], 23552\n" \
"s_add_u32 s66, %[so0], 188416\n" \
"buffer_load_dwordx4 %[wvo], %[rw], s66 offen lds\n" \
"s_waitcnt vmcnt(2)\n" \
"s_add_u32 m0, %[wbase], 24576\n" \
"s_add_u32 s66, %[so0], 196608\n" \
"buffer_load_dwordx4 %[wvo], %[rw], s66 offen lds\n" \
"s_waitcnt vmcnt(2)\n" \
"s_add_u32 m0, %[wbase], 25600\n" \
"s_add_u32 s66, %[so0], 204800\n" \
"buffer_load_dwordx4 %[wvo], %[rw], s66 offen lds\n" \
"s_waitcnt vmcnt(2)\n" \
"s_add_u32 m0, %[wbase], 26624\n" \
"s_add_u32 s66, %[so0], 212992\n" \
"buffer_load_dwordx4 %[wvo], %[rw], s66 offen lds\n" \
"ds_read_b64_tr_b16 a[64:65], v20 offset:16384\n" \
"ds_read_b64_tr_b16 a[66:67], v20 offset:17408\n" \
"ds_read_b64_tr_b16 a[68:69], v21 offset:16384\n" \
"ds_read_b64_tr_b16 a[70:71], v21 offset:17408\n" \
"ds_read_b64_tr_b16 a[72:73], v22 offset:16384\n" \
"ds_read_b64_tr_b16 a[74:75], v22 offset:17408\n" \
"ds_read_b64_tr_b16 a[76:77], v23 offset:16384\n" \
"ds_read_b64_tr_b16 a[78:79], v23 offset:17408\n" \
"ds_read_b64_tr_b16 a[80:81], v24 offset:16384\n" \
"ds_read_b64_tr_b16 a[82:83], v24 offset:17408\n" \
"ds_read_b64_tr_b16 a[84:85], v25 offset:16384\n" \
"ds_read_b64_tr_b16 a[86:87], v25 offset:17408\n" \
"ds_read_b64_tr_b16 a[88:89], v26 offset:16384\n" \
"ds_read_b64_tr_b16 a[90:91], v26 offset:17408\n" \
"ds_read_b64_tr_b16 a[92:93], v27 offset:16384\n" \
"ds_read_b64_tr_b16 a[94:95], v27 offset:17408\n" \
"s_waitcnt lgkmcnt(0)\n" \
"s_waitcnt vmcnt(2)\n" \
"s_add_u32 m0, %[wbase], 27648\n" \
"s_add_u32 s66, %[so0], 221184\n" \
"buffer_load_dwordx4 %[wvo], %[rw], s66 offen lds\n" \
"s_waitcnt vmcnt(2)\n" \
"s_add_u32 m0, %[wbase], 28672\n" \
"s_add_u32 s66, %[so0], 229376\n" \
"buffer_load_dwordx4 %[wvo], %[rw], s66 offen lds\n" \
"s_waitcnt vmcnt(2)\n" \
"s_add_u32 m0, %[wbase], 29696\n" \
"s_add_u32 s66, %[so0], 237568\n" \
"buffer_load_dwordx4 %[wvo], %[rw], s66 offen lds\n" \
"s_waitcnt vmcnt(2)\n" \
"s_add_u32 m0, %[wbase], 30720\n" \
"s_add_u32 s66, %[so0], 245760\n" \
"buffer_load_dwordx4 %[wvo], %[rw], s66 offen lds\n" \
"s_waitcnt vmcnt(2)\n" \
"s_add_u32 m0, %[wbase], 31744\n" \
"s_add_u32 s66, %[so0], 253952\n" \
"buffer_load_dwordx4 %[wvo], %[rw], s66 offen lds\n" \
"s_waitcnt vmcnt(2)\n" \
"s_add_u32 m0, %[wbase], 0\n" \
"s_add_u32 s66, %[so0], 262144\n" \
"buffer_load_dwordx4 %[wvo], %[rw], s66 offen lds\n" \
"s_waitcnt vmcnt(2)\n" \
"s_add_u32 m0, %[wbase], 1024\n" \
"s_add_u32 s66, %[so0], 270336\n" \
"buffer_load_dwordx4 %[wvo], %[rw], s66 offen lds\n" \
"s_waitcnt vmcnt(2)\n" \
"s_add_u32 m0, %[wbase], 2048\n" \
"s_add_u32 s66, %[so0], 278528\n" \
"buffer_load_dwordx4 %[wvo], %[rw], s66 offen lds\n" \
"ds_read_b64_tr_b16 a[96:97], v20 offset:24576\n" \
"ds_read_b64_tr_b16 a[98:99], v20 offset:25600\n" \
"ds_read_b64_tr_b16 a[100:101], v21 offset:24576\n" \
"ds_read_b64_tr_b16 a[102:103], v21 offset:25600\n" \
"ds_read_b64_tr_b16 a[104:105], v22 offset:24576\n" \
"ds_read_b64_tr_b16 a[106:107], v22 offset:25600\n" \
"ds_read_b64_tr_b16 a[108:109], v23 offset:24576\n" \
"ds_read_b64_tr_b16 a[110:111], v23 offset:25600\n" \
"ds_read_b64_tr_b16 a[112:113], v24 offset:24576\n" \
"ds_read_b64_tr_b16 a[114:115], v24 offset:25600\n" \
"ds_read_b64_tr_b16 a[116:117], v25 offset:24576\n" \
"ds_read_b64_tr_b16 a[118:119], v25 offset:25600\n" \
"ds_read_b64_tr_b16 a[120:121], v26 offset:24576\n" \
"ds_read_b64_tr_b16 a[122:123], v26 offset:25600\n" \
"ds_read_b64_tr_b16 a[124:125], v27 offset:24576\n" \
"ds_read_b64_tr_b16 a[126:127], v27 offset:25600\n" \
"s_waitcnt lgkmcnt(0)\n" \
"s_waitcnt vmcnt(2)\n" \
"s_add_u32 m0, %[wbase], 3072\n" \
"s_add_u32 s66, %[so0], 286720\n" \
"buffer_load_dwordx4 %[wvo], %[rw], s66 offen lds\n" \
"s_waitcnt vmcnt(2)\n" \
"s_add_u32 m0, %[wbase], 4096\n" \
"s_add_u32 s66, %[so0], 294912\n" \
"buffer_load_dwordx4 %[wvo], %[rw], s66 offen lds\n" \
"s_waitcnt vmcnt(2)\n" \
"s_add_u32 m0, %[wbase], 5120\n" \
"s_add_u32 s66, %[so0], 303104\n" \
"buffer_load_dwordx4 %[wvo], %[rw], s66 offen lds\n" \
"s_waitcnt vmcnt(2)\n" \
"s_add_u32 m0, %[wbase], 6144\n" \
"s_add_u32 s66, %[so0], 311296\n" \
"buffer_load_dwordx4 %[wvo], %[rw], s66 offen lds\n" \
"s_waitcnt vmcnt(2)\n" \
"s_add_u32 m0, %[wbase], 7168\n" \
"s_add_u32 s66, %[so0], 319488\n" \
"buffer_load_dwordx4 %[wvo], %[rw], s66 offen lds\n" \
"s_waitcnt vmcnt(2)\n" \
"s_add_u32 m0, %[wbase], 8192\n" \
"s_add_u32 s66, %[so0], 327680\n" \
"buffer_load_dwordx4 %[wvo], %[rw], s66 offen lds\n" \
"s_waitcnt vmcnt(2)\n" \
"s_add_u32 m0, %[wbase], 9216\n" \
"s_add_u32 s66, %[so0], 335872\n" \
"buffer_load_dwordx4 %[wvo], %[rw], s66 offen lds\n" \
"s_waitcnt vmcnt(2)\n" \
"s_add_u32 m0, %[wbase], 10240\n" \
"s_add_u32 s66, %[so0], 344064\n" \
"buffer_load_dwordx4 %[wvo], %[rw], s66 offen lds\n" \
"ds_read_b64_tr_b16 a[128:129], v20 offset:0\n" \
"ds_read_b64_tr_b16 a[130:131], v20 offset:1024\n" \
"ds_read_b64_tr_b16 a[132:133], v21 offset:0\n" \
"ds_read_b64_tr_b16 a[134:135], v21 offset:1024\n" \
"ds_read_b64_tr_b16 a[136:137], v22 offset:0\n" \
"ds_read_b64_tr_b16 a[138:139], v22 offset:1024\n" \
"ds_read_b64_tr_b16 a[140:141], v23 offset:0\n" \
"ds_read_b64_tr_b16 a[142:143], v23 offset:1024\n" \
"ds_read_b64_tr_b16 a[144:145], v24 offset:0\n" \
"ds_read_b64_tr_b16 a[146:147], v24 offset:1024\n" \
"ds_read_b64_tr_b16 a[148:149], v25 offset:0\n" \
"ds_read_b64_tr_b16 a[150:151], v25 offset:1024\n" \
"ds_read_b64_tr_b16 a[152:153], v26 offset:0\n" \
"ds_read_b64_tr_b16 a[154:155], v26 offset:1024\n" \
"ds_read_b64_tr_b16 a[156:157], v27 offset:0\n" \
"ds_read_b64_tr_b16 a[158:159], v27 offset:1024\n" \
"s_waitcnt lgkmcnt(0)\n" \
"s_waitcnt vmcnt(2)\n" \
"s_add_u32 m0, %[wbase], 11264\n" \
"s_add_u32 s66, %[so0], 352256\n" \
"buffer_load_dwordx4 %[wvo], %[rw], s66 offen lds\n" \
"s_waitcnt vmcnt(2)\n" \
"s_add_u32 m0, %[wbase], 12288\n" \
"s_add_u32 s66, %[so0], 360448\n" \
"buffer_load_dwordx4 %[wvo], %[rw], s66 offen lds\n" \
"s_waitcnt vmcnt(2)\n" \
"s_add_u32 m0, %[wbase], 13312\n" \
"s_add_u32 s66, %[so0], 368640\n" \
"buffer_load_dwordx4 %[wvo], %[rw], s66 offen lds\n" \
"s_waitcnt vmcnt(2)\n" \
"s_add_u32 m0, %[wbase], 14336\n" \
"s_add_u32 s66, %[so0], 376832\n" \
"buffer_load_dwordx4 %[wvo], %[rw], s66 offen lds\n" \
"s_waitcnt vmcnt(2)\n" \
"s_add_u32 m0, %[wbase], 15360\n" \
"s_add_u32 s66, %[so0], 385024\n" \
"buffer_load_dwordx4 %[wvo], %[rw], s66 offen lds\n" \
"s_waitcnt vmcnt(2)\n" \
"s_add_u32 m0, %[wbase], 16384\n" \
"s_add_u32 s66, %[so0], 393216\n" \
"buffer_load_dwordx4 %[wvo], %[rw], s66 offen lds\n" \
"s_waitcnt vmcnt(2)\n" \
"s_add_u32 m0, %[wbase], 17408\n" \
"s_add_u32 s66, %[so0], 401408\n" \
"buffer_load_dwordx4 %[wvo], %[rw], s66 offen lds\n" \
"s_waitcnt vmcnt(2)\n" \
"s_add_u32 m0, %[wbase], 18432\n" \
"s_add_u32 s66, %[so0], 409600\n" \
"buffer_load_dwordx4 %[wvo], %[rw], s66 offen lds\n" \
"ds_read_b64_tr_b16 a[160:161], v20 offset:8192\n" \
"ds_read_b64_tr_b16 a[162:163], v20 offset:9216\n" \
"ds_read_b64_tr_b16 a[164:165], v21 offset:8192\n" \
"ds_read_b64_tr_b16 a[166:167], v21 offset:9216\n" \
"ds_read_b64_tr_b16 a[168:169], v22 offset:8192\n" \
"ds_read_b64_tr_b16 a[170:171], v22 offset:9216\n" \
"ds_read_b64_tr_b16 a[172:173], v23 offset:8192\n" \
"ds_read_b64_tr_b16 a[174:175], v23 offset:9216\n" \
"ds_read_b64_tr_b16 a[176:177], v24 offset:8192\n" \
"ds_read_b64_tr_b16 a[178:179], v24 offset:9216\n" \
"ds_read_b64_tr_b16 a[180:181], v25 offset:8192\n" \
"ds_read_b64_tr_b16 a[182:183], v25 offset:9216\n" \
"ds_read_b64_tr_b16 a[184:185], v26 offset:8192\n" \
"ds_read_b64_tr_b16 a[186:187], v26 offset:9216\n" \
"ds_read_b64_tr_b16 a[188:189], v27 offset:8192\n" \
"ds_read_b64_tr_b16 a[190:191], v27 offset:9216\n" \
"s_waitcnt lgkmcnt(0)\n" \
"s_waitcnt vmcnt(2)\n" \
"s_add_u32 m0, %[wbase], 19456\n" \
"s_add_u32 s66, %[so0], 417792\n" \
"buffer_load_dwordx4 %[wvo], %[rw], s66 offen lds\n" \
"s_waitcnt vmcnt(2)\n" \
"s_add_u32 m0, %[wbase], 20480\n" \
"s_add_u32 s66, %[so0], 425984\n" \
"buffer_load_dwordx4 %[wvo], %[rw], s66 offen lds\n" \
"s_waitcnt vmcnt(2)\n" \
"s_add_u32 m0, %[wbase], 21504\n" \
"s_add_u32 s66, %[so0], 434176\n" \
"buffer_load_dwordx4 %[wvo], %[rw], s66 offen lds\n" \
"s_waitcnt vmcnt(2)\n" \
"s_add_u32 m0, %[wbase], 22528\n" \
"s_add_u32 s66, %[so0], 442368\n" \
"buffer_load_dwordx4 %[wvo], %[rw], s66 offen lds\n" \
"s_waitcnt vmcnt(2)\n" \
"s_add_u32 m0, %[wbase], 23552\n" \
"s_add_u32 s66, %[so0], 450560\n" \
"buffer_load_dwordx4 %[wvo], %[rw], s66 offen lds\n" \
"s_waitcnt vmcnt(2)\n" \
"s_add_u32 m0, %[wbase], 24576\n" \
"s_add_u32 s66, %[so0], 458752\n" \
"buffer_load_dwordx4 %[wvo], %[rw], s66 offen lds\n" \
"s_waitcnt vmcnt(2)\n" \
"s_add_u32 m0, %[wbase], 25600\n" \
"s_add_u32 s66, %[so0], 466944\n" \
"buffer_load_dwordx4 %[wvo], %[rw], s66 offen lds\n" \
"s_waitcnt vmcnt(2)\n" \
"s_add_u32 m0, %[wbase], 26624\n" \
"s_add_u32 s66, %[so0], 475136\n" \
"buffer_load_dwordx4 %[wvo], %[rw], s66 offen lds\n" \
"ds_read_b64_tr_b16 a[192:193], v20 offset:16384\n" \
"ds_read_b64_tr_b16 a[194:195], v20 offset:17408\n" \
"ds_read_b64_tr_b16 a[196:197], v21 offset:16384\n" \
"ds_read_b64_tr_b16 a[198:199], v21 offset:17408\n" \
"ds_read_b64_tr_b16 a[200:201], v22 offset:16384\n" \
"ds_read_b64_tr_b16 a[202:203], v22 offset:17408\n" \
"ds_read_b64_tr_b16 a[204:205], v23 offset:16384\n" \
"ds_read_b64_tr_b16 a[206:207], v23 offset:17408\n" \
"ds_read_b64_tr_b16 a[208:209], v24 offset:16384\n" \
"ds_read_b64_tr_b16 a[210:211], v24 offset:17408\n" \
"ds_read_b64_tr_b16 a[212:213], v25 offset:16384\n" \
"ds_read_b64_tr_b16 a[214:215], v25 offset:17408\n" \
"ds_read_b64_tr_b16 a[216:217], v26 offset:16384\n" \
"ds_read_b64_tr_b16 a[218:219], v26 offset:17408\n" \
"ds_read_b64_tr_b16 a[220:221], v27 offset:16384\n" \
"ds_read_b64_tr_b16 a[222:223], v27 offset:17408\n" \
"s_waitcnt lgkmcnt(0)\n" \
"s_waitcnt vmcnt(2)\n" \
"s_add_u32 m0, %[wbase], 27648\n" \
"s_add_u32 s66, %[so0], 483328\n" \
"buffer_load_dwordx4 %[wvo], %[rw], s66 offen lds\n" \
"s_waitcnt vmcnt(2)\n" \
"s_add_u32 m0, %[wbase], 28672\n" \
"s_add_u32 s66, %[so0], 491520\n" \
"buffer_load_dwordx4 %[wvo], %[rw], s66 offen lds\n" \
"s_waitcnt vmcnt(2)\n" \
"s_add_u32 m0, %[wbase], 29696\n" \
"s_add_u32 s66, %[so0], 499712\n" \
"buffer_load_dwordx4 %[wvo], %[rw], s66 offen lds\n" \
"s_waitcnt vmcnt(2)\n" \
"s_add_u32 m0, %[wbase], 30720\n" \
"s_add_u32 s66, %[so0], 507904\n" \
"buffer_load_dwordx4 %[wvo], %[rw], s66 offen lds\n" \
"s_waitcnt vmcnt(2)\n" \
"s_add_u32 m0, %[wbase], 31744\n" \
"s_add_u32 s66, %[so0], 516096\n" \
"buffer_load_dwordx4 %[wvo], %[rw], s66 offen lds\n" \
"s_waitcnt vmcnt(0)\n" \
"ds_read_b64_tr_b16 a[224:225], v20 offset:24576\n" \
"ds_read_b64_tr_b16 a[226:227], v20 offset:25600\n" \
"ds_read_b64_tr_b16 a[228:229], v21 offset:24576\n" \
"ds_read_b64_tr_b16 a[230:231], v21 offset:25600\n" \
"ds_read_b64_tr_b16 a[232:233], v22 offset:24576\n" \
"ds_read_b64_tr_b16 a[234:235], v22 offset:25600\n" \
"ds_read_b64_tr_b16 a[236:237], v23 offset:24576\n" \
"ds_read_b64_tr_b16 a[238:239], v23 offset:25600\n" \
"ds_read_b64_tr_b16 a[240:241], v24 offset:24576\n" \
"ds_read_b64_tr_b16 a[242:243], v24 offset:25600\n" \
"ds_read_b64_tr_b16 a[244:245], v25 offset:24576\n" \
"ds_read_b64_tr_b16 a[246:247], v25 offset:25600\n" \
"ds_read_b64_tr_b16 a[248:249], v26 offset:24576\n" \
"ds_read_b64_tr_b16 a[250:251], v26 offset:25600\n" \
"ds_read_b64_tr_b16 a[252:253], v27 offset:24576\n" \
"ds_read_b64_tr_b16 a[254:255], v27 offset:25600\n" \
"s_waitcnt lgkmcnt(0)\n" \
"v_xor_b32 v212, 8192, %[xr]\n" \
"v_xor_b32 v213, 16384, %[xr]\n" \
"v_xor_b32 v214, 24576, %[xr]\n" \
"s_barrier\n" \
"s_mov_b32 s76, 0xbdd2d3e8\n" \
"s_mov_b32 s77, 0xbdd2d3e8\n" \
"v_mov_b32 v180, 0xc0135761\n" \
"v_mov_b32 v181, 0xc0135761\n" \
"s_mov_b32 s78, 0x3727c5ac\n" \
"v_mov_b32 v156, 0x3f803f80\n" \
"v_mov_b32 v157, 0x3f803f80\n" \
"v_mov_b32 v158, 0x3f803f80\n" \
"v_mov_b32 v159, 0x3f803f80\n" \
"s_nop 1\n" \
"v_mfma_f32_16x16x32_bf16 v[100:103], a[0:3], v[156:159], 0\n" \
"v_mfma_f32_16x16x32_bf16 v[104:107], a[4:7], v[156:159], 0\n" \
"v_mfma_f32_16x16x32_bf16 v[108:111], a[8:11], v[156:159], 0\n" \
"v_mfma_f32_16x16x32_bf16 v[112:115], a[12:15], v[156:159], 0\n" \
"v_mfma_f32_16x16x32_bf16 v[116:119], a[16:19], v[156:159], 0\n" \
"v_mfma_f32_16x16x32_bf16 v[120:123], a[20:23], v[156:159], 0\n" \
"v_mfma_f32_16x16x32_bf16 v[124:127], a[24:27], v[156:159], 0\n" \
"v_mfma_f32_16x16x32_bf16 v[128:131], a[28:31], v[156:159], 0\n" \
"v_mfma_f32_16x16x32_bf16 v[100:103], a[32:35], v[156:159], v[100:103]\n" \
"v_mfma_f32_16x16x32_bf16 v[104:107], a[36:39], v[156:159], v[104:107]\n" \
"v_mfma_f32_16x16x32_bf16 v[108:111], a[40:43], v[156:159], v[108:111]\n" \
"v_mfma_f32_16x16x32_bf16 v[112:115], a[44:47], v[156:159], v[112:115]\n" \
"v_mfma_f32_16x16x32_bf16 v[116:119], a[48:51], v[156:159], v[116:119]\n" \
"v_mfma_f32_16x16x32_bf16 v[120:123], a[52:55], v[156:159], v[120:123]\n" \
"v_mfma_f32_16x16x32_bf16 v[124:127], a[56:59], v[156:159], v[124:127]\n" \
"v_mfma_f32_16x16x32_bf16 v[128:131], a[60:63], v[156:159], v[128:131]\n" \
"v_mfma_f32_16x16x32_bf16 v[100:103], a[64:67], v[156:159], v[100:103]\n" \
"v_mfma_f32_16x16x32_bf16 v[104:107], a[68:71], v[156:159], v[104:107]\n" \
"v_mfma_f32_16x16x32_bf16 v[108:111], a[72:75], v[156:159], v[108:111]\n" \
"v_mfma_f32_16x16x32_bf16 v[112:115], a[76:79], v[156:159], v[112:115]\n" \
"v_mfma_f32_16x16x32_bf16 v[116:119], a[80:83], v[156:159], v[116:119]\n" \
"v_mfma_f32_16x16x32_bf16 v[120:123], a[84:87], v[156:159], v[120:123]\n" \
"v_mfma_f32_16x16x32_bf16 v[124:127], a[88:91], v[156:159], v[124:127]\n" \
"v_mfma_f32_16x16x32_bf16 v[128:131], a[92:95], v[156:159], v[128:131]\n" \
"v_mfma_f32_16x16x32_bf16 v[100:103], a[96:99], v[156:159], v[100:103]\n" \
"v_mfma_f32_16x16x32_bf16 v[104:107], a[100:103], v[156:159], v[104:107]\n" \
"v_mfma_f32_16x16x32_bf16 v[108:111], a[104:107], v[156:159], v[108:111]\n" \
"v_mfma_f32_16x16x32_bf16 v[112:115], a[108:111], v[156:159], v[112:115]\n" \
"v_mfma_f32_16x16x32_bf16 v[116:119], a[112:115], v[156:159], v[116:119]\n" \
"v_mfma_f32_16x16x32_bf16 v[120:123], a[116:119], v[156:159], v[120:123]\n" \
"v_mfma_f32_16x16x32_bf16 v[124:127], a[120:123], v[156:159], v[124:127]\n" \
"v_mfma_f32_16x16x32_bf16 v[128:131], a[124:127], v[156:159], v[128:131]\n" \
"v_mfma_f32_16x16x32_bf16 v[100:103], a[128:131], v[156:159], v[100:103]\n" \
"v_mfma_f32_16x16x32_bf16 v[104:107], a[132:135], v[156:159], v[104:107]\n" \
"v_mfma_f32_16x16x32_bf16 v[108:111], a[136:139], v[156:159], v[108:111]\n" \
"v_mfma_f32_16x16x32_bf16 v[112:115], a[140:143], v[156:159], v[112:115]\n" \
"v_mfma_f32_16x16x32_bf16 v[116:119], a[144:147], v[156:159], v[116:119]\n" \
"v_mfma_f32_16x16x32_bf16 v[120:123], a[148:151], v[156:159], v[120:123]\n" \
"v_mfma_f32_16x16x32_bf16 v[124:127], a[152:155], v[156:159], v[124:127]\n" \
"v_mfma_f32_16x16x32_bf16 v[128:131], a[156:159], v[156:159], v[128:131]\n" \
"v_mfma_f32_16x16x32_bf16 v[100:103], a[160:163], v[156:159], v[100:103]\n" \
"v_mfma_f32_16x16x32_bf16 v[104:107], a[164:167], v[156:159], v[104:107]\n" \
"v_mfma_f32_16x16x32_bf16 v[108:111], a[168:171], v[156:159], v[108:111]\n" \
"v_mfma_f32_16x16x32_bf16 v[112:115], a[172:175], v[156:159], v[112:115]\n" \
"v_mfma_f32_16x16x32_bf16 v[116:119], a[176:179], v[156:159], v[116:119]\n" \
"v_mfma_f32_16x16x32_bf16 v[120:123], a[180:183], v[156:159], v[120:123]\n" \
"v_mfma_f32_16x16x32_bf16 v[124:127], a[184:187], v[156:159], v[124:127]\n" \
"v_mfma_f32_16x16x32_bf16 v[128:131], a[188:191], v[156:159], v[128:131]\n" \
"v_mfma_f32_16x16x32_bf16 v[100:103], a[192:195], v[156:159], v[100:103]\n" \
"v_mfma_f32_16x16x32_bf16 v[104:107], a[196:199], v[156:159], v[104:107]\n" \
"v_mfma_f32_16x16x32_bf16 v[108:111], a[200:203], v[156:159], v[108:111]\n" \
"v_mfma_f32_16x16x32_bf16 v[112:115], a[204:207], v[156:159], v[112:115]\n" \
"v_mfma_f32_16x16x32_bf16 v[116:119], a[208:211], v[156:159], v[116:119]\n" \
"v_mfma_f32_16x16x32_bf16 v[120:123], a[212:215], v[156:159], v[120:123]\n" \
"v_mfma_f32_16x16x32_bf16 v[124:127], a[216:219], v[156:159], v[124:127]\n" \
"v_mfma_f32_16x16x32_bf16 v[128:131], a[220:223], v[156:159], v[128:131]\n" \
"v_mfma_f32_16x16x32_bf16 v[100:103], a[224:227], v[156:159], v[100:103]\n" \
"v_mfma_f32_16x16x32_bf16 v[104:107], a[228:231], v[156:159], v[104:107]\n" \
"v_mfma_f32_16x16x32_bf16 v[108:111], a[232:235], v[156:159], v[108:111]\n" \
"v_mfma_f32_16x16x32_bf16 v[112:115], a[236:239], v[156:159], v[112:115]\n" \
"v_mfma_f32_16x16x32_bf16 v[116:119], a[240:243], v[156:159], v[116:119]\n" \
"v_mfma_f32_16x16x32_bf16 v[120:123], a[244:247], v[156:159], v[120:123]\n" \
"v_mfma_f32_16x16x32_bf16 v[124:127], a[248:251], v[156:159], v[124:127]\n" \
"v_mfma_f32_16x16x32_bf16 v[128:131], a[252:255], v[156:159], v[128:131]\n" \
"s_nop 2\n" \
"ds_write_b128 v212, v[108:111] offset:34816\n" \
"ds_write_b128 v212, v[112:115] offset:35840\n" \
"ds_write_b128 v213, v[116:119] offset:36864\n" \
"ds_write_b128 v213, v[120:123] offset:37888\n" \
"ds_write_b128 v214, v[124:127] offset:38912\n" \
"ds_write_b128 v214, v[128:131] offset:39936\n" \
"s_waitcnt lgkmcnt(0)\n" \
"s_barrier\n" \
"ds_read_b128 v[132:135], %[xr] offset:34816\n" \
"ds_read_b128 v[136:139], %[xr] offset:35840\n" \
"ds_read_b128 v[140:143], %[xr] offset:36864\n" \
"ds_read_b128 v[144:147], %[xr] offset:37888\n" \
"ds_read_b128 v[148:151], %[xr] offset:38912\n" \
"ds_read_b128 v[152:155], %[xr] offset:39936\n" \
"s_waitcnt lgkmcnt(5)\n" \
"v_add_f32 v164, v100, v132\n" \
"s_waitcnt lgkmcnt(1)\n" \
"v_add_f32 v140, v140, v148\n" \
"v_add_f32 v164, v164, v140\n" \
"v_add_f32 v165, v101, v133\n" \
"v_add_f32 v141, v141, v149\n" \
"v_add_f32 v165, v165, v141\n" \
"v_add_f32 v166, v102, v134\n" \
"v_add_f32 v142, v142, v150\n" \
"v_add_f32 v166, v166, v142\n" \
"v_add_f32 v167, v103, v135\n" \
"v_add_f32 v143, v143, v151\n" \
"v_add_f32 v167, v167, v143\n" \
"v_add_f32 v168, v104, v136\n" \
"s_waitcnt lgkmcnt(0)\n" \
"v_add_f32 v144, v144, v152\n" \
"v_add_f32 v168, v168, v144\n" \
"v_add_f32 v169, v105, v137\n" \
"v_add_f32 v145, v145, v153\n" \
"v_add_f32 v169, v169, v145\n" \
"v_add_f32 v170, v106, v138\n" \
"v_add_f32 v146, v146, v154\n" \
"v_add_f32 v170, v170, v146\n" \
"v_add_f32 v171, v107, v139\n" \
"v_add_f32 v147, v147, v155\n" \
"v_add_f32 v171, v171, v147\n" \
"s_add_u32 s81, %[dst], 3375104\n" \
"v_mov_b32 v220, -1\n" \
"v_mov_b32 v221, -1\n" \
"v_mov_b32 v222, -1\n" \
"v_mov_b32 v223, -1\n" \
"s_mov_b32 s80, 128\n" \
"s_mov_b32 s65, 25\n" \
"Lw1_poi_%=:\n" \
"buffer_store_dwordx4 v[220:223], %[so], %[ws], s81 offen sc1\n" \
"s_waitcnt vmcnt(2)\n" \
"s_add_u32 s81, s81, 32768\n" \
"s_sub_u32 s65, s65, 1\n" \
"s_cbranch_scc0 Lw1_poi_%=\n" \
"s_waitcnt vmcnt(0)\n" \
"s_mov_b32 s64, 262144\n" \
"s_mov_b32 s65, 0\n" \
"s_mov_b32 s74, 1\n" \
"s_mov_b32 s69, 1073741824\n" \
"s_mov_b32 s67, %[src]\n" \
"s_mov_b32 s68, %[dst]\n" \
"s_mov_b32 s70, %[ox0]\n" \
"v_mov_b32 v202, 0\n" \
"v_mov_b32 v203, 0\n" \
"v_mov_b32 v208, 0\n" \
"v_mov_b32 v209, 0\n" \
"v_mov_b32 v210, 0\n" \
"v_mov_b32 v211, 0\n" \
"Lw1_top0_%=:\n" \
"v_cmp_gt_u32 vcc, s74, v202\n" \
"s_mov_b32 s72, s67\n" \
"s_mov_b32 s75, s70\n" \
"s_add_u32 s73, s67, 4096\n" \
"s_cbranch_vccnz Lw1_sgo0_%=\n" \
"Lw1_go0_%=:\n" \
"buffer_load_dwordx4 v[20:23], %[lo], %[ws], s72 offen offset:0 sc1\n" \
"buffer_load_dwordx4 v[24:27], %[lo], %[ws], s72 offen offset:1024 sc1\n" \
"buffer_load_dwordx2 v[84:85], %[sl], %[ws], s75 offen offset:0 sc1\n" \
"buffer_load_dwordx4 v[28:31], %[lo], %[ws], s72 offen offset:2048 sc1\n" \
"buffer_load_dwordx4 v[32:35], %[lo], %[ws], s72 offen offset:3072 sc1\n" \
"buffer_load_dwordx4 v[36:39], %[lo], %[ws], s73 offen offset:0 sc1\n" \
"buffer_load_dwordx4 v[40:43], %[lo], %[ws], s73 offen offset:1024 sc1\n" \
"buffer_load_dwordx4 v[44:47], %[lo], %[ws], s73 offen offset:2048 sc1\n" \
"buffer_load_dwordx4 v[48:51], %[lo], %[ws], s73 offen offset:3072 sc1\n" \
"s_waitcnt vmcnt(9)\n" \
"buffer_store_dword v203, %[fo], %[ws], s69 offen sc1\n" \
"s_mov_b32 s69, 1073741824\n" \
"Lw1_body0_%=:\n" \
"s_waitcnt vmcnt(9)\n" \
"v_mfma_f32_16x16x32_bf16 v[108:111], a[8:11], v[20:23], 0\n" \
"s_nop 2\n" \
"v_mfma_f32_16x16x32_bf16 v[112:115], a[12:15], v[20:23], 0\n" \
"s_nop 2\n" \
"v_mfma_f32_16x16x32_bf16 v[116:119], a[16:19], v[20:23], 0\n" \
"s_add_u32 s74, s74, 1\n" \
"s_add_u32 s67, s67, 32768\n" \
"s_nop 0\n" \
"v_mfma_f32_16x16x32_bf16 v[120:123], a[20:23], v[20:23], 0\n" \
"s_add_u32 s65, s65, 1\n" \
"s_add_u32 s70, s70, 2048\n" \
"s_nop 0\n" \
"v_mfma_f32_16x16x32_bf16 v[124:127], a[24:27], v[20:23], 0\n" \
"s_cmp_gt_u32 s65, 104\n" \
"s_cselect_b32 s79, 1073741824, 0\n" \
"s_cselect_b32 s69, 0, s69\n" \
"v_mfma_f32_16x16x32_bf16 v[128:131], a[28:31], v[20:23], 0\n" \
"s_waitcnt vmcnt(8)\n" \
"s_addc_u32 s66, s65, -1\n" \
"s_nop 0\n" \
"v_mfma_f32_16x16x32_bf16 v[108:111], a[40:43], v[24:27], v[108:111]\n" \
"s_nop 0\n" \
"buffer_load_dword v202, %[fvo], %[ws], 0 offen sc1\n" \
"v_mov_b32 v203, s66\n" \
"v_mfma_f32_16x16x32_bf16 v[112:115], a[44:47], v[24:27], v[112:115]\n" \
"s_waitcnt vmcnt(8)\n" \
"v_cmp_u_f32 vcc, v84, v85\n" \
"v_mfma_f32_16x16x32_bf16 v[116:119], a[48:51], v[24:27], v[116:119]\n" \
"s_cbranch_vccnz Lw1_sk00_%=\n" \
"Lw1_k00_%=:\n" \
"v_mfma_f32_16x16x32_bf16 v[120:123], a[52:55], v[24:27], v[120:123]\n" \
"s_nop 2\n" \
"v_mfma_f32_16x16x32_bf16 v[124:127], a[56:59], v[24:27], v[124:127]\n" \
"s_nop 0\n" \
"v_permlane32_swap_b32 v84, v85\n" \
"v_mfma_f32_16x16x32_bf16 v[128:131], a[60:63], v[24:27], v[128:131]\n" \
"s_waitcnt vmcnt(7)\n" \
"v_add_f32 v86, v84, v85\n" \
"v_add_f32 v84, v84, v85\n" \
"v_mfma_f32_16x16x32_bf16 v[108:111], a[72:75], v[28:31], v[108:111]\n" \
"s_nop 0\n" \
"v_permlane16_swap_b32 v84, v86\n" \
"v_mfma_f32_16x16x32_bf16 v[112:115], a[76:79], v[28:31], v[112:115]\n" \
"s_nop 0\n" \
"v_add_f32 v84, v84, v86\n" \
"v_mfma_f32_16x16x32_bf16 v[116:119], a[80:83], v[28:31], v[116:119]\n" \
"s_nop 0\n" \
"ds_write_b32 %[sw], v84 offset:0\n" \
"v_mfma_f32_16x16x32_bf16 v[120:123], a[84:87], v[28:31], v[120:123]\n" \
"s_nop 2\n" \
"v_mfma_f32_16x16x32_bf16 v[124:127], a[88:91], v[28:31], v[124:127]\n" \
"s_nop 2\n" \
"v_mfma_f32_16x16x32_bf16 v[128:131], a[92:95], v[28:31], v[128:131]\n" \
"s_waitcnt vmcnt(6) lgkmcnt(0)\n" \
"s_barrier\n" \
"v_mfma_f32_16x16x32_bf16 v[108:111], a[104:107], v[32:35], v[108:111]\n" \
"s_nop 0\n" \
"ds_read_b128 v[190:193], %[sr] offset:0\n" \
"v_mfma_f32_16x16x32_bf16 v[112:115], a[108:111], v[32:35], v[112:115]\n" \
"s_nop 0\n" \
"ds_read_b128 v[194:197], %[sr] offset:16\n" \
"v_mfma_f32_16x16x32_bf16 v[116:119], a[112:115], v[32:35], v[116:119]\n" \
"s_nop 2\n" \
"v_mfma_f32_16x16x32_bf16 v[120:123], a[116:119], v[32:35], v[120:123]\n" \
"s_nop 2\n" \
"v_mfma_f32_16x16x32_bf16 v[124:127], a[120:123], v[32:35], v[124:127]\n" \
"s_nop 2\n" \
"v_mfma_f32_16x16x32_bf16 v[128:131], a[124:127], v[32:35], v[128:131]\n" \
"s_waitcnt vmcnt(5) lgkmcnt(1)\n" \
"v_add_f32 v190, v190, v191\n" \
"v_add_f32 v192, v192, v193\n" \
"v_mfma_f32_16x16x32_bf16 v[108:111], a[136:139], v[36:39], v[108:111]\n" \
"s_waitcnt lgkmcnt(0)\n" \
"v_add_f32 v194, v194, v195\n" \
"v_add_f32 v196, v196, v197\n" \
"v_mfma_f32_16x16x32_bf16 v[112:115], a[140:143], v[36:39], v[112:115]\n" \
"s_nop 0\n" \
"v_add_f32 v190, v190, v192\n" \
"v_add_f32 v194, v194, v196\n" \
"v_mfma_f32_16x16x32_bf16 v[116:119], a[144:147], v[36:39], v[116:119]\n" \
"s_nop 0\n" \
"v_mul_f32 v190, 0xba800000, v190\n" \
"v_mul_f32 v194, 0x3a800000, v194\n" \
"v_mfma_f32_16x16x32_bf16 v[120:123], a[148:151], v[36:39], v[120:123]\n" \
"s_nop 0\n" \
"v_mul_f32 v156, v190, v164\n" \
"v_mul_f32 v157, v190, v165\n" \
"v_mfma_f32_16x16x32_bf16 v[124:127], a[152:155], v[36:39], v[124:127]\n" \
"s_nop 0\n" \
"v_mul_f32 v158, v190, v166\n" \
"v_mul_f32 v159, v190, v167\n" \
"v_mfma_f32_16x16x32_bf16 v[128:131], a[156:159], v[36:39], v[128:131]\n" \
"s_waitcnt vmcnt(4)\n" \
"v_mul_f32 v160, v190, v168\n" \
"v_mul_f32 v161, v190, v169\n" \
"v_mfma_f32_16x16x32_bf16 v[108:111], a[168:171], v[40:43], v[108:111]\n" \
"s_waitcnt vmcnt(3)\n" \
"v_mul_f32 v162, v190, v170\n" \
"v_mul_f32 v163, v190, v171\n" \
"v_mfma_f32_16x16x32_bf16 v[108:111], a[200:203], v[44:47], v[108:111]\n" \
"s_waitcnt vmcnt(2)\n" \
"v_fma_f32 v194, -v190, v190, v194\n" \
"v_max_f32 v194, 0, v194\n" \
"v_mfma_f32_16x16x32_bf16 v[108:111], a[232:235], v[48:51], v[108:111]\n" \
"s_nop 0\n" \
"v_add_f32 v192, 0x3727c5ac, v194\n" \
"v_rsq_f32 v192, v192\n" \
"v_mfma_f32_16x16x32_bf16 v[112:115], a[172:175], v[40:43], v[112:115]\n" \
"s_nop 0\n" \
"v_mul_f32 v194, v194, v192\n" \
"v_fma_f32 v194, v194, v192, s78\n" \
"v_mfma_f32_16x16x32_bf16 v[112:115], a[204:207], v[44:47], v[112:115]\n" \
"s_nop 0\n" \
"ds_write_b128 v212, v[108:111] offset:2048\n" \
"v_mfma_f32_16x16x32_bf16 v[112:115], a[236:239], v[48:51], v[112:115]\n" \
"s_nop 0\n" \
"v_cmp_u_f32 vcc, v108, v108\n" \
"v_rsq_f32 v194, v194\n" \
"v_mfma_f32_16x16x32_bf16 v[116:119], a[176:179], v[40:43], v[116:119]\n" \
"s_cbranch_vccnz Lw1_sn0_%=\n" \
"Lw1_n0_%=:\n" \
"v_mul_f32 v192, v192, v194\n" \
"v_mfma_f32_16x16x32_bf16 v[116:119], a[208:211], v[44:47], v[116:119]\n" \
"s_nop 0\n" \
"ds_write_b128 v212, v[112:115] offset:3072\n" \
"v_mfma_f32_16x16x32_bf16 v[116:119], a[240:243], v[48:51], v[116:119]\n" \
"s_nop 2\n" \
"v_mfma_f32_16x16x32_bf16 v[120:123], a[180:183], v[40:43], v[120:123]\n" \
"s_nop 2\n" \
"v_mfma_f32_16x16x32_bf16 v[120:123], a[212:215], v[44:47], v[120:123]\n" \
"s_nop 0\n" \
"ds_write_b128 v213, v[116:119] offset:4096\n" \
"v_mfma_f32_16x16x32_bf16 v[120:123], a[244:247], v[48:51], v[120:123]\n" \
"s_waitcnt vmcnt(0)\n" \
"v_cmp_gt_u32 vcc, s74, v202\n" \
"v_mfma_f32_16x16x32_bf16 v[124:127], a[184:187], v[40:43], v[124:127]\n" \
"s_nop 0\n" \
"buffer_load_dword v202, %[fvo], %[ws], 0 offen sc1\n" \
"v_mfma_f32_16x16x32_bf16 v[124:127], a[216:219], v[44:47], v[124:127]\n" \
"s_cmp_eq_u64 vcc, 0\n" \
"s_nop 0\n" \
"ds_write_b128 v213, v[120:123] offset:5120\n" \
"v_mfma_f32_16x16x32_bf16 v[124:127], a[248:251], v[48:51], v[124:127]\n" \
"s_cselect_b32 s72, s67, 1073741824\n" \
"buffer_load_dwordx4 v[52:55], %[lo], %[ws], s72 offen offset:0 sc1\n" \
"v_mfma_f32_16x16x32_bf16 v[128:131], a[188:191], v[40:43], v[128:131]\n" \
"s_cselect_b32 s75, s70, 1073741824\n" \
"s_add_u32 s73, s72, 4096\n" \
"s_nop 0\n" \
"v_mfma_f32_16x16x32_bf16 v[128:131], a[220:223], v[44:47], v[128:131]\n" \
"s_nop 0\n" \
"ds_write_b128 v214, v[124:127] offset:6144\n" \
"v_mfma_f32_16x16x32_bf16 v[128:131], a[252:255], v[48:51], v[128:131]\n" \
"s_waitcnt lgkmcnt(1)\n" \
"s_barrier\n" \
"v_mfma_f32_16x16x32_bf16 v[100:103], a[0:3], v[20:23], v[156:159]\n" \
"s_nop 0\n" \
"ds_read_b128 v[132:135], %[xr] offset:2048\n" \
"buffer_load_dwordx4 v[56:59], %[lo], %[ws], s72 offen offset:1024 sc1\n" \
"v_mfma_f32_16x16x32_bf16 v[104:107], a[4:7], v[20:23], v[160:163]\n" \
"s_nop 0\n" \
"ds_write_b128 v214, v[128:131] offset:7168\n" \
"v_mfma_f32_16x16x32_bf16 v[100:103], a[32:35], v[24:27], v[100:103]\n" \
"s_nop 0\n" \
"ds_read_b128 v[136:139], %[xr] offset:3072\n" \
"v_mfma_f32_16x16x32_bf16 v[104:107], a[36:39], v[24:27], v[104:107]\n" \
"s_nop 0\n" \
"ds_read_b128 v[140:143], %[xr] offset:4096\n" \
"buffer_store_dword v203, %[fo], %[ws], s69 offen sc1\n" \
"v_mfma_f32_16x16x32_bf16 v[100:103], a[64:67], v[28:31], v[100:103]\n" \
"s_nop 0\n" \
"ds_read_b128 v[144:147], %[xr] offset:5120\n" \
"buffer_load_dwordx2 v[92:93], %[sl], %[ws], s75 offen offset:0 sc1\n" \
"v_mfma_f32_16x16x32_bf16 v[104:107], a[68:71], v[28:31], v[104:107]\n" \
"s_waitcnt lgkmcnt(3)\n" \
"s_barrier\n" \
"v_mfma_f32_16x16x32_bf16 v[100:103], a[96:99], v[32:35], v[100:103]\n" \
"s_nop 0\n" \
"ds_read_b128 v[148:151], %[xr] offset:6144\n" \
"buffer_load_dwordx4 v[60:63], %[lo], %[ws], s72 offen offset:2048 sc1\n" \
"v_mfma_f32_16x16x32_bf16 v[104:107], a[100:103], v[32:35], v[104:107]\n" \
"s_nop 0\n" \
"ds_read_b128 v[152:155], %[xr] offset:7168\n" \
"v_mfma_f32_16x16x32_bf16 v[100:103], a[128:131], v[36:39], v[100:103]\n" \
"s_waitcnt lgkmcnt(3)\n" \
"v_add_f32 v132, v132, v140\n" \
"v_add_f32 v133, v133, v141\n" \
"v_mfma_f32_16x16x32_bf16 v[104:107], a[132:135], v[36:39], v[104:107]\n" \
"s_nop 0\n" \
"v_add_f32 v134, v134, v142\n" \
"v_add_f32 v135, v135, v143\n" \
"v_mfma_f32_16x16x32_bf16 v[100:103], a[160:163], v[40:43], v[100:103]\n" \
"s_waitcnt lgkmcnt(2)\n" \
"buffer_load_dwordx4 v[64:67], %[lo], %[ws], s72 offen offset:3072 sc1\n" \
"v_add_f32 v136, v136, v144\n" \
"v_add_f32 v137, v137, v145\n" \
"v_mfma_f32_16x16x32_bf16 v[104:107], a[164:167], v[40:43], v[104:107]\n" \
"s_nop 0\n" \
"v_add_f32 v138, v138, v146\n" \
"v_add_f32 v139, v139, v147\n" \
"v_mfma_f32_16x16x32_bf16 v[100:103], a[192:195], v[44:47], v[100:103]\n" \
"s_waitcnt lgkmcnt(1)\n" \
"v_add_f32 v132, v132, v148\n" \
"v_add_f32 v133, v133, v149\n" \
"v_mfma_f32_16x16x32_bf16 v[104:107], a[196:199], v[44:47], v[104:107]\n" \
"s_nop 0\n" \
"v_add_f32 v134, v134, v150\n" \
"v_add_f32 v135, v135, v151\n" \
"v_mfma_f32_16x16x32_bf16 v[100:103], a[224:227], v[48:51], v[100:103]\n" \
"s_waitcnt lgkmcnt(0)\n" \
"buffer_load_dwordx4 v[68:71], %[lo], %[ws], s73 offen offset:0 sc1\n" \
"v_add_f32 v136, v136, v152\n" \
"v_add_f32 v137, v137, v153\n" \
"v_mfma_f32_16x16x32_bf16 v[104:107], a[228:231], v[48:51], v[104:107]\n" \
"s_nop 0\n" \
"v_add_f32 v138, v138, v154\n" \
"v_add_f32 v139, v139, v155\n" \
"v_mov_b32 v203, s65\n" \
"s_mov_b32 s69, s79\n" \
"buffer_load_dwordx4 v[72:75], %[lo], %[ws], s73 offen offset:1024 sc1\n" \
"v_pk_add_f32 v[100:101], v[100:101], v[132:133]\n" \
"v_pk_add_f32 v[102:103], v[102:103], v[134:135]\n" \
"v_pk_add_f32 v[104:105], v[104:105], v[136:137]\n" \
"v_pk_add_f32 v[106:107], v[106:107], v[138:139]\n" \
"v_pk_mul_f32 v[100:101], v[100:101], v[192:193] op_sel_hi:[1,0]\n" \
"v_pk_mul_f32 v[102:103], v[102:103], v[192:193] op_sel_hi:[1,0]\n" \
"v_pk_mul_f32 v[104:105], v[104:105], v[192:193] op_sel_hi:[1,0]\n" \
"v_pk_mul_f32 v[106:107], v[106:107], v[192:193] op_sel_hi:[1,0]\n" \
"v_pk_mul_f32 v[172:173], v[100:101], v[100:101]\n" \
"v_pk_mul_f32 v[174:175], v[102:103], v[102:103]\n" \
"v_pk_mul_f32 v[176:177], v[104:105], v[104:105]\n" \
"v_pk_mul_f32 v[178:179], v[106:107], v[106:107]\n" \
"buffer_load_dwordx4 v[76:79], %[lo], %[ws], s73 offen offset:2048 sc1\n" \
"v_pk_fma_f32 v[172:173], v[172:173], s[76:77], v[180:181]\n" \
"v_pk_fma_f32 v[174:175], v[174:175], s[76:77], v[180:181]\n" \
"v_pk_fma_f32 v[176:177], v[176:177], s[76:77], v[180:181]\n" \
"v_pk_fma_f32 v[178:179], v[178:179], s[76:77], v[180:181]\n" \
"v_pk_mul_f32 v[172:173], v[172:173], v[100:101]\n" \
"v_pk_mul_f32 v[174:175], v[174:175], v[102:103]\n" \
"v_pk_mul_f32 v[176:177], v[176:177], v[104:105]\n" \
"v_pk_mul_f32 v[178:179], v[178:179], v[106:107]\n" \
"buffer_load_dwordx4 v[80:83], %[lo], %[ws], s73 offen offset:3072 sc1\n" \
"v_exp_f32 v172, v172\n" \
"v_exp_f32 v173, v173\n" \
"v_exp_f32 v174, v174\n" \
"v_exp_f32 v175, v175\n" \
"v_exp_f32 v176, v176\n" \
"v_exp_f32 v177, v177\n" \
"v_exp_f32 v178, v178\n" \
"v_exp_f32 v179, v179\n" \
"v_pk_add_f32 v[172:173], v[172:173], 1.0 op_sel_hi:[1,0]\n" \
"v_pk_add_f32 v[174:175], v[174:175], 1.0 op_sel_hi:[1,0]\n" \
"v_pk_add_f32 v[176:177], v[176:177], 1.0 op_sel_hi:[1,0]\n" \
"v_pk_add_f32 v[178:179], v[178:179], 1.0 op_sel_hi:[1,0]\n" \
"v_rcp_f32 v172, v172\n" \
"v_rcp_f32 v173, v173\n" \
"v_rcp_f32 v174, v174\n" \
"v_rcp_f32 v175, v175\n" \
"v_rcp_f32 v176, v176\n" \
"v_rcp_f32 v177, v177\n" \
"v_rcp_f32 v178, v178\n" \
"v_rcp_f32 v179, v179\n" \
"v_pk_mul_f32 v[100:101], v[100:101], v[172:173]\n" \
"v_pk_mul_f32 v[102:103], v[102:103], v[174:175]\n" \
"v_pk_mul_f32 v[104:105], v[104:105], v[176:177]\n" \
"v_pk_mul_f32 v[106:107], v[106:107], v[178:179]\n" \
"v_cvt_pk_bf16_f32 v204, v100, v101\n" \
"v_cvt_pk_bf16_f32 v205, v102, v103\n" \
"v_cvt_pk_bf16_f32 v206, v104, v105\n" \
"v_cvt_pk_bf16_f32 v207, v106, v107\n" \
"v_mov_b32_dpp v208, v204 row_shr:%c[rot] row_mask:0xf bank_mask:0xf\n" \
"v_mov_b32_dpp v209, v205 row_shr:%c[rot] row_mask:0xf bank_mask:0xf\n" \
"v_mov_b32_dpp v210, v206 row_shr:%c[rot] row_mask:0xf bank_mask:0xf\n" \
"v_mov_b32_dpp v211, v207 row_shr:%c[rot] row_mask:0xf bank_mask:0xf\n" \
"buffer_store_dwordx4 v[208:211], %[so], %[ws], s68 offen sc1\n" \
"s_nop 1\n" \
"v_mov_b32_dpp v208, v204 row_shl:%c[tor] row_mask:0xf bank_mask:0xf\n" \
"v_mov_b32_dpp v209, v205 row_shl:%c[tor] row_mask:0xf bank_mask:0xf\n" \
"v_mov_b32_dpp v210, v206 row_shl:%c[tor] row_mask:0xf bank_mask:0xf\n" \
"v_mov_b32_dpp v211, v207 row_shl:%c[tor] row_mask:0xf bank_mask:0xf\n" \
"s_add_u32 s68, s68, 32768\n" \
"s_cmp_eq_u32 s72, s67\n" \
"s_cbranch_scc1 Lw1_body1_%=\n" \
"s_waitcnt vmcnt(11)\n" \
"Lw1_top1_%=:\n" \
"v_cmp_gt_u32 vcc, s74, v202\n" \
"s_mov_b32 s72, s67\n" \
"s_mov_b32 s75, s70\n" \
"s_add_u32 s73, s67, 4096\n" \
"s_cbranch_vccnz Lw1_sgo1_%=\n" \
"Lw1_go1_%=:\n" \
"buffer_load_dwordx4 v[52:55], %[lo], %[ws], s72 offen offset:0 sc1\n" \
"buffer_load_dwordx4 v[56:59], %[lo], %[ws], s72 offen offset:1024 sc1\n" \
"buffer_load_dwordx2 v[92:93], %[sl], %[ws], s75 offen offset:0 sc1\n" \
"buffer_load_dwordx4 v[60:63], %[lo], %[ws], s72 offen offset:2048 sc1\n" \
"buffer_load_dwordx4 v[64:67], %[lo], %[ws], s72 offen offset:3072 sc1\n" \
"buffer_load_dwordx4 v[68:71], %[lo], %[ws], s73 offen offset:0 sc1\n" \
"buffer_load_dwordx4 v[72:75], %[lo], %[ws], s73 offen offset:1024 sc1\n" \
"buffer_load_dwordx4 v[76:79], %[lo], %[ws], s73 offen offset:2048 sc1\n" \
"buffer_load_dwordx4 v[80:83], %[lo], %[ws], s73 offen offset:3072 sc1\n" \
"s_waitcnt vmcnt(9)\n" \
"buffer_store_dword v203, %[fo], %[ws], s69 offen sc1\n" \
"s_mov_b32 s69, 1073741824\n" \
"Lw1_body1_%=:\n" \
"s_waitcnt vmcnt(9)\n" \
"v_mfma_f32_16x16x32_bf16 v[108:111], a[8:11], v[52:55], 0\n" \
"s_nop 2\n" \
"v_mfma_f32_16x16x32_bf16 v[112:115], a[12:15], v[52:55], 0\n" \
"s_nop 2\n" \
"v_mfma_f32_16x16x32_bf16 v[116:119], a[16:19], v[52:55], 0\n" \
"s_add_u32 s74, s74, 1\n" \
"s_add_u32 s67, s67, 32768\n" \
"s_nop 0\n" \
"v_mfma_f32_16x16x32_bf16 v[120:123], a[20:23], v[52:55], 0\n" \
"s_add_u32 s65, s65, 1\n" \
"s_add_u32 s70, s70, 2048\n" \
"s_nop 0\n" \
"v_mfma_f32_16x16x32_bf16 v[124:127], a[24:27], v[52:55], 0\n" \
"s_cmp_gt_u32 s65, 104\n" \
"s_cselect_b32 s79, 1073741824, 0\n" \
"s_cselect_b32 s69, 0, s69\n" \
"v_mfma_f32_16x16x32_bf16 v[128:131], a[28:31], v[52:55], 0\n" \
"s_waitcnt vmcnt(8)\n" \
"s_addc_u32 s66, s65, -1\n" \
"s_nop 0\n" \
"v_mfma_f32_16x16x32_bf16 v[108:111], a[40:43], v[56:59], v[108:111]\n" \
"s_nop 0\n" \
"buffer_load_dword v202, %[fvo], %[ws], 0 offen sc1\n" \
"v_mov_b32 v203, s66\n" \
"v_mfma_f32_16x16x32_bf16 v[112:115], a[44:47], v[56:59], v[112:115]\n" \
"s_waitcnt vmcnt(8)\n" \
"v_cmp_u_f32 vcc, v92, v93\n" \
"v_mfma_f32_16x16x32_bf16 v[116:119], a[48:51], v[56:59], v[116:119]\n" \
"s_cbranch_vccnz Lw1_sk10_%=\n" \
"Lw1_k10_%=:\n" \
"v_mfma_f32_16x16x32_bf16 v[120:123], a[52:55], v[56:59], v[120:123]\n" \
"s_nop 2\n" \
"v_mfma_f32_16x16x32_bf16 v[124:127], a[56:59], v[56:59], v[124:127]\n" \
"s_nop 0\n" \
"v_permlane32_swap_b32 v92, v93\n" \
"v_mfma_f32_16x16x32_bf16 v[128:131], a[60:63], v[56:59], v[128:131]\n" \
"s_waitcnt vmcnt(7)\n" \
"v_add_f32 v94, v92, v93\n" \
"v_add_f32 v92, v92, v93\n" \
"v_mfma_f32_16x16x32_bf16 v[108:111], a[72:75], v[60:63], v[108:111]\n" \
"s_nop 0\n" \
"v_permlane16_swap_b32 v92, v94\n" \
"v_mfma_f32_16x16x32_bf16 v[112:115], a[76:79], v[60:63], v[112:115]\n" \
"s_nop 0\n" \
"v_add_f32 v92, v92, v94\n" \
"v_mfma_f32_16x16x32_bf16 v[116:119], a[80:83], v[60:63], v[116:119]\n" \
"s_nop 0\n" \
"ds_write_b32 %[sw], v92 offset:32768\n" \
"v_mfma_f32_16x16x32_bf16 v[120:123], a[84:87], v[60:63], v[120:123]\n" \
"s_nop 2\n" \
"v_mfma_f32_16x16x32_bf16 v[124:127], a[88:91], v[60:63], v[124:127]\n" \
"s_nop 2\n" \
"v_mfma_f32_16x16x32_bf16 v[128:131], a[92:95], v[60:63], v[128:131]\n" \
"s_waitcnt vmcnt(6) lgkmcnt(0)\n" \
"s_barrier\n" \
"v_mfma_f32_16x16x32_bf16 v[108:111], a[104:107], v[64:67], v[108:111]\n" \
"s_nop 0\n" \
"ds_read_b128 v[190:193], %[sr] offset:32768\n" \
"v_mfma_f32_16x16x32_bf16 v[112:115], a[108:111], v[64:67], v[112:115]\n" \
"s_nop 0\n" \
"ds_read_b128 v[194:197], %[sr] offset:32784\n" \
"v_mfma_f32_16x16x32_bf16 v[116:119], a[112:115], v[64:67], v[116:119]\n" \
"s_nop 2\n" \
"v_mfma_f32_16x16x32_bf16 v[120:123], a[116:119], v[64:67], v[120:123]\n" \
"s_nop 2\n" \
"v_mfma_f32_16x16x32_bf16 v[124:127], a[120:123], v[64:67], v[124:127]\n" \
"s_nop 2\n" \
"v_mfma_f32_16x16x32_bf16 v[128:131], a[124:127], v[64:67], v[128:131]\n" \
"s_waitcnt vmcnt(5) lgkmcnt(1)\n" \
"v_add_f32 v190, v190, v191\n" \
"v_add_f32 v192, v192, v193\n" \
"v_mfma_f32_16x16x32_bf16 v[108:111], a[136:139], v[68:71], v[108:111]\n" \
"s_waitcnt lgkmcnt(0)\n" \
"v_add_f32 v194, v194, v195\n" \
"v_add_f32 v196, v196, v197\n" \
"v_mfma_f32_16x16x32_bf16 v[112:115], a[140:143], v[68:71], v[112:115]\n" \
"s_nop 0\n" \
"v_add_f32 v190, v190, v192\n" \
"v_add_f32 v194, v194, v196\n" \
"v_mfma_f32_16x16x32_bf16 v[116:119], a[144:147], v[68:71], v[116:119]\n" \
"s_nop 0\n" \
"v_mul_f32 v190, 0xba800000, v190\n" \
"v_mul_f32 v194, 0x3a800000, v194\n" \
"v_mfma_f32_16x16x32_bf16 v[120:123], a[148:151], v[68:71], v[120:123]\n" \
"s_nop 0\n" \
"v_mul_f32 v156, v190, v164\n" \
"v_mul_f32 v157, v190, v165\n" \
"v_mfma_f32_16x16x32_bf16 v[124:127], a[152:155], v[68:71], v[124:127]\n" \
"s_nop 0\n" \
"v_mul_f32 v158, v190, v166\n" \
"v_mul_f32 v159, v190, v167\n" \
"v_mfma_f32_16x16x32_bf16 v[128:131], a[156:159], v[68:71], v[128:131]\n" \
"s_waitcnt vmcnt(4)\n" \
"v_mul_f32 v160, v190, v168\n" \
"v_mul_f32 v161, v190, v169\n" \
"v_mfma_f32_16x16x32_bf16 v[108:111], a[168:171], v[72:75], v[108:111]\n" \
"s_waitcnt vmcnt(3)\n" \
"v_mul_f32 v162, v190, v170\n" \
"v_mul_f32 v163, v190, v171\n" \
"v_mfma_f32_16x16x32_bf16 v[108:111], a[200:203], v[76:79], v[108:111]\n" \
"s_waitcnt vmcnt(2)\n" \
"v_fma_f32 v194, -v190, v190, v194\n" \
"v_max_f32 v194, 0, v194\n" \
"v_mfma_f32_16x16x32_bf16 v[108:111], a[232:235], v[80:83], v[108:111]\n" \
"s_nop 0\n" \
"v_add_f32 v192, 0x3727c5ac, v194\n" \
"v_rsq_f32 v192, v192\n" \
"v_mfma_f32_16x16x32_bf16 v[112:115], a[172:175], v[72:75], v[112:115]\n" \
"s_nop 0\n" \
"v_mul_f32 v194, v194, v192\n" \
"v_fma_f32 v194, v194, v192, s78\n" \
"v_mfma_f32_16x16x32_bf16 v[112:115], a[204:207], v[76:79], v[112:115]\n" \
"s_nop 0\n" \
"ds_write_b128 v212, v[108:111] offset:34816\n" \
"v_mfma_f32_16x16x32_bf16 v[112:115], a[236:239], v[80:83], v[112:115]\n" \
"s_nop 0\n" \
"v_cmp_u_f32 vcc, v108, v108\n" \
"v_rsq_f32 v194, v194\n" \
"v_mfma_f32_16x16x32_bf16 v[116:119], a[176:179], v[72:75], v[116:119]\n" \
"s_cbranch_vccnz Lw1_sn1_%=\n" \
"Lw1_n1_%=:\n" \
"v_mul_f32 v192, v192, v194\n" \
"v_mfma_f32_16x16x32_bf16 v[116:119], a[208:211], v[76:79], v[116:119]\n" \
"s_nop 0\n" \
"ds_write_b128 v212, v[112:115] offset:35840\n" \
"v_mfma_f32_16x16x32_bf16 v[116:119], a[240:243], v[80:83], v[116:119]\n" \
"s_nop 2\n" \
"v_mfma_f32_16x16x32_bf16 v[120:123], a[180:183], v[72:75], v[120:123]\n" \
"s_nop 2\n" \
"v_mfma_f32_16x16x32_bf16 v[120:123], a[212:215], v[76:79], v[120:123]\n" \
"s_nop 0\n" \
"ds_write_b128 v213, v[116:119] offset:36864\n" \
"v_mfma_f32_16x16x32_bf16 v[120:123], a[244:247], v[80:83], v[120:123]\n" \
"s_waitcnt vmcnt(0)\n" \
"v_cmp_gt_u32 vcc, s74, v202\n" \
"v_mfma_f32_16x16x32_bf16 v[124:127], a[184:187], v[72:75], v[124:127]\n" \
"s_nop 0\n" \
"buffer_load_dword v202, %[fvo], %[ws], 0 offen sc1\n" \
"v_mfma_f32_16x16x32_bf16 v[124:127], a[216:219], v[76:79], v[124:127]\n" \
"s_cmp_eq_u64 vcc, 0\n" \
"s_nop 0\n" \
"ds_write_b128 v213, v[120:123] offset:37888\n" \
"v_mfma_f32_16x16x32_bf16 v[124:127], a[248:251], v[80:83], v[124:127]\n" \
"s_cselect_b32 s72, s67, 1073741824\n" \
"buffer_load_dwordx4 v[20:23], %[lo], %[ws], s72 offen offset:0 sc1\n" \
"v_mfma_f32_16x16x32_bf16 v[128:131], a[188:191], v[72:75], v[128:131]\n" \
"s_cselect_b32 s75, s70, 1073741824\n" \
"s_add_u32 s73, s72, 4096\n" \
"s_nop 0\n" \
"v_mfma_f32_16x16x32_bf16 v[128:131], a[220:223], v[76:79], v[128:131]\n" \
"s_nop 0\n" \
"ds_write_b128 v214, v[124:127] offset:38912\n" \
"v_mfma_f32_16x16x32_bf16 v[128:131], a[252:255], v[80:83], v[128:131]\n" \
"s_waitcnt lgkmcnt(1)\n" \
"s_barrier\n" \
"v_mfma_f32_16x16x32_bf16 v[100:103], a[0:3], v[52:55], v[156:159]\n" \
"s_nop 0\n" \
"ds_read_b128 v[132:135], %[xr] offset:34816\n" \
"buffer_load_dwordx4 v[24:27], %[lo], %[ws], s72 offen offset:1024 sc1\n" \
"v_mfma_f32_16x16x32_bf16 v[104:107], a[4:7], v[52:55], v[160:163]\n" \
"s_nop 0\n" \
"ds_write_b128 v214, v[128:131] offset:39936\n" \
"v_mfma_f32_16x16x32_bf16 v[100:103], a[32:35], v[56:59], v[100:103]\n" \
"s_nop 0\n" \
"ds_read_b128 v[136:139], %[xr] offset:35840\n" \
"v_mfma_f32_16x16x32_bf16 v[104:107], a[36:39], v[56:59], v[104:107]\n" \
"s_nop 0\n" \
"ds_read_b128 v[140:143], %[xr] offset:36864\n" \
"buffer_store_dword v203, %[fo], %[ws], s69 offen sc1\n" \
"v_mfma_f32_16x16x32_bf16 v[100:103], a[64:67], v[60:63], v[100:103]\n" \
"s_nop 0\n" \
"ds_read_b128 v[144:147], %[xr] offset:37888\n" \
"buffer_load_dwordx2 v[84:85], %[sl], %[ws], s75 offen offset:0 sc1\n" \
"v_mfma_f32_16x16x32_bf16 v[104:107], a[68:71], v[60:63], v[104:107]\n" \
"s_waitcnt lgkmcnt(3)\n" \
"s_barrier\n" \
"v_mfma_f32_16x16x32_bf16 v[100:103], a[96:99], v[64:67], v[100:103]\n" \
"s_nop 0\n" \
"ds_read_b128 v[148:151], %[xr] offset:38912\n" \
"buffer_load_dwordx4 v[28:31], %[lo], %[ws], s72 offen offset:2048 sc1\n" \
"v_mfma_f32_16x16x32_bf16 v[104:107], a[100:103], v[64:67], v[104:107]\n" \
"s_nop 0\n" \
"ds_read_b128 v[152:155], %[xr] offset:39936\n" \
"v_mfma_f32_16x16x32_bf16 v[100:103], a[128:131], v[68:71], v[100:103]\n" \
"s_waitcnt lgkmcnt(3)\n" \
"v_add_f32 v132, v132, v140\n" \
"v_add_f32 v133, v133, v141\n" \
"v_mfma_f32_16x16x32_bf16 v[104:107], a[132:135], v[68:71], v[104:107]\n" \
"s_nop 0\n" \
"v_add_f32 v134, v134, v142\n" \
"v_add_f32 v135, v135, v143\n" \
"v_mfma_f32_16x16x32_bf16 v[100:103], a[160:163], v[72:75], v[100:103]\n" \
"s_waitcnt lgkmcnt(2)\n" \
"buffer_load_dwordx4 v[32:35], %[lo], %[ws], s72 offen offset:3072 sc1\n" \
"v_add_f32 v136, v136, v144\n" \
"v_add_f32 v137, v137, v145\n" \
"v_mfma_f32_16x16x32_bf16 v[104:107], a[164:167], v[72:75], v[104:107]\n" \
"s_nop 0\n" \
"v_add_f32 v138, v138, v146\n" \
"v_add_f32 v139, v139, v147\n" \
"v_mfma_f32_16x16x32_bf16 v[100:103], a[192:195], v[76:79], v[100:103]\n" \
"s_waitcnt lgkmcnt(1)\n" \
"v_add_f32 v132, v132, v148\n" \
"v_add_f32 v133, v133, v149\n" \
"v_mfma_f32_16x16x32_bf16 v[104:107], a[196:199], v[76:79], v[104:107]\n" \
"s_nop 0\n" \
"v_add_f32 v134, v134, v150\n" \
"v_add_f32 v135, v135, v151\n" \
"v_mfma_f32_16x16x32_bf16 v[100:103], a[224:227], v[80:83], v[100:103]\n" \
"s_waitcnt lgkmcnt(0)\n" \
"buffer_load_dwordx4 v[36:39], %[lo], %[ws], s73 offen offset:0 sc1\n" \
"v_add_f32 v136, v136, v152\n" \
"v_add_f32 v137, v137, v153\n" \
"v_mfma_f32_16x16x32_bf16 v[104:107], a[228:231], v[80:83], v[104:107]\n" \
"s_nop 0\n" \
"v_add_f32 v138, v138, v154\n" \
"v_add_f32 v139, v139, v155\n" \
"v_mov_b32 v203, s65\n" \
"s_mov_b32 s69, s79\n" \
"buffer_load_dwordx4 v[40:43], %[lo], %[ws], s73 offen offset:1024 sc1\n" \
"v_pk_add_f32 v[100:101], v[100:101], v[132:133]\n" \
"v_pk_add_f32 v[102:103], v[102:103], v[134:135]\n" \
"v_pk_add_f32 v[104:105], v[104:105], v[136:137]\n" \
"v_pk_add_f32 v[106:107], v[106:107], v[138:139]\n" \
"v_pk_mul_f32 v[100:101], v[100:101], v[192:193] op_sel_hi:[1,0]\n" \
"v_pk_mul_f32 v[102:103], v[102:103], v[192:193] op_sel_hi:[1,0]\n" \
"v_pk_mul_f32 v[104:105], v[104:105], v[192:193] op_sel_hi:[1,0]\n" \
"v_pk_mul_f32 v[106:107], v[106:107], v[192:193] op_sel_hi:[1,0]\n" \
"v_pk_mul_f32 v[172:173], v[100:101], v[100:101]\n" \
"v_pk_mul_f32 v[174:175], v[102:103], v[102:103]\n" \
"v_pk_mul_f32 v[176:177], v[104:105], v[104:105]\n" \
"v_pk_mul_f32 v[178:179], v[106:107], v[106:107]\n" \
"buffer_load_dwordx4 v[44:47], %[lo], %[ws], s73 offen offset:2048 sc1\n" \
"v_pk_fma_f32 v[172:173], v[172:173], s[76:77], v[180:181]\n" \
"v_pk_fma_f32 v[174:175], v[174:175], s[76:77], v[180:181]\n" \
"v_pk_fma_f32 v[176:177], v[176:177], s[76:77], v[180:181]\n" \
"v_pk_fma_f32 v[178:179], v[178:179], s[76:77], v[180:181]\n" \
"v_pk_mul_f32 v[172:173], v[172:173], v[100:101]\n" \
"v_pk_mul_f32 v[174:175], v[174:175], v[102:103]\n" \
"v_pk_mul_f32 v[176:177], v[176:177], v[104:105]\n" \
"v_pk_mul_f32 v[178:179], v[178:179], v[106:107]\n" \
"buffer_load_dwordx4 v[48:51], %[lo], %[ws], s73 offen offset:3072 sc1\n" \
"v_exp_f32 v172, v172\n" \
"v_exp_f32 v173, v173\n" \
"v_exp_f32 v174, v174\n" \
"v_exp_f32 v175, v175\n" \
"v_exp_f32 v176, v176\n" \
"v_exp_f32 v177, v177\n" \
"v_exp_f32 v178, v178\n" \
"v_exp_f32 v179, v179\n" \
"v_pk_add_f32 v[172:173], v[172:173], 1.0 op_sel_hi:[1,0]\n" \
"v_pk_add_f32 v[174:175], v[174:175], 1.0 op_sel_hi:[1,0]\n" \
"v_pk_add_f32 v[176:177], v[176:177], 1.0 op_sel_hi:[1,0]\n" \
"v_pk_add_f32 v[178:179], v[178:179], 1.0 op_sel_hi:[1,0]\n" \
"v_rcp_f32 v172, v172\n" \
"v_rcp_f32 v173, v173\n" \
"v_rcp_f32 v174, v174\n" \
"v_rcp_f32 v175, v175\n" \
"v_rcp_f32 v176, v176\n" \
"v_rcp_f32 v177, v177\n" \
"v_rcp_f32 v178, v178\n" \
"v_rcp_f32 v179, v179\n" \
"v_pk_mul_f32 v[100:101], v[100:101], v[172:173]\n" \
"v_pk_mul_f32 v[102:103], v[102:103], v[174:175]\n" \
"v_pk_mul_f32 v[104:105], v[104:105], v[176:177]\n" \
"v_pk_mul_f32 v[106:107], v[106:107], v[178:179]\n" \
"v_cvt_pk_bf16_f32 v204, v100, v101\n" \
"v_cvt_pk_bf16_f32 v205, v102, v103\n" \
"v_cvt_pk_bf16_f32 v206, v104, v105\n" \
"v_cvt_pk_bf16_f32 v207, v106, v107\n" \
"v_mov_b32_dpp v208, v204 row_shr:%c[rot] row_mask:0xf bank_mask:0xf\n" \
"v_mov_b32_dpp v209, v205 row_shr:%c[rot] row_mask:0xf bank_mask:0xf\n" \
"v_mov_b32_dpp v210, v206 row_shr:%c[rot] row_mask:0xf bank_mask:0xf\n" \
"v_mov_b32_dpp v211, v207 row_shr:%c[rot] row_mask:0xf bank_mask:0xf\n" \
"buffer_store_dwordx4 v[208:211], %[so], %[ws], s68 offen sc1\n" \
"s_nop 1\n" \
"v_mov_b32_dpp v208, v204 row_shl:%c[tor] row_mask:0xf bank_mask:0xf\n" \
"v_mov_b32_dpp v209, v205 row_shl:%c[tor] row_mask:0xf bank_mask:0xf\n" \
"v_mov_b32_dpp v210, v206 row_shl:%c[tor] row_mask:0xf bank_mask:0xf\n" \
"v_mov_b32_dpp v211, v207 row_shl:%c[tor] row_mask:0xf bank_mask:0xf\n" \
"s_add_u32 s68, s68, 32768\n" \
"s_cmp_eq_u32 s65, s80\n" \
"s_cbranch_scc1 Lw1_end_%=\n" \
"Lw1_on_%=:\n" \
"s_cmp_eq_u32 s72, s67\n" \
"s_cbranch_scc1 Lw1_body0_%=\n" \
"s_waitcnt vmcnt(11)\n" \
"s_branch Lw1_top0_%=\n" \
"Lw1_end_%=:\n" \
"v_mov_b32 v204, 0\n" \
"s_nop 1\n" \
"v_mov_b32_dpp v208, v204 row_shr:%c[rot] row_mask:0xf bank_mask:0xf\n" \
"v_mov_b32_dpp v209, v204 row_shr:%c[rot] row_mask:0xf bank_mask:0xf\n" \
"v_mov_b32_dpp v210, v204 row_shr:%c[rot] row_mask:0xf bank_mask:0xf\n" \
"v_mov_b32_dpp v211, v204 row_shr:%c[rot] row_mask:0xf bank_mask:0xf\n" \
"buffer_store_dwordx4 v[208:211], %[so], %[ws], s68 offen sc1\n" \
"s_endpgm\n" \
"Lw1_sgo0_%=:\n" \
"s_waitcnt vmcnt(0)\n" \
"buffer_load_dword v202, %[fvo], %[ws], 0 offen sc1\n" \
"buffer_store_dword v203, %[fo], %[ws], s69 offen sc1\n" \
"s_mov_b32 s69, 1073741824\n" \
"s_waitcnt vmcnt(1)\n" \
"s_branch Lw1_cgo0_%=\n" \
"Lw1_pgo0_%=:\n" \
"s_sub_u32 s64, s64, 1\n" \
"s_cbranch_scc1 Lw1_dead_%=\n" \
"buffer_load_dword v202, %[fvo], %[ws], 0 offen sc1\n" \
"s_waitcnt vmcnt(0)\n" \
"Lw1_cgo0_%=:\n" \
"v_cmp_gt_u32 vcc, s74, v202\n" \
"s_cbranch_vccz Lw1_go0_%=\n" \
"s_sleep 1\n" \
"s_branch Lw1_pgo0_%=\n" \
"Lw1_sk00_%=:\n" \
"v_max3_u32 v215, v84, v85, v85\n" \
"v_cmp_eq_u32 vcc, -1, v215\n" \
"s_nop 1\n" \
"s_cbranch_vccz Lw1_k00_%=\n" \
"Lw1_rk00_%=:\n" \
"s_sub_u32 s64, s64, 1\n" \
"s_cbranch_scc1 Lw1_dead_%=\n" \
"buffer_load_dwordx2 v[84:85], %[sl], %[ws], s75 offen offset:0 sc1\n" \
"s_waitcnt vmcnt(0)\n" \
"v_max3_u32 v215, v84, v85, v85\n" \
"v_cmp_eq_u32 vcc, -1, v215\n" \
"s_nop 1\n" \
"s_cbranch_vccnz Lw1_rk00_%=\n" \
"s_branch Lw1_k00_%=\n" \
"Lw1_sn0_%=:\n" \
"v_max3_u32 v215, v20, v21, v22\n" \
"v_max3_u32 v215, v215, v23, v24\n" \
"v_max3_u32 v215, v215, v25, v26\n" \
"v_max3_u32 v215, v215, v27, v28\n" \
"v_max3_u32 v215, v215, v29, v30\n" \
"v_max3_u32 v215, v215, v31, v32\n" \
"v_max3_u32 v215, v215, v33, v34\n" \
"v_max3_u32 v215, v215, v35, v36\n" \
"v_max3_u32 v215, v215, v37, v38\n" \
"v_max3_u32 v215, v215, v39, v40\n" \
"v_max3_u32 v215, v215, v41, v42\n" \
"v_max3_u32 v215, v215, v43, v44\n" \
"v_max3_u32 v215, v215, v45, v46\n" \
"v_max3_u32 v215, v215, v47, v48\n" \
"v_max3_u32 v215, v215, v49, v50\n" \
"v_max3_u32 v215, v215, v51, v51\n" \
"v_cmp_eq_u32 vcc, -1, v215\n" \
"s_nop 1\n" \
"s_cbranch_vccz Lw1_n0_%=\n" \
"Lw1_rn0_%=:\n" \
"s_sub_u32 s64, s64, 1\n" \
"s_cbranch_scc1 Lw1_dead_%=\n" \
"buffer_load_dwordx4 v[20:23], %[lo], %[ws], s72 offen offset:0 sc1\n" \
"buffer_load_dwordx4 v[24:27], %[lo], %[ws], s72 offen offset:1024 sc1\n" \
"buffer_load_dwordx4 v[28:31], %[lo], %[ws], s72 offen offset:2048 sc1\n" \
"buffer_load_dwordx4 v[32:35], %[lo], %[ws], s72 offen offset:3072 sc1\n" \
"buffer_load_dwordx4 v[36:39], %[lo], %[ws], s73 offen offset:0 sc1\n" \
"buffer_load_dwordx4 v[40:43], %[lo], %[ws], s73 offen offset:1024 sc1\n" \
"buffer_load_dwordx4 v[44:47], %[lo], %[ws], s73 offen offset:2048 sc1\n" \
"buffer_load_dwordx4 v[48:51], %[lo], %[ws], s73 offen offset:3072 sc1\n" \
"s_waitcnt vmcnt(0)\n" \
"v_max3_u32 v215, v20, v21, v22\n" \
"v_max3_u32 v215, v215, v23, v24\n" \
"v_max3_u32 v215, v215, v25, v26\n" \
"v_max3_u32 v215, v215, v27, v28\n" \
"v_max3_u32 v215, v215, v29, v30\n" \
"v_max3_u32 v215, v215, v31, v32\n" \
"v_max3_u32 v215, v215, v33, v34\n" \
"v_max3_u32 v215, v215, v35, v36\n" \
"v_max3_u32 v215, v215, v37, v38\n" \
"v_max3_u32 v215, v215, v39, v40\n" \
"v_max3_u32 v215, v215, v41, v42\n" \
"v_max3_u32 v215, v215, v43, v44\n" \
"v_max3_u32 v215, v215, v45, v46\n" \
"v_max3_u32 v215, v215, v47, v48\n" \
"v_max3_u32 v215, v215, v49, v50\n" \
"v_max3_u32 v215, v215, v51, v51\n" \
"v_cmp_eq_u32 vcc, -1, v215\n" \
"s_nop 1\n" \
"s_cbranch_vccnz Lw1_rn0_%=\n" \
"v_mfma_f32_16x16x32_bf16 v[108:111], a[8:11], v[20:23], 0\n" \
"v_mfma_f32_16x16x32_bf16 v[112:115], a[12:15], v[20:23], 0\n" \
"v_mfma_f32_16x16x32_bf16 v[116:119], a[16:19], v[20:23], 0\n" \
"v_mfma_f32_16x16x32_bf16 v[120:123], a[20:23], v[20:23], 0\n" \
"v_mfma_f32_16x16x32_bf16 v[124:127], a[24:27], v[20:23], 0\n" \
"v_mfma_f32_16x16x32_bf16 v[128:131], a[28:31], v[20:23], 0\n" \
"v_mfma_f32_16x16x32_bf16 v[108:111], a[40:43], v[24:27], v[108:111]\n" \
"v_mfma_f32_16x16x32_bf16 v[112:115], a[44:47], v[24:27], v[112:115]\n" \
"v_mfma_f32_16x16x32_bf16 v[116:119], a[48:51], v[24:27], v[116:119]\n" \
"v_mfma_f32_16x16x32_bf16 v[120:123], a[52:55], v[24:27], v[120:123]\n" \
"v_mfma_f32_16x16x32_bf16 v[124:127], a[56:59], v[24:27], v[124:127]\n" \
"v_mfma_f32_16x16x32_bf16 v[128:131], a[60:63], v[24:27], v[128:131]\n" \
"v_mfma_f32_16x16x32_bf16 v[108:111], a[72:75], v[28:31], v[108:111]\n" \
"v_mfma_f32_16x16x32_bf16 v[112:115], a[76:79], v[28:31], v[112:115]\n" \
"v_mfma_f32_16x16x32_bf16 v[116:119], a[80:83], v[28:31], v[116:119]\n" \
"v_mfma_f32_16x16x32_bf16 v[120:123], a[84:87], v[28:31], v[120:123]\n" \
"v_mfma_f32_16x16x32_bf16 v[124:127], a[88:91], v[28:31], v[124:127]\n" \
"v_mfma_f32_16x16x32_bf16 v[128:131], a[92:95], v[28:31], v[128:131]\n" \
"v_mfma_f32_16x16x32_bf16 v[108:111], a[104:107], v[32:35], v[108:111]\n" \
"v_mfma_f32_16x16x32_bf16 v[112:115], a[108:111], v[32:35], v[112:115]\n" \
"v_mfma_f32_16x16x32_bf16 v[116:119], a[112:115], v[32:35], v[116:119]\n" \
"v_mfma_f32_16x16x32_bf16 v[120:123], a[116:119], v[32:35], v[120:123]\n" \
"v_mfma_f32_16x16x32_bf16 v[124:127], a[120:123], v[32:35], v[124:127]\n" \
"v_mfma_f32_16x16x32_bf16 v[128:131], a[124:127], v[32:35], v[128:131]\n" \
"v_mfma_f32_16x16x32_bf16 v[108:111], a[136:139], v[36:39], v[108:111]\n" \
"v_mfma_f32_16x16x32_bf16 v[112:115], a[140:143], v[36:39], v[112:115]\n" \
"v_mfma_f32_16x16x32_bf16 v[116:119], a[144:147], v[36:39], v[116:119]\n" \
"v_mfma_f32_16x16x32_bf16 v[120:123], a[148:151], v[36:39], v[120:123]\n" \
"v_mfma_f32_16x16x32_bf16 v[124:127], a[152:155], v[36:39], v[124:127]\n" \
"v_mfma_f32_16x16x32_bf16 v[128:131], a[156:159], v[36:39], v[128:131]\n" \
"v_mfma_f32_16x16x32_bf16 v[108:111], a[168:171], v[40:43], v[108:111]\n" \
"v_mfma_f32_16x16x32_bf16 v[108:111], a[200:203], v[44:47], v[108:111]\n" \
"v_mfma_f32_16x16x32_bf16 v[108:111], a[232:235], v[48:51], v[108:111]\n" \
"v_mfma_f32_16x16x32_bf16 v[112:115], a[172:175], v[40:43], v[112:115]\n" \
"v_mfma_f32_16x16x32_bf16 v[112:115], a[204:207], v[44:47], v[112:115]\n" \
"v_mfma_f32_16x16x32_bf16 v[112:115], a[236:239], v[48:51], v[112:115]\n" \
"v_mfma_f32_16x16x32_bf16 v[116:119], a[176:179], v[40:43], v[116:119]\n" \
"s_nop 7\n" \
"ds_write_b128 v212, v[108:111] offset:2048\n" \
"s_branch Lw1_n0_%=\n" \
"Lw1_sgo1_%=:\n" \
"s_waitcnt vmcnt(0)\n" \
"buffer_load_dword v202, %[fvo], %[ws], 0 offen sc1\n" \
"buffer_store_dword v203, %[fo], %[ws], s69 offen sc1\n" \
"s_mov_b32 s69, 1073741824\n" \
"s_waitcnt vmcnt(1)\n" \
"s_branch Lw1_cgo1_%=\n" \
"Lw1_pgo1_%=:\n" \
"s_sub_u32 s64, s64, 1\n" \
"s_cbranch_scc1 Lw1_dead_%=\n" \
"buffer_load_dword v202, %[fvo], %[ws], 0 offen sc1\n" \
"s_waitcnt vmcnt(0)\n" \
"Lw1_cgo1_%=:\n" \
"v_cmp_gt_u32 vcc, s74, v202\n" \
"s_cbranch_vccz Lw1_go1_%=\n" \
"s_sleep 1\n" \
"s_branch Lw1_pgo1_%=\n" \
"Lw1_sk10_%=:\n" \
"v_max3_u32 v215, v92, v93, v93\n" \
"v_cmp_eq_u32 vcc, -1, v215\n" \
"s_nop 1\n" \
"s_cbranch_vccz Lw1_k10_%=\n" \
"Lw1_rk10_%=:\n" \
"s_sub_u32 s64, s64, 1\n" \
"s_cbranch_scc1 Lw1_dead_%=\n" \
"buffer_load_dwordx2 v[92:93], %[sl], %[ws], s75 offen offset:0 sc1\n" \
"s_waitcnt vmcnt(0)\n" \
"v_max3_u32 v215, v92, v93, v93\n" \
"v_cmp_eq_u32 vcc, -1, v215\n" \
"s_nop 1\n" \
"s_cbranch_vccnz Lw1_rk10_%=\n" \
"s_branch Lw1_k10_%=\n" \
"Lw1_sn1_%=:\n" \
"v_max3_u32 v215, v52, v53, v54\n" \
"v_max3_u32 v215, v215, v55, v56\n" \
"v_max3_u32 v215, v215, v57, v58\n" \
"v_max3_u32 v215, v215, v59, v60\n" \
"v_max3_u32 v215, v215, v61, v62\n" \
"v_max3_u32 v215, v215, v63, v64\n" \
"v_max3_u32 v215, v215, v65, v66\n" \
"v_max3_u32 v215, v215, v67, v68\n" \
"v_max3_u32 v215, v215, v69, v70\n" \
"v_max3_u32 v215, v215, v71, v72\n" \
"v_max3_u32 v215, v215, v73, v74\n" \
"v_max3_u32 v215, v215, v75, v76\n" \
"v_max3_u32 v215, v215, v77, v78\n" \
"v_max3_u32 v215, v215, v79, v80\n" \
"v_max3_u32 v215, v215, v81, v82\n" \
"v_max3_u32 v215, v215, v83, v83\n" \
"v_cmp_eq_u32 vcc, -1, v215\n" \
"s_nop 1\n" \
"s_cbranch_vccz Lw1_n1_%=\n" \
"Lw1_rn1_%=:\n" \
"s_sub_u32 s64, s64, 1\n" \
"s_cbranch_scc1 Lw1_dead_%=\n" \
"buffer_load_dwordx4 v[52:55], %[lo], %[ws], s72 offen offset:0 sc1\n" \
"buffer_load_dwordx4 v[56:59], %[lo], %[ws], s72 offen offset:1024 sc1\n" \
"buffer_load_dwordx4 v[60:63], %[lo], %[ws], s72 offen offset:2048 sc1\n" \
"buffer_load_dwordx4 v[64:67], %[lo], %[ws], s72 offen offset:3072 sc1\n" \
"buffer_load_dwordx4 v[68:71], %[lo], %[ws], s73 offen offset:0 sc1\n" \
"buffer_load_dwordx4 v[72:75], %[lo], %[ws], s73 offen offset:1024 sc1\n" \
"buffer_load_dwordx4 v[76:79], %[lo], %[ws], s73 offen offset:2048 sc1\n" \
"buffer_load_dwordx4 v[80:83], %[lo], %[ws], s73 offen offset:3072 sc1\n" \
"s_waitcnt vmcnt(0)\n" \
"v_max3_u32 v215, v52, v53, v54\n" \
"v_max3_u32 v215, v215, v55, v56\n" \
"v_max3_u32 v215, v215, v57, v58\n" \
"v_max3_u32 v215, v215, v59, v60\n" \
"v_max3_u32 v215, v215, v61, v62\n" \
"v_max3_u32 v215, v215, v63, v64\n" \
"v_max3_u32 v215, v215, v65, v66\n" \
"v_max3_u32 v215, v215, v67, v68\n" \
"v_max3_u32 v215, v215, v69, v70\n" \
"v_max3_u32 v215, v215, v71, v72\n" \
"v_max3_u32 v215, v215, v73, v74\n" \
"v_max3_u32 v215, v215, v75, v76\n" \
"v_max3_u32 v215, v215, v77, v78\n" \
"v_max3_u32 v215, v215, v79, v80\n" \
"v_max3_u32 v215, v215, v81, v82\n" \
"v_max3_u32 v215, v215, v83, v83\n" \
"v_cmp_eq_u32 vcc, -1, v215\n" \
"s_nop 1\n" \
"s_cbranch_vccnz Lw1_rn1_%=\n" \
"v_mfma_f32_16x16x32_bf16 v[108:111], a[8:11], v[52:55], 0\n" \
"v_mfma_f32_16x16x32_bf16 v[112:115], a[12:15], v[52:55], 0\n" \
"v_mfma_f32_16x16x32_bf16 v[116:119], a[16:19], v[52:55], 0\n" \
"v_mfma_f32_16x16x32_bf16 v[120:123], a[20:23], v[52:55], 0\n" \
"v_mfma_f32_16x16x32_bf16 v[124:127], a[24:27], v[52:55], 0\n" \
"v_mfma_f32_16x16x32_bf16 v[128:131], a[28:31], v[52:55], 0\n" \
"v_mfma_f32_16x16x32_bf16 v[108:111], a[40:43], v[56:59], v[108:111]\n" \
"v_mfma_f32_16x16x32_bf16 v[112:115], a[44:47], v[56:59], v[112:115]\n" \
"v_mfma_f32_16x16x32_bf16 v[116:119], a[48:51], v[56:59], v[116:119]\n" \
"v_mfma_f32_16x16x32_bf16 v[120:123], a[52:55], v[56:59], v[120:123]\n" \
"v_mfma_f32_16x16x32_bf16 v[124:127], a[56:59], v[56:59], v[124:127]\n" \
"v_mfma_f32_16x16x32_bf16 v[128:131], a[60:63], v[56:59], v[128:131]\n" \
"v_mfma_f32_16x16x32_bf16 v[108:111], a[72:75], v[60:63], v[108:111]\n" \
"v_mfma_f32_16x16x32_bf16 v[112:115], a[76:79], v[60:63], v[112:115]\n" \
"v_mfma_f32_16x16x32_bf16 v[116:119], a[80:83], v[60:63], v[116:119]\n" \
"v_mfma_f32_16x16x32_bf16 v[120:123], a[84:87], v[60:63], v[120:123]\n" \
"v_mfma_f32_16x16x32_bf16 v[124:127], a[88:91], v[60:63], v[124:127]\n" \
"v_mfma_f32_16x16x32_bf16 v[128:131], a[92:95], v[60:63], v[128:131]\n" \
"v_mfma_f32_16x16x32_bf16 v[108:111], a[104:107], v[64:67], v[108:111]\n" \
"v_mfma_f32_16x16x32_bf16 v[112:115], a[108:111], v[64:67], v[112:115]\n" \
"v_mfma_f32_16x16x32_bf16 v[116:119], a[112:115], v[64:67], v[116:119]\n" \
"v_mfma_f32_16x16x32_bf16 v[120:123], a[116:119], v[64:67], v[120:123]\n" \
"v_mfma_f32_16x16x32_bf16 v[124:127], a[120:123], v[64:67], v[124:127]\n" \
"v_mfma_f32_16x16x32_bf16 v[128:131], a[124:127], v[64:67], v[128:131]\n" \
"v_mfma_f32_16x16x32_bf16 v[108:111], a[136:139], v[68:71], v[108:111]\n" \
"v_mfma_f32_16x16x32_bf16 v[112:115], a[140:143], v[68:71], v[112:115]\n" \
"v_mfma_f32_16x16x32_bf16 v[116:119], a[144:147], v[68:71], v[116:119]\n" \
"v_mfma_f32_16x16x32_bf16 v[120:123], a[148:151], v[68:71], v[120:123]\n" \
"v_mfma_f32_16x16x32_bf16 v[124:127], a[152:155], v[68:71], v[124:127]\n" \
"v_mfma_f32_16x16x32_bf16 v[128:131], a[156:159], v[68:71], v[128:131]\n" \
"v_mfma_f32_16x16x32_bf16 v[108:111], a[168:171], v[72:75], v[108:111]\n" \
"v_mfma_f32_16x16x32_bf16 v[108:111], a[200:203], v[76:79], v[108:111]\n" \
"v_mfma_f32_16x16x32_bf16 v[108:111], a[232:235], v[80:83], v[108:111]\n" \
"v_mfma_f32_16x16x32_bf16 v[112:115], a[172:175], v[72:75], v[112:115]\n" \
"v_mfma_f32_16x16x32_bf16 v[112:115], a[204:207], v[76:79], v[112:115]\n" \
"v_mfma_f32_16x16x32_bf16 v[112:115], a[236:239], v[80:83], v[112:115]\n" \
"v_mfma_f32_16x16x32_bf16 v[116:119], a[176:179], v[72:75], v[116:119]\n" \
"s_nop 7\n" \
"ds_write_b128 v212, v[108:111] offset:34816\n" \
"s_branch Lw1_n1_%=\n" \
"Lw1_dead_%=:\n" \
"v_mov_b32 v202, 29185\n" \
"buffer_store_dword v202, off, %[ws], 0 sc1\n" \
"s_endpgm\n" \
    :  : [wvo] "v"(wvo), [tr0] "v"(tr0), [fo] "v"(fo), [xr] "v"(xr), [fvo] "v"(fvo), [lo] "v"(lo), [so] "v"(so), [sl] "v"(sl), [sw] "v"(sw), [sr] "v"(sr), [ws] "s"(ws), [rw] "s"(rw), [so0] "s"(so0), [wbase] "s"(wbase), [src] "s"(src), [dst] "s"(dst), [d0] "s"(d0), [ox0] "s"(ox0), [rot] "n"(ROT), [tor] "n"(16 - ROT) \
    : CLOB20)

#define W2_ASM() asm volatile( \
"s_nop 4\n" \
"s_mov_b32 s66, %[d0]\n" \
"Lw2_dly_%=:\n" \
"s_sleep 8\n" \
"s_sub_u32 s66, s66, 1\n" \
"s_cbranch_scc0 Lw2_dly_%=\n" \
"v_xor_b32 v20, 0, %[tr0]\n" \
"v_xor_b32 v21, 8, %[tr0]\n" \
"v_xor_b32 v22, 64, %[tr0]\n" \
"v_xor_b32 v23, 72, %[tr0]\n" \
"v_xor_b32 v24, 128, %[tr0]\n" \
"v_xor_b32 v25, 136, %[tr0]\n" \
"v_xor_b32 v26, 192, %[tr0]\n" \
"v_xor_b32 v27, 200, %[tr0]\n" \
"s_add_u32 m0, %[wbase], 0\n" \
"s_add_u32 s66, %[so0], 0\n" \
"buffer_load_dwordx4 %[wvo], %[rw], s66 offen lds\n" \
"s_add_u32 m0, %[wbase], 1024\n" \
"s_add_u32 s66, %[so0], 8192\n" \
"buffer_load_dwordx4 %[wvo], %[rw], s66 offen lds\n" \
"s_add_u32 m0, %[wbase], 2048\n" \
"s_add_u32 s66, %[so0], 16384\n" \
"buffer_load_dwordx4 %[wvo], %[rw], s66 offen lds\n" \
"s_waitcnt vmcnt(2)\n" \
"s_add_u32 m0, %[wbase], 3072\n" \
"s_add_u32 s66, %[so0], 24576\n" \
"buffer_load_dwordx4 %[wvo], %[rw], s66 offen lds\n" \
"s_waitcnt vmcnt(2)\n" \
"s_add_u32 m0, %[wbase], 4096\n" \
"s_add_u32 s66, %[so0], 32768\n" \
"buffer_load_dwordx4 %[wvo], %[rw], s66 offen lds\n" \
"s_waitcnt vmcnt(2)\n" \
"s_add_u32 m0, %[wbase], 5120\n" \
"s_add_u32 s66, %[so0], 40960\n" \
"buffer_load_dwordx4 %[wvo], %[rw], s66 offen lds\n" \
"s_waitcnt vmcnt(2)\n" \
"s_add_u32 m0, %[wbase], 6144\n" \
"s_add_u32 s66, %[so0], 49152\n" \
"buffer_load_dwordx4 %[wvo], %[rw], s66 offen lds\n" \
"s_waitcnt vmcnt(2)\n" \
"s_add_u32 m0, %[wbase], 7168\n" \
"s_add_u32 s66, %[so0], 57344\n" \
"buffer_load_dwordx4 %[wvo], %[rw], s66 offen lds\n" \
"s_waitcnt vmcnt(2)\n" \
"s_add_u32 m0, %[wbase], 8192\n" \
"s_add_u32 s66, %[so0], 65536\n" \
"buffer_load_dwordx4 %[wvo], %[rw], s66 offen lds\n" \
"s_waitcnt vmcnt(2)\n" \
"s_add_u32 m0, %[wbase], 9216\n" \
"s_add_u32 s66, %[so0], 73728\n" \
"buffer_load_dwordx4 %[wvo], %[rw], s66 offen lds\n" \
"s_waitcnt vmcnt(2)\n" \
"s_add_u32 m0, %[wbase], 10240\n" \
"s_add_u32 s66, %[so0], 81920\n" \
"buffer_load_dwordx4 %[wvo], %[rw], s66 offen lds\n" \
"ds_read_b64_tr_b16 a[0:1], v20 offset:0\n" \
"ds_read_b64_tr_b16 a[2:3], v20 offset:1024\n" \
"ds_read_b64_tr_b16 a[4:5], v21 offset:0\n" \
"ds_read_b64_tr_b16 a[6:7], v21 offset:1024\n" \
"ds_read_b64_tr_b16 a[8:9], v22 offset:0\n" \
"ds_read_b64_tr_b16 a[10:11], v22 offset:1024\n" \
"ds_read_b64_tr_b16 a[12:13], v23 offset:0\n" \
"ds_read_b64_tr_b16 a[14:15], v23 offset:1024\n" \
"ds_read_b64_tr_b16 a[16:17], v24 offset:0\n" \
"ds_read_b64_tr_b16 a[18:19], v24 offset:1024\n" \
"ds_read_b64_tr_b16 a[20:21], v25 offset:0\n" \
"ds_read_b64_tr_b16 a[22:23], v25 offset:1024\n" \
"ds_read_b64_tr_b16 a[24:25], v26 offset:0\n" \
"ds_read_b64_tr_b16 a[26:27], v26 offset:1024\n" \
"ds_read_b64_tr_b16 a[28:29], v27 offset:0\n" \
"ds_read_b64_tr_b16 a[30:31], v27 offset:1024\n" \
"s_waitcnt lgkmcnt(0)\n" \
"s_waitcnt vmcnt(2)\n" \
"s_add_u32 m0, %[wbase], 11264\n" \
"s_add_u32 s66, %[so0], 90112\n" \
"buffer_load_dwordx4 %[wvo], %[rw], s66 offen lds\n" \
"s_waitcnt vmcnt(2)\n" \
"s_add_u32 m0, %[wbase], 12288\n" \
"s_add_u32 s66, %[so0], 98304\n" \
"buffer_load_dwordx4 %[wvo], %[rw], s66 offen lds\n" \
"s_waitcnt vmcnt(2)\n" \
"s_add_u32 m0, %[wbase], 13312\n" \
"s_add_u32 s66, %[so0], 106496\n" \
"buffer_load_dwordx4 %[wvo], %[rw], s66 offen lds\n" \
"s_waitcnt vmcnt(2)\n" \
"s_add_u32 m0, %[wbase], 14336\n" \
"s_add_u32 s66, %[so0], 114688\n" \
"buffer_load_dwordx4 %[wvo], %[rw], s66 offen lds\n" \
"s_waitcnt vmcnt(2)\n" \
"s_add_u32 m0, %[wbase], 15360\n" \
"s_add_u32 s66, %[so0], 122880\n" \
"buffer_load_dwordx4 %[wvo], %[rw], s66 offen lds\n" \
"s_waitcnt vmcnt(2)\n" \
"s_add_u32 m0, %[wbase], 16384\n" \
"s_add_u32 s66, %[so0], 131072\n" \
"buffer_load_dwordx4 %[wvo], %[rw], s66 offen lds\n" \
"s_waitcnt vmcnt(2)\n" \
"s_add_u32 m0, %[wbase], 17408\n" \
"s_add_u32 s66, %[so0], 139264\n" \
"buffer_load_dwordx4 %[wvo], %[rw], s66 offen lds\n" \
"s_waitcnt vmcnt(2)\n" \
"s_add_u32 m0, %[wbase], 18432\n" \
"s_add_u32 s66, %[so0], 147456\n" \
"buffer_load_dwordx4 %[wvo], %[rw], s66 offen lds\n" \
"ds_read_b64_tr_b16 a[32:33], v20 offset:8192\n" \
"ds_read_b64_tr_b16 a[34:35], v20 offset:9216\n" \
"ds_read_b64_tr_b16 a[36:37], v21 offset:8192\n" \
"ds_read_b64_tr_b16 a[38:39], v21 offset:9216\n" \
"ds_read_b64_tr_b16 a[40:41], v22 offset:8192\n" \
"ds_read_b64_tr_b16 a[42:43], v22 offset:9216\n" \
"ds_read_b64_tr_b16 a[44:45], v23 offset:8192\n" \
"ds_read_b64_tr_b16 a[46:47], v23 offset:9216\n" \
"ds_read_b64_tr_b16 a[48:49], v24 offset:8192\n" \
"ds_read_b64_tr_b16 a[50:51], v24 offset:9216\n" \
"ds_read_b64_tr_b16 a[52:53], v25 offset:8192\n" \
"ds_read_b64_tr_b16 a[54:55], v25 offset:9216\n" \
"ds_read_b64_tr_b16 a[56:57], v26 offset:8192\n" \
"ds_read_b64_tr_b16 a[58:59], v26 offset:9216\n" \
"ds_read_b64_tr_b16 a[60:61], v27 offset:8192\n" \
"ds_read_b64_tr_b16 a[62:63], v27 offset:9216\n" \
"s_waitcnt lgkmcnt(0)\n" \
"s_waitcnt vmcnt(2)\n" \
"s_add_u32 m0, %[wbase], 19456\n" \
"s_add_u32 s66, %[so0], 155648\n" \
"buffer_load_dwordx4 %[wvo], %[rw], s66 offen lds\n" \
"s_waitcnt vmcnt(2)\n" \
"s_add_u32 m0, %[wbase], 20480\n" \
"s_add_u32 s66, %[so0], 163840\n" \
"buffer_load_dwordx4 %[wvo], %[rw], s66 offen lds\n" \
"s_waitcnt vmcnt(2)\n" \
"s_add_u32 m0, %[wbase], 21504\n" \
"s_add_u32 s66, %[so0], 172032\n" \
"buffer_load_dwordx4 %[wvo], %[rw], s66 offen lds\n" \
"s_waitcnt vmcnt(2)\n" \
"s_add_u32 m0, %[wbase], 22528\n" \
"s_add_u32 s66, %[so0], 180224\n" \
"buffer_load_dwordx4 %[wvo], %[rw], s66 offen lds\n" \
"s_waitcnt vmcnt(2)\n" \
"s_add_u32 m0, %[wbase], 23552\n" \
"s_add_u32 s66, %[so0], 188416\n" \
"buffer_load_dwordx4 %[wvo], %[rw], s66 offen lds\n" \
"s_waitcnt vmcnt(2)\n" \
"s_add_u32 m0, %[wbase], 24576\n" \
"s_add_u32 s66, %[so0], 196608\n" \
"buffer_load_dwordx4 %[wvo], %[rw], s66 offen lds\n" \
"s_waitcnt vmcnt(2)\n" \
"s_add_u32 m0, %[wbase], 25600\n" \
"s_add_u32 s66, %[so0], 204800\n" \
"buffer_load_dwordx4 %[wvo], %[rw], s66 offen lds\n" \
"s_waitcnt vmcnt(2)\n" \
"s_add_u32 m0, %[wbase], 26624\n" \
"s_add_u32 s66, %[so0], 212992\n" \
"buffer_load_dwordx4 %[wvo], %[rw], s66 offen lds\n" \
"ds_read_b64_tr_b16 a[64:65], v20 offset:16384\n" \
"ds_read_b64_tr_b16 a[66:67], v20 offset:17408\n" \
"ds_read_b64_tr_b16 a[68:69], v21 offset:16384\n" \
"ds_read_b64_tr_b16 a[70:71], v21 offset:17408\n" \
"ds_read_b64_tr_b16 a[72:73], v22 offset:16384\n" \
"ds_read_b64_tr_b16 a[74:75], v22 offset:17408\n" \
"ds_read_b64_tr_b16 a[76:77], v23 offset:16384\n" \
"ds_read_b64_tr_b16 a[78:79], v23 offset:17408\n" \
"ds_read_b64_tr_b16 a[80:81], v24 offset:16384\n" \
"ds_read_b64_tr_b16 a[82:83], v24 offset:17408\n" \
"ds_read_b64_tr_b16 a[84:85], v25 offset:16384\n" \
"ds_read_b64_tr_b16 a[86:87], v25 offset:17408\n" \
"ds_read_b64_tr_b16 a[88:89], v26 offset:16384\n" \
"ds_read_b64_tr_b16 a[90:91], v26 offset:17408\n" \
"ds_read_b64_tr_b16 a[92:93], v27 offset:16384\n" \
"ds_read_b64_tr_b16 a[94:95], v27 offset:17408\n" \
"s_waitcnt lgkmcnt(0)\n" \
"s_waitcnt vmcnt(2)\n" \
"s_add_u32 m0, %[wbase], 27648\n" \
"s_add_u32 s66, %[so0], 221184\n" \
"buffer_load_dwordx4 %[wvo], %[rw], s66 offen lds\n" \
"s_waitcnt vmcnt(2)\n" \
"s_add_u32 m0, %[wbase], 28672\n" \
"s_add_u32 s66, %[so0], 229376\n" \
"buffer_load_dwordx4 %[wvo], %[rw], s66 offen lds\n" \
"s_waitcnt vmcnt(2)\n" \
"s_add_u32 m0, %[wbase], 29696\n" \
"s_add_u32 s66, %[so0], 237568\n" \
"buffer_load_dwordx4 %[wvo], %[rw], s66 offen lds\n" \
"s_waitcnt vmcnt(2)\n" \
"s_add_u32 m0, %[wbase], 30720\n" \
"s_add_u32 s66, %[so0], 245760\n" \
"buffer_load_dwordx4 %[wvo], %[rw], s66 offen lds\n" \
"s_waitcnt vmcnt(2)\n" \
"s_add_u32 m0, %[wbase], 31744\n" \
"s_add_u32 s66, %[so0], 253952\n" \
"buffer_load_dwordx4 %[wvo], %[rw], s66 offen lds\n" \
"s_waitcnt vmcnt(2)\n" \
"s_add_u32 m0, %[wbase], 0\n" \
"s_add_u32 s66, %[so0], 262144\n" \
"buffer_load_dwordx4 %[wvo], %[rw], s66 offen lds\n" \
"s_waitcnt vmcnt(2)\n" \
"s_add_u32 m0, %[wbase], 1024\n" \
"s_add_u32 s66, %[so0], 270336\n" \
"buffer_load_dwordx4 %[wvo], %[rw], s66 offen lds\n" \
"s_waitcnt vmcnt(2)\n" \
"s_add_u32 m0, %[wbase], 2048\n" \
"s_add_u32 s66, %[so0], 278528\n" \
"buffer_load_dwordx4 %[wvo], %[rw], s66 offen lds\n" \
"ds_read_b64_tr_b16 a[96:97], v20 offset:24576\n" \
"ds_read_b64_tr_b16 a[98:99], v20 offset:25600\n" \
"ds_read_b64_tr_b16 a[100:101], v21 offset:24576\n" \
"ds_read_b64_tr_b16 a[102:103], v21 offset:25600\n" \
"ds_read_b64_tr_b16 a[104:105], v22 offset:24576\n" \
"ds_read_b64_tr_b16 a[106:107], v22 offset:25600\n" \
"ds_read_b64_tr_b16 a[108:109], v23 offset:24576\n" \
"ds_read_b64_tr_b16 a[110:111], v23 offset:25600\n" \
"ds_read_b64_tr_b16 a[112:113], v24 offset:24576\n" \
"ds_read_b64_tr_b16 a[114:115], v24 offset:25600\n" \
"ds_read_b64_tr_b16 a[116:117], v25 offset:24576\n" \
"ds_read_b64_tr_b16 a[118:119], v25 offset:25600\n" \
"ds_read_b64_tr_b16 a[120:121], v26 offset:24576\n" \
"ds_read_b64_tr_b16 a[122:123], v26 offset:25600\n" \
"ds_read_b64_tr_b16 a[124:125], v27 offset:24576\n" \
"ds_read_b64_tr_b16 a[126:127], v27 offset:25600\n" \
"s_waitcnt lgkmcnt(0)\n" \
"s_waitcnt vmcnt(2)\n" \
"s_add_u32 m0, %[wbase], 3072\n" \
"s_add_u32 s66, %[so0], 286720\n" \
"buffer_load_dwordx4 %[wvo], %[rw], s66 offen lds\n" \
"s_waitcnt vmcnt(2)\n" \
"s_add_u32 m0, %[wbase], 4096\n" \
"s_add_u32 s66, %[so0], 294912\n" \
"buffer_load_dwordx4 %[wvo], %[rw], s66 offen lds\n" \
"s_waitcnt vmcnt(2)\n" \
"s_add_u32 m0, %[wbase], 5120\n" \
"s_add_u32 s66, %[so0], 303104\n" \
"buffer_load_dwordx4 %[wvo], %[rw], s66 offen lds\n" \
"s_waitcnt vmcnt(2)\n" \
"s_add_u32 m0, %[wbase], 6144\n" \
"s_add_u32 s66, %[so0], 311296\n" \
"buffer_load_dwordx4 %[wvo], %[rw], s66 offen lds\n" \
"s_waitcnt vmcnt(2)\n" \
"s_add_u32 m0, %[wbase], 7168\n" \
"s_add_u32 s66, %[so0], 319488\n" \
"buffer_load_dwordx4 %[wvo], %[rw], s66 offen lds\n" \
"s_waitcnt vmcnt(2)\n" \
"s_add_u32 m0, %[wbase], 8192\n" \
"s_add_u32 s66, %[so0], 327680\n" \
"buffer_load_dwordx4 %[wvo], %[rw], s66 offen lds\n" \
"s_waitcnt vmcnt(2)\n" \
"s_add_u32 m0, %[wbase], 9216\n" \
"s_add_u32 s66, %[so0], 335872\n" \
"buffer_load_dwordx4 %[wvo], %[rw], s66 offen lds\n" \
"s_waitcnt vmcnt(2)\n" \
"s_add_u32 m0, %[wbase], 10240\n" \
"s_add_u32 s66, %[so0], 344064\n" \
"buffer_load_dwordx4 %[wvo], %[rw], s66 offen lds\n" \
"ds_read_b64_tr_b16 a[128:129], v20 offset:0\n" \
"ds_read_b64_tr_b16 a[130:131], v20 offset:1024\n" \
"ds_read_b64_tr_b16 a[132:133], v21 offset:0\n" \
"ds_read_b64_tr_b16 a[134:135], v21 offset:1024\n" \
"ds_read_b64_tr_b16 a[136:137], v22 offset:0\n" \
"ds_read_b64_tr_b16 a[138:139], v22 offset:1024\n" \
"ds_read_b64_tr_b16 a[140:141], v23 offset:0\n" \
"ds_read_b64_tr_b16 a[142:143], v23 offset:1024\n" \
"ds_read_b64_tr_b16 a[144:145], v24 offset:0\n" \
"ds_read_b64_tr_b16 a[146:147], v24 offset:1024\n" \
"ds_read_b64_tr_b16 a[148:149], v25 offset:0\n" \
"ds_read_b64_tr_b16 a[150:151], v25 offset:1024\n" \
"ds_read_b64_tr_b16 a[152:153], v26 offset:0\n" \
"ds_read_b64_tr_b16 a[154:155], v26 offset:1024\n" \
"ds_read_b64_tr_b16 a[156:157], v27 offset:0\n" \
"ds_read_b64_tr_b16 a[158:159], v27 offset:1024\n" \
"s_waitcnt lgkmcnt(0)\n" \
"s_waitcnt vmcnt(2)\n" \
"s_add_u32 m0, %[wbase], 11264\n" \
"s_add_u32 s66, %[so0], 352256\n" \
"buffer_load_dwordx4 %[wvo], %[rw], s66 offen lds\n" \
"s_waitcnt vmcnt(2)\n" \
"s_add_u32 m0, %[wbase], 12288\n" \
"s_add_u32 s66, %[so0], 360448\n" \
"buffer_load_dwordx4 %[wvo], %[rw], s66 offen lds\n" \
"s_waitcnt vmcnt(2)\n" \
"s_add_u32 m0, %[wbase], 13312\n" \
"s_add_u32 s66, %[so0], 368640\n" \
"buffer_load_dwordx4 %[wvo], %[rw], s66 offen lds\n" \
"s_waitcnt vmcnt(2)\n" \
"s_add_u32 m0, %[wbase], 14336\n" \
"s_add_u32 s66, %[so0], 376832\n" \
"buffer_load_dwordx4 %[wvo], %[rw], s66 offen lds\n" \
"s_waitcnt vmcnt(2)\n" \
"s_add_u32 m0, %[wbase], 15360\n" \
"s_add_u32 s66, %[so0], 385024\n" \
"buffer_load_dwordx4 %[wvo], %[rw], s66 offen lds\n" \
"s_waitcnt vmcnt(2)\n" \
"s_add_u32 m0, %[wbase], 16384\n" \
"s_add_u32 s66, %[so0], 393216\n" \
"buffer_load_dwordx4 %[wvo], %[rw], s66 offen lds\n" \
"s_waitcnt vmcnt(2)\n" \
"s_add_u32 m0, %[wbase], 17408\n" \
"s_add_u32 s66, %[so0], 401408\n" \
"buffer_load_dwordx4 %[wvo], %[rw], s66 offen lds\n" \
"s_waitcnt vmcnt(2)\n" \
"s_add_u32 m0, %[wbase], 18432\n" \
"s_add_u32 s66, %[so0], 409600\n" \
"buffer_load_dwordx4 %[wvo], %[rw], s66 offen lds\n" \
"ds_read_b64_tr_b16 a[160:161], v20 offset:8192\n" \
"ds_read_b64_tr_b16 a[162:163], v20 offset:9216\n" \
"ds_read_b64_tr_b16 a[164:165], v21 offset:8192\n" \
"ds_read_b64_tr_b16 a[166:167], v21 offset:9216\n" \
"ds_read_b64_tr_b16 a[168:169], v22 offset:8192\n" \
"ds_read_b64_tr_b16 a[170:171], v22 offset:9216\n" \
"ds_read_b64_tr_b16 a[172:173], v23 offset:8192\n" \
"ds_read_b64_tr_b16 a[174:175], v23 offset:9216\n" \
"ds_read_b64_tr_b16 a[176:177], v24 offset:8192\n" \
"ds_read_b64_tr_b16 a[178:179], v24 offset:9216\n" \
"ds_read_b64_tr_b16 a[180:181], v25 offset:8192\n" \
"ds_read_b64_tr_b16 a[182:183], v25 offset:9216\n" \
"ds_read_b64_tr_b16 a[184:185], v26 offset:8192\n" \
"ds_read_b64_tr_b16 a[186:187], v26 offset:9216\n" \
"ds_read_b64_tr_b16 a[188:189], v27 offset:8192\n" \
"ds_read_b64_tr_b16 a[190:191], v27 offset:9216\n" \
"s_waitcnt lgkmcnt(0)\n" \
"s_waitcnt vmcnt(2)\n" \
"s_add_u32 m0, %[wbase], 19456\n" \
"s_add_u32 s66, %[so0], 417792\n" \
"buffer_load_dwordx4 %[wvo], %[rw], s66 offen lds\n" \
"s_waitcnt vmcnt(2)\n" \
"s_add_u32 m0, %[wbase], 20480\n" \
"s_add_u32 s66, %[so0], 425984\n" \
"buffer_load_dwordx4 %[wvo], %[rw], s66 offen lds\n" \
"s_waitcnt vmcnt(2)\n" \
"s_add_u32 m0, %[wbase], 21504\n" \
"s_add_u32 s66, %[so0], 434176\n" \
"buffer_load_dwordx4 %[wvo], %[rw], s66 offen lds\n" \
"s_waitcnt vmcnt(2)\n" \
"s_add_u32 m0, %[wbase], 22528\n" \
"s_add_u32 s66, %[so0], 442368\n" \
"buffer_load_dwordx4 %[wvo], %[rw], s66 offen lds\n" \
"s_waitcnt vmcnt(2)\n" \
"s_add_u32 m0, %[wbase], 23552\n" \
"s_add_u32 s66, %[so0], 450560\n" \
"buffer_load_dwordx4 %[wvo], %[rw], s66 offen lds\n" \
"s_waitcnt vmcnt(2)\n" \
"s_add_u32 m0, %[wbase], 24576\n" \
"s_add_u32 s66, %[so0], 458752\n" \
"buffer_load_dwordx4 %[wvo], %[rw], s66 offen lds\n" \
"s_waitcnt vmcnt(2)\n" \
"s_add_u32 m0, %[wbase], 25600\n" \
"s_add_u32 s66, %[so0], 466944\n" \
"buffer_load_dwordx4 %[wvo], %[rw], s66 offen lds\n" \
"s_waitcnt vmcnt(2)\n" \
"s_add_u32 m0, %[wbase], 26624\n" \
"s_add_u32 s66, %[so0], 475136\n" \
"buffer_load_dwordx4 %[wvo], %[rw], s66 offen lds\n" \
"ds_read_b64_tr_b16 a[192:193], v20 offset:16384\n" \
"ds_read_b64_tr_b16 a[194:195], v20 offset:17408\n" \
"ds_read_b64_tr_b16 a[196:197], v21 offset:16384\n" \
"ds_read_b64_tr_b16 a[198:199], v21 offset:17408\n" \
"ds_read_b64_tr_b16 a[200:201], v22 offset:16384\n" \
"ds_read_b64_tr_b16 a[202:203], v22 offset:17408\n" \
"ds_read_b64_tr_b16 a[204:205], v23 offset:16384\n" \
"ds_read_b64_tr_b16 a[206:207], v23 offset:17408\n" \
"ds_read_b64_tr_b16 a[208:209], v24 offset:16384\n" \
"ds_read_b64_tr_b16 a[210:211], v24 offset:17408\n" \
"ds_read_b64_tr_b16 a[212:213], v25 offset:16384\n" \
"ds_read_b64_tr_b16 a[214:215], v25 offset:17408\n" \
"ds_read_b64_tr_b16 a[216:217], v26 offset:16384\n" \
"ds_read_b64_tr_b16 a[218:219], v26 offset:17408\n" \
"ds_read_b64_tr_b16 a[220:221], v27 offset:16384\n" \
"ds_read_b64_tr_b16 a[222:223], v27 offset:17408\n" \
"s_waitcnt lgkmcnt(0)\n" \
"s_waitcnt vmcnt(2)\n" \
"s_add_u32 m0, %[wbase], 27648\n" \
"s_add_u32 s66, %[so0], 483328\n" \
"buffer_load_dwordx4 %[wvo], %[rw], s66 offen lds\n" \
"s_waitcnt vmcnt(2)\n" \
"s_add_u32 m0, %[wbase], 28672\n" \
"s_add_u32 s66, %[so0], 491520\n" \
"buffer_load_dwordx4 %[wvo], %[rw], s66 offen lds\n" \
"s_waitcnt vmcnt(2)\n" \
"s_add_u32 m0, %[wbase], 29696\n" \
"s_add_u32 s66, %[so0], 499712\n" \
"buffer_load_dwordx4 %[wvo], %[rw], s66 offen lds\n" \
"s_waitcnt vmcnt(2)\n" \
"s_add_u32 m0, %[wbase], 30720\n" \
"s_add_u32 s66, %[so0], 507904\n" \
"buffer_load_dwordx4 %[wvo], %[rw], s66 offen lds\n" \
"s_waitcnt vmcnt(2)\n" \
"s_add_u32 m0, %[wbase], 31744\n" \
"s_add_u32 s66, %[so0], 516096\n" \
"buffer_load_dwordx4 %[wvo], %[rw], s66 offen lds\n" \
"s_waitcnt vmcnt(0)\n" \
"ds_read_b64_tr_b16 a[224:225], v20 offset:24576\n" \
"ds_read_b64_tr_b16 a[226:227], v20 offset:25600\n" \
"ds_read_b64_tr_b16 a[228:229], v21 offset:24576\n" \
"ds_read_b64_tr_b16 a[230:231], v21 offset:25600\n" \
"ds_read_b64_tr_b16 a[232:233], v22 offset:24576\n" \
"ds_read_b64_tr_b16 a[234:235], v22 offset:25600\n" \
"ds_read_b64_tr_b16 a[236:237], v23 offset:24576\n" \
"ds_read_b64_tr_b16 a[238:239], v23 offset:25600\n" \
"ds_read_b64_tr_b16 a[240:241], v24 offset:24576\n" \
"ds_read_b64_tr_b16 a[242:243], v24 offset:25600\n" \
"ds_read_b64_tr_b16 a[244:245], v25 offset:24576\n" \
"ds_read_b64_tr_b16 a[246:247], v25 offset:25600\n" \
"ds_read_b64_tr_b16 a[248:249], v26 offset:24576\n" \
"ds_read_b64_tr_b16 a[250:251], v26 offset:25600\n" \
"ds_read_b64_tr_b16 a[252:253], v27 offset:24576\n" \
"ds_read_b64_tr_b16 a[254:255], v27 offset:25600\n" \
"s_waitcnt lgkmcnt(0)\n" \
"v_xor_b32 v212, 8192, %[xr]\n" \
"v_xor_b32 v213, 16384, %[xr]\n" \
"v_xor_b32 v214, 24576, %[xr]\n" \
"s_barrier\n" \
"s_add_u32 s81, %[dst], 3342336\n" \
"s_add_u32 s77, %[ox2], 417792\n" \
"v_mov_b32 v220, -1\n" \
"v_mov_b32 v221, -1\n" \
"v_mov_b32 v222, -1\n" \
"v_mov_b32 v223, -1\n" \
"s_mov_b32 s80, 128\n" \
"s_mov_b32 s65, 25\n" \
"Lw2_poi_%=:\n" \
"buffer_store_dwordx4 v[220:223], %[so], %[ws], s81 offen sc1\n" \
"buffer_store_dwordx4 v[220:223], %[yo], %[ws], s81 offen sc1\n" \
"buffer_store_dword v220, %[sto], %[ws], s77 offen sc1\n" \
"s_waitcnt vmcnt(2)\n" \
"s_add_u32 s81, s81, 32768\n" \
"s_add_u32 s77, s77, 4096\n" \
"s_sub_u32 s65, s65, 1\n" \
"s_cbranch_scc0 Lw2_poi_%=\n" \
"s_waitcnt vmcnt(0)\n" \
"s_mov_b32 s64, 262144\n" \
"s_mov_b32 s65, 0\n" \
"s_mov_b32 s74, 2\n" \
"s_mov_b32 s69, 1073741824\n" \
"s_mov_b32 s67, %[src]\n" \
"s_mov_b32 s68, %[dst]\n" \
"s_mov_b32 s70, %[ox0]\n" \
"s_mov_b32 s71, %[ox1]\n" \
"s_mov_b32 s76, %[ox2]\n" \
"v_mov_b32 v202, 0\n" \
"v_mov_b32 v203, 0\n" \
"Lw2_top0_%=:\n" \
"v_cmp_gt_u32 vcc, s74, v202\n" \
"s_mov_b32 s72, s67\n" \
"s_mov_b32 s75, s70\n" \
"s_mov_b32 s82, s71\n" \
"s_add_u32 s73, s67, 4096\n" \
"s_cbranch_vccnz Lw2_sgo0_%=\n" \
"Lw2_go0_%=:\n" \
"buffer_load_dwordx4 v[20:23], %[lo], %[ws], s72 offen offset:0 sc1\n" \
"buffer_load_dwordx4 v[24:27], %[lo], %[ws], s72 offen offset:1024 sc1\n" \
"buffer_load_dwordx2 v[84:85], %[sl], %[ws], s75 offen offset:0 sc1\n" \
"buffer_load_dwordx4 v[88:91], %[so], %[ws], s82 offen sc1\n" \
"buffer_load_dwordx4 v[28:31], %[lo], %[ws], s72 offen offset:2048 sc1\n" \
"buffer_load_dwordx4 v[32:35], %[lo], %[ws], s72 offen offset:3072 sc1\n" \
"buffer_load_dwordx4 v[36:39], %[lo], %[ws], s73 offen offset:0 sc1\n" \
"buffer_load_dwordx4 v[40:43], %[lo], %[ws], s73 offen offset:1024 sc1\n" \
"buffer_load_dwordx4 v[44:47], %[lo], %[ws], s73 offen offset:2048 sc1\n" \
"buffer_load_dwordx4 v[48:51], %[lo], %[ws], s73 offen offset:3072 sc1\n" \
"s_waitcnt vmcnt(10)\n" \
"buffer_store_dword v203, %[fo], %[ws], s69 offen sc1\n" \
"s_mov_b32 s69, 1073741824\n" \
"buffer_store_dword v203, %[fo], %[ws], s69 offen sc1\n" \
"buffer_store_dword v203, %[fo], %[ws], s69 offen sc1\n" \
"Lw2_body0_%=:\n" \
"s_waitcnt vmcnt(12)\n" \
"v_mfma_f32_16x16x32_bf16 v[108:111], a[8:11], v[20:23], 0\n" \
"s_nop 2\n" \
"v_mfma_f32_16x16x32_bf16 v[112:115], a[12:15], v[20:23], 0\n" \
"s_nop 2\n" \
"v_mfma_f32_16x16x32_bf16 v[116:119], a[16:19], v[20:23], 0\n" \
"s_add_u32 s74, s74, 1\n" \
"s_add_u32 s67, s67, 32768\n" \
"s_min_u32 s74, s74, 128\n" \
"v_mfma_f32_16x16x32_bf16 v[120:123], a[20:23], v[20:23], 0\n" \
"s_add_u32 s65, s65, 1\n" \
"s_add_u32 s70, s70, 2048\n" \
"s_add_u32 s71, s71, 32768\n" \
"v_mfma_f32_16x16x32_bf16 v[124:127], a[24:27], v[20:23], 0\n" \
"s_cmp_gt_u32 s65, 104\n" \
"s_cselect_b32 s79, 1073741824, 0\n" \
"s_cselect_b32 s69, 0, s69\n" \
"v_mfma_f32_16x16x32_bf16 v[128:131], a[28:31], v[20:23], 0\n" \
"s_waitcnt vmcnt(11)\n" \
"s_addc_u32 s66, s65, -1\n" \
"s_nop 0\n" \
"v_mfma_f32_16x16x32_bf16 v[108:111], a[40:43], v[24:27], v[108:111]\n" \
"s_nop 0\n" \
"buffer_load_dword v202, %[fvo], %[ws], 0 offen sc1\n" \
"v_mov_b32 v203, s66\n" \
"v_mfma_f32_16x16x32_bf16 v[112:115], a[44:47], v[24:27], v[112:115]\n" \
"s_waitcnt vmcnt(10)\n" \
"v_max3_u32 v215, v84, v85, v88\n" \
"v_mfma_f32_16x16x32_bf16 v[116:119], a[48:51], v[24:27], v[116:119]\n" \
"s_nop 0\n" \
"v_max3_u32 v215, v215, v89, v90\n" \
"v_cmp_u_f32 vcc, v215, v91\n" \
"v_mfma_f32_16x16x32_bf16 v[120:123], a[52:55], v[24:27], v[120:123]\n" \
"s_cbranch_vccnz Lw2_sk00_%=\n" \
"Lw2_k00_%=:\n" \
"v_mfma_f32_16x16x32_bf16 v[124:127], a[56:59], v[24:27], v[124:127]\n" \
"s_nop 0\n" \
"v_permlane32_swap_b32 v84, v85\n" \
"v_mfma_f32_16x16x32_bf16 v[128:131], a[60:63], v[24:27], v[128:131]\n" \
"s_waitcnt vmcnt(9)\n" \
"v_add_f32 v86, v84, v85\n" \
"v_add_f32 v84, v84, v85\n" \
"v_mfma_f32_16x16x32_bf16 v[108:111], a[72:75], v[28:31], v[108:111]\n" \
"s_nop 0\n" \
"v_permlane16_swap_b32 v84, v86\n" \
"v_mfma_f32_16x16x32_bf16 v[112:115], a[76:79], v[28:31], v[112:115]\n" \
"s_nop 0\n" \
"v_add_f32 v84, v84, v86\n" \
"v_mfma_f32_16x16x32_bf16 v[116:119], a[80:83], v[28:31], v[116:119]\n" \
"s_nop 0\n" \
"ds_write_b32 %[sw], v84 offset:0\n" \
"v_mfma_f32_16x16x32_bf16 v[120:123], a[84:87], v[28:31], v[120:123]\n" \
"s_nop 2\n" \
"v_mfma_f32_16x16x32_bf16 v[124:127], a[88:91], v[28:31], v[124:127]\n" \
"s_nop 2\n" \
"v_mfma_f32_16x16x32_bf16 v[128:131], a[92:95], v[28:31], v[128:131]\n" \
"s_waitcnt vmcnt(8) lgkmcnt(0)\n" \
"s_barrier\n" \
"v_mfma_f32_16x16x32_bf16 v[108:111], a[104:107], v[32:35], v[108:111]\n" \
"s_nop 0\n" \
"ds_read_b128 v[190:193], %[sr] offset:0\n" \
"v_mfma_f32_16x16x32_bf16 v[112:115], a[108:111], v[32:35], v[112:115]\n" \
"s_nop 0\n" \
"ds_read_b128 v[194:197], %[sr] offset:16\n" \
"v_mfma_f32_16x16x32_bf16 v[116:119], a[112:115], v[32:35], v[116:119]\n" \
"s_nop 2\n" \
"v_mfma_f32_16x16x32_bf16 v[120:123], a[116:119], v[32:35], v[120:123]\n" \
"s_nop 2\n" \
"v_mfma_f32_16x16x32_bf16 v[124:127], a[120:123], v[32:35], v[124:127]\n" \
"s_nop 2\n" \
"v_mfma_f32_16x16x32_bf16 v[128:131], a[124:127], v[32:35], v[128:131]\n" \
"s_waitcnt vmcnt(7) lgkmcnt(1)\n" \
"v_add_f32 v190, v190, v191\n" \
"v_add_f32 v192, v192, v193\n" \
"v_mfma_f32_16x16x32_bf16 v[108:111], a[136:139], v[36:39], v[108:111]\n" \
"s_waitcnt lgkmcnt(0)\n" \
"v_add_f32 v194, v194, v195\n" \
"v_add_f32 v196, v196, v197\n" \
"v_mfma_f32_16x16x32_bf16 v[112:115], a[140:143], v[36:39], v[112:115]\n" \
"s_nop 0\n" \
"v_add_f32 v190, v190, v192\n" \
"v_add_f32 v194, v194, v196\n" \
"v_mfma_f32_16x16x32_bf16 v[116:119], a[144:147], v[36:39], v[116:119]\n" \
"s_nop 0\n" \
"v_mul_f32 v190, 0xba800000, v190\n" \
"v_mul_f32 v194, 0x3a800000, v194\n" \
"v_mfma_f32_16x16x32_bf16 v[120:123], a[148:151], v[36:39], v[120:123]\n" \
"s_nop 0\n" \
"v_fma_f32 v194, -v190, v190, v194\n" \
"v_max_f32 v194, 0, v194\n" \
"v_mfma_f32_16x16x32_bf16 v[124:127], a[152:155], v[36:39], v[124:127]\n" \
"s_nop 0\n" \
"v_add_f32 v192, 0x3727c5ac, v194\n" \
"v_rsq_f32 v192, v192\n" \
"v_mfma_f32_16x16x32_bf16 v[128:131], a[156:159], v[36:39], v[128:131]\n" \
"s_waitcnt vmcnt(6)\n" \
"v_lshlrev_b32 v172, 16, v88\n" \
"v_mul_f32 v190, v190, v192\n" \
"v_mfma_f32_16x16x32_bf16 v[108:111], a[168:171], v[40:43], v[108:111]\n" \
"s_waitcnt vmcnt(5)\n" \
"v_and_b32 v173, 0xffff0000, v88\n" \
"v_lshlrev_b32 v174, 16, v89\n" \
"v_mfma_f32_16x16x32_bf16 v[108:111], a[200:203], v[44:47], v[108:111]\n" \
"s_waitcnt vmcnt(4)\n" \
"v_and_b32 v175, 0xffff0000, v89\n" \
"v_lshlrev_b32 v176, 16, v90\n" \
"v_mfma_f32_16x16x32_bf16 v[108:111], a[232:235], v[48:51], v[108:111]\n" \
"s_nop 0\n" \
"v_and_b32 v177, 0xffff0000, v90\n" \
"v_lshlrev_b32 v178, 16, v91\n" \
"v_mfma_f32_16x16x32_bf16 v[112:115], a[172:175], v[40:43], v[112:115]\n" \
"s_nop 0\n" \
"v_and_b32 v179, 0xffff0000, v91\n" \
"v_fma_f32 v156, v172, v192, v190\n" \
"v_mfma_f32_16x16x32_bf16 v[112:115], a[204:207], v[44:47], v[112:115]\n" \
"s_nop 0\n" \
"ds_write_b128 v212, v[108:111] offset:2048\n" \
"v_mfma_f32_16x16x32_bf16 v[112:115], a[236:239], v[48:51], v[112:115]\n" \
"s_nop 0\n" \
"v_cmp_u_f32 vcc, v108, v108\n" \
"v_fma_f32 v157, v173, v192, v190\n" \
"v_mfma_f32_16x16x32_bf16 v[116:119], a[176:179], v[40:43], v[116:119]\n" \
"s_cbranch_vccnz Lw2_sn0_%=\n" \
"Lw2_n0_%=:\n" \
"v_fma_f32 v158, v174, v192, v190\n" \
"v_mfma_f32_16x16x32_bf16 v[116:119], a[208:211], v[44:47], v[116:119]\n" \
"s_nop 0\n" \
"ds_write_b128 v212, v[112:115] offset:3072\n" \
"v_mfma_f32_16x16x32_bf16 v[116:119], a[240:243], v[48:51], v[116:119]\n" \
"s_nop 0\n" \
"v_fma_f32 v159, v175, v192, v190\n" \
"v_fma_f32 v160, v176, v192, v190\n" \
"v_mfma_f32_16x16x32_bf16 v[120:123], a[180:183], v[40:43], v[120:123]\n" \
"s_nop 0\n" \
"v_fma_f32 v161, v177, v192, v190\n" \
"v_fma_f32 v162, v178, v192, v190\n" \
"v_mfma_f32_16x16x32_bf16 v[120:123], a[212:215], v[44:47], v[120:123]\n" \
"s_nop 0\n" \
"ds_write_b128 v213, v[116:119] offset:4096\n" \
"v_fma_f32 v163, v179, v192, v190\n" \
"v_mfma_f32_16x16x32_bf16 v[120:123], a[244:247], v[48:51], v[120:123]\n" \
"s_waitcnt vmcnt(0)\n" \
"v_cmp_gt_u32 vcc, s74, v202\n" \
"v_mfma_f32_16x16x32_bf16 v[124:127], a[184:187], v[40:43], v[124:127]\n" \
"s_nop 0\n" \
"buffer_load_dword v202, %[fvo], %[ws], 0 offen sc1\n" \
"v_mfma_f32_16x16x32_bf16 v[124:127], a[216:219], v[44:47], v[124:127]\n" \
"s_cmp_eq_u64 vcc, 0\n" \
"s_nop 0\n" \
"ds_write_b128 v213, v[120:123] offset:5120\n" \
"v_mfma_f32_16x16x32_bf16 v[124:127], a[248:251], v[48:51], v[124:127]\n" \
"s_cselect_b32 s72, s67, 1073741824\n" \
"buffer_load_dwordx4 v[52:55], %[lo], %[ws], s72 offen offset:0 sc1\n" \
"v_mfma_f32_16x16x32_bf16 v[128:131], a[188:191], v[40:43], v[128:131]\n" \
"s_cselect_b32 s75, s70, 1073741824\n" \
"s_cselect_b32 s82, s71, 1073741824\n" \
"s_add_u32 s73, s72, 4096\n" \
"v_mfma_f32_16x16x32_bf16 v[128:131], a[220:223], v[44:47], v[128:131]\n" \
"s_nop 0\n" \
"ds_write_b128 v214, v[124:127] offset:6144\n" \
"v_mfma_f32_16x16x32_bf16 v[128:131], a[252:255], v[48:51], v[128:131]\n" \
"s_waitcnt lgkmcnt(1)\n" \
"s_barrier\n" \
"v_mfma_f32_16x16x32_bf16 v[100:103], a[0:3], v[20:23], v[156:159]\n" \
"s_nop 0\n" \
"ds_read_b128 v[132:135], %[xr] offset:2048\n" \
"buffer_load_dwordx4 v[56:59], %[lo], %[ws], s72 offen offset:1024 sc1\n" \
"v_mfma_f32_16x16x32_bf16 v[104:107], a[4:7], v[20:23], v[160:163]\n" \
"s_nop 0\n" \
"ds_write_b128 v214, v[128:131] offset:7168\n" \
"v_mfma_f32_16x16x32_bf16 v[100:103], a[32:35], v[24:27], v[100:103]\n" \
"s_nop 0\n" \
"ds_read_b128 v[136:139], %[xr] offset:3072\n" \
"v_mfma_f32_16x16x32_bf16 v[104:107], a[36:39], v[24:27], v[104:107]\n" \
"s_nop 0\n" \
"ds_read_b128 v[140:143], %[xr] offset:4096\n" \
"buffer_store_dword v203, %[fo], %[ws], s69 offen sc1\n" \
"v_mfma_f32_16x16x32_bf16 v[100:103], a[64:67], v[28:31], v[100:103]\n" \
"s_nop 0\n" \
"ds_read_b128 v[144:147], %[xr] offset:5120\n" \
"buffer_load_dwordx2 v[92:93], %[sl], %[ws], s75 offen offset:0 sc1\n" \
"v_mfma_f32_16x16x32_bf16 v[104:107], a[68:71], v[28:31], v[104:107]\n" \
"s_waitcnt lgkmcnt(3)\n" \
"s_barrier\n" \
"v_mfma_f32_16x16x32_bf16 v[100:103], a[96:99], v[32:35], v[100:103]\n" \
"s_nop 0\n" \
"ds_read_b128 v[148:151], %[xr] offset:6144\n" \
"buffer_load_dwordx4 v[96:99], %[so], %[ws], s82 offen sc1\n" \
"v_mfma_f32_16x16x32_bf16 v[104:107], a[100:103], v[32:35], v[104:107]\n" \
"s_nop 0\n" \
"ds_read_b128 v[152:155], %[xr] offset:7168\n" \
"v_mfma_f32_16x16x32_bf16 v[100:103], a[128:131], v[36:39], v[100:103]\n" \
"s_waitcnt lgkmcnt(3)\n" \
"v_add_f32 v132, v132, v140\n" \
"v_add_f32 v133, v133, v141\n" \
"v_mfma_f32_16x16x32_bf16 v[104:107], a[132:135], v[36:39], v[104:107]\n" \
"s_nop 0\n" \
"v_add_f32 v134, v134, v142\n" \
"v_add_f32 v135, v135, v143\n" \
"v_mfma_f32_16x16x32_bf16 v[100:103], a[160:163], v[40:43], v[100:103]\n" \
"s_waitcnt lgkmcnt(2)\n" \
"buffer_load_dwordx4 v[60:63], %[lo], %[ws], s72 offen offset:2048 sc1\n" \
"v_add_f32 v136, v136, v144\n" \
"v_add_f32 v137, v137, v145\n" \
"v_mfma_f32_16x16x32_bf16 v[104:107], a[164:167], v[40:43], v[104:107]\n" \
"s_nop 0\n" \
"v_add_f32 v138, v138, v146\n" \
"v_add_f32 v139, v139, v147\n" \
"v_mfma_f32_16x16x32_bf16 v[100:103], a[192:195], v[44:47], v[100:103]\n" \
"s_waitcnt lgkmcnt(1)\n" \
"v_add_f32 v132, v132, v148\n" \
"v_add_f32 v133, v133, v149\n" \
"v_mfma_f32_16x16x32_bf16 v[104:107], a[196:199], v[44:47], v[104:107]\n" \
"s_nop 0\n" \
"v_add_f32 v134, v134, v150\n" \
"v_add_f32 v135, v135, v151\n" \
"v_mfma_f32_16x16x32_bf16 v[100:103], a[224:227], v[48:51], v[100:103]\n" \
"s_waitcnt lgkmcnt(0)\n" \
"buffer_load_dwordx4 v[64:67], %[lo], %[ws], s72 offen offset:3072 sc1\n" \
"v_add_f32 v136, v136, v152\n" \
"v_add_f32 v137, v137, v153\n" \
"v_mfma_f32_16x16x32_bf16 v[104:107], a[228:231], v[48:51], v[104:107]\n" \
"s_nop 0\n" \
"v_add_f32 v138, v138, v154\n" \
"v_add_f32 v139, v139, v155\n" \
"v_mov_b32 v203, s65\n" \
"s_mov_b32 s69, s79\n" \
"buffer_load_dwordx4 v[68:71], %[lo], %[ws], s73 offen offset:0 sc1\n" \
"v_pk_add_f32 v[100:101], v[100:101], v[132:133]\n" \
"v_pk_add_f32 v[102:103], v[102:103], v[134:135]\n" \
"v_pk_add_f32 v[104:105], v[104:105], v[136:137]\n" \
"v_pk_add_f32 v[106:107], v[106:107], v[138:139]\n" \
"buffer_load_dwordx4 v[72:75], %[lo], %[ws], s73 offen offset:1024 sc1\n" \
"buffer_load_dwordx4 v[76:79], %[lo], %[ws], s73 offen offset:2048 sc1\n" \
"buffer_load_dwordx4 v[80:83], %[lo], %[ws], s73 offen offset:3072 sc1\n" \
"v_cvt_pk_bf16_f32 v204, v100, v101\n" \
"v_cvt_pk_bf16_f32 v205, v102, v103\n" \
"v_cvt_pk_bf16_f32 v206, v104, v105\n" \
"v_cvt_pk_bf16_f32 v207, v106, v107\n" \
"buffer_store_dwordx4 v[204:207], %[so], %[ws], s68 offen sc1\n" \
"v_pk_mul_f32 v[134:135], v[100:101], v[100:101]\n" \
"v_pk_add_f32 v[132:133], v[100:101], v[102:103]\n" \
"v_pk_fma_f32 v[134:135], v[102:103], v[102:103], v[134:135]\n" \
"v_pk_fma_f32 v[134:135], v[104:105], v[104:105], v[134:135]\n" \
"v_pk_add_f32 v[132:133], v[132:133], v[104:105]\n" \
"v_pk_fma_f32 v[134:135], v[106:107], v[106:107], v[134:135]\n" \
"v_pk_add_f32 v[132:133], v[132:133], v[106:107]\n" \
"v_add_f32 v132, v132, v133\n" \
"v_add_f32 v134, v134, v135\n" \
"v_pk_add_f32 v[172:173], v[172:173], v[100:101]\n" \
"v_pk_add_f32 v[174:175], v[174:175], v[102:103]\n" \
"v_permlane32_swap_b32 v132, v134\n" \
"v_add_f32 v133, v132, v134\n" \
"v_add_f32 v132, v132, v134\n" \
"v_pk_add_f32 v[176:177], v[176:177], v[104:105]\n" \
"v_pk_add_f32 v[178:179], v[178:179], v[106:107]\n" \
"v_permlane16_swap_b32 v132, v133\n" \
"v_add_f32 v132, v132, v133\n" \
"v_cvt_pk_bf16_f32 v208, v172, v173\n" \
"v_cvt_pk_bf16_f32 v209, v174, v175\n" \
"v_cvt_pk_bf16_f32 v210, v176, v177\n" \
"v_cvt_pk_bf16_f32 v211, v178, v179\n" \
"buffer_store_dword v132, %[sto], %[ws], s76 offen sc1\n" \
"buffer_store_dwordx4 v[208:211], %[yo], %[ws], s68 offen sc1\n" \
"s_add_u32 s76, s76, 4096\n" \
"s_add_u32 s68, s68, 32768\n" \
"s_cmp_eq_u32 s72, s67\n" \
"s_cbranch_scc1 Lw2_body1_%=\n" \
"s_waitcnt vmcnt(14)\n" \
"Lw2_top1_%=:\n" \
"v_cmp_gt_u32 vcc, s74, v202\n" \
"s_mov_b32 s72, s67\n" \
"s_mov_b32 s75, s70\n" \
"s_mov_b32 s82, s71\n" \
"s_add_u32 s73, s67, 4096\n" \
"s_cbranch_vccnz Lw2_sgo1_%=\n" \
"Lw2_go1_%=:\n" \
"buffer_load_dwordx4 v[52:55], %[lo], %[ws], s72 offen offset:0 sc1\n" \
"buffer_load_dwordx4 v[56:59], %[lo], %[ws], s72 offen offset:1024 sc1\n" \
"buffer_load_dwordx2 v[92:93], %[sl], %[ws], s75 offen offset:0 sc1\n" \
"buffer_load_dwordx4 v[96:99], %[so], %[ws], s82 offen sc1\n" \
"buffer_load_dwordx4 v[60:63], %[lo], %[ws], s72 offen offset:2048 sc1\n" \
"buffer_load_dwordx4 v[64:67], %[lo], %[ws], s72 offen offset:3072 sc1\n" \
"buffer_load_dwordx4 v[68:71], %[lo], %[ws], s73 offen offset:0 sc1\n" \
"buffer_load_dwordx4 v[72:75], %[lo], %[ws], s73 offen offset:1024 sc1\n" \
"buffer_load_dwordx4 v[76:79], %[lo], %[ws], s73 offen offset:2048 sc1\n" \
"buffer_load_dwordx4 v[80:83], %[lo], %[ws], s73 offen offset:3072 sc1\n" \
"s_waitcnt vmcnt(10)\n" \
"buffer_store_dword v203, %[fo], %[ws], s69 offen sc1\n" \
"s_mov_b32 s69, 1073741824\n" \
"buffer_store_dword v203, %[fo], %[ws], s69 offen sc1\n" \
"buffer_store_dword v203, %[fo], %[ws], s69 offen sc1\n" \
"Lw2_body1_%=:\n" \
"s_waitcnt vmcnt(12)\n" \
"v_mfma_f32_16x16x32_bf16 v[108:111], a[8:11], v[52:55], 0\n" \
"s_nop 2\n" \
"v_mfma_f32_16x16x32_bf16 v[112:115], a[12:15], v[52:55], 0\n" \
"s_nop 2\n" \
"v_mfma_f32_16x16x32_bf16 v[116:119], a[16:19], v[52:55], 0\n" \
"s_add_u32 s74, s74, 1\n" \
"s_add_u32 s67, s67, 32768\n" \
"s_min_u32 s74, s74, 128\n" \
"v_mfma_f32_16x16x32_bf16 v[120:123], a[20:23], v[52:55], 0\n" \
"s_add_u32 s65, s65, 1\n" \
"s_add_u32 s70, s70, 2048\n" \
"s_add_u32 s71, s71, 32768\n" \
"v_mfma_f32_16x16x32_bf16 v[124:127], a[24:27], v[52:55], 0\n" \
"s_cmp_gt_u32 s65, 104\n" \
"s_cselect_b32 s79, 1073741824, 0\n" \
"s_cselect_b32 s69, 0, s69\n" \
"v_mfma_f32_16x16x32_bf16 v[128:131], a[28:31], v[52:55], 0\n" \
"s_waitcnt vmcnt(11)\n" \
"s_addc_u32 s66, s65, -1\n" \
"s_nop 0\n" \
"v_mfma_f32_16x16x32_bf16 v[108:111], a[40:43], v[56:59], v[108:111]\n" \
"s_nop 0\n" \
"buffer_load_dword v202, %[fvo], %[ws], 0 offen sc1\n" \
"v_mov_b32 v203, s66\n" \
"v_mfma_f32_16x16x32_bf16 v[112:115], a[44:47], v[56:59], v[112:115]\n" \
"s_waitcnt vmcnt(10)\n" \
"v_max3_u32 v215, v92, v93, v96\n" \
"v_mfma_f32_16x16x32_bf16 v[116:119], a[48:51], v[56:59], v[116:119]\n" \
"s_nop 0\n" \
"v_max3_u32 v215, v215, v97, v98\n" \
"v_cmp_u_f32 vcc, v215, v99\n" \
"v_mfma_f32_16x16x32_bf16 v[120:123], a[52:55], v[56:59], v[120:123]\n" \
"s_cbranch_vccnz Lw2_sk10_%=\n" \
"Lw2_k10_%=:\n" \
"v_mfma_f32_16x16x32_bf16 v[124:127], a[56:59], v[56:59], v[124:127]\n" \
"s_nop 0\n" \
"v_permlane32_swap_b32 v92, v93\n" \
"v_mfma_f32_16x16x32_bf16 v[128:131], a[60:63], v[56:59], v[128:131]\n" \
"s_waitcnt vmcnt(9)\n" \
"v_add_f32 v94, v92, v93\n" \
"v_add_f32 v92, v92, v93\n" \
"v_mfma_f32_16x16x32_bf16 v[108:111], a[72:75], v[60:63], v[108:111]\n" \
"s_nop 0\n" \
"v_permlane16_swap_b32 v92, v94\n" \
"v_mfma_f32_16x16x32_bf16 v[112:115], a[76:79], v[60:63], v[112:115]\n" \
"s_nop 0\n" \
"v_add_f32 v92, v92, v94\n" \
"v_mfma_f32_16x16x32_bf16 v[116:119], a[80:83], v[60:63], v[116:119]\n" \
"s_nop 0\n" \
"ds_write_b32 %[sw], v92 offset:32768\n" \
"v_mfma_f32_16x16x32_bf16 v[120:123], a[84:87], v[60:63], v[120:123]\n" \
"s_nop 2\n" \
"v_mfma_f32_16x16x32_bf16 v[124:127], a[88:91], v[60:63], v[124:127]\n" \
"s_nop 2\n" \
"v_mfma_f32_16x16x32_bf16 v[128:131], a[92:95], v[60:63], v[128:131]\n" \
"s_waitcnt vmcnt(8) lgkmcnt(0)\n" \
"s_barrier\n" \
"v_mfma_f32_16x16x32_bf16 v[108:111], a[104:107], v[64:67], v[108:111]\n" \
"s_nop 0\n" \
"ds_read_b128 v[190:193], %[sr] offset:32768\n" \
"v_mfma_f32_16x16x32_bf16 v[112:115], a[108:111], v[64:67], v[112:115]\n" \
"s_nop 0\n" \
"ds_read_b128 v[194:197], %[sr] offset:32784\n" \
"v_mfma_f32_16x16x32_bf16 v[116:119], a[112:115], v[64:67], v[116:119]\n" \
"s_nop 2\n" \
"v_mfma_f32_16x16x32_bf16 v[120:123], a[116:119], v[64:67], v[120:123]\n" \
"s_nop 2\n" \
"v_mfma_f32_16x16x32_bf16 v[124:127], a[120:123], v[64:67], v[124:127]\n" \
"s_nop 2\n" \
"v_mfma_f32_16x16x32_bf16 v[128:131], a[124:127], v[64:67], v[128:131]\n" \
"s_waitcnt vmcnt(7) lgkmcnt(1)\n" \
"v_add_f32 v190, v190, v191\n" \
"v_add_f32 v192, v192, v193\n" \
"v_mfma_f32_16x16x32_bf16 v[108:111], a[136:139], v[68:71], v[108:111]\n" \
"s_waitcnt lgkmcnt(0)\n" \
"v_add_f32 v194, v194, v195\n" \
"v_add_f32 v196, v196, v197\n" \
"v_mfma_f32_16x16x32_bf16 v[112:115], a[140:143], v[68:71], v[112:115]\n" \
"s_nop 0\n" \
"v_add_f32 v190, v190, v192\n" \
"v_add_f32 v194, v194, v196\n" \
"v_mfma_f32_16x16x32_bf16 v[116:119], a[144:147], v[68:71], v[116:119]\n" \
"s_nop 0\n" \
"v_mul_f32 v190, 0xba800000, v190\n" \
"v_mul_f32 v194, 0x3a800000, v194\n" \
"v_mfma_f32_16x16x32_bf16 v[120:123], a[148:151], v[68:71], v[120:123]\n" \
"s_nop 0\n" \
"v_fma_f32 v194, -v190, v190, v194\n" \
"v_max_f32 v194, 0, v194\n" \
"v_mfma_f32_16x16x32_bf16 v[124:127], a[152:155], v[68:71], v[124:127]\n" \
"s_nop 0\n" \
"v_add_f32 v192, 0x3727c5ac, v194\n" \
"v_rsq_f32 v192, v192\n" \
"v_mfma_f32_16x16x32_bf16 v[128:131], a[156:159], v[68:71], v[128:131]\n" \
"s_waitcnt vmcnt(6)\n" \
"v_lshlrev_b32 v172, 16, v96\n" \
"v_mul_f32 v190, v190, v192\n" \
"v_mfma_f32_16x16x32_bf16 v[108:111], a[168:171], v[72:75], v[108:111]\n" \
"s_waitcnt vmcnt(5)\n" \
"v_and_b32 v173, 0xffff0000, v96\n" \
"v_lshlrev_b32 v174, 16, v97\n" \
"v_mfma_f32_16x16x32_bf16 v[108:111], a[200:203], v[76:79], v[108:111]\n" \
"s_waitcnt vmcnt(4)\n" \
"v_and_b32 v175, 0xffff0000, v97\n" \
"v_lshlrev_b32 v176, 16, v98\n" \
"v_mfma_f32_16x16x32_bf16 v[108:111], a[232:235], v[80:83], v[108:111]\n" \
"s_nop 0\n" \
"v_and_b32 v177, 0xffff0000, v98\n" \
"v_lshlrev_b32 v178, 16, v99\n" \
"v_mfma_f32_16x16x32_bf16 v[112:115], a[172:175], v[72:75], v[112:115]\n" \
"s_nop 0\n" \
"v_and_b32 v179, 0xffff0000, v99\n" \
"v_fma_f32 v156, v172, v192, v190\n" \
"v_mfma_f32_16x16x32_bf16 v[112:115], a[204:207], v[76:79], v[112:115]\n" \
"s_nop 0\n" \
"ds_write_b128 v212, v[108:111] offset:34816\n" \
"v_mfma_f32_16x16x32_bf16 v[112:115], a[236:239], v[80:83], v[112:115]\n" \
"s_nop 0\n" \
"v_cmp_u_f32 vcc, v108, v108\n" \
"v_fma_f32 v157, v173, v192, v190\n" \
"v_mfma_f32_16x16x32_bf16 v[116:119], a[176:179], v[72:75], v[116:119]\n" \
"s_cbranch_vccnz Lw2_sn1_%=\n" \
"Lw2_n1_%=:\n" \
"v_fma_f32 v158, v174, v192, v190\n" \
"v_mfma_f32_16x16x32_bf16 v[116:119], a[208:211], v[76:79], v[116:119]\n" \
"s_nop 0\n" \
"ds_write_b128 v212, v[112:115] offset:35840\n" \
"v_mfma_f32_16x16x32_bf16 v[116:119], a[240:243], v[80:83], v[116:119]\n" \
"s_nop 0\n" \
"v_fma_f32 v159, v175, v192, v190\n" \
"v_fma_f32 v160, v176, v192, v190\n" \
"v_mfma_f32_16x16x32_bf16 v[120:123], a[180:183], v[72:75], v[120:123]\n" \
"s_nop 0\n" \
"v_fma_f32 v161, v177, v192, v190\n" \
"v_fma_f32 v162, v178, v192, v190\n" \
"v_mfma_f32_16x16x32_bf16 v[120:123], a[212:215], v[76:79], v[120:123]\n" \
"s_nop 0\n" \
"ds_write_b128 v213, v[116:119] offset:36864\n" \
"v_fma_f32 v163, v179, v192, v190\n" \
"v_mfma_f32_16x16x32_bf16 v[120:123], a[244:247], v[80:83], v[120:123]\n" \
"s_waitcnt vmcnt(0)\n" \
"v_cmp_gt_u32 vcc, s74, v202\n" \
"v_mfma_f32_16x16x32_bf16 v[124:127], a[184:187], v[72:75], v[124:127]\n" \
"s_nop 0\n" \
"buffer_load_dword v202, %[fvo], %[ws], 0 offen sc1\n" \
"v_mfma_f32_16x16x32_bf16 v[124:127], a[216:219], v[76:79], v[124:127]\n" \
"s_cmp_eq_u64 vcc, 0\n" \
"s_nop 0\n" \
"ds_write_b128 v213, v[120:123] offset:37888\n" \
"v_mfma_f32_16x16x32_bf16 v[124:127], a[248:251], v[80:83], v[124:127]\n" \
"s_cselect_b32 s72, s67, 1073741824\n" \
"buffer_load_dwordx4 v[20:23], %[lo], %[ws], s72 offen offset:0 sc1\n" \
"v_mfma_f32_16x16x32_bf16 v[128:131], a[188:191], v[72:75], v[128:131]\n" \
"s_cselect_b32 s75, s70, 1073741824\n" \
"s_cselect_b32 s82, s71, 1073741824\n" \
"s_add_u32 s73, s72, 4096\n" \
"v_mfma_f32_16x16x32_bf16 v[128:131], a[220:223], v[76:79], v[128:131]\n" \
"s_nop 0\n" \
"ds_write_b128 v214, v[124:127] offset:38912\n" \
"v_mfma_f32_16x16x32_bf16 v[128:131], a[252:255], v[80:83], v[128:131]\n" \
"s_waitcnt lgkmcnt(1)\n" \
"s_barrier\n" \
"v_mfma_f32_16x16x32_bf16 v[100:103], a[0:3], v[52:55], v[156:159]\n" \
"s_nop 0\n" \
"ds_read_b128 v[132:135], %[xr] offset:34816\n" \
"buffer_load_dwordx4 v[24:27], %[lo], %[ws], s72 offen offset:1024 sc1\n" \
"v_mfma_f32_16x16x32_bf16 v[104:107], a[4:7], v[52:55], v[160:163]\n" \
"s_nop 0\n" \
"ds_write_b128 v214, v[128:131] offset:39936\n" \
"v_mfma_f32_16x16x32_bf16 v[100:103], a[32:35], v[56:59], v[100:103]\n" \
"s_nop 0\n" \
"ds_read_b128 v[136:139], %[xr] offset:35840\n" \
"v_mfma_f32_16x16x32_bf16 v[104:107], a[36:39], v[56:59], v[104:107]\n" \
"s_nop 0\n" \
"ds_read_b128 v[140:143], %[xr] offset:36864\n" \
"buffer_store_dword v203, %[fo], %[ws], s69 offen sc1\n" \
"v_mfma_f32_16x16x32_bf16 v[100:103], a[64:67], v[60:63], v[100:103]\n" \
"s_nop 0\n" \
"ds_read_b128 v[144:147], %[xr] offset:37888\n" \
"buffer_load_dwordx2 v[84:85], %[sl], %[ws], s75 offen offset:0 sc1\n" \
"v_mfma_f32_16x16x32_bf16 v[104:107], a[68:71], v[60:63], v[104:107]\n" \
"s_waitcnt lgkmcnt(3)\n" \
"s_barrier\n" \
"v_mfma_f32_16x16x32_bf16 v[100:103], a[96:99], v[64:67], v[100:103]\n" \
"s_nop 0\n" \
"ds_read_b128 v[148:151], %[xr] offset:38912\n" \
"buffer_load_dwordx4 v[88:91], %[so], %[ws], s82 offen sc1\n" \
"v_mfma_f32_16x16x32_bf16 v[104:107], a[100:103], v[64:67], v[104:107]\n" \
"s_nop 0\n" \
"ds_read_b128 v[152:155], %[xr] offset:39936\n" \
"v_mfma_f32_16x16x32_bf16 v[100:103], a[128:131], v[68:71], v[100:103]\n" \
"s_waitcnt lgkmcnt(3)\n" \
"v_add_f32 v132, v132, v140\n" \
"v_add_f32 v133, v133, v141\n" \
"v_mfma_f32_16x16x32_bf16 v[104:107], a[132:135], v[68:71], v[104:107]\n" \
"s_nop 0\n" \
"v_add_f32 v134, v134, v142\n" \
"v_add_f32 v135, v135, v143\n" \
"v_mfma_f32_16x16x32_bf16 v[100:103], a[160:163], v[72:75], v[100:103]\n" \
"s_waitcnt lgkmcnt(2)\n" \
"buffer_load_dwordx4 v[28:31], %[lo], %[ws], s72 offen offset:2048 sc1\n" \
"v_add_f32 v136, v136, v144\n" \
"v_add_f32 v137, v137, v145\n" \
"v_mfma_f32_16x16x32_bf16 v[104:107], a[164:167], v[72:75], v[104:107]\n" \
"s_nop 0\n" \
"v_add_f32 v138, v138, v146\n" \
"v_add_f32 v139, v139, v147\n" \
"v_mfma_f32_16x16x32_bf16 v[100:103], a[192:195], v[76:79], v[100:103]\n" \
"s_waitcnt lgkmcnt(1)\n" \
"v_add_f32 v132, v132, v148\n" \
"v_add_f32 v133, v133, v149\n" \
"v_mfma_f32_16x16x32_bf16 v[104:107], a[196:199], v[76:79], v[104:107]\n" \
"s_nop 0\n" \
"v_add_f32 v134, v134, v150\n" \
"v_add_f32 v135, v135, v151\n" \
"v_mfma_f32_16x16x32_bf16 v[100:103], a[224:227], v[80:83], v[100:103]\n" \
"s_waitcnt lgkmcnt(0)\n" \
"buffer_load_dwordx4 v[32:35], %[lo], %[ws], s72 offen offset:3072 sc1\n" \
"v_add_f32 v136, v136, v152\n" \
"v_add_f32 v137, v137, v153\n" \
"v_mfma_f32_16x16x32_bf16 v[104:107], a[228:231], v[80:83], v[104:107]\n" \
"s_nop 0\n" \
"v_add_f32 v138, v138, v154\n" \
"v_add_f32 v139, v139, v155\n" \
"v_mov_b32 v203, s65\n" \
"s_mov_b32 s69, s79\n" \
"buffer_load_dwordx4 v[36:39], %[lo], %[ws], s73 offen offset:0 sc1\n" \
"v_pk_add_f32 v[100:101], v[100:101], v[132:133]\n" \
"v_pk_add_f32 v[102:103], v[102:103], v[134:135]\n" \
"v_pk_add_f32 v[104:105], v[104:105], v[136:137]\n" \
"v_pk_add_f32 v[106:107], v[106:107], v[138:139]\n" \
"buffer_load_dwordx4 v[40:43], %[lo], %[ws], s73 offen offset:1024 sc1\n" \
"buffer_load_dwordx4 v[44:47], %[lo], %[ws], s73 offen offset:2048 sc1\n" \
"buffer_load_dwordx4 v[48:51], %[lo], %[ws], s73 offen offset:3072 sc1\n" \
"v_cvt_pk_bf16_f32 v204, v100, v101\n" \
"v_cvt_pk_bf16_f32 v205, v102, v103\n" \
"v_cvt_pk_bf16_f32 v206, v104, v105\n" \
"v_cvt_pk_bf16_f32 v207, v106, v107\n" \
"buffer_store_dwordx4 v[204:207], %[so], %[ws], s68 offen sc1\n" \
"v_pk_mul_f32 v[134:135], v[100:101], v[100:101]\n" \
"v_pk_add_f32 v[132:133], v[100:101], v[102:103]\n" \
"v_pk_fma_f32 v[134:135], v[102:103], v[102:103], v[134:135]\n" \
"v_pk_fma_f32 v[134:135], v[104:105], v[104:105], v[134:135]\n" \
"v_pk_add_f32 v[132:133], v[132:133], v[104:105]\n" \
"v_pk_fma_f32 v[134:135], v[106:107], v[106:107], v[134:135]\n" \
"v_pk_add_f32 v[132:133], v[132:133], v[106:107]\n" \
"v_add_f32 v132, v132, v133\n" \
"v_add_f32 v134, v134, v135\n" \
"v_pk_add_f32 v[172:173], v[172:173], v[100:101]\n" \
"v_pk_add_f32 v[174:175], v[174:175], v[102:103]\n" \
"v_permlane32_swap_b32 v132, v134\n" \
"v_add_f32 v133, v132, v134\n" \
"v_add_f32 v132, v132, v134\n" \
"v_pk_add_f32 v[176:177], v[176:177], v[104:105]\n" \
"v_pk_add_f32 v[178:179], v[178:179], v[106:107]\n" \
"v_permlane16_swap_b32 v132, v133\n" \
"v_add_f32 v132, v132, v133\n" \
"v_cvt_pk_bf16_f32 v208, v172, v173\n" \
"v_cvt_pk_bf16_f32 v209, v174, v175\n" \
"v_cvt_pk_bf16_f32 v210, v176, v177\n" \
"v_cvt_pk_bf16_f32 v211, v178, v179\n" \
"buffer_store_dword v132, %[sto], %[ws], s76 offen sc1\n" \
"buffer_store_dwordx4 v[208:211], %[yo], %[ws], s68 offen sc1\n" \
"s_add_u32 s76, s76, 4096\n" \
"s_add_u32 s68, s68, 32768\n" \
"s_cmp_eq_u32 s65, s80\n" \
"s_cbranch_scc1 Lw2_end_%=\n" \
"Lw2_on_%=:\n" \
"s_cmp_eq_u32 s72, s67\n" \
"s_cbranch_scc1 Lw2_body0_%=\n" \
"s_waitcnt vmcnt(14)\n" \
"s_branch Lw2_top0_%=\n" \
"Lw2_end_%=:\n" \
"s_endpgm\n" \
"Lw2_sgo0_%=:\n" \
"s_waitcnt vmcnt(0)\n" \
"buffer_load_dword v202, %[fvo], %[ws], 0 offen sc1\n" \
"buffer_store_dword v203, %[fo], %[ws], s69 offen sc1\n" \
"s_mov_b32 s69, 1073741824\n" \
"s_waitcnt vmcnt(1)\n" \
"s_branch Lw2_cgo0_%=\n" \
"Lw2_pgo0_%=:\n" \
"s_sub_u32 s64, s64, 1\n" \
"s_cbranch_scc1 Lw2_dead_%=\n" \
"buffer_load_dword v202, %[fvo], %[ws], 0 offen sc1\n" \
"s_waitcnt vmcnt(0)\n" \
"Lw2_cgo0_%=:\n" \
"v_cmp_gt_u32 vcc, s74, v202\n" \
"s_cbranch_vccz Lw2_go0_%=\n" \
"s_sleep 1\n" \
"s_branch Lw2_pgo0_%=\n" \
"Lw2_sk00_%=:\n" \
"v_max3_u32 v215, v84, v85, v88\n" \
"v_max3_u32 v215, v215, v89, v90\n" \
"v_max3_u32 v215, v215, v91, v91\n" \
"v_cmp_eq_u32 vcc, -1, v215\n" \
"s_nop 1\n" \
"s_cbranch_vccz Lw2_k00_%=\n" \
"Lw2_rk00_%=:\n" \
"s_sub_u32 s64, s64, 1\n" \
"s_cbranch_scc1 Lw2_dead_%=\n" \
"buffer_load_dwordx2 v[84:85], %[sl], %[ws], s75 offen offset:0 sc1\n" \
"buffer_load_dwordx4 v[88:91], %[so], %[ws], s82 offen sc1\n" \
"s_waitcnt vmcnt(0)\n" \
"v_max3_u32 v215, v84, v85, v88\n" \
"v_max3_u32 v215, v215, v89, v90\n" \
"v_max3_u32 v215, v215, v91, v91\n" \
"v_cmp_eq_u32 vcc, -1, v215\n" \
"s_nop 1\n" \
"s_cbranch_vccnz Lw2_rk00_%=\n" \
"s_branch Lw2_k00_%=\n" \
"Lw2_sn0_%=:\n" \
"v_max3_u32 v215, v20, v21, v22\n" \
"v_max3_u32 v215, v215, v23, v24\n" \
"v_max3_u32 v215, v215, v25, v26\n" \
"v_max3_u32 v215, v215, v27, v28\n" \
"v_max3_u32 v215, v215, v29, v30\n" \
"v_max3_u32 v215, v215, v31, v32\n" \
"v_max3_u32 v215, v215, v33, v34\n" \
"v_max3_u32 v215, v215, v35, v36\n" \
"v_max3_u32 v215, v215, v37, v38\n" \
"v_max3_u32 v215, v215, v39, v40\n" \
"v_max3_u32 v215, v215, v41, v42\n" \
"v_max3_u32 v215, v215, v43, v44\n" \
"v_max3_u32 v215, v215, v45, v46\n" \
"v_max3_u32 v215, v215, v47, v48\n" \
"v_max3_u32 v215, v215, v49, v50\n" \
"v_max3_u32 v215, v215, v51, v51\n" \
"v_cmp_eq_u32 vcc, -1, v215\n" \
"s_nop 1\n" \
"s_cbranch_vccz Lw2_n0_%=\n" \
"Lw2_rn0_%=:\n" \
"s_sub_u32 s64, s64, 1\n" \
"s_cbranch_scc1 Lw2_dead_%=\n" \
"buffer_load_dwordx4 v[20:23], %[lo], %[ws], s72 offen offset:0 sc1\n" \
"buffer_load_dwordx4 v[24:27], %[lo], %[ws], s72 offen offset:1024 sc1\n" \
"buffer_load_dwordx4 v[28:31], %[lo], %[ws], s72 offen offset:2048 sc1\n" \
"buffer_load_dwordx4 v[32:35], %[lo], %[ws], s72 offen offset:3072 sc1\n" \
"buffer_load_dwordx4 v[36:39], %[lo], %[ws], s73 offen offset:0 sc1\n" \
"buffer_load_dwordx4 v[40:43], %[lo], %[ws], s73 offen offset:1024 sc1\n" \
"buffer_load_dwordx4 v[44:47], %[lo], %[ws], s73 offen offset:2048 sc1\n" \
"buffer_load_dwordx4 v[48:51], %[lo], %[ws], s73 offen offset:3072 sc1\n" \
"s_waitcnt vmcnt(0)\n" \
"v_max3_u32 v215, v20, v21, v22\n" \
"v_max3_u32 v215, v215, v23, v24\n" \
"v_max3_u32 v215, v215, v25, v26\n" \
"v_max3_u32 v215, v215, v27, v28\n" \
"v_max3_u32 v215, v215, v29, v30\n" \
"v_max3_u32 v215, v215, v31, v32\n" \
"v_max3_u32 v215, v215, v33, v34\n" \
"v_max3_u32 v215, v215, v35, v36\n" \
"v_max3_u32 v215, v215, v37, v38\n" \
"v_max3_u32 v215, v215, v39, v40\n" \
"v_max3_u32 v215, v215, v41, v42\n" \
"v_max3_u32 v215, v215, v43, v44\n" \
"v_max3_u32 v215, v215, v45, v46\n" \
"v_max3_u32 v215, v215, v47, v48\n" \
"v_max3_u32 v215, v215, v49, v50\n" \
"v_max3_u32 v215, v215, v51, v51\n" \
"v_cmp_eq_u32 vcc, -1, v215\n" \
"s_nop 1\n" \
"s_cbranch_vccnz Lw2_rn0_%=\n" \
"v_mfma_f32_16x16x32_bf16 v[108:111], a[8:11], v[20:23], 0\n" \
"v_mfma_f32_16x16x32_bf16 v[112:115], a[12:15], v[20:23], 0\n" \
"v_mfma_f32_16x16x32_bf16 v[116:119], a[16:19], v[20:23], 0\n" \
"v_mfma_f32_16x16x32_bf16 v[120:123], a[20:23], v[20:23], 0\n" \
"v_mfma_f32_16x16x32_bf16 v[124:127], a[24:27], v[20:23], 0\n" \
"v_mfma_f32_16x16x32_bf16 v[128:131], a[28:31], v[20:23], 0\n" \
"v_mfma_f32_16x16x32_bf16 v[108:111], a[40:43], v[24:27], v[108:111]\n" \
"v_mfma_f32_16x16x32_bf16 v[112:115], a[44:47], v[24:27], v[112:115]\n" \
"v_mfma_f32_16x16x32_bf16 v[116:119], a[48:51], v[24:27], v[116:119]\n" \
"v_mfma_f32_16x16x32_bf16 v[120:123], a[52:55], v[24:27], v[120:123]\n" \
"v_mfma_f32_16x16x32_bf16 v[124:127], a[56:59], v[24:27], v[124:127]\n" \
"v_mfma_f32_16x16x32_bf16 v[128:131], a[60:63], v[24:27], v[128:131]\n" \
"v_mfma_f32_16x16x32_bf16 v[108:111], a[72:75], v[28:31], v[108:111]\n" \
"v_mfma_f32_16x16x32_bf16 v[112:115], a[76:79], v[28:31], v[112:115]\n" \
"v_mfma_f32_16x16x32_bf16 v[116:119], a[80:83], v[28:31], v[116:119]\n" \
"v_mfma_f32_16x16x32_bf16 v[120:123], a[84:87], v[28:31], v[120:123]\n" \
"v_mfma_f32_16x16x32_bf16 v[124:127], a[88:91], v[28:31], v[124:127]\n" \
"v_mfma_f32_16x16x32_bf16 v[128:131], a[92:95], v[28:31], v[128:131]\n" \
"v_mfma_f32_16x16x32_bf16 v[108:111], a[104:107], v[32:35], v[108:111]\n" \
"v_mfma_f32_16x16x32_bf16 v[112:115], a[108:111], v[32:35], v[112:115]\n" \
"v_mfma_f32_16x16x32_bf16 v[116:119], a[112:115], v[32:35], v[116:119]\n" \
"v_mfma_f32_16x16x32_bf16 v[120:123], a[116:119], v[32:35], v[120:123]\n" \
"v_mfma_f32_16x16x32_bf16 v[124:127], a[120:123], v[32:35], v[124:127]\n" \
"v_mfma_f32_16x16x32_bf16 v[128:131], a[124:127], v[32:35], v[128:131]\n" \
"v_mfma_f32_16x16x32_bf16 v[108:111], a[136:139], v[36:39], v[108:111]\n" \
"v_mfma_f32_16x16x32_bf16 v[112:115], a[140:143], v[36:39], v[112:115]\n" \
"v_mfma_f32_16x16x32_bf16 v[116:119], a[144:147], v[36:39], v[116:119]\n" \
"v_mfma_f32_16x16x32_bf16 v[120:123], a[148:151], v[36:39], v[120:123]\n" \
"v_mfma_f32_16x16x32_bf16 v[124:127], a[152:155], v[36:39], v[124:127]\n" \
"v_mfma_f32_16x16x32_bf16 v[128:131], a[156:159], v[36:39], v[128:131]\n" \
"v_mfma_f32_16x16x32_bf16 v[108:111], a[168:171], v[40:43], v[108:111]\n" \
"v_mfma_f32_16x16x32_bf16 v[108:111], a[200:203], v[44:47], v[108:111]\n" \
"v_mfma_f32_16x16x32_bf16 v[108:111], a[232:235], v[48:51], v[108:111]\n" \
"v_mfma_f32_16x16x32_bf16 v[112:115], a[172:175], v[40:43], v[112:115]\n" \
"v_mfma_f32_16x16x32_bf16 v[112:115], a[204:207], v[44:47], v[112:115]\n" \
"v_mfma_f32_16x16x32_bf16 v[112:115], a[236:239], v[48:51], v[112:115]\n" \
"v_mfma_f32_16x16x32_bf16 v[116:119], a[176:179], v[40:43], v[116:119]\n" \
"s_nop 7\n" \
"ds_write_b128 v212, v[108:111] offset:2048\n" \
"s_branch Lw2_n0_%=\n" \
"Lw2_sgo1_%=:\n" \
"s_waitcnt vmcnt(0)\n" \
"buffer_load_dword v202, %[fvo], %[ws], 0 offen sc1\n" \
"buffer_store_dword v203, %[fo], %[ws], s69 offen sc1\n" \
"s_mov_b32 s69, 1073741824\n" \
"s_waitcnt vmcnt(1)\n" \
"s_branch Lw2_cgo1_%=\n" \
"Lw2_pgo1_%=:\n" \
"s_sub_u32 s64, s64, 1\n" \
"s_cbranch_scc1 Lw2_dead_%=\n" \
"buffer_load_dword v202, %[fvo], %[ws], 0 offen sc1\n" \
"s_waitcnt vmcnt(0)\n" \
"Lw2_cgo1_%=:\n" \
"v_cmp_gt_u32 vcc, s74, v202\n" \
"s_cbranch_vccz Lw2_go1_%=\n" \
"s_sleep 1\n" \
"s_branch Lw2_pgo1_%=\n" \
"Lw2_sk10_%=:\n" \
"v_max3_u32 v215, v92, v93, v96\n" \
"v_max3_u32 v215, v215, v97, v98\n" \
"v_max3_u32 v215, v215, v99, v99\n" \
"v_cmp_eq_u32 vcc, -1, v215\n" \
"s_nop 1\n" \
"s_cbranch_vccz Lw2_k10_%=\n" \
"Lw2_rk10_%=:\n" \
"s_sub_u32 s64, s64, 1\n" \
"s_cbranch_scc1 Lw2_dead_%=\n" \
"buffer_load_dwordx2 v[92:93], %[sl], %[ws], s75 offen offset:0 sc1\n" \
"buffer_load_dwordx4 v[96:99], %[so], %[ws], s82 offen sc1\n" \
"s_waitcnt vmcnt(0)\n" \
"v_max3_u32 v215, v92, v93, v96\n" \
"v_max3_u32 v215, v215, v97, v98\n" \
"v_max3_u32 v215, v215, v99, v99\n" \
"v_cmp_eq_u32 vcc, -1, v215\n" \
"s_nop 1\n" \
"s_cbranch_vccnz Lw2_rk10_%=\n" \
"s_branch Lw2_k10_%=\n" \
"Lw2_sn1_%=:\n" \
"v_max3_u32 v215, v52, v53, v54\n" \
"v_max3_u32 v215, v215, v55, v56\n" \
"v_max3_u32 v215, v215, v57, v58\n" \
"v_max3_u32 v215, v215, v59, v60\n" \
"v_max3_u32 v215, v215, v61, v62\n" \
"v_max3_u32 v215, v215, v63, v64\n" \
"v_max3_u32 v215, v215, v65, v66\n" \
"v_max3_u32 v215, v215, v67, v68\n" \
"v_max3_u32 v215, v215, v69, v70\n" \
"v_max3_u32 v215, v215, v71, v72\n" \
"v_max3_u32 v215, v215, v73, v74\n" \
"v_max3_u32 v215, v215, v75, v76\n" \
"v_max3_u32 v215, v215, v77, v78\n" \
"v_max3_u32 v215, v215, v79, v80\n" \
"v_max3_u32 v215, v215, v81, v82\n" \
"v_max3_u32 v215, v215, v83, v83\n" \
"v_cmp_eq_u32 vcc, -1, v215\n" \
"s_nop 1\n" \
"s_cbranch_vccz Lw2_n1_%=\n" \
"Lw2_rn1_%=:\n" \
"s_sub_u32 s64, s64, 1\n" \
"s_cbranch_scc1 Lw2_dead_%=\n" \
"buffer_load_dwordx4 v[52:55], %[lo], %[ws], s72 offen offset:0 sc1\n" \
"buffer_load_dwordx4 v[56:59], %[lo], %[ws], s72 offen offset:1024 sc1\n" \
"buffer_load_dwordx4 v[60:63], %[lo], %[ws], s72 offen offset:2048 sc1\n" \
"buffer_load_dwordx4 v[64:67], %[lo], %[ws], s72 offen offset:3072 sc1\n" \
"buffer_load_dwordx4 v[68:71], %[lo], %[ws], s73 offen offset:0 sc1\n" \
"buffer_load_dwordx4 v[72:75], %[lo], %[ws], s73 offen offset:1024 sc1\n" \
"buffer_load_dwordx4 v[76:79], %[lo], %[ws], s73 offen offset:2048 sc1\n" \
"buffer_load_dwordx4 v[80:83], %[lo], %[ws], s73 offen offset:3072 sc1\n" \
"s_waitcnt vmcnt(0)\n" \
"v_max3_u32 v215, v52, v53, v54\n" \
"v_max3_u32 v215, v215, v55, v56\n" \
"v_max3_u32 v215, v215, v57, v58\n" \
"v_max3_u32 v215, v215, v59, v60\n" \
"v_max3_u32 v215, v215, v61, v62\n" \
"v_max3_u32 v215, v215, v63, v64\n" \
"v_max3_u32 v215, v215, v65, v66\n" \
"v_max3_u32 v215, v215, v67, v68\n" \
"v_max3_u32 v215, v215, v69, v70\n" \
"v_max3_u32 v215, v215, v71, v72\n" \
"v_max3_u32 v215, v215, v73, v74\n" \
"v_max3_u32 v215, v215, v75, v76\n" \
"v_max3_u32 v215, v215, v77, v78\n" \
"v_max3_u32 v215, v215, v79, v80\n" \
"v_max3_u32 v215, v215, v81, v82\n" \
"v_max3_u32 v215, v215, v83, v83\n" \
"v_cmp_eq_u32 vcc, -1, v215\n" \
"s_nop 1\n" \
"s_cbranch_vccnz Lw2_rn1_%=\n" \
"v_mfma_f32_16x16x32_bf16 v[108:111], a[8:11], v[52:55], 0\n" \
"v_mfma_f32_16x16x32_bf16 v[112:115], a[12:15], v[52:55], 0\n" \
"v_mfma_f32_16x16x32_bf16 v[116:119], a[16:19], v[52:55], 0\n" \
"v_mfma_f32_16x16x32_bf16 v[120:123], a[20:23], v[52:55], 0\n" \
"v_mfma_f32_16x16x32_bf16 v[124:127], a[24:27], v[52:55], 0\n" \
"v_mfma_f32_16x16x32_bf16 v[128:131], a[28:31], v[52:55], 0\n" \
"v_mfma_f32_16x16x32_bf16 v[108:111], a[40:43], v[56:59], v[108:111]\n" \
"v_mfma_f32_16x16x32_bf16 v[112:115], a[44:47], v[56:59], v[112:115]\n" \
"v_mfma_f32_16x16x32_bf16 v[116:119], a[48:51], v[56:59], v[116:119]\n" \
"v_mfma_f32_16x16x32_bf16 v[120:123], a[52:55], v[56:59], v[120:123]\n" \
"v_mfma_f32_16x16x32_bf16 v[124:127], a[56:59], v[56:59], v[124:127]\n" \
"v_mfma_f32_16x16x32_bf16 v[128:131], a[60:63], v[56:59], v[128:131]\n" \
"v_mfma_f32_16x16x32_bf16 v[108:111], a[72:75], v[60:63], v[108:111]\n" \
"v_mfma_f32_16x16x32_bf16 v[112:115], a[76:79], v[60:63], v[112:115]\n" \
"v_mfma_f32_16x16x32_bf16 v[116:119], a[80:83], v[60:63], v[116:119]\n" \
"v_mfma_f32_16x16x32_bf16 v[120:123], a[84:87], v[60:63], v[120:123]\n" \
"v_mfma_f32_16x16x32_bf16 v[124:127], a[88:91], v[60:63], v[124:127]\n" \
"v_mfma_f32_16x16x32_bf16 v[128:131], a[92:95], v[60:63], v[128:131]\n" \
"v_mfma_f32_16x16x32_bf16 v[108:111], a[104:107], v[64:67], v[108:111]\n" \
"v_mfma_f32_16x16x32_bf16 v[112:115], a[108:111], v[64:67], v[112:115]\n" \
"v_mfma_f32_16x16x32_bf16 v[116:119], a[112:115], v[64:67], v[116:119]\n" \
"v_mfma_f32_16x16x32_bf16 v[120:123], a[116:119], v[64:67], v[120:123]\n" \
"v_mfma_f32_16x16x32_bf16 v[124:127], a[120:123], v[64:67], v[124:127]\n" \
"v_mfma_f32_16x16x32_bf16 v[128:131], a[124:127], v[64:67], v[128:131]\n" \
"v_mfma_f32_16x16x32_bf16 v[108:111], a[136:139], v[68:71], v[108:111]\n" \
"v_mfma_f32_16x16x32_bf16 v[112:115], a[140:143], v[68:71], v[112:115]\n" \
"v_mfma_f32_16x16x32_bf16 v[116:119], a[144:147], v[68:71], v[116:119]\n" \
"v_mfma_f32_16x16x32_bf16 v[120:123], a[148:151], v[68:71], v[120:123]\n" \
"v_mfma_f32_16x16x32_bf16 v[124:127], a[152:155], v[68:71], v[124:127]\n" \
"v_mfma_f32_16x16x32_bf16 v[128:131], a[156:159], v[68:71], v[128:131]\n" \
"v_mfma_f32_16x16x32_bf16 v[108:111], a[168:171], v[72:75], v[108:111]\n" \
"v_mfma_f32_16x16x32_bf16 v[108:111], a[200:203], v[76:79], v[108:111]\n" \
"v_mfma_f32_16x16x32_bf16 v[108:111], a[232:235], v[80:83], v[108:111]\n" \
"v_mfma_f32_16x16x32_bf16 v[112:115], a[172:175], v[72:75], v[112:115]\n" \
"v_mfma_f32_16x16x32_bf16 v[112:115], a[204:207], v[76:79], v[112:115]\n" \
"v_mfma_f32_16x16x32_bf16 v[112:115], a[236:239], v[80:83], v[112:115]\n" \
"v_mfma_f32_16x16x32_bf16 v[116:119], a[176:179], v[72:75], v[116:119]\n" \
"s_nop 7\n" \
"ds_write_b128 v212, v[108:111] offset:34816\n" \
"s_branch Lw2_n1_%=\n" \
"Lw2_dead_%=:\n" \
"v_mov_b32 v202, 29186\n" \
"buffer_store_dword v202, off, %[ws], 0 sc1\n" \
"s_endpgm\n" \
    :  : [wvo] "v"(wvo), [tr0] "v"(tr0), [fo] "v"(fo), [xr] "v"(xr), [fvo] "v"(fvo), [lo] "v"(lo), [so] "v"(so), [sl] "v"(sl), [sw] "v"(sw), [sr] "v"(sr), [sto] "v"(sto), [yo] "v"(yo), [ws] "s"(ws), [rw] "s"(rw), [so0] "s"(so0), [wbase] "s"(wbase), [src] "s"(src), [dst] "s"(dst), [d0] "s"(d0), [ox0] "s"(ox0), [ox1] "s"(ox1), [ox2] "s"(ox2) \
    : CLOB20)

#define W3_ASM() asm volatile( \
"s_nop 4\n" \
"s_mov_b32 s66, %[d0]\n" \
"Lw3_dly_%=:\n" \
"s_sleep 8\n" \
"s_sub_u32 s66, s66, 1\n" \
"s_cbranch_scc0 Lw3_dly_%=\n" \
"v_xor_b32 v20, 0, %[tr0]\n" \
"v_xor_b32 v21, 8, %[tr0]\n" \
"v_xor_b32 v22, 64, %[tr0]\n" \
"v_xor_b32 v23, 72, %[tr0]\n" \
"v_xor_b32 v24, 128, %[tr0]\n" \
"v_xor_b32 v25, 136, %[tr0]\n" \
"v_xor_b32 v26, 192, %[tr0]\n" \
"v_xor_b32 v27, 200, %[tr0]\n" \
"s_add_u32 m0, %[wbase], 0\n" \
"s_add_u32 s66, %[so0], 0\n" \
"buffer_load_dwordx4 %[wvo], %[rw], s66 offen lds\n" \
"s_add_u32 m0, %[wbase], 1024\n" \
"s_add_u32 s66, %[so0], 28672\n" \
"buffer_load_dwordx4 %[wvo], %[rw], s66 offen lds\n" \
"s_add_u32 m0, %[wbase], 2048\n" \
"s_add_u32 s66, %[so0], 57344\n" \
"buffer_load_dwordx4 %[wvo], %[rw], s66 offen lds\n" \
"s_waitcnt vmcnt(2)\n" \
"s_add_u32 m0, %[wbase], 3072\n" \
"s_add_u32 s66, %[so0], 86016\n" \
"buffer_load_dwordx4 %[wvo], %[rw], s66 offen lds\n" \
"s_waitcnt vmcnt(2)\n" \
"s_add_u32 m0, %[wbase], 4096\n" \
"s_add_u32 s66, %[so0], 114688\n" \
"buffer_load_dwordx4 %[wvo], %[rw], s66 offen lds\n" \
"s_waitcnt vmcnt(2)\n" \
"s_add_u32 m0, %[wbase], 5120\n" \
"s_add_u32 s66, %[so0], 143360\n" \
"buffer_load_dwordx4 %[wvo], %[rw], s66 offen lds\n" \
"s_waitcnt vmcnt(2)\n" \
"s_add_u32 m0, %[wbase], 6144\n" \
"s_add_u32 s66, %[so0], 172032\n" \
"buffer_load_dwordx4 %[wvo], %[rw], s66 offen lds\n" \
"s_waitcnt vmcnt(2)\n" \
"s_add_u32 m0, %[wbase], 7168\n" \
"s_add_u32 s66, %[so0], 200704\n" \
"buffer_load_dwordx4 %[wvo], %[rw], s66 offen lds\n" \
"s_waitcnt vmcnt(2)\n" \
"s_add_u32 m0, %[wbase], 8192\n" \
"s_add_u32 s66, %[so0], 229376\n" \
"buffer_load_dwordx4 %[wvo], %[rw], s66 offen lds\n" \
"s_waitcnt vmcnt(2)\n" \
"s_add_u32 m0, %[wbase], 9216\n" \
"s_add_u32 s66, %[so0], 258048\n" \
"buffer_load_dwordx4 %[wvo], %[rw], s66 offen lds\n" \
"s_waitcnt vmcnt(2)\n" \
"s_add_u32 m0, %[wbase], 10240\n" \
"s_add_u32 s66, %[so0], 286720\n" \
"buffer_load_dwordx4 %[wvo], %[rw], s66 offen lds\n" \
"ds_read_b64_tr_b16 a[0:1], v20 offset:0\n" \
"ds_read_b64_tr_b16 a[2:3], v20 offset:1024\n" \
"ds_read_b64_tr_b16 a[4:5], v21 offset:0\n" \
"ds_read_b64_tr_b16 a[6:7], v21 offset:1024\n" \
"ds_read_b64_tr_b16 a[8:9], v22 offset:0\n" \
"ds_read_b64_tr_b16 a[10:11], v22 offset:1024\n" \
"ds_read_b64_tr_b16 a[12:13], v23 offset:0\n" \
"ds_read_b64_tr_b16 a[14:15], v23 offset:1024\n" \
"ds_read_b64_tr_b16 a[16:17], v24 offset:0\n" \
"ds_read_b64_tr_b16 a[18:19], v24 offset:1024\n" \
"ds_read_b64_tr_b16 a[20:21], v25 offset:0\n" \
"ds_read_b64_tr_b16 a[22:23], v25 offset:1024\n" \
"ds_read_b64_tr_b16 a[24:25], v26 offset:0\n" \
"ds_read_b64_tr_b16 a[26:27], v26 offset:1024\n" \
"ds_read_b64_tr_b16 a[28:29], v27 offset:0\n" \
"ds_read_b64_tr_b16 a[30:31], v27 offset:1024\n" \
"s_waitcnt lgkmcnt(0)\n" \
"s_waitcnt vmcnt(2)\n" \
"s_add_u32 m0, %[wbase], 11264\n" \
"s_add_u32 s66, %[so0], 315392\n" \
"buffer_load_dwordx4 %[wvo], %[rw], s66 offen lds\n" \
"s_waitcnt vmcnt(2)\n" \
"s_add_u32 m0, %[wbase], 12288\n" \
"s_add_u32 s66, %[so0], 344064\n" \
"buffer_load_dwordx4 %[wvo], %[rw], s66 offen lds\n" \
"s_waitcnt vmcnt(2)\n" \
"s_add_u32 m0, %[wbase], 13312\n" \
"s_add_u32 s66, %[so0], 372736\n" \
"buffer_load_dwordx4 %[wvo], %[rw], s66 offen lds\n" \
"s_waitcnt vmcnt(2)\n" \
"s_add_u32 m0, %[wbase], 14336\n" \
"s_add_u32 s66, %[so0], 401408\n" \
"buffer_load_dwordx4 %[wvo], %[rw], s66 offen lds\n" \
"s_waitcnt vmcnt(2)\n" \
"s_add_u32 m0, %[wbase], 15360\n" \
"s_add_u32 s66, %[so0], 430080\n" \
"buffer_load_dwordx4 %[wvo], %[rw], s66 offen lds\n" \
"s_waitcnt vmcnt(2)\n" \
"s_add_u32 m0, %[wbase], 16384\n" \
"s_add_u32 s66, %[so0], 458752\n" \
"buffer_load_dwordx4 %[wvo], %[rw], s66 offen lds\n" \
"s_waitcnt vmcnt(2)\n" \
"s_add_u32 m0, %[wbase], 17408\n" \
"s_add_u32 s66, %[so0], 487424\n" \
"buffer_load_dwordx4 %[wvo], %[rw], s66 offen lds\n" \
"s_waitcnt vmcnt(2)\n" \
"s_add_u32 m0, %[wbase], 18432\n" \
"s_add_u32 s66, %[so0], 516096\n" \
"buffer_load_dwordx4 %[wvo], %[rw], s66 offen lds\n" \
"ds_read_b64_tr_b16 a[32:33], v20 offset:8192\n" \
"ds_read_b64_tr_b16 a[34:35], v20 offset:9216\n" \
"ds_read_b64_tr_b16 a[36:37], v21 offset:8192\n" \
"ds_read_b64_tr_b16 a[38:39], v21 offset:9216\n" \
"ds_read_b64_tr_b16 a[40:41], v22 offset:8192\n" \
"ds_read_b64_tr_b16 a[42:43], v22 offset:9216\n" \
"ds_read_b64_tr_b16 a[44:45], v23 offset:8192\n" \
"ds_read_b64_tr_b16 a[46:47], v23 offset:9216\n" \
"ds_read_b64_tr_b16 a[48:49], v24 offset:8192\n" \
"ds_read_b64_tr_b16 a[50:51], v24 offset:9216\n" \
"ds_read_b64_tr_b16 a[52:53], v25 offset:8192\n" \
"ds_read_b64_tr_b16 a[54:55], v25 offset:9216\n" \
"ds_read_b64_tr_b16 a[56:57], v26 offset:8192\n" \
"ds_read_b64_tr_b16 a[58:59], v26 offset:9216\n" \
"ds_read_b64_tr_b16 a[60:61], v27 offset:8192\n" \
"ds_read_b64_tr_b16 a[62:63], v27 offset:9216\n" \
"s_waitcnt lgkmcnt(0)\n" \
"s_waitcnt vmcnt(2)\n" \
"s_add_u32 m0, %[wbase], 19456\n" \
"s_add_u32 s66, %[so0], 544768\n" \
"buffer_load_dwordx4 %[wvo], %[rw], s66 offen lds\n" \
"s_waitcnt vmcnt(2)\n" \
"s_add_u32 m0, %[wbase], 20480\n" \
"s_add_u32 s66, %[so0], 573440\n" \
"buffer_load_dwordx4 %[wvo], %[rw], s66 offen lds\n" \
"s_waitcnt vmcnt(2)\n" \
"s_add_u32 m0, %[wbase], 21504\n" \
"s_add_u32 s66, %[so0], 602112\n" \
"buffer_load_dwordx4 %[wvo], %[rw], s66 offen lds\n" \
"s_waitcnt vmcnt(2)\n" \
"s_add_u32 m0, %[wbase], 22528\n" \
"s_add_u32 s66, %[so0], 630784\n" \
"buffer_load_dwordx4 %[wvo], %[rw], s66 offen lds\n" \
"s_waitcnt vmcnt(2)\n" \
"s_add_u32 m0, %[wbase], 23552\n" \
"s_add_u32 s66, %[so0], 659456\n" \
"buffer_load_dwordx4 %[wvo], %[rw], s66 offen lds\n" \
"s_waitcnt vmcnt(2)\n" \
"s_add_u32 m0, %[wbase], 24576\n" \
"s_add_u32 s66, %[so0], 688128\n" \
"buffer_load_dwordx4 %[wvo], %[rw], s66 offen lds\n" \
"s_waitcnt vmcnt(2)\n" \
"s_add_u32 m0, %[wbase], 25600\n" \
"s_add_u32 s66, %[so0], 716800\n" \
"buffer_load_dwordx4 %[wvo], %[rw], s66 offen lds\n" \
"s_waitcnt vmcnt(2)\n" \
"s_add_u32 m0, %[wbase], 26624\n" \
"s_add_u32 s66, %[so0], 745472\n" \
"buffer_load_dwordx4 %[wvo], %[rw], s66 offen lds\n" \
"ds_read_b64_tr_b16 a[64:65], v20 offset:16384\n" \
"ds_read_b64_tr_b16 a[66:67], v20 offset:17408\n" \
"ds_read_b64_tr_b16 a[68:69], v21 offset:16384\n" \
"ds_read_b64_tr_b16 a[70:71], v21 offset:17408\n" \
"ds_read_b64_tr_b16 a[72:73], v22 offset:16384\n" \
"ds_read_b64_tr_b16 a[74:75], v22 offset:17408\n" \
"ds_read_b64_tr_b16 a[76:77], v23 offset:16384\n" \
"ds_read_b64_tr_b16 a[78:79], v23 offset:17408\n" \
"ds_read_b64_tr_b16 a[80:81], v24 offset:16384\n" \
"ds_read_b64_tr_b16 a[82:83], v24 offset:17408\n" \
"ds_read_b64_tr_b16 a[84:85], v25 offset:16384\n" \
"ds_read_b64_tr_b16 a[86:87], v25 offset:17408\n" \
"ds_read_b64_tr_b16 a[88:89], v26 offset:16384\n" \
"ds_read_b64_tr_b16 a[90:91], v26 offset:17408\n" \
"ds_read_b64_tr_b16 a[92:93], v27 offset:16384\n" \
"ds_read_b64_tr_b16 a[94:95], v27 offset:17408\n" \
"s_waitcnt lgkmcnt(0)\n" \
"s_waitcnt vmcnt(2)\n" \
"s_add_u32 m0, %[wbase], 27648\n" \
"s_add_u32 s66, %[so0], 774144\n" \
"buffer_load_dwordx4 %[wvo], %[rw], s66 offen lds\n" \
"s_waitcnt vmcnt(2)\n" \
"s_add_u32 m0, %[wbase], 28672\n" \
"s_add_u32 s66, %[so0], 802816\n" \
"buffer_load_dwordx4 %[wvo], %[rw], s66 offen lds\n" \
"s_waitcnt vmcnt(2)\n" \
"s_add_u32 m0, %[wbase], 29696\n" \
"s_add_u32 s66, %[so0], 831488\n" \
"buffer_load_dwordx4 %[wvo], %[rw], s66 offen lds\n" \
"s_waitcnt vmcnt(2)\n" \
"s_add_u32 m0, %[wbase], 30720\n" \
"s_add_u32 s66, %[so0], 860160\n" \
"buffer_load_dwordx4 %[wvo], %[rw], s66 offen lds\n" \
"s_waitcnt vmcnt(2)\n" \
"s_add_u32 m0, %[wbase], 31744\n" \
"s_add_u32 s66, %[so0], 888832\n" \
"buffer_load_dwordx4 %[wvo], %[rw], s66 offen lds\n" \
"s_waitcnt vmcnt(2)\n" \
"s_add_u32 m0, %[wbase], 0\n" \
"s_add_u32 s66, %[so0], 917504\n" \
"buffer_load_dwordx4 %[wvo], %[rw], s66 offen lds\n" \
"s_waitcnt vmcnt(2)\n" \
"s_add_u32 m0, %[wbase], 1024\n" \
"s_add_u32 s66, %[so0], 946176\n" \
"buffer_load_dwordx4 %[wvo], %[rw], s66 offen lds\n" \
"s_waitcnt vmcnt(2)\n" \
"s_add_u32 m0, %[wbase], 2048\n" \
"s_add_u32 s66, %[so0], 974848\n" \
"buffer_load_dwordx4 %[wvo], %[rw], s66 offen lds\n" \
"ds_read_b64_tr_b16 a[96:97], v20 offset:24576\n" \
"ds_read_b64_tr_b16 a[98:99], v20 offset:25600\n" \
"ds_read_b64_tr_b16 a[100:101], v21 offset:24576\n" \
"ds_read_b64_tr_b16 a[102:103], v21 offset:25600\n" \
"ds_read_b64_tr_b16 a[104:105], v22 offset:24576\n" \
"ds_read_b64_tr_b16 a[106:107], v22 offset:25600\n" \
"ds_read_b64_tr_b16 a[108:109], v23 offset:24576\n" \
"ds_read_b64_tr_b16 a[110:111], v23 offset:25600\n" \
"ds_read_b64_tr_b16 a[112:113], v24 offset:24576\n" \
"ds_read_b64_tr_b16 a[114:115], v24 offset:25600\n" \
"ds_read_b64_tr_b16 a[116:117], v25 offset:24576\n" \
"ds_read_b64_tr_b16 a[118:119], v25 offset:25600\n" \
"ds_read_b64_tr_b16 a[120:121], v26 offset:24576\n" \
"ds_read_b64_tr_b16 a[122:123], v26 offset:25600\n" \
"ds_read_b64_tr_b16 a[124:125], v27 offset:24576\n" \
"ds_read_b64_tr_b16 a[126:127], v27 offset:25600\n" \
"s_waitcnt lgkmcnt(0)\n" \
"s_waitcnt vmcnt(2)\n" \
"s_add_u32 m0, %[wbase], 3072\n" \
"s_add_u32 s66, %[so0], 1003520\n" \
"buffer_load_dwordx4 %[wvo], %[rw], s66 offen lds\n" \
"s_waitcnt vmcnt(2)\n" \
"s_add_u32 m0, %[wbase], 4096\n" \
"s_add_u32 s66, %[so0], 1032192\n" \
"buffer_load_dwordx4 %[wvo], %[rw], s66 offen lds\n" \
"s_waitcnt vmcnt(2)\n" \
"s_add_u32 m0, %[wbase], 5120\n" \
"s_add_u32 s66, %[so0], 1060864\n" \
"buffer_load_dwordx4 %[wvo], %[rw], s66 offen lds\n" \
"s_waitcnt vmcnt(2)\n" \
"s_add_u32 m0, %[wbase], 6144\n" \
"s_add_u32 s66, %[so0], 1089536\n" \
"buffer_load_dwordx4 %[wvo], %[rw], s66 offen lds\n" \
"s_waitcnt vmcnt(2)\n" \
"s_add_u32 m0, %[wbase], 7168\n" \
"s_add_u32 s66, %[so0], 1118208\n" \
"buffer_load_dwordx4 %[wvo], %[rw], s66 offen lds\n" \
"s_waitcnt vmcnt(2)\n" \
"s_add_u32 m0, %[wbase], 8192\n" \
"s_add_u32 s66, %[so0], 1146880\n" \
"buffer_load_dwordx4 %[wvo], %[rw], s66 offen lds\n" \
"s_waitcnt vmcnt(2)\n" \
"s_add_u32 m0, %[wbase], 9216\n" \
"s_add_u32 s66, %[so0], 1175552\n" \
"buffer_load_dwordx4 %[wvo], %[rw], s66 offen lds\n" \
"s_waitcnt vmcnt(2)\n" \
"s_add_u32 m0, %[wbase], 10240\n" \
"s_add_u32 s66, %[so0], 1204224\n" \
"buffer_load_dwordx4 %[wvo], %[rw], s66 offen lds\n" \
"ds_read_b64_tr_b16 a[128:129], v20 offset:0\n" \
"ds_read_b64_tr_b16 a[130:131], v20 offset:1024\n" \
"ds_read_b64_tr_b16 a[132:133], v21 offset:0\n" \
"ds_read_b64_tr_b16 a[134:135], v21 offset:1024\n" \
"ds_read_b64_tr_b16 a[136:137], v22 offset:0\n" \
"ds_read_b64_tr_b16 a[138:139], v22 offset:1024\n" \
"ds_read_b64_tr_b16 a[140:141], v23 offset:0\n" \
"ds_read_b64_tr_b16 a[142:143], v23 offset:1024\n" \
"ds_read_b64_tr_b16 a[144:145], v24 offset:0\n" \
"ds_read_b64_tr_b16 a[146:147], v24 offset:1024\n" \
"ds_read_b64_tr_b16 a[148:149], v25 offset:0\n" \
"ds_read_b64_tr_b16 a[150:151], v25 offset:1024\n" \
"ds_read_b64_tr_b16 a[152:153], v26 offset:0\n" \
"ds_read_b64_tr_b16 a[154:155], v26 offset:1024\n" \
"ds_read_b64_tr_b16 a[156:157], v27 offset:0\n" \
"ds_read_b64_tr_b16 a[158:159], v27 offset:1024\n" \
"s_waitcnt lgkmcnt(0)\n" \
"s_waitcnt vmcnt(2)\n" \
"s_add_u32 m0, %[wbase], 11264\n" \
"s_add_u32 s66, %[so0], 1232896\n" \
"buffer_load_dwordx4 %[wvo], %[rw], s66 offen lds\n" \
"s_waitcnt vmcnt(2)\n" \
"s_add_u32 m0, %[wbase], 12288\n" \
"s_add_u32 s66, %[so0], 1261568\n" \
"buffer_load_dwordx4 %[wvo], %[rw], s66 offen lds\n" \
"s_waitcnt vmcnt(2)\n" \
"s_add_u32 m0, %[wbase], 13312\n" \
"s_add_u32 s66, %[so0], 1290240\n" \
"buffer_load_dwordx4 %[wvo], %[rw], s66 offen lds\n" \
"s_waitcnt vmcnt(2)\n" \
"s_add_u32 m0, %[wbase], 14336\n" \
"s_add_u32 s66, %[so0], 1318912\n" \
"buffer_load_dwordx4 %[wvo], %[rw], s66 offen lds\n" \
"s_waitcnt vmcnt(2)\n" \
"s_add_u32 m0, %[wbase], 15360\n" \
"s_add_u32 s66, %[so0], 1347584\n" \
"buffer_load_dwordx4 %[wvo], %[rw], s66 offen lds\n" \
"s_waitcnt vmcnt(2)\n" \
"s_add_u32 m0, %[wbase], 16384\n" \
"s_add_u32 s66, %[so0], 1376256\n" \
"buffer_load_dwordx4 %[wvo], %[rw], s66 offen lds\n" \
"s_waitcnt vmcnt(2)\n" \
"s_add_u32 m0, %[wbase], 17408\n" \
"s_add_u32 s66, %[so0], 1404928\n" \
"buffer_load_dwordx4 %[wvo], %[rw], s66 offen lds\n" \
"s_waitcnt vmcnt(2)\n" \
"s_add_u32 m0, %[wbase], 18432\n" \
"s_add_u32 s66, %[so0], 1433600\n" \
"buffer_load_dwordx4 %[wvo], %[rw], s66 offen lds\n" \
"ds_read_b64_tr_b16 a[160:161], v20 offset:8192\n" \
"ds_read_b64_tr_b16 a[162:163], v20 offset:9216\n" \
"ds_read_b64_tr_b16 a[164:165], v21 offset:8192\n" \
"ds_read_b64_tr_b16 a[166:167], v21 offset:9216\n" \
"ds_read_b64_tr_b16 a[168:169], v22 offset:8192\n" \
"ds_read_b64_tr_b16 a[170:171], v22 offset:9216\n" \
"ds_read_b64_tr_b16 a[172:173], v23 offset:8192\n" \
"ds_read_b64_tr_b16 a[174:175], v23 offset:9216\n" \
"ds_read_b64_tr_b16 a[176:177], v24 offset:8192\n" \
"ds_read_b64_tr_b16 a[178:179], v24 offset:9216\n" \
"ds_read_b64_tr_b16 a[180:181], v25 offset:8192\n" \
"ds_read_b64_tr_b16 a[182:183], v25 offset:9216\n" \
"ds_read_b64_tr_b16 a[184:185], v26 offset:8192\n" \
"ds_read_b64_tr_b16 a[186:187], v26 offset:9216\n" \
"ds_read_b64_tr_b16 a[188:189], v27 offset:8192\n" \
"ds_read_b64_tr_b16 a[190:191], v27 offset:9216\n" \
"s_waitcnt lgkmcnt(0)\n" \
"s_waitcnt vmcnt(2)\n" \
"s_add_u32 m0, %[wbase], 19456\n" \
"s_add_u32 s66, %[so0], 1462272\n" \
"buffer_load_dwordx4 %[wvo], %[rw], s66 offen lds\n" \
"s_waitcnt vmcnt(2)\n" \
"s_add_u32 m0, %[wbase], 20480\n" \
"s_add_u32 s66, %[so0], 1490944\n" \
"buffer_load_dwordx4 %[wvo], %[rw], s66 offen lds\n" \
"s_waitcnt vmcnt(2)\n" \
"s_add_u32 m0, %[wbase], 21504\n" \
"s_add_u32 s66, %[so0], 1519616\n" \
"buffer_load_dwordx4 %[wvo], %[rw], s66 offen lds\n" \
"s_waitcnt vmcnt(2)\n" \
"s_add_u32 m0, %[wbase], 22528\n" \
"s_add_u32 s66, %[so0], 1548288\n" \
"buffer_load_dwordx4 %[wvo], %[rw], s66 offen lds\n" \
"s_waitcnt vmcnt(2)\n" \
"s_add_u32 m0, %[wbase], 23552\n" \
"s_add_u32 s66, %[so0], 1576960\n" \
"buffer_load_dwordx4 %[wvo], %[rw], s66 offen lds\n" \
"s_waitcnt vmcnt(2)\n" \
"s_add_u32 m0, %[wbase], 24576\n" \
"s_add_u32 s66, %[so0], 1605632\n" \
"buffer_load_dwordx4 %[wvo], %[rw], s66 offen lds\n" \
"s_waitcnt vmcnt(2)\n" \
"s_add_u32 m0, %[wbase], 25600\n" \
"s_add_u32 s66, %[so0], 1634304\n" \
"buffer_load_dwordx4 %[wvo], %[rw], s66 offen lds\n" \
"s_waitcnt vmcnt(2)\n" \
"s_add_u32 m0, %[wbase], 26624\n" \
"s_add_u32 s66, %[so0], 1662976\n" \
"buffer_load_dwordx4 %[wvo], %[rw], s66 offen lds\n" \
"ds_read_b64_tr_b16 a[192:193], v20 offset:16384\n" \
"ds_read_b64_tr_b16 a[194:195], v20 offset:17408\n" \
"ds_read_b64_tr_b16 a[196:197], v21 offset:16384\n" \
"ds_read_b64_tr_b16 a[198:199], v21 offset:17408\n" \
"ds_read_b64_tr_b16 a[200:201], v22 offset:16384\n" \
"ds_read_b64_tr_b16 a[202:203], v22 offset:17408\n" \
"ds_read_b64_tr_b16 a[204:205], v23 offset:16384\n" \
"ds_read_b64_tr_b16 a[206:207], v23 offset:17408\n" \
"ds_read_b64_tr_b16 a[208:209], v24 offset:16384\n" \
"ds_read_b64_tr_b16 a[210:211], v24 offset:17408\n" \
"ds_read_b64_tr_b16 a[212:213], v25 offset:16384\n" \
"ds_read_b64_tr_b16 a[214:215], v25 offset:17408\n" \
"ds_read_b64_tr_b16 a[216:217], v26 offset:16384\n" \
"ds_read_b64_tr_b16 a[218:219], v26 offset:17408\n" \
"ds_read_b64_tr_b16 a[220:221], v27 offset:16384\n" \
"ds_read_b64_tr_b16 a[222:223], v27 offset:17408\n" \
"s_waitcnt lgkmcnt(0)\n" \
"s_waitcnt vmcnt(2)\n" \
"s_add_u32 m0, %[wbase], 27648\n" \
"s_add_u32 s66, %[so0], 1691648\n" \
"buffer_load_dwordx4 %[wvo], %[rw], s66 offen lds\n" \
"s_waitcnt vmcnt(2)\n" \
"s_add_u32 m0, %[wbase], 28672\n" \
"s_add_u32 s66, %[so0], 1720320\n" \
"buffer_load_dwordx4 %[wvo], %[rw], s66 offen lds\n" \
"s_waitcnt vmcnt(2)\n" \
"s_add_u32 m0, %[wbase], 29696\n" \
"s_add_u32 s66, %[so0], 1748992\n" \
"buffer_load_dwordx4 %[wvo], %[rw], s66 offen lds\n" \
"s_waitcnt vmcnt(2)\n" \
"s_add_u32 m0, %[wbase], 30720\n" \
"s_add_u32 s66, %[so0], 1777664\n" \
"buffer_load_dwordx4 %[wvo], %[rw], s66 offen lds\n" \
"s_waitcnt vmcnt(2)\n" \
"s_add_u32 m0, %[wbase], 31744\n" \
"s_add_u32 s66, %[so0], 1806336\n" \
"buffer_load_dwordx4 %[wvo], %[rw], s66 offen lds\n" \
"s_waitcnt vmcnt(0)\n" \
"ds_read_b64_tr_b16 a[224:225], v20 offset:24576\n" \
"ds_read_b64_tr_b16 a[226:227], v20 offset:25600\n" \
"ds_read_b64_tr_b16 a[228:229], v21 offset:24576\n" \
"ds_read_b64_tr_b16 a[230:231], v21 offset:25600\n" \
"ds_read_b64_tr_b16 a[232:233], v22 offset:24576\n" \
"ds_read_b64_tr_b16 a[234:235], v22 offset:25600\n" \
"ds_read_b64_tr_b16 a[236:237], v23 offset:24576\n" \
"ds_read_b64_tr_b16 a[238:239], v23 offset:25600\n" \
"ds_read_b64_tr_b16 a[240:241], v24 offset:24576\n" \
"ds_read_b64_tr_b16 a[242:243], v24 offset:25600\n" \
"ds_read_b64_tr_b16 a[244:245], v25 offset:24576\n" \
"ds_read_b64_tr_b16 a[246:247], v25 offset:25600\n" \
"ds_read_b64_tr_b16 a[248:249], v26 offset:24576\n" \
"ds_read_b64_tr_b16 a[250:251], v26 offset:25600\n" \
"ds_read_b64_tr_b16 a[252:253], v27 offset:24576\n" \
"ds_read_b64_tr_b16 a[254:255], v27 offset:25600\n" \
"s_waitcnt lgkmcnt(0)\n" \
"v_xor_b32 v212, 8192, %[xr]\n" \
"v_xor_b32 v213, 16384, %[xr]\n" \
"v_xor_b32 v214, 24576, %[xr]\n" \
"s_barrier\n" \
"s_mov_b32 s76, 0xbdd2d3e8\n" \
"s_mov_b32 s77, 0xbdd2d3e8\n" \
"v_mov_b32 v180, 0xc0135761\n" \
"v_mov_b32 v181, 0xc0135761\n" \
"s_mov_b32 s78, 0x3727c5ac\n" \
"v_mov_b32 v156, 0x3f803f80\n" \
"v_mov_b32 v157, 0x3f803f80\n" \
"v_mov_b32 v158, 0x3f803f80\n" \
"v_mov_b32 v159, 0x3f803f80\n" \
"s_nop 1\n" \
"v_mfma_f32_16x16x32_bf16 v[100:103], a[0:3], v[156:159], 0\n" \
"v_mfma_f32_16x16x32_bf16 v[104:107], a[4:7], v[156:159], 0\n" \
"v_mfma_f32_16x16x32_bf16 v[108:111], a[8:11], v[156:159], 0\n" \
"v_mfma_f32_16x16x32_bf16 v[112:115], a[12:15], v[156:159], 0\n" \
"v_mfma_f32_16x16x32_bf16 v[116:119], a[16:19], v[156:159], 0\n" \
"v_mfma_f32_16x16x32_bf16 v[120:123], a[20:23], v[156:159], 0\n" \
"v_mfma_f32_16x16x32_bf16 v[124:127], a[24:27], v[156:159], 0\n" \
"v_mfma_f32_16x16x32_bf16 v[128:131], a[28:31], v[156:159], 0\n" \
"v_mfma_f32_16x16x32_bf16 v[100:103], a[32:35], v[156:159], v[100:103]\n" \
"v_mfma_f32_16x16x32_bf16 v[104:107], a[36:39], v[156:159], v[104:107]\n" \
"v_mfma_f32_16x16x32_bf16 v[108:111], a[40:43], v[156:159], v[108:111]\n" \
"v_mfma_f32_16x16x32_bf16 v[112:115], a[44:47], v[156:159], v[112:115]\n" \
"v_mfma_f32_16x16x32_bf16 v[116:119], a[48:51], v[156:159], v[116:119]\n" \
"v_mfma_f32_16x16x32_bf16 v[120:123], a[52:55], v[156:159], v[120:123]\n" \
"v_mfma_f32_16x16x32_bf16 v[124:127], a[56:59], v[156:159], v[124:127]\n" \
"v_mfma_f32_16x16x32_bf16 v[128:131], a[60:63], v[156:159], v[128:131]\n" \
"v_mfma_f32_16x16x32_bf16 v[100:103], a[64:67], v[156:159], v[100:103]\n" \
"v_mfma_f32_16x16x32_bf16 v[104:107], a[68:71], v[156:159], v[104:107]\n" \
"v_mfma_f32_16x16x32_bf16 v[108:111], a[72:75], v[156:159], v[108:111]\n" \
"v_mfma_f32_16x16x32_bf16 v[112:115], a[76:79], v[156:159], v[112:115]\n" \
"v_mfma_f32_16x16x32_bf16 v[116:119], a[80:83], v[156:159], v[116:119]\n" \
"v_mfma_f32_16x16x32_bf16 v[120:123], a[84:87], v[156:159], v[120:123]\n" \
"v_mfma_f32_16x16x32_bf16 v[124:127], a[88:91], v[156:159], v[124:127]\n" \
"v_mfma_f32_16x16x32_bf16 v[128:131], a[92:95], v[156:159], v[128:131]\n" \
"v_mfma_f32_16x16x32_bf16 v[100:103], a[96:99], v[156:159], v[100:103]\n" \
"v_mfma_f32_16x16x32_bf16 v[104:107], a[100:103], v[156:159], v[104:107]\n" \
"v_mfma_f32_16x16x32_bf16 v[108:111], a[104:107], v[156:159], v[108:111]\n" \
"v_mfma_f32_16x16x32_bf16 v[112:115], a[108:111], v[156:159], v[112:115]\n" \
"v_mfma_f32_16x16x32_bf16 v[116:119], a[112:115], v[156:159], v[116:119]\n" \
"v_mfma_f32_16x16x32_bf16 v[120:123], a[116:119], v[156:159], v[120:123]\n" \
"v_mfma_f32_16x16x32_bf16 v[124:127], a[120:123], v[156:159], v[124:127]\n" \
"v_mfma_f32_16x16x32_bf16 v[128:131], a[124:127], v[156:159], v[128:131]\n" \
"v_mfma_f32_16x16x32_bf16 v[100:103], a[128:131], v[156:159], v[100:103]\n" \
"v_mfma_f32_16x16x32_bf16 v[104:107], a[132:135], v[156:159], v[104:107]\n" \
"v_mfma_f32_16x16x32_bf16 v[108:111], a[136:139], v[156:159], v[108:111]\n" \
"v_mfma_f32_16x16x32_bf16 v[112:115], a[140:143], v[156:159], v[112:115]\n" \
"v_mfma_f32_16x16x32_bf16 v[116:119], a[144:147], v[156:159], v[116:119]\n" \
"v_mfma_f32_16x16x32_bf16 v[120:123], a[148:151], v[156:159], v[120:123]\n" \
"v_mfma_f32_16x16x32_bf16 v[124:127], a[152:155], v[156:159], v[124:127]\n" \
"v_mfma_f32_16x16x32_bf16 v[128:131], a[156:159], v[156:159], v[128:131]\n" \
"v_mfma_f32_16x16x32_bf16 v[100:103], a[160:163], v[156:159], v[100:103]\n" \
"v_mfma_f32_16x16x32_bf16 v[104:107], a[164:167], v[156:159], v[104:107]\n" \
"v_mfma_f32_16x16x32_bf16 v[108:111], a[168:171], v[156:159], v[108:111]\n" \
"v_mfma_f32_16x16x32_bf16 v[112:115], a[172:175], v[156:159], v[112:115]\n" \
"v_mfma_f32_16x16x32_bf16 v[116:119], a[176:179], v[156:159], v[116:119]\n" \
"v_mfma_f32_16x16x32_bf16 v[120:123], a[180:183], v[156:159], v[120:123]\n" \
"v_mfma_f32_16x16x32_bf16 v[124:127], a[184:187], v[156:159], v[124:127]\n" \
"v_mfma_f32_16x16x32_bf16 v[128:131], a[188:191], v[156:159], v[128:131]\n" \
"v_mfma_f32_16x16x32_bf16 v[100:103], a[192:195], v[156:159], v[100:103]\n" \
"v_mfma_f32_16x16x32_bf16 v[104:107], a[196:199], v[156:159], v[104:107]\n" \
"v_mfma_f32_16x16x32_bf16 v[108:111], a[200:203], v[156:159], v[108:111]\n" \
"v_mfma_f32_16x16x32_bf16 v[112:115], a[204:207], v[156:159], v[112:115]\n" \
"v_mfma_f32_16x16x32_bf16 v[116:119], a[208:211], v[156:159], v[116:119]\n" \
"v_mfma_f32_16x16x32_bf16 v[120:123], a[212:215], v[156:159], v[120:123]\n" \
"v_mfma_f32_16x16x32_bf16 v[124:127], a[216:219], v[156:159], v[124:127]\n" \
"v_mfma_f32_16x16x32_bf16 v[128:131], a[220:223], v[156:159], v[128:131]\n" \
"v_mfma_f32_16x16x32_bf16 v[100:103], a[224:227], v[156:159], v[100:103]\n" \
"v_mfma_f32_16x16x32_bf16 v[104:107], a[228:231], v[156:159], v[104:107]\n" \
"v_mfma_f32_16x16x32_bf16 v[108:111], a[232:235], v[156:159], v[108:111]\n" \
"v_mfma_f32_16x16x32_bf16 v[112:115], a[236:239], v[156:159], v[112:115]\n" \
"v_mfma_f32_16x16x32_bf16 v[116:119], a[240:243], v[156:159], v[116:119]\n" \
"v_mfma_f32_16x16x32_bf16 v[120:123], a[244:247], v[156:159], v[120:123]\n" \
"v_mfma_f32_16x16x32_bf16 v[124:127], a[248:251], v[156:159], v[124:127]\n" \
"v_mfma_f32_16x16x32_bf16 v[128:131], a[252:255], v[156:159], v[128:131]\n" \
"s_nop 2\n" \
"ds_write_b128 v212, v[108:111] offset:34816\n" \
"ds_write_b128 v212, v[112:115] offset:35840\n" \
"ds_write_b128 v213, v[116:119] offset:36864\n" \
"ds_write_b128 v213, v[120:123] offset:37888\n" \
"ds_write_b128 v214, v[124:127] offset:38912\n" \
"ds_write_b128 v214, v[128:131] offset:39936\n" \
"s_waitcnt lgkmcnt(0)\n" \
"s_barrier\n" \
"ds_read_b128 v[132:135], %[xr] offset:34816\n" \
"ds_read_b128 v[136:139], %[xr] offset:35840\n" \
"ds_read_b128 v[140:143], %[xr] offset:36864\n" \
"ds_read_b128 v[144:147], %[xr] offset:37888\n" \
"ds_read_b128 v[148:151], %[xr] offset:38912\n" \
"ds_read_b128 v[152:155], %[xr] offset:39936\n" \
"s_waitcnt lgkmcnt(5)\n" \
"v_add_f32 v164, v100, v132\n" \
"s_waitcnt lgkmcnt(1)\n" \
"v_add_f32 v140, v140, v148\n" \
"v_add_f32 v164, v164, v140\n" \
"v_add_f32 v165, v101, v133\n" \
"v_add_f32 v141, v141, v149\n" \
"v_add_f32 v165, v165, v141\n" \
"v_add_f32 v166, v102, v134\n" \
"v_add_f32 v142, v142, v150\n" \
"v_add_f32 v166, v166, v142\n" \
"v_add_f32 v167, v103, v135\n" \
"v_add_f32 v143, v143, v151\n" \
"v_add_f32 v167, v167, v143\n" \
"v_add_f32 v168, v104, v136\n" \
"s_waitcnt lgkmcnt(0)\n" \
"v_add_f32 v144, v144, v152\n" \
"v_add_f32 v168, v168, v144\n" \
"v_add_f32 v169, v105, v137\n" \
"v_add_f32 v145, v145, v153\n" \
"v_add_f32 v169, v169, v145\n" \
"v_add_f32 v170, v106, v138\n" \
"v_add_f32 v146, v146, v154\n" \
"v_add_f32 v170, v170, v146\n" \
"v_add_f32 v171, v107, v139\n" \
"v_add_f32 v147, v147, v155\n" \
"v_add_f32 v171, v171, v147\n" \
"s_add_u32 s81, %[dst], 11698176\n" \
"v_mov_b32 v220, -1\n" \
"v_mov_b32 v221, -1\n" \
"v_mov_b32 v222, -1\n" \
"v_mov_b32 v223, -1\n" \
"s_mov_b32 s80, 128\n" \
"s_mov_b32 s65, 25\n" \
"Lw3_poi_%=:\n" \
"buffer_store_dwordx4 v[220:223], %[so], %[ws], s81 offen sc1\n" \
"s_waitcnt vmcnt(2)\n" \
"s_add_u32 s81, s81, 114688\n" \
"s_sub_u32 s65, s65, 1\n" \
"s_cbranch_scc0 Lw3_poi_%=\n" \
"s_waitcnt vmcnt(0)\n" \
"s_mov_b32 s64, 262144\n" \
"s_mov_b32 s65, 0\n" \
"s_mov_b32 s74, 1\n" \
"s_mov_b32 s69, 1073741824\n" \
"s_mov_b32 s67, %[src]\n" \
"s_mov_b32 s68, %[dst]\n" \
"s_mov_b32 s70, %[ox0]\n" \
"v_mov_b32 v202, 0\n" \
"v_mov_b32 v203, 0\n" \
"Lw3_top0_%=:\n" \
"v_cmp_gt_u32 vcc, s74, v202\n" \
"s_mov_b32 s72, s67\n" \
"s_mov_b32 s75, s70\n" \
"s_add_u32 s73, s67, 4096\n" \
"s_cbranch_vccnz Lw3_sgo0_%=\n" \
"Lw3_go0_%=:\n" \
"buffer_load_dwordx4 v[20:23], %[lo], %[ws], s72 offen offset:0 sc1\n" \
"buffer_load_dwordx4 v[24:27], %[lo], %[ws], s72 offen offset:1024 sc1\n" \
"buffer_load_dwordx2 v[84:85], %[sl], %[ws], s75 offen offset:0 sc1\n" \
"buffer_load_dwordx2 v[86:87], %[sl], %[ws], s75 offen offset:512 sc1\n" \
"buffer_load_dwordx4 v[28:31], %[lo], %[ws], s72 offen offset:2048 sc1\n" \
"buffer_load_dwordx4 v[32:35], %[lo], %[ws], s72 offen offset:3072 sc1\n" \
"buffer_load_dwordx4 v[36:39], %[lo], %[ws], s73 offen offset:0 sc1\n" \
"buffer_load_dwordx4 v[40:43], %[lo], %[ws], s73 offen offset:1024 sc1\n" \
"buffer_load_dwordx4 v[44:47], %[lo], %[ws], s73 offen offset:2048 sc1\n" \
"buffer_load_dwordx4 v[48:51], %[lo], %[ws], s73 offen offset:3072 sc1\n" \
"s_waitcnt vmcnt(10)\n" \
"buffer_store_dword v203, %[fo], %[ws], s69 offen sc1\n" \
"s_mov_b32 s69, 1073741824\n" \
"Lw3_body0_%=:\n" \
"s_waitcnt vmcnt(10)\n" \
"v_mfma_f32_16x16x32_bf16 v[108:111], a[8:11], v[20:23], 0\n" \
"s_nop 2\n" \
"v_mfma_f32_16x16x32_bf16 v[112:115], a[12:15], v[20:23], 0\n" \
"s_nop 2\n" \
"v_mfma_f32_16x16x32_bf16 v[116:119], a[16:19], v[20:23], 0\n" \
"s_add_u32 s74, s74, 1\n" \
"s_add_u32 s67, s67, 32768\n" \
"s_nop 0\n" \
"v_mfma_f32_16x16x32_bf16 v[120:123], a[20:23], v[20:23], 0\n" \
"s_add_u32 s65, s65, 1\n" \
"s_add_u32 s70, s70, 4096\n" \
"s_nop 0\n" \
"v_mfma_f32_16x16x32_bf16 v[124:127], a[24:27], v[20:23], 0\n" \
"s_cmp_gt_u32 s65, 104\n" \
"s_cselect_b32 s79, 1073741824, 0\n" \
"s_cselect_b32 s69, 0, s69\n" \
"v_mfma_f32_16x16x32_bf16 v[128:131], a[28:31], v[20:23], 0\n" \
"s_waitcnt vmcnt(9)\n" \
"s_addc_u32 s66, s65, -1\n" \
"s_nop 0\n" \
"v_mfma_f32_16x16x32_bf16 v[108:111], a[40:43], v[24:27], v[108:111]\n" \
"s_nop 0\n" \
"buffer_load_dword v202, %[fvo], %[ws], 0 offen sc1\n" \
"v_mov_b32 v203, s66\n" \
"v_mfma_f32_16x16x32_bf16 v[112:115], a[44:47], v[24:27], v[112:115]\n" \
"s_waitcnt vmcnt(8)\n" \
"v_max3_u32 v215, v84, v85, v86\n" \
"v_cmp_u_f32 vcc, v215, v87\n" \
"v_mfma_f32_16x16x32_bf16 v[116:119], a[48:51], v[24:27], v[116:119]\n" \
"s_cbranch_vccnz Lw3_sk00_%=\n" \
"Lw3_k00_%=:\n" \
"v_mfma_f32_16x16x32_bf16 v[120:123], a[52:55], v[24:27], v[120:123]\n" \
"s_nop 2\n" \
"v_mfma_f32_16x16x32_bf16 v[124:127], a[56:59], v[24:27], v[124:127]\n" \
"s_nop 0\n" \
"v_add_f32 v84, v84, v86\n" \
"v_add_f32 v85, v85, v87\n" \
"v_mfma_f32_16x16x32_bf16 v[128:131], a[60:63], v[24:27], v[128:131]\n" \
"s_waitcnt vmcnt(7)\n" \
"v_permlane32_swap_b32 v84, v85\n" \
"v_mfma_f32_16x16x32_bf16 v[108:111], a[72:75], v[28:31], v[108:111]\n" \
"s_nop 0\n" \
"v_add_f32 v86, v84, v85\n" \
"v_add_f32 v84, v84, v85\n" \
"v_mfma_f32_16x16x32_bf16 v[112:115], a[76:79], v[28:31], v[112:115]\n" \
"s_nop 0\n" \
"v_permlane16_swap_b32 v84, v86\n" \
"v_mfma_f32_16x16x32_bf16 v[116:119], a[80:83], v[28:31], v[116:119]\n" \
"s_nop 0\n" \
"v_add_f32 v84, v84, v86\n" \
"v_mfma_f32_16x16x32_bf16 v[120:123], a[84:87], v[28:31], v[120:123]\n" \
"s_nop 0\n" \
"ds_write_b32 %[sw], v84 offset:0\n" \
"v_mfma_f32_16x16x32_bf16 v[124:127], a[88:91], v[28:31], v[124:127]\n" \
"s_nop 2\n" \
"v_mfma_f32_16x16x32_bf16 v[128:131], a[92:95], v[28:31], v[128:131]\n" \
"s_waitcnt vmcnt(6)\n" \
"s_nop 1\n" \
"v_mfma_f32_16x16x32_bf16 v[108:111], a[104:107], v[32:35], v[108:111]\n" \
"s_waitcnt lgkmcnt(0)\n" \
"s_barrier\n" \
"v_mfma_f32_16x16x32_bf16 v[112:115], a[108:111], v[32:35], v[112:115]\n" \
"s_nop 0\n" \
"ds_read_b128 v[190:193], %[sr] offset:0\n" \
"v_mfma_f32_16x16x32_bf16 v[116:119], a[112:115], v[32:35], v[116:119]\n" \
"s_nop 0\n" \
"ds_read_b128 v[194:197], %[sr] offset:16\n" \
"v_mfma_f32_16x16x32_bf16 v[120:123], a[116:119], v[32:35], v[120:123]\n" \
"s_nop 2\n" \
"v_mfma_f32_16x16x32_bf16 v[124:127], a[120:123], v[32:35], v[124:127]\n" \
"s_nop 2\n" \
"v_mfma_f32_16x16x32_bf16 v[128:131], a[124:127], v[32:35], v[128:131]\n" \
"s_waitcnt vmcnt(5)\n" \
"s_nop 1\n" \
"v_mfma_f32_16x16x32_bf16 v[108:111], a[136:139], v[36:39], v[108:111]\n" \
"s_waitcnt lgkmcnt(1)\n" \
"v_add_f32 v190, v190, v191\n" \
"v_add_f32 v192, v192, v193\n" \
"v_mfma_f32_16x16x32_bf16 v[112:115], a[140:143], v[36:39], v[112:115]\n" \
"s_waitcnt lgkmcnt(0)\n" \
"v_add_f32 v194, v194, v195\n" \
"v_add_f32 v196, v196, v197\n" \
"v_mfma_f32_16x16x32_bf16 v[116:119], a[144:147], v[36:39], v[116:119]\n" \
"s_nop 0\n" \
"v_add_f32 v190, v190, v192\n" \
"v_add_f32 v194, v194, v196\n" \
"v_mfma_f32_16x16x32_bf16 v[120:123], a[148:151], v[36:39], v[120:123]\n" \
"s_nop 0\n" \
"v_mul_f32 v190, 0xba800000, v190\n" \
"v_mul_f32 v194, 0x3a800000, v194\n" \
"v_mfma_f32_16x16x32_bf16 v[124:127], a[152:155], v[36:39], v[124:127]\n" \
"s_nop 0\n" \
"v_mul_f32 v156, v190, v164\n" \
"v_mul_f32 v157, v190, v165\n" \
"v_mfma_f32_16x16x32_bf16 v[128:131], a[156:159], v[36:39], v[128:131]\n" \
"s_waitcnt vmcnt(4)\n" \
"v_mul_f32 v158, v190, v166\n" \
"v_mul_f32 v159, v190, v167\n" \
"v_mfma_f32_16x16x32_bf16 v[108:111], a[168:171], v[40:43], v[108:111]\n" \
"s_waitcnt vmcnt(3)\n" \
"v_mul_f32 v160, v190, v168\n" \
"v_mul_f32 v161, v190, v169\n" \
"v_mfma_f32_16x16x32_bf16 v[108:111], a[200:203], v[44:47], v[108:111]\n" \
"s_waitcnt vmcnt(2)\n" \
"v_mul_f32 v162, v190, v170\n" \
"v_mul_f32 v163, v190, v171\n" \
"v_mfma_f32_16x16x32_bf16 v[108:111], a[232:235], v[48:51], v[108:111]\n" \
"s_nop 0\n" \
"v_fma_f32 v194, -v190, v190, v194\n" \
"v_max_f32 v194, 0, v194\n" \
"v_mfma_f32_16x16x32_bf16 v[112:115], a[172:175], v[40:43], v[112:115]\n" \
"s_nop 0\n" \
"v_add_f32 v192, 0x3727c5ac, v194\n" \
"v_rsq_f32 v192, v192\n" \
"v_mfma_f32_16x16x32_bf16 v[112:115], a[204:207], v[44:47], v[112:115]\n" \
"s_nop 0\n" \
"ds_write_b128 v212, v[108:111] offset:2048\n" \
"v_mfma_f32_16x16x32_bf16 v[112:115], a[236:239], v[48:51], v[112:115]\n" \
"s_nop 0\n" \
"v_cmp_u_f32 vcc, v108, v108\n" \
"v_mfma_f32_16x16x32_bf16 v[116:119], a[176:179], v[40:43], v[116:119]\n" \
"s_cbranch_vccnz Lw3_sn0_%=\n" \
"Lw3_n0_%=:\n" \
"v_mfma_f32_16x16x32_bf16 v[116:119], a[208:211], v[44:47], v[116:119]\n" \
"s_nop 0\n" \
"s_nop 1\n" \
"ds_write_b128 v212, v[112:115] offset:3072\n" \
"v_mfma_f32_16x16x32_bf16 v[116:119], a[240:243], v[48:51], v[116:119]\n" \
"s_nop 2\n" \
"v_mfma_f32_16x16x32_bf16 v[120:123], a[180:183], v[40:43], v[120:123]\n" \
"s_nop 2\n" \
"v_mfma_f32_16x16x32_bf16 v[120:123], a[212:215], v[44:47], v[120:123]\n" \
"s_nop 0\n" \
"ds_write_b128 v213, v[116:119] offset:4096\n" \
"v_mfma_f32_16x16x32_bf16 v[120:123], a[244:247], v[48:51], v[120:123]\n" \
"s_waitcnt vmcnt(0)\n" \
"v_cmp_gt_u32 vcc, s74, v202\n" \
"v_mfma_f32_16x16x32_bf16 v[124:127], a[184:187], v[40:43], v[124:127]\n" \
"s_nop 0\n" \
"buffer_load_dword v202, %[fvo], %[ws], 0 offen sc1\n" \
"v_mfma_f32_16x16x32_bf16 v[124:127], a[216:219], v[44:47], v[124:127]\n" \
"s_cmp_eq_u64 vcc, 0\n" \
"s_nop 0\n" \
"ds_write_b128 v213, v[120:123] offset:5120\n" \
"v_mfma_f32_16x16x32_bf16 v[124:127], a[248:251], v[48:51], v[124:127]\n" \
"s_cselect_b32 s72, s67, 1073741824\n" \
"buffer_load_dwordx4 v[52:55], %[lo], %[ws], s72 offen offset:0 sc1\n" \
"v_mfma_f32_16x16x32_bf16 v[128:131], a[188:191], v[40:43], v[128:131]\n" \
"s_cselect_b32 s75, s70, 1073741824\n" \
"s_add_u32 s73, s72, 4096\n" \
"s_nop 0\n" \
"v_mfma_f32_16x16x32_bf16 v[128:131], a[220:223], v[44:47], v[128:131]\n" \
"s_nop 0\n" \
"ds_write_b128 v214, v[124:127] offset:6144\n" \
"v_mfma_f32_16x16x32_bf16 v[128:131], a[252:255], v[48:51], v[128:131]\n" \
"s_waitcnt lgkmcnt(1)\n" \
"s_barrier\n" \
"v_mfma_f32_16x16x32_bf16 v[100:103], a[0:3], v[20:23], v[156:159]\n" \
"s_nop 0\n" \
"ds_read_b128 v[132:135], %[xr] offset:2048\n" \
"buffer_load_dwordx4 v[56:59], %[lo], %[ws], s72 offen offset:1024 sc1\n" \
"v_mfma_f32_16x16x32_bf16 v[104:107], a[4:7], v[20:23], v[160:163]\n" \
"s_nop 0\n" \
"ds_write_b128 v214, v[128:131] offset:7168\n" \
"v_mfma_f32_16x16x32_bf16 v[100:103], a[32:35], v[24:27], v[100:103]\n" \
"s_nop 0\n" \
"ds_read_b128 v[136:139], %[xr] offset:3072\n" \
"v_mfma_f32_16x16x32_bf16 v[104:107], a[36:39], v[24:27], v[104:107]\n" \
"s_nop 0\n" \
"ds_read_b128 v[140:143], %[xr] offset:4096\n" \
"buffer_store_dword v203, %[fo], %[ws], s69 offen sc1\n" \
"v_mfma_f32_16x16x32_bf16 v[100:103], a[64:67], v[28:31], v[100:103]\n" \
"s_nop 0\n" \
"ds_read_b128 v[144:147], %[xr] offset:5120\n" \
"buffer_load_dwordx2 v[92:93], %[sl], %[ws], s75 offen offset:0 sc1\n" \
"v_mfma_f32_16x16x32_bf16 v[104:107], a[68:71], v[28:31], v[104:107]\n" \
"s_waitcnt lgkmcnt(3)\n" \
"s_barrier\n" \
"v_mfma_f32_16x16x32_bf16 v[100:103], a[96:99], v[32:35], v[100:103]\n" \
"s_nop 0\n" \
"ds_read_b128 v[148:151], %[xr] offset:6144\n" \
"buffer_load_dwordx2 v[94:95], %[sl], %[ws], s75 offen offset:512 sc1\n" \
"v_mfma_f32_16x16x32_bf16 v[104:107], a[100:103], v[32:35], v[104:107]\n" \
"s_nop 0\n" \
"ds_read_b128 v[152:155], %[xr] offset:7168\n" \
"v_mfma_f32_16x16x32_bf16 v[100:103], a[128:131], v[36:39], v[100:103]\n" \
"s_waitcnt lgkmcnt(3)\n" \
"buffer_load_dwordx4 v[60:63], %[lo], %[ws], s72 offen offset:2048 sc1\n" \
"v_add_f32 v132, v132, v140\n" \
"v_add_f32 v133, v133, v141\n" \
"v_mfma_f32_16x16x32_bf16 v[104:107], a[132:135], v[36:39], v[104:107]\n" \
"s_nop 0\n" \
"v_add_f32 v134, v134, v142\n" \
"v_add_f32 v135, v135, v143\n" \
"v_mfma_f32_16x16x32_bf16 v[100:103], a[160:163], v[40:43], v[100:103]\n" \
"s_waitcnt lgkmcnt(2)\n" \
"v_add_f32 v136, v136, v144\n" \
"v_add_f32 v137, v137, v145\n" \
"v_mfma_f32_16x16x32_bf16 v[104:107], a[164:167], v[40:43], v[104:107]\n" \
"s_nop 0\n" \
"v_add_f32 v138, v138, v146\n" \
"v_add_f32 v139, v139, v147\n" \
"v_mfma_f32_16x16x32_bf16 v[100:103], a[192:195], v[44:47], v[100:103]\n" \
"s_waitcnt lgkmcnt(1)\n" \
"buffer_load_dwordx4 v[64:67], %[lo], %[ws], s72 offen offset:3072 sc1\n" \
"v_add_f32 v132, v132, v148\n" \
"v_add_f32 v133, v133, v149\n" \
"v_mfma_f32_16x16x32_bf16 v[104:107], a[196:199], v[44:47], v[104:107]\n" \
"s_nop 0\n" \
"v_add_f32 v134, v134, v150\n" \
"v_add_f32 v135, v135, v151\n" \
"v_mfma_f32_16x16x32_bf16 v[100:103], a[224:227], v[48:51], v[100:103]\n" \
"s_waitcnt lgkmcnt(0)\n" \
"v_add_f32 v136, v136, v152\n" \
"v_add_f32 v137, v137, v153\n" \
"v_mfma_f32_16x16x32_bf16 v[104:107], a[228:231], v[48:51], v[104:107]\n" \
"s_nop 0\n" \
"v_add_f32 v138, v138, v154\n" \
"v_add_f32 v139, v139, v155\n" \
"v_mov_b32 v203, s65\n" \
"s_mov_b32 s69, s79\n" \
"buffer_load_dwordx4 v[68:71], %[lo], %[ws], s73 offen offset:0 sc1\n" \
"v_pk_add_f32 v[100:101], v[100:101], v[132:133]\n" \
"v_pk_add_f32 v[102:103], v[102:103], v[134:135]\n" \
"v_pk_add_f32 v[104:105], v[104:105], v[136:137]\n" \
"v_pk_add_f32 v[106:107], v[106:107], v[138:139]\n" \
"v_pk_mul_f32 v[100:101], v[100:101], v[192:193] op_sel_hi:[1,0]\n" \
"v_pk_mul_f32 v[102:103], v[102:103], v[192:193] op_sel_hi:[1,0]\n" \
"v_pk_mul_f32 v[104:105], v[104:105], v[192:193] op_sel_hi:[1,0]\n" \
"v_pk_mul_f32 v[106:107], v[106:107], v[192:193] op_sel_hi:[1,0]\n" \
"v_pk_mul_f32 v[172:173], v[100:101], v[100:101]\n" \
"v_pk_mul_f32 v[174:175], v[102:103], v[102:103]\n" \
"v_pk_mul_f32 v[176:177], v[104:105], v[104:105]\n" \
"v_pk_mul_f32 v[178:179], v[106:107], v[106:107]\n" \
"buffer_load_dwordx4 v[72:75], %[lo], %[ws], s73 offen offset:1024 sc1\n" \
"v_pk_fma_f32 v[172:173], v[172:173], s[76:77], v[180:181]\n" \
"v_pk_fma_f32 v[174:175], v[174:175], s[76:77], v[180:181]\n" \
"v_pk_fma_f32 v[176:177], v[176:177], s[76:77], v[180:181]\n" \
"v_pk_fma_f32 v[178:179], v[178:179], s[76:77], v[180:181]\n" \
"v_pk_mul_f32 v[172:173], v[172:173], v[100:101]\n" \
"v_pk_mul_f32 v[174:175], v[174:175], v[102:103]\n" \
"v_pk_mul_f32 v[176:177], v[176:177], v[104:105]\n" \
"v_pk_mul_f32 v[178:179], v[178:179], v[106:107]\n" \
"buffer_load_dwordx4 v[76:79], %[lo], %[ws], s73 offen offset:2048 sc1\n" \
"v_exp_f32 v172, v172\n" \
"v_exp_f32 v173, v173\n" \
"v_exp_f32 v174, v174\n" \
"v_exp_f32 v175, v175\n" \
"v_exp_f32 v176, v176\n" \
"v_exp_f32 v177, v177\n" \
"v_exp_f32 v178, v178\n" \
"v_exp_f32 v179, v179\n" \
"buffer_load_dwordx4 v[80:83], %[lo], %[ws], s73 offen offset:3072 sc1\n" \
"v_pk_add_f32 v[172:173], v[172:173], 1.0 op_sel_hi:[1,0]\n" \
"v_pk_add_f32 v[174:175], v[174:175], 1.0 op_sel_hi:[1,0]\n" \
"v_pk_add_f32 v[176:177], v[176:177], 1.0 op_sel_hi:[1,0]\n" \
"v_pk_add_f32 v[178:179], v[178:179], 1.0 op_sel_hi:[1,0]\n" \
"v_rcp_f32 v172, v172\n" \
"v_rcp_f32 v173, v173\n" \
"v_rcp_f32 v174, v174\n" \
"v_rcp_f32 v175, v175\n" \
"v_rcp_f32 v176, v176\n" \
"v_rcp_f32 v177, v177\n" \
"v_rcp_f32 v178, v178\n" \
"v_rcp_f32 v179, v179\n" \
"v_pk_mul_f32 v[100:101], v[100:101], v[172:173]\n" \
"v_pk_mul_f32 v[102:103], v[102:103], v[174:175]\n" \
"v_pk_mul_f32 v[104:105], v[104:105], v[176:177]\n" \
"v_pk_mul_f32 v[106:107], v[106:107], v[178:179]\n" \
"v_cvt_pk_bf16_f32 v204, v100, v101\n" \
"v_cvt_pk_bf16_f32 v205, v102, v103\n" \
"v_cvt_pk_bf16_f32 v206, v104, v105\n" \
"v_cvt_pk_bf16_f32 v207, v106, v107\n" \
"buffer_store_dwordx4 v[204:207], %[so], %[ws], s68 offen sc1\n" \
"s_add_u32 s68, s68, 114688\n" \
"s_cmp_eq_u32 s72, s67\n" \
"s_cbranch_scc1 Lw3_body1_%=\n" \
"s_waitcnt vmcnt(12)\n" \
"Lw3_top1_%=:\n" \
"v_cmp_gt_u32 vcc, s74, v202\n" \
"s_mov_b32 s72, s67\n" \
"s_mov_b32 s75, s70\n" \
"s_add_u32 s73, s67, 4096\n" \
"s_cbranch_vccnz Lw3_sgo1_%=\n" \
"Lw3_go1_%=:\n" \
"buffer_load_dwordx4 v[52:55], %[lo], %[ws], s72 offen offset:0 sc1\n" \
"buffer_load_dwordx4 v[56:59], %[lo], %[ws], s72 offen offset:1024 sc1\n" \
"buffer_load_dwordx2 v[92:93], %[sl], %[ws], s75 offen offset:0 sc1\n" \
"buffer_load_dwordx2 v[94:95], %[sl], %[ws], s75 offen offset:512 sc1\n" \
"buffer_load_dwordx4 v[60:63], %[lo], %[ws], s72 offen offset:2048 sc1\n" \
"buffer_load_dwordx4 v[64:67], %[lo], %[ws], s72 offen offset:3072 sc1\n" \
"buffer_load_dwordx4 v[68:71], %[lo], %[ws], s73 offen offset:0 sc1\n" \
"buffer_load_dwordx4 v[72:75], %[lo], %[ws], s73 offen offset:1024 sc1\n" \
"buffer_load_dwordx4 v[76:79], %[lo], %[ws], s73 offen offset:2048 sc1\n" \
"buffer_load_dwordx4 v[80:83], %[lo], %[ws], s73 offen offset:3072 sc1\n" \
"s_waitcnt vmcnt(10)\n" \
"buffer_store_dword v203, %[fo], %[ws], s69 offen sc1\n" \
"s_mov_b32 s69, 1073741824\n" \
"Lw3_body1_%=:\n" \
"s_waitcnt vmcnt(10)\n" \
"v_mfma_f32_16x16x32_bf16 v[108:111], a[8:11], v[52:55], 0\n" \
"s_nop 2\n" \
"v_mfma_f32_16x16x32_bf16 v[112:115], a[12:15], v[52:55], 0\n" \
"s_nop 2\n" \
"v_mfma_f32_16x16x32_bf16 v[116:119], a[16:19], v[52:55], 0\n" \
"s_add_u32 s74, s74, 1\n" \
"s_add_u32 s67, s67, 32768\n" \
"s_nop 0\n" \
"v_mfma_f32_16x16x32_bf16 v[120:123], a[20:23], v[52:55], 0\n" \
"s_add_u32 s65, s65, 1\n" \
"s_add_u32 s70, s70, 4096\n" \
"s_nop 0\n" \
"v_mfma_f32_16x16x32_bf16 v[124:127], a[24:27], v[52:55], 0\n" \
"s_cmp_gt_u32 s65, 104\n" \
"s_cselect_b32 s79, 1073741824, 0\n" \
"s_cselect_b32 s69, 0, s69\n" \
"v_mfma_f32_16x16x32_bf16 v[128:131], a[28:31], v[52:55], 0\n" \
"s_waitcnt vmcnt(9)\n" \
"s_addc_u32 s66, s65, -1\n" \
"s_nop 0\n" \
"v_mfma_f32_16x16x32_bf16 v[108:111], a[40:43], v[56:59], v[108:111]\n" \
"s_nop 0\n" \
"buffer_load_dword v202, %[fvo], %[ws], 0 offen sc1\n" \
"v_mov_b32 v203, s66\n" \
"v_mfma_f32_16x16x32_bf16 v[112:115], a[44:47], v[56:59], v[112:115]\n" \
"s_waitcnt vmcnt(8)\n" \
"v_max3_u32 v215, v92, v93, v94\n" \
"v_cmp_u_f32 vcc, v215, v95\n" \
"v_mfma_f32_16x16x32_bf16 v[116:119], a[48:51], v[56:59], v[116:119]\n" \
"s_cbranch_vccnz Lw3_sk10_%=\n" \
"Lw3_k10_%=:\n" \
"v_mfma_f32_16x16x32_bf16 v[120:123], a[52:55], v[56:59], v[120:123]\n" \
"s_nop 2\n" \
"v_mfma_f32_16x16x32_bf16 v[124:127], a[56:59], v[56:59], v[124:127]\n" \
"s_nop 0\n" \
"v_add_f32 v92, v92, v94\n" \
"v_add_f32 v93, v93, v95\n" \
"v_mfma_f32_16x16x32_bf16 v[128:131], a[60:63], v[56:59], v[128:131]\n" \
"s_waitcnt vmcnt(7)\n" \
"v_permlane32_swap_b32 v92, v93\n" \
"v_mfma_f32_16x16x32_bf16 v[108:111], a[72:75], v[60:63], v[108:111]\n" \
"s_nop 0\n" \
"v_add_f32 v94, v92, v93\n" \
"v_add_f32 v92, v92, v93\n" \
"v_mfma_f32_16x16x32_bf16 v[112:115], a[76:79], v[60:63], v[112:115]\n" \
"s_nop 0\n" \
"v_permlane16_swap_b32 v92, v94\n" \
"v_mfma_f32_16x16x32_bf16 v[116:119], a[80:83], v[60:63], v[116:119]\n" \
"s_nop 0\n" \
"v_add_f32 v92, v92, v94\n" \
"v_mfma_f32_16x16x32_bf16 v[120:123], a[84:87], v[60:63], v[120:123]\n" \
"s_nop 0\n" \
"ds_write_b32 %[sw], v92 offset:32768\n" \
"v_mfma_f32_16x16x32_bf16 v[124:127], a[88:91], v[60:63], v[124:127]\n" \
"s_nop 2\n" \
"v_mfma_f32_16x16x32_bf16 v[128:131], a[92:95], v[60:63], v[128:131]\n" \
"s_waitcnt vmcnt(6)\n" \
"s_nop 1\n" \
"v_mfma_f32_16x16x32_bf16 v[108:111], a[104:107], v[64:67], v[108:111]\n" \
"s_waitcnt lgkmcnt(0)\n" \
"s_barrier\n" \
"v_mfma_f32_16x16x32_bf16 v[112:115], a[108:111], v[64:67], v[112:115]\n" \
"s_nop 0\n" \
"ds_read_b128 v[190:193], %[sr] offset:32768\n" \
"v_mfma_f32_16x16x32_bf16 v[116:119], a[112:115], v[64:67], v[116:119]\n" \
"s_nop 0\n" \
"ds_read_b128 v[194:197], %[sr] offset:32784\n" \
"v_mfma_f32_16x16x32_bf16 v[120:123], a[116:119], v[64:67], v[120:123]\n" \
"s_nop 2\n" \
"v_mfma_f32_16x16x32_bf16 v[124:127], a[120:123], v[64:67], v[124:127]\n" \
"s_nop 2\n" \
"v_mfma_f32_16x16x32_bf16 v[128:131], a[124:127], v[64:67], v[128:131]\n" \
"s_waitcnt vmcnt(5)\n" \
"s_nop 1\n" \
"v_mfma_f32_16x16x32_bf16 v[108:111], a[136:139], v[68:71], v[108:111]\n" \
"s_waitcnt lgkmcnt(1)\n" \
"v_add_f32 v190, v190, v191\n" \
"v_add_f32 v192, v192, v193\n" \
"v_mfma_f32_16x16x32_bf16 v[112:115], a[140:143], v[68:71], v[112:115]\n" \
"s_waitcnt lgkmcnt(0)\n" \
"v_add_f32 v194, v194, v195\n" \
"v_add_f32 v196, v196, v197\n" \
"v_mfma_f32_16x16x32_bf16 v[116:119], a[144:147], v[68:71], v[116:119]\n" \
"s_nop 0\n" \
"v_add_f32 v190, v190, v192\n" \
"v_add_f32 v194, v194, v196\n" \
"v_mfma_f32_16x16x32_bf16 v[120:123], a[148:151], v[68:71], v[120:123]\n" \
"s_nop 0\n" \
"v_mul_f32 v190, 0xba800000, v190\n" \
"v_mul_f32 v194, 0x3a800000, v194\n" \
"v_mfma_f32_16x16x32_bf16 v[124:127], a[152:155], v[68:71], v[124:127]\n" \
"s_nop 0\n" \
"v_mul_f32 v156, v190, v164\n" \
"v_mul_f32 v157, v190, v165\n" \
"v_mfma_f32_16x16x32_bf16 v[128:131], a[156:159], v[68:71], v[128:131]\n" \
"s_waitcnt vmcnt(4)\n" \
"v_mul_f32 v158, v190, v166\n" \
"v_mul_f32 v159, v190, v167\n" \
"v_mfma_f32_16x16x32_bf16 v[108:111], a[168:171], v[72:75], v[108:111]\n" \
"s_waitcnt vmcnt(3)\n" \
"v_mul_f32 v160, v190, v168\n" \
"v_mul_f32 v161, v190, v169\n" \
"v_mfma_f32_16x16x32_bf16 v[108:111], a[200:203], v[76:79], v[108:111]\n" \
"s_waitcnt vmcnt(2)\n" \
"v_mul_f32 v162, v190, v170\n" \
"v_mul_f32 v163, v190, v171\n" \
"v_mfma_f32_16x16x32_bf16 v[108:111], a[232:235], v[80:83], v[108:111]\n" \
"s_nop 0\n" \
"v_fma_f32 v194, -v190, v190, v194\n" \
"v_max_f32 v194, 0, v194\n" \
"v_mfma_f32_16x16x32_bf16 v[112:115], a[172:175], v[72:75], v[112:115]\n" \
"s_nop 0\n" \
"v_add_f32 v192, 0x3727c5ac, v194\n" \
"v_rsq_f32 v192, v192\n" \
"v_mfma_f32_16x16x32_bf16 v[112:115], a[204:207], v[76:79], v[112:115]\n" \
"s_nop 0\n" \
"ds_write_b128 v212, v[108:111] offset:34816\n" \
"v_mfma_f32_16x16x32_bf16 v[112:115], a[236:239], v[80:83], v[112:115]\n" \
"s_nop 0\n" \
"v_cmp_u_f32 vcc, v108, v108\n" \
"v_mfma_f32_16x16x32_bf16 v[116:119], a[176:179], v[72:75], v[116:119]\n" \
"s_cbranch_vccnz Lw3_sn1_%=\n" \
"Lw3_n1_%=:\n" \
"v_mfma_f32_16x16x32_bf16 v[116:119], a[208:211], v[76:79], v[116:119]\n" \
"s_nop 0\n" \
"s_nop 1\n" \
"ds_write_b128 v212, v[112:115] offset:35840\n" \
"v_mfma_f32_16x16x32_bf16 v[116:119], a[240:243], v[80:83], v[116:119]\n" \
"s_nop 2\n" \
"v_mfma_f32_16x16x32_bf16 v[120:123], a[180:183], v[72:75], v[120:123]\n" \
"s_nop 2\n" \
"v_mfma_f32_16x16x32_bf16 v[120:123], a[212:215], v[76:79], v[120:123]\n" \
"s_nop 0\n" \
"ds_write_b128 v213, v[116:119] offset:36864\n" \
"v_mfma_f32_16x16x32_bf16 v[120:123], a[244:247], v[80:83], v[120:123]\n" \
"s_waitcnt vmcnt(0)\n" \
"v_cmp_gt_u32 vcc, s74, v202\n" \
"v_mfma_f32_16x16x32_bf16 v[124:127], a[184:187], v[72:75], v[124:127]\n" \
"s_nop 0\n" \
"buffer_load_dword v202, %[fvo], %[ws], 0 offen sc1\n" \
"v_mfma_f32_16x16x32_bf16 v[124:127], a[216:219], v[76:79], v[124:127]\n" \
"s_cmp_eq_u64 vcc, 0\n" \
"s_nop 0\n" \
"ds_write_b128 v213, v[120:123] offset:37888\n" \
"v_mfma_f32_16x16x32_bf16 v[124:127], a[248:251], v[80:83], v[124:127]\n" \
"s_cselect_b32 s72, s67, 1073741824\n" \
"buffer_load_dwordx4 v[20:23], %[lo], %[ws], s72 offen offset:0 sc1\n" \
"v_mfma_f32_16x16x32_bf16 v[128:131], a[188:191], v[72:75], v[128:131]\n" \
"s_cselect_b32 s75, s70, 1073741824\n" \
"s_add_u32 s73, s72, 4096\n" \
"s_nop 0\n" \
"v_mfma_f32_16x16x32_bf16 v[128:131], a[220:223], v[76:79], v[128:131]\n" \
"s_nop 0\n" \
"ds_write_b128 v214, v[124:127] offset:38912\n" \
"v_mfma_f32_16x16x32_bf16 v[128:131], a[252:255], v[80:83], v[128:131]\n" \
"s_waitcnt lgkmcnt(1)\n" \
"s_barrier\n" \
"v_mfma_f32_16x16x32_bf16 v[100:103], a[0:3], v[52:55], v[156:159]\n" \
"s_nop 0\n" \
"ds_read_b128 v[132:135], %[xr] offset:34816\n" \
"buffer_load_dwordx4 v[24:27], %[lo], %[ws], s72 offen offset:1024 sc1\n" \
"v_mfma_f32_16x16x32_bf16 v[104:107], a[4:7], v[52:55], v[160:163]\n" \
"s_nop 0\n" \
"ds_write_b128 v214, v[128:131] offset:39936\n" \
"v_mfma_f32_16x16x32_bf16 v[100:103], a[32:35], v[56:59], v[100:103]\n" \
"s_nop 0\n" \
"ds_read_b128 v[136:139], %[xr] offset:35840\n" \
"v_mfma_f32_16x16x32_bf16 v[104:107], a[36:39], v[56:59], v[104:107]\n" \
"s_nop 0\n" \
"ds_read_b128 v[140:143], %[xr] offset:36864\n" \
"buffer_store_dword v203, %[fo], %[ws], s69 offen sc1\n" \
"v_mfma_f32_16x16x32_bf16 v[100:103], a[64:67], v[60:63], v[100:103]\n" \
"s_nop 0\n" \
"ds_read_b128 v[144:147], %[xr] offset:37888\n" \
"buffer_load_dwordx2 v[84:85], %[sl], %[ws], s75 offen offset:0 sc1\n" \
"v_mfma_f32_16x16x32_bf16 v[104:107], a[68:71], v[60:63], v[104:107]\n" \
"s_waitcnt lgkmcnt(3)\n" \
"s_barrier\n" \
"v_mfma_f32_16x16x32_bf16 v[100:103], a[96:99], v[64:67], v[100:103]\n" \
"s_nop 0\n" \
"ds_read_b128 v[148:151], %[xr] offset:38912\n" \
"buffer_load_dwordx2 v[86:87], %[sl], %[ws], s75 offen offset:512 sc1\n" \
"v_mfma_f32_16x16x32_bf16 v[104:107], a[100:103], v[64:67], v[104:107]\n" \
"s_nop 0\n" \
"ds_read_b128 v[152:155], %[xr] offset:39936\n" \
"v_mfma_f32_16x16x32_bf16 v[100:103], a[128:131], v[68:71], v[100:103]\n" \
"s_waitcnt lgkmcnt(3)\n" \
"buffer_load_dwordx4 v[28:31], %[lo], %[ws], s72 offen offset:2048 sc1\n" \
"v_add_f32 v132, v132, v140\n" \
"v_add_f32 v133, v133, v141\n" \
"v_mfma_f32_16x16x32_bf16 v[104:107], a[132:135], v[68:71], v[104:107]\n" \
"s_nop 0\n" \
"v_add_f32 v134, v134, v142\n" \
"v_add_f32 v135, v135, v143\n" \
"v_mfma_f32_16x16x32_bf16 v[100:103], a[160:163], v[72:75], v[100:103]\n" \
"s_waitcnt lgkmcnt(2)\n" \
"v_add_f32 v136, v136, v144\n" \
"v_add_f32 v137, v137, v145\n" \
"v_mfma_f32_16x16x32_bf16 v[104:107], a[164:167], v[72:75], v[104:107]\n" \
"s_nop 0\n" \
"v_add_f32 v138, v138, v146\n" \
"v_add_f32 v139, v139, v147\n" \
"v_mfma_f32_16x16x32_bf16 v[100:103], a[192:195], v[76:79], v[100:103]\n" \
"s_waitcnt lgkmcnt(1)\n" \
"buffer_load_dwordx4 v[32:35], %[lo], %[ws], s72 offen offset:3072 sc1\n" \
"v_add_f32 v132, v132, v148\n" \
"v_add_f32 v133, v133, v149\n" \
"v_mfma_f32_16x16x32_bf16 v[104:107], a[196:199], v[76:79], v[104:107]\n" \
"s_nop 0\n" \
"v_add_f32 v134, v134, v150\n" \
"v_add_f32 v135, v135, v151\n" \
"v_mfma_f32_16x16x32_bf16 v[100:103], a[224:227], v[80:83], v[100:103]\n" \
"s_waitcnt lgkmcnt(0)\n" \
"v_add_f32 v136, v136, v152\n" \
"v_add_f32 v137, v137, v153\n" \
"v_mfma_f32_16x16x32_bf16 v[104:107], a[228:231], v[80:83], v[104:107]\n" \
"s_nop 0\n" \
"v_add_f32 v138, v138, v154\n" \
"v_add_f32 v139, v139, v155\n" \
"v_mov_b32 v203, s65\n" \
"s_mov_b32 s69, s79\n" \
"buffer_load_dwordx4 v[36:39], %[lo], %[ws], s73 offen offset:0 sc1\n" \
"v_pk_add_f32 v[100:101], v[100:101], v[132:133]\n" \
"v_pk_add_f32 v[102:103], v[102:103], v[134:135]\n" \
"v_pk_add_f32 v[104:105], v[104:105], v[136:137]\n" \
"v_pk_add_f32 v[106:107], v[106:107], v[138:139]\n" \
"v_pk_mul_f32 v[100:101], v[100:101], v[192:193] op_sel_hi:[1,0]\n" \
"v_pk_mul_f32 v[102:103], v[102:103], v[192:193] op_sel_hi:[1,0]\n" \
"v_pk_mul_f32 v[104:105], v[104:105], v[192:193] op_sel_hi:[1,0]\n" \
"v_pk_mul_f32 v[106:107], v[106:107], v[192:193] op_sel_hi:[1,0]\n" \
"v_pk_mul_f32 v[172:173], v[100:101], v[100:101]\n" \
"v_pk_mul_f32 v[174:175], v[102:103], v[102:103]\n" \
"v_pk_mul_f32 v[176:177], v[104:105], v[104:105]\n" \
"v_pk_mul_f32 v[178:179], v[106:107], v[106:107]\n" \
"buffer_load_dwordx4 v[40:43], %[lo], %[ws], s73 offen offset:1024 sc1\n" \
"v_pk_fma_f32 v[172:173], v[172:173], s[76:77], v[180:181]\n" \
"v_pk_fma_f32 v[174:175], v[174:175], s[76:77], v[180:181]\n" \
"v_pk_fma_f32 v[176:177], v[176:177], s[76:77], v[180:181]\n" \
"v_pk_fma_f32 v[178:179], v[178:179], s[76:77], v[180:181]\n" \
"v_pk_mul_f32 v[172:173], v[172:173], v[100:101]\n" \
"v_pk_mul_f32 v[174:175], v[174:175], v[102:103]\n" \
"v_pk_mul_f32 v[176:177], v[176:177], v[104:105]\n" \
"v_pk_mul_f32 v[178:179], v[178:179], v[106:107]\n" \
"buffer_load_dwordx4 v[44:47], %[lo], %[ws], s73 offen offset:2048 sc1\n" \
"v_exp_f32 v172, v172\n" \
"v_exp_f32 v173, v173\n" \
"v_exp_f32 v174, v174\n" \
"v_exp_f32 v175, v175\n" \
"v_exp_f32 v176, v176\n" \
"v_exp_f32 v177, v177\n" \
"v_exp_f32 v178, v178\n" \
"v_exp_f32 v179, v179\n" \
"buffer_load_dwordx4 v[48:51], %[lo], %[ws], s73 offen offset:3072 sc1\n" \
"v_pk_add_f32 v[172:173], v[172:173], 1.0 op_sel_hi:[1,0]\n" \
"v_pk_add_f32 v[174:175], v[174:175], 1.0 op_sel_hi:[1,0]\n" \
"v_pk_add_f32 v[176:177], v[176:177], 1.0 op_sel_hi:[1,0]\n" \
"v_pk_add_f32 v[178:179], v[178:179], 1.0 op_sel_hi:[1,0]\n" \
"v_rcp_f32 v172, v172\n" \
"v_rcp_f32 v173, v173\n" \
"v_rcp_f32 v174, v174\n" \
"v_rcp_f32 v175, v175\n" \
"v_rcp_f32 v176, v176\n" \
"v_rcp_f32 v177, v177\n" \
"v_rcp_f32 v178, v178\n" \
"v_rcp_f32 v179, v179\n" \
"v_pk_mul_f32 v[100:101], v[100:101], v[172:173]\n" \
"v_pk_mul_f32 v[102:103], v[102:103], v[174:175]\n" \
"v_pk_mul_f32 v[104:105], v[104:105], v[176:177]\n" \
"v_pk_mul_f32 v[106:107], v[106:107], v[178:179]\n" \
"v_cvt_pk_bf16_f32 v204, v100, v101\n" \
"v_cvt_pk_bf16_f32 v205, v102, v103\n" \
"v_cvt_pk_bf16_f32 v206, v104, v105\n" \
"v_cvt_pk_bf16_f32 v207, v106, v107\n" \
"buffer_store_dwordx4 v[204:207], %[so], %[ws], s68 offen sc1\n" \
"s_add_u32 s68, s68, 114688\n" \
"s_cmp_eq_u32 s65, s80\n" \
"s_cbranch_scc1 Lw3_end_%=\n" \
"Lw3_on_%=:\n" \
"s_cmp_eq_u32 s72, s67\n" \
"s_cbranch_scc1 Lw3_body0_%=\n" \
"s_waitcnt vmcnt(12)\n" \
"s_branch Lw3_top0_%=\n" \
"Lw3_end_%=:\n" \
"s_endpgm\n" \
"Lw3_sgo0_%=:\n" \
"s_waitcnt vmcnt(0)\n" \
"buffer_load_dword v202, %[fvo], %[ws], 0 offen sc1\n" \
"buffer_store_dword v203, %[fo], %[ws], s69 offen sc1\n" \
"s_mov_b32 s69, 1073741824\n" \
"s_waitcnt vmcnt(1)\n" \
"s_branch Lw3_cgo0_%=\n" \
"Lw3_pgo0_%=:\n" \
"s_sub_u32 s64, s64, 1\n" \
"s_cbranch_scc1 Lw3_dead_%=\n" \
"buffer_load_dword v202, %[fvo], %[ws], 0 offen sc1\n" \
"s_waitcnt vmcnt(0)\n" \
"Lw3_cgo0_%=:\n" \
"v_cmp_gt_u32 vcc, s74, v202\n" \
"s_cbranch_vccz Lw3_go0_%=\n" \
"s_sleep 1\n" \
"s_branch Lw3_pgo0_%=\n" \
"Lw3_sk00_%=:\n" \
"v_max3_u32 v215, v84, v85, v86\n" \
"v_max3_u32 v215, v215, v87, v87\n" \
"v_cmp_eq_u32 vcc, -1, v215\n" \
"s_nop 1\n" \
"s_cbranch_vccz Lw3_k00_%=\n" \
"Lw3_rk00_%=:\n" \
"s_sub_u32 s64, s64, 1\n" \
"s_cbranch_scc1 Lw3_dead_%=\n" \
"buffer_load_dwordx2 v[84:85], %[sl], %[ws], s75 offen offset:0 sc1\n" \
"buffer_load_dwordx2 v[86:87], %[sl], %[ws], s75 offen offset:512 sc1\n" \
"s_waitcnt vmcnt(0)\n" \
"v_max3_u32 v215, v84, v85, v86\n" \
"v_max3_u32 v215, v215, v87, v87\n" \
"v_cmp_eq_u32 vcc, -1, v215\n" \
"s_nop 1\n" \
"s_cbranch_vccnz Lw3_rk00_%=\n" \
"s_branch Lw3_k00_%=\n" \
"Lw3_sn0_%=:\n" \
"v_max3_u32 v215, v20, v21, v22\n" \
"v_max3_u32 v215, v215, v23, v24\n" \
"v_max3_u32 v215, v215, v25, v26\n" \
"v_max3_u32 v215, v215, v27, v28\n" \
"v_max3_u32 v215, v215, v29, v30\n" \
"v_max3_u32 v215, v215, v31, v32\n" \
"v_max3_u32 v215, v215, v33, v34\n" \
"v_max3_u32 v215, v215, v35, v36\n" \
"v_max3_u32 v215, v215, v37, v38\n" \
"v_max3_u32 v215, v215, v39, v40\n" \
"v_max3_u32 v215, v215, v41, v42\n" \
"v_max3_u32 v215, v215, v43, v44\n" \
"v_max3_u32 v215, v215, v45, v46\n" \
"v_max3_u32 v215, v215, v47, v48\n" \
"v_max3_u32 v215, v215, v49, v50\n" \
"v_max3_u32 v215, v215, v51, v51\n" \
"v_cmp_eq_u32 vcc, -1, v215\n" \
"s_nop 1\n" \
"s_cbranch_vccz Lw3_n0_%=\n" \
"Lw3_rn0_%=:\n" \
"s_sub_u32 s64, s64, 1\n" \
"s_cbranch_scc1 Lw3_dead_%=\n" \
"buffer_load_dwordx4 v[20:23], %[lo], %[ws], s72 offen offset:0 sc1\n" \
"buffer_load_dwordx4 v[24:27], %[lo], %[ws], s72 offen offset:1024 sc1\n" \
"buffer_load_dwordx4 v[28:31], %[lo], %[ws], s72 offen offset:2048 sc1\n" \
"buffer_load_dwordx4 v[32:35], %[lo], %[ws], s72 offen offset:3072 sc1\n" \
"buffer_load_dwordx4 v[36:39], %[lo], %[ws], s73 offen offset:0 sc1\n" \
"buffer_load_dwordx4 v[40:43], %[lo], %[ws], s73 offen offset:1024 sc1\n" \
"buffer_load_dwordx4 v[44:47], %[lo], %[ws], s73 offen offset:2048 sc1\n" \
"buffer_load_dwordx4 v[48:51], %[lo], %[ws], s73 offen offset:3072 sc1\n" \
"s_waitcnt vmcnt(0)\n" \
"v_max3_u32 v215, v20, v21, v22\n" \
"v_max3_u32 v215, v215, v23, v24\n" \
"v_max3_u32 v215, v215, v25, v26\n" \
"v_max3_u32 v215, v215, v27, v28\n" \
"v_max3_u32 v215, v215, v29, v30\n" \
"v_max3_u32 v215, v215, v31, v32\n" \
"v_max3_u32 v215, v215, v33, v34\n" \
"v_max3_u32 v215, v215, v35, v36\n" \
"v_max3_u32 v215, v215, v37, v38\n" \
"v_max3_u32 v215, v215, v39, v40\n" \
"v_max3_u32 v215, v215, v41, v42\n" \
"v_max3_u32 v215, v215, v43, v44\n" \
"v_max3_u32 v215, v215, v45, v46\n" \
"v_max3_u32 v215, v215, v47, v48\n" \
"v_max3_u32 v215, v215, v49, v50\n" \
"v_max3_u32 v215, v215, v51, v51\n" \
"v_cmp_eq_u32 vcc, -1, v215\n" \
"s_nop 1\n" \
"s_cbranch_vccnz Lw3_rn0_%=\n" \
"v_mfma_f32_16x16x32_bf16 v[108:111], a[8:11], v[20:23], 0\n" \
"v_mfma_f32_16x16x32_bf16 v[112:115], a[12:15], v[20:23], 0\n" \
"v_mfma_f32_16x16x32_bf16 v[116:119], a[16:19], v[20:23], 0\n" \
"v_mfma_f32_16x16x32_bf16 v[120:123], a[20:23], v[20:23], 0\n" \
"v_mfma_f32_16x16x32_bf16 v[124:127], a[24:27], v[20:23], 0\n" \
"v_mfma_f32_16x16x32_bf16 v[128:131], a[28:31], v[20:23], 0\n" \
"v_mfma_f32_16x16x32_bf16 v[108:111], a[40:43], v[24:27], v[108:111]\n" \
"v_mfma_f32_16x16x32_bf16 v[112:115], a[44:47], v[24:27], v[112:115]\n" \
"v_mfma_f32_16x16x32_bf16 v[116:119], a[48:51], v[24:27], v[116:119]\n" \
"v_mfma_f32_16x16x32_bf16 v[120:123], a[52:55], v[24:27], v[120:123]\n" \
"v_mfma_f32_16x16x32_bf16 v[124:127], a[56:59], v[24:27], v[124:127]\n" \
"v_mfma_f32_16x16x32_bf16 v[128:131], a[60:63], v[24:27], v[128:131]\n" \
"v_mfma_f32_16x16x32_bf16 v[108:111], a[72:75], v[28:31], v[108:111]\n" \
"v_mfma_f32_16x16x32_bf16 v[112:115], a[76:79], v[28:31], v[112:115]\n" \
"v_mfma_f32_16x16x32_bf16 v[116:119], a[80:83], v[28:31], v[116:119]\n" \
"v_mfma_f32_16x16x32_bf16 v[120:123], a[84:87], v[28:31], v[120:123]\n" \
"v_mfma_f32_16x16x32_bf16 v[124:127], a[88:91], v[28:31], v[124:127]\n" \
"v_mfma_f32_16x16x32_bf16 v[128:131], a[92:95], v[28:31], v[128:131]\n" \
"v_mfma_f32_16x16x32_bf16 v[108:111], a[104:107], v[32:35], v[108:111]\n" \
"v_mfma_f32_16x16x32_bf16 v[112:115], a[108:111], v[32:35], v[112:115]\n" \
"v_mfma_f32_16x16x32_bf16 v[116:119], a[112:115], v[32:35], v[116:119]\n" \
"v_mfma_f32_16x16x32_bf16 v[120:123], a[116:119], v[32:35], v[120:123]\n" \
"v_mfma_f32_16x16x32_bf16 v[124:127], a[120:123], v[32:35], v[124:127]\n" \
"v_mfma_f32_16x16x32_bf16 v[128:131], a[124:127], v[32:35], v[128:131]\n" \
"v_mfma_f32_16x16x32_bf16 v[108:111], a[136:139], v[36:39], v[108:111]\n" \
"v_mfma_f32_16x16x32_bf16 v[112:115], a[140:143], v[36:39], v[112:115]\n" \
"v_mfma_f32_16x16x32_bf16 v[116:119], a[144:147], v[36:39], v[116:119]\n" \
"v_mfma_f32_16x16x32_bf16 v[120:123], a[148:151], v[36:39], v[120:123]\n" \
"v_mfma_f32_16x16x32_bf16 v[124:127], a[152:155], v[36:39], v[124:127]\n" \
"v_mfma_f32_16x16x32_bf16 v[128:131], a[156:159], v[36:39], v[128:131]\n" \
"v_mfma_f32_16x16x32_bf16 v[108:111], a[168:171], v[40:43], v[108:111]\n" \
"v_mfma_f32_16x16x32_bf16 v[108:111], a[200:203], v[44:47], v[108:111]\n" \
"v_mfma_f32_16x16x32_bf16 v[108:111], a[232:235], v[48:51], v[108:111]\n" \
"v_mfma_f32_16x16x32_bf16 v[112:115], a[172:175], v[40:43], v[112:115]\n" \
"v_mfma_f32_16x16x32_bf16 v[112:115], a[204:207], v[44:47], v[112:115]\n" \
"v_mfma_f32_16x16x32_bf16 v[112:115], a[236:239], v[48:51], v[112:115]\n" \
"v_mfma_f32_16x16x32_bf16 v[116:119], a[176:179], v[40:43], v[116:119]\n" \
"s_nop 7\n" \
"ds_write_b128 v212, v[108:111] offset:2048\n" \
"s_branch Lw3_n0_%=\n" \
"Lw3_sgo1_%=:\n" \
"s_waitcnt vmcnt(0)\n" \
"buffer_load_dword v202, %[fvo], %[ws], 0 offen sc1\n" \
"buffer_store_dword v203, %[fo], %[ws], s69 offen sc1\n" \
"s_mov_b32 s69, 1073741824\n" \
"s_waitcnt vmcnt(1)\n" \
"s_branch Lw3_cgo1_%=\n" \
"Lw3_pgo1_%=:\n" \
"s_sub_u32 s64, s64, 1\n" \
"s_cbranch_scc1 Lw3_dead_%=\n" \
"buffer_load_dword v202, %[fvo], %[ws], 0 offen sc1\n" \
"s_waitcnt vmcnt(0)\n" \
"Lw3_cgo1_%=:\n" \
"v_cmp_gt_u32 vcc, s74, v202\n" \
"s_cbranch_vccz Lw3_go1_%=\n" \
"s_sleep 1\n" \
"s_branch Lw3_pgo1_%=\n" \
"Lw3_sk10_%=:\n" \
"v_max3_u32 v215, v92, v93, v94\n" \
"v_max3_u32 v215, v215, v95, v95\n" \
"v_cmp_eq_u32 vcc, -1, v215\n" \
"s_nop 1\n" \
"s_cbranch_vccz Lw3_k10_%=\n" \
"Lw3_rk10_%=:\n" \
"s_sub_u32 s64, s64, 1\n" \
"s_cbranch_scc1 Lw3_dead_%=\n" \
"buffer_load_dwordx2 v[92:93], %[sl], %[ws], s75 offen offset:0 sc1\n" \
"buffer_load_dwordx2 v[94:95], %[sl], %[ws], s75 offen offset:512 sc1\n" \
"s_waitcnt vmcnt(0)\n" \
"v_max3_u32 v215, v92, v93, v94\n" \
"v_max3_u32 v215, v215, v95, v95\n" \
"v_cmp_eq_u32 vcc, -1, v215\n" \
"s_nop 1\n" \
"s_cbranch_vccnz Lw3_rk10_%=\n" \
"s_branch Lw3_k10_%=\n" \
"Lw3_sn1_%=:\n" \
"v_max3_u32 v215, v52, v53, v54\n" \
"v_max3_u32 v215, v215, v55, v56\n" \
"v_max3_u32 v215, v215, v57, v58\n" \
"v_max3_u32 v215, v215, v59, v60\n" \
"v_max3_u32 v215, v215, v61, v62\n" \
"v_max3_u32 v215, v215, v63, v64\n" \
"v_max3_u32 v215, v215, v65, v66\n" \
"v_max3_u32 v215, v215, v67, v68\n" \
"v_max3_u32 v215, v215, v69, v70\n" \
"v_max3_u32 v215, v215, v71, v72\n" \
"v_max3_u32 v215, v215, v73, v74\n" \
"v_max3_u32 v215, v215, v75, v76\n" \
"v_max3_u32 v215, v215, v77, v78\n" \
"v_max3_u32 v215, v215, v79, v80\n" \
"v_max3_u32 v215, v215, v81, v82\n" \
"v_max3_u32 v215, v215, v83, v83\n" \
"v_cmp_eq_u32 vcc, -1, v215\n" \
"s_nop 1\n" \
"s_cbranch_vccz Lw3_n1_%=\n" \
"Lw3_rn1_%=:\n" \
"s_sub_u32 s64, s64, 1\n" \
"s_cbranch_scc1 Lw3_dead_%=\n" \
"buffer_load_dwordx4 v[52:55], %[lo], %[ws], s72 offen offset:0 sc1\n" \
"buffer_load_dwordx4 v[56:59], %[lo], %[ws], s72 offen offset:1024 sc1\n" \
"buffer_load_dwordx4 v[60:63], %[lo], %[ws], s72 offen offset:2048 sc1\n" \
"buffer_load_dwordx4 v[64:67], %[lo], %[ws], s72 offen offset:3072 sc1\n" \
"buffer_load_dwordx4 v[68:71], %[lo], %[ws], s73 offen offset:0 sc1\n" \
"buffer_load_dwordx4 v[72:75], %[lo], %[ws], s73 offen offset:1024 sc1\n" \
"buffer_load_dwordx4 v[76:79], %[lo], %[ws], s73 offen offset:2048 sc1\n" \
"buffer_load_dwordx4 v[80:83], %[lo], %[ws], s73 offen offset:3072 sc1\n" \
"s_waitcnt vmcnt(0)\n" \
"v_max3_u32 v215, v52, v53, v54\n" \
"v_max3_u32 v215, v215, v55, v56\n" \
"v_max3_u32 v215, v215, v57, v58\n" \
"v_max3_u32 v215, v215, v59, v60\n" \
"v_max3_u32 v215, v215, v61, v62\n" \
"v_max3_u32 v215, v215, v63, v64\n" \
"v_max3_u32 v215, v215, v65, v66\n" \
"v_max3_u32 v215, v215, v67, v68\n" \
"v_max3_u32 v215, v215, v69, v70\n" \
"v_max3_u32 v215, v215, v71, v72\n" \
"v_max3_u32 v215, v215, v73, v74\n" \
"v_max3_u32 v215, v215, v75, v76\n" \
"v_max3_u32 v215, v215, v77, v78\n" \
"v_max3_u32 v215, v215, v79, v80\n" \
"v_max3_u32 v215, v215, v81, v82\n" \
"v_max3_u32 v215, v215, v83, v83\n" \
"v_cmp_eq_u32 vcc, -1, v215\n" \
"s_nop 1\n" \
"s_cbranch_vccnz Lw3_rn1_%=\n" \
"v_mfma_f32_16x16x32_bf16 v[108:111], a[8:11], v[52:55], 0\n" \
"v_mfma_f32_16x16x32_bf16 v[112:115], a[12:15], v[52:55], 0\n" \
"v_mfma_f32_16x16x32_bf16 v[116:119], a[16:19], v[52:55], 0\n" \
"v_mfma_f32_16x16x32_bf16 v[120:123], a[20:23], v[52:55], 0\n" \
"v_mfma_f32_16x16x32_bf16 v[124:127], a[24:27], v[52:55], 0\n" \
"v_mfma_f32_16x16x32_bf16 v[128:131], a[28:31], v[52:55], 0\n" \
"v_mfma_f32_16x16x32_bf16 v[108:111], a[40:43], v[56:59], v[108:111]\n" \
"v_mfma_f32_16x16x32_bf16 v[112:115], a[44:47], v[56:59], v[112:115]\n" \
"v_mfma_f32_16x16x32_bf16 v[116:119], a[48:51], v[56:59], v[116:119]\n" \
"v_mfma_f32_16x16x32_bf16 v[120:123], a[52:55], v[56:59], v[120:123]\n" \
"v_mfma_f32_16x16x32_bf16 v[124:127], a[56:59], v[56:59], v[124:127]\n" \
"v_mfma_f32_16x16x32_bf16 v[128:131], a[60:63], v[56:59], v[128:131]\n" \
"v_mfma_f32_16x16x32_bf16 v[108:111], a[72:75], v[60:63], v[108:111]\n" \
"v_mfma_f32_16x16x32_bf16 v[112:115], a[76:79], v[60:63], v[112:115]\n" \
"v_mfma_f32_16x16x32_bf16 v[116:119], a[80:83], v[60:63], v[116:119]\n" \
"v_mfma_f32_16x16x32_bf16 v[120:123], a[84:87], v[60:63], v[120:123]\n" \
"v_mfma_f32_16x16x32_bf16 v[124:127], a[88:91], v[60:63], v[124:127]\n" \
"v_mfma_f32_16x16x32_bf16 v[128:131], a[92:95], v[60:63], v[128:131]\n" \
"v_mfma_f32_16x16x32_bf16 v[108:111], a[104:107], v[64:67], v[108:111]\n" \
"v_mfma_f32_16x16x32_bf16 v[112:115], a[108:111], v[64:67], v[112:115]\n" \
"v_mfma_f32_16x16x32_bf16 v[116:119], a[112:115], v[64:67], v[116:119]\n" \
"v_mfma_f32_16x16x32_bf16 v[120:123], a[116:119], v[64:67], v[120:123]\n" \
"v_mfma_f32_16x16x32_bf16 v[124:127], a[120:123], v[64:67], v[124:127]\n" \
"v_mfma_f32_16x16x32_bf16 v[128:131], a[124:127], v[64:67], v[128:131]\n" \
"v_mfma_f32_16x16x32_bf16 v[108:111], a[136:139], v[68:71], v[108:111]\n" \
"v_mfma_f32_16x16x32_bf16 v[112:115], a[140:143], v[68:71], v[112:115]\n" \
"v_mfma_f32_16x16x32_bf16 v[116:119], a[144:147], v[68:71], v[116:119]\n" \
"v_mfma_f32_16x16x32_bf16 v[120:123], a[148:151], v[68:71], v[120:123]\n" \
"v_mfma_f32_16x16x32_bf16 v[124:127], a[152:155], v[68:71], v[124:127]\n" \
"v_mfma_f32_16x16x32_bf16 v[128:131], a[156:159], v[68:71], v[128:131]\n" \
"v_mfma_f32_16x16x32_bf16 v[108:111], a[168:171], v[72:75], v[108:111]\n" \
"v_mfma_f32_16x16x32_bf16 v[108:111], a[200:203], v[76:79], v[108:111]\n" \
"v_mfma_f32_16x16x32_bf16 v[108:111], a[232:235], v[80:83], v[108:111]\n" \
"v_mfma_f32_16x16x32_bf16 v[112:115], a[172:175], v[72:75], v[112:115]\n" \
"v_mfma_f32_16x16x32_bf16 v[112:115], a[204:207], v[76:79], v[112:115]\n" \
"v_mfma_f32_16x16x32_bf16 v[112:115], a[236:239], v[80:83], v[112:115]\n" \
"v_mfma_f32_16x16x32_bf16 v[116:119], a[176:179], v[72:75], v[116:119]\n" \
"s_nop 7\n" \
"ds_write_b128 v212, v[108:111] offset:34816\n" \
"s_branch Lw3_n1_%=\n" \
"Lw3_dead_%=:\n" \
"v_mov_b32 v202, 29187\n" \
"buffer_store_dword v202, off, %[ws], 0 sc1\n" \
"s_endpgm\n" \
    :  : [wvo] "v"(wvo), [tr0] "v"(tr0), [fo] "v"(fo), [xr] "v"(xr), [fvo] "v"(fvo), [lo] "v"(lo), [so] "v"(so), [sl] "v"(sl), [sw] "v"(sw), [sr] "v"(sr), [ws] "s"(ws), [rw] "s"(rw), [so0] "s"(so0), [wbase] "s"(wbase), [src] "s"(src), [dst] "s"(dst), [d0] "s"(d0), [ox0] "s"(ox0) \
    : CLOB20)

#define W4_ASM() asm volatile( \
"s_nop 4\n" \
"s_mov_b32 s66, %[d0]\n" \
"Lw4_dly_%=:\n" \
"s_sleep 8\n" \
"s_sub_u32 s66, s66, 1\n" \
"s_cbranch_scc0 Lw4_dly_%=\n" \
"v_xor_b32 v20, 0, %[tr0]\n" \
"v_xor_b32 v21, 64, %[tr0]\n" \
"v_xor_b32 v22, 8, %[tr0]\n" \
"v_xor_b32 v23, 72, %[tr0]\n" \
"v_xor_b32 v24, 128, %[tr0]\n" \
"v_xor_b32 v25, 192, %[tr0]\n" \
"v_xor_b32 v26, 136, %[tr0]\n" \
"v_xor_b32 v27, 200, %[tr0]\n" \
"s_add_u32 m0, %[wbase], 0\n" \
"s_add_u32 s66, %[so0], 0\n" \
"buffer_load_dwordx4 %[wvo], %[rw], s66 offen lds\n" \
"s_add_u32 m0, %[wbase], 1024\n" \
"s_add_u32 s66, %[so0], 8192\n" \
"buffer_load_dwordx4 %[wvo], %[rw], s66 offen lds\n" \
"s_add_u32 m0, %[wbase], 2048\n" \
"s_add_u32 s66, %[so0], 16384\n" \
"buffer_load_dwordx4 %[wvo], %[rw], s66 offen lds\n" \
"s_waitcnt vmcnt(2)\n" \
"s_add_u32 m0, %[wbase], 3072\n" \
"s_add_u32 s66, %[so0], 24576\n" \
"buffer_load_dwordx4 %[wvo], %[rw], s66 offen lds\n" \
"s_waitcnt vmcnt(2)\n" \
"s_add_u32 m0, %[wbase], 4096\n" \
"s_add_u32 s66, %[so0], 32768\n" \
"buffer_load_dwordx4 %[wvo], %[rw], s66 offen lds\n" \
"s_waitcnt vmcnt(2)\n" \
"s_add_u32 m0, %[wbase], 5120\n" \
"s_add_u32 s66, %[so0], 40960\n" \
"buffer_load_dwordx4 %[wvo], %[rw], s66 offen lds\n" \
"s_waitcnt vmcnt(2)\n" \
"s_add_u32 m0, %[wbase], 6144\n" \
"s_add_u32 s66, %[so0], 49152\n" \
"buffer_load_dwordx4 %[wvo], %[rw], s66 offen lds\n" \
"s_waitcnt vmcnt(2)\n" \
"s_add_u32 m0, %[wbase], 7168\n" \
"s_add_u32 s66, %[so0], 57344\n" \
"buffer_load_dwordx4 %[wvo], %[rw], s66 offen lds\n" \
"s_waitcnt vmcnt(2)\n" \
"s_add_u32 m0, %[wbase], 8192\n" \
"s_add_u32 s66, %[so0], 65536\n" \
"buffer_load_dwordx4 %[wvo], %[rw], s66 offen lds\n" \
"s_waitcnt vmcnt(2)\n" \
"s_add_u32 m0, %[wbase], 9216\n" \
"s_add_u32 s66, %[so0], 73728\n" \
"buffer_load_dwordx4 %[wvo], %[rw], s66 offen lds\n" \
"s_waitcnt vmcnt(2)\n" \
"s_add_u32 m0, %[wbase], 10240\n" \
"s_add_u32 s66, %[so0], 81920\n" \
"buffer_load_dwordx4 %[wvo], %[rw], s66 offen lds\n" \
"ds_read_b64_tr_b16 a[0:1], v20 offset:0\n" \
"ds_read_b64_tr_b16 a[2:3], v20 offset:1024\n" \
"ds_read_b64_tr_b16 a[4:5], v21 offset:0\n" \
"ds_read_b64_tr_b16 a[6:7], v21 offset:1024\n" \
"ds_read_b64_tr_b16 a[8:9], v22 offset:0\n" \
"ds_read_b64_tr_b16 a[10:11], v22 offset:1024\n" \
"ds_read_b64_tr_b16 a[12:13], v23 offset:0\n" \
"ds_read_b64_tr_b16 a[14:15], v23 offset:1024\n" \
"ds_read_b64_tr_b16 a[16:17], v24 offset:0\n" \
"ds_read_b64_tr_b16 a[18:19], v24 offset:1024\n" \
"ds_read_b64_tr_b16 a[20:21], v25 offset:0\n" \
"ds_read_b64_tr_b16 a[22:23], v25 offset:1024\n" \
"ds_read_b64_tr_b16 a[24:25], v26 offset:0\n" \
"ds_read_b64_tr_b16 a[26:27], v26 offset:1024\n" \
"ds_read_b64_tr_b16 a[28:29], v27 offset:0\n" \
"ds_read_b64_tr_b16 a[30:31], v27 offset:1024\n" \
"s_waitcnt lgkmcnt(0)\n" \
"s_waitcnt vmcnt(2)\n" \
"s_add_u32 m0, %[wbase], 11264\n" \
"s_add_u32 s66, %[so0], 90112\n" \
"buffer_load_dwordx4 %[wvo], %[rw], s66 offen lds\n" \
"s_waitcnt vmcnt(2)\n" \
"s_add_u32 m0, %[wbase], 12288\n" \
"s_add_u32 s66, %[so0], 98304\n" \
"buffer_load_dwordx4 %[wvo], %[rw], s66 offen lds\n" \
"s_waitcnt vmcnt(2)\n" \
"s_add_u32 m0, %[wbase], 13312\n" \
"s_add_u32 s66, %[so0], 106496\n" \
"buffer_load_dwordx4 %[wvo], %[rw], s66 offen lds\n" \
"s_waitcnt vmcnt(2)\n" \
"s_add_u32 m0, %[wbase], 14336\n" \
"s_add_u32 s66, %[so0], 114688\n" \
"buffer_load_dwordx4 %[wvo], %[rw], s66 offen lds\n" \
"s_waitcnt vmcnt(2)\n" \
"s_add_u32 m0, %[wbase], 15360\n" \
"s_add_u32 s66, %[so0], 122880\n" \
"buffer_load_dwordx4 %[wvo], %[rw], s66 offen lds\n" \
"s_waitcnt vmcnt(2)\n" \
"s_add_u32 m0, %[wbase], 16384\n" \
"s_add_u32 s66, %[so0], 131072\n" \
"buffer_load_dwordx4 %[wvo], %[rw], s66 offen lds\n" \
"s_waitcnt vmcnt(2)\n" \
"s_add_u32 m0, %[wbase], 17408\n" \
"s_add_u32 s66, %[so0], 139264\n" \
"buffer_load_dwordx4 %[wvo], %[rw], s66 offen lds\n" \
"s_waitcnt vmcnt(2)\n" \
"s_add_u32 m0, %[wbase], 18432\n" \
"s_add_u32 s66, %[so0], 147456\n" \
"buffer_load_dwordx4 %[wvo], %[rw], s66 offen lds\n" \
"ds_read_b64_tr_b16 a[32:33], v20 offset:8192\n" \
"ds_read_b64_tr_b16 a[34:35], v20 offset:9216\n" \
"ds_read_b64_tr_b16 a[36:37], v21 offset:8192\n" \
"ds_read_b64_tr_b16 a[38:39], v21 offset:9216\n" \
"ds_read_b64_tr_b16 a[40:41], v22 offset:8192\n" \
"ds_read_b64_tr_b16 a[42:43], v22 offset:9216\n" \
"ds_read_b64_tr_b16 a[44:45], v23 offset:8192\n" \
"ds_read_b64_tr_b16 a[46:47], v23 offset:9216\n" \
"ds_read_b64_tr_b16 a[48:49], v24 offset:8192\n" \
"ds_read_b64_tr_b16 a[50:51], v24 offset:9216\n" \
"ds_read_b64_tr_b16 a[52:53], v25 offset:8192\n" \
"ds_read_b64_tr_b16 a[54:55], v25 offset:9216\n" \
"ds_read_b64_tr_b16 a[56:57], v26 offset:8192\n" \
"ds_read_b64_tr_b16 a[58:59], v26 offset:9216\n" \
"ds_read_b64_tr_b16 a[60:61], v27 offset:8192\n" \
"ds_read_b64_tr_b16 a[62:63], v27 offset:9216\n" \
"s_waitcnt lgkmcnt(0)\n" \
"s_waitcnt vmcnt(2)\n" \
"s_add_u32 m0, %[wbase], 19456\n" \
"s_add_u32 s66, %[so0], 155648\n" \
"buffer_load_dwordx4 %[wvo], %[rw], s66 offen lds\n" \
"s_waitcnt vmcnt(2)\n" \
"s_add_u32 m0, %[wbase], 20480\n" \
"s_add_u32 s66, %[so0], 163840\n" \
"buffer_load_dwordx4 %[wvo], %[rw], s66 offen lds\n" \
"s_waitcnt vmcnt(2)\n" \
"s_add_u32 m0, %[wbase], 21504\n" \
"s_add_u32 s66, %[so0], 172032\n" \
"buffer_load_dwordx4 %[wvo], %[rw], s66 offen lds\n" \
"s_waitcnt vmcnt(2)\n" \
"s_add_u32 m0, %[wbase], 22528\n" \
"s_add_u32 s66, %[so0], 180224\n" \
"buffer_load_dwordx4 %[wvo], %[rw], s66 offen lds\n" \
"s_waitcnt vmcnt(2)\n" \
"s_add_u32 m0, %[wbase], 23552\n" \
"s_add_u32 s66, %[so0], 188416\n" \
"buffer_load_dwordx4 %[wvo], %[rw], s66 offen lds\n" \
"s_waitcnt vmcnt(2)\n" \
"s_add_u32 m0, %[wbase], 24576\n" \
"s_add_u32 s66, %[so0], 196608\n" \
"buffer_load_dwordx4 %[wvo], %[rw], s66 offen lds\n" \
"s_waitcnt vmcnt(2)\n" \
"s_add_u32 m0, %[wbase], 25600\n" \
"s_add_u32 s66, %[so0], 204800\n" \
"buffer_load_dwordx4 %[wvo], %[rw], s66 offen lds\n" \
"s_waitcnt vmcnt(2)\n" \
"s_add_u32 m0, %[wbase], 26624\n" \
"s_add_u32 s66, %[so0], 212992\n" \
"buffer_load_dwordx4 %[wvo], %[rw], s66 offen lds\n" \
"ds_read_b64_tr_b16 a[64:65], v20 offset:16384\n" \
"ds_read_b64_tr_b16 a[66:67], v20 offset:17408\n" \
"ds_read_b64_tr_b16 a[68:69], v21 offset:16384\n" \
"ds_read_b64_tr_b16 a[70:71], v21 offset:17408\n" \
"ds_read_b64_tr_b16 a[72:73], v22 offset:16384\n" \
"ds_read_b64_tr_b16 a[74:75], v22 offset:17408\n" \
"ds_read_b64_tr_b16 a[76:77], v23 offset:16384\n" \
"ds_read_b64_tr_b16 a[78:79], v23 offset:17408\n" \
"ds_read_b64_tr_b16 a[80:81], v24 offset:16384\n" \
"ds_read_b64_tr_b16 a[82:83], v24 offset:17408\n" \
"ds_read_b64_tr_b16 a[84:85], v25 offset:16384\n" \
"ds_read_b64_tr_b16 a[86:87], v25 offset:17408\n" \
"ds_read_b64_tr_b16 a[88:89], v26 offset:16384\n" \
"ds_read_b64_tr_b16 a[90:91], v26 offset:17408\n" \
"ds_read_b64_tr_b16 a[92:93], v27 offset:16384\n" \
"ds_read_b64_tr_b16 a[94:95], v27 offset:17408\n" \
"s_waitcnt lgkmcnt(0)\n" \
"s_waitcnt vmcnt(2)\n" \
"s_add_u32 m0, %[wbase], 27648\n" \
"s_add_u32 s66, %[so0], 221184\n" \
"buffer_load_dwordx4 %[wvo], %[rw], s66 offen lds\n" \
"s_waitcnt vmcnt(2)\n" \
"s_add_u32 m0, %[wbase], 28672\n" \
"s_add_u32 s66, %[so0], 229376\n" \
"buffer_load_dwordx4 %[wvo], %[rw], s66 offen lds\n" \
"s_waitcnt vmcnt(2)\n" \
"s_add_u32 m0, %[wbase], 29696\n" \
"s_add_u32 s66, %[so0], 237568\n" \
"buffer_load_dwordx4 %[wvo], %[rw], s66 offen lds\n" \
"s_waitcnt vmcnt(2)\n" \
"s_add_u32 m0, %[wbase], 30720\n" \
"s_add_u32 s66, %[so0], 245760\n" \
"buffer_load_dwordx4 %[wvo], %[rw], s66 offen lds\n" \
"s_waitcnt vmcnt(2)\n" \
"s_add_u32 m0, %[wbase], 31744\n" \
"s_add_u32 s66, %[so0], 253952\n" \
"buffer_load_dwordx4 %[wvo], %[rw], s66 offen lds\n" \
"s_waitcnt vmcnt(2)\n" \
"s_add_u32 m0, %[wbase], 0\n" \
"s_add_u32 s66, %[so0], 262144\n" \
"buffer_load_dwordx4 %[wvo], %[rw], s66 offen lds\n" \
"s_waitcnt vmcnt(2)\n" \
"s_add_u32 m0, %[wbase], 1024\n" \
"s_add_u32 s66, %[so0], 270336\n" \
"buffer_load_dwordx4 %[wvo], %[rw], s66 offen lds\n" \
"s_waitcnt vmcnt(2)\n" \
"s_add_u32 m0, %[wbase], 2048\n" \
"s_add_u32 s66, %[so0], 278528\n" \
"buffer_load_dwordx4 %[wvo], %[rw], s66 offen lds\n" \
"ds_read_b64_tr_b16 a[96:97], v20 offset:24576\n" \
"ds_read_b64_tr_b16 a[98:99], v20 offset:25600\n" \
"ds_read_b64_tr_b16 a[100:101], v21 offset:24576\n" \
"ds_read_b64_tr_b16 a[102:103], v21 offset:25600\n" \
"ds_read_b64_tr_b16 a[104:105], v22 offset:24576\n" \
"ds_read_b64_tr_b16 a[106:107], v22 offset:25600\n" \
"ds_read_b64_tr_b16 a[108:109], v23 offset:24576\n" \
"ds_read_b64_tr_b16 a[110:111], v23 offset:25600\n" \
"ds_read_b64_tr_b16 a[112:113], v24 offset:24576\n" \
"ds_read_b64_tr_b16 a[114:115], v24 offset:25600\n" \
"ds_read_b64_tr_b16 a[116:117], v25 offset:24576\n" \
"ds_read_b64_tr_b16 a[118:119], v25 offset:25600\n" \
"ds_read_b64_tr_b16 a[120:121], v26 offset:24576\n" \
"ds_read_b64_tr_b16 a[122:123], v26 offset:25600\n" \
"ds_read_b64_tr_b16 a[124:125], v27 offset:24576\n" \
"ds_read_b64_tr_b16 a[126:127], v27 offset:25600\n" \
"s_waitcnt lgkmcnt(0)\n" \
"s_waitcnt vmcnt(2)\n" \
"s_add_u32 m0, %[wbase], 3072\n" \
"s_add_u32 s66, %[so0], 286720\n" \
"buffer_load_dwordx4 %[wvo], %[rw], s66 offen lds\n" \
"s_waitcnt vmcnt(2)\n" \
"s_add_u32 m0, %[wbase], 4096\n" \
"s_add_u32 s66, %[so0], 294912\n" \
"buffer_load_dwordx4 %[wvo], %[rw], s66 offen lds\n" \
"s_waitcnt vmcnt(2)\n" \
"s_add_u32 m0, %[wbase], 5120\n" \
"s_add_u32 s66, %[so0], 303104\n" \
"buffer_load_dwordx4 %[wvo], %[rw], s66 offen lds\n" \
"s_waitcnt vmcnt(2)\n" \
"s_add_u32 m0, %[wbase], 6144\n" \
"s_add_u32 s66, %[so0], 311296\n" \
"buffer_load_dwordx4 %[wvo], %[rw], s66 offen lds\n" \
"s_waitcnt vmcnt(2)\n" \
"s_add_u32 m0, %[wbase], 7168\n" \
"s_add_u32 s66, %[so0], 319488\n" \
"buffer_load_dwordx4 %[wvo], %[rw], s66 offen lds\n" \
"s_waitcnt vmcnt(2)\n" \
"s_add_u32 m0, %[wbase], 8192\n" \
"s_add_u32 s66, %[so0], 327680\n" \
"buffer_load_dwordx4 %[wvo], %[rw], s66 offen lds\n" \
"s_waitcnt vmcnt(2)\n" \
"s_add_u32 m0, %[wbase], 9216\n" \
"s_add_u32 s66, %[so0], 335872\n" \
"buffer_load_dwordx4 %[wvo], %[rw], s66 offen lds\n" \
"s_waitcnt vmcnt(2)\n" \
"s_add_u32 m0, %[wbase], 10240\n" \
"s_add_u32 s66, %[so0], 344064\n" \
"buffer_load_dwordx4 %[wvo], %[rw], s66 offen lds\n" \
"ds_read_b64_tr_b16 a[128:129], v20 offset:0\n" \
"ds_read_b64_tr_b16 a[130:131], v20 offset:1024\n" \
"ds_read_b64_tr_b16 a[132:133], v21 offset:0\n" \
"ds_read_b64_tr_b16 a[134:135], v21 offset:1024\n" \
"ds_read_b64_tr_b16 a[136:137], v22 offset:0\n" \
"ds_read_b64_tr_b16 a[138:139], v22 offset:1024\n" \
"ds_read_b64_tr_b16 a[140:141], v23 offset:0\n" \
"ds_read_b64_tr_b16 a[142:143], v23 offset:1024\n" \
"ds_read_b64_tr_b16 a[144:145], v24 offset:0\n" \
"ds_read_b64_tr_b16 a[146:147], v24 offset:1024\n" \
"ds_read_b64_tr_b16 a[148:149], v25 offset:0\n" \
"ds_read_b64_tr_b16 a[150:151], v25 offset:1024\n" \
"ds_read_b64_tr_b16 a[152:153], v26 offset:0\n" \
"ds_read_b64_tr_b16 a[154:155], v26 offset:1024\n" \
"ds_read_b64_tr_b16 a[156:157], v27 offset:0\n" \
"ds_read_b64_tr_b16 a[158:159], v27 offset:1024\n" \
"s_waitcnt lgkmcnt(0)\n" \
"s_waitcnt vmcnt(2)\n" \
"s_add_u32 m0, %[wbase], 11264\n" \
"s_add_u32 s66, %[so0], 352256\n" \
"buffer_load_dwordx4 %[wvo], %[rw], s66 offen lds\n" \
"s_waitcnt vmcnt(2)\n" \
"s_add_u32 m0, %[wbase], 12288\n" \
"s_add_u32 s66, %[so0], 360448\n" \
"buffer_load_dwordx4 %[wvo], %[rw], s66 offen lds\n" \
"s_waitcnt vmcnt(2)\n" \
"s_add_u32 m0, %[wbase], 13312\n" \
"s_add_u32 s66, %[so0], 368640\n" \
"buffer_load_dwordx4 %[wvo], %[rw], s66 offen lds\n" \
"s_waitcnt vmcnt(2)\n" \
"s_add_u32 m0, %[wbase], 14336\n" \
"s_add_u32 s66, %[so0], 376832\n" \
"buffer_load_dwordx4 %[wvo], %[rw], s66 offen lds\n" \
"s_waitcnt vmcnt(2)\n" \
"s_add_u32 m0, %[wbase], 15360\n" \
"s_add_u32 s66, %[so0], 385024\n" \
"buffer_load_dwordx4 %[wvo], %[rw], s66 offen lds\n" \
"s_waitcnt vmcnt(2)\n" \
"s_add_u32 m0, %[wbase], 16384\n" \
"s_add_u32 s66, %[so0], 393216\n" \
"buffer_load_dwordx4 %[wvo], %[rw], s66 offen lds\n" \
"s_waitcnt vmcnt(2)\n" \
"s_add_u32 m0, %[wbase], 17408\n" \
"s_add_u32 s66, %[so0], 401408\n" \
"buffer_load_dwordx4 %[wvo], %[rw], s66 offen lds\n" \
"s_waitcnt vmcnt(2)\n" \
"s_add_u32 m0, %[wbase], 18432\n" \
"s_add_u32 s66, %[so0], 409600\n" \
"buffer_load_dwordx4 %[wvo], %[rw], s66 offen lds\n" \
"ds_read_b64_tr_b16 a[160:161], v20 offset:8192\n" \
"ds_read_b64_tr_b16 a[162:163], v20 offset:9216\n" \
"ds_read_b64_tr_b16 a[164:165], v21 offset:8192\n" \
"ds_read_b64_tr_b16 a[166:167], v21 offset:9216\n" \
"ds_read_b64_tr_b16 a[168:169], v22 offset:8192\n" \
"ds_read_b64_tr_b16 a[170:171], v22 offset:9216\n" \
"ds_read_b64_tr_b16 a[172:173], v23 offset:8192\n" \
"ds_read_b64_tr_b16 a[174:175], v23 offset:9216\n" \
"ds_read_b64_tr_b16 a[176:177], v24 offset:8192\n" \
"ds_read_b64_tr_b16 a[178:179], v24 offset:9216\n" \
"ds_read_b64_tr_b16 a[180:181], v25 offset:8192\n" \
"ds_read_b64_tr_b16 a[182:183], v25 offset:9216\n" \
"ds_read_b64_tr_b16 a[184:185], v26 offset:8192\n" \
"ds_read_b64_tr_b16 a[186:187], v26 offset:9216\n" \
"ds_read_b64_tr_b16 a[188:189], v27 offset:8192\n" \
"ds_read_b64_tr_b16 a[190:191], v27 offset:9216\n" \
"s_waitcnt lgkmcnt(0)\n" \
"s_waitcnt vmcnt(2)\n" \
"s_add_u32 m0, %[wbase], 19456\n" \
"s_add_u32 s66, %[so0], 417792\n" \
"buffer_load_dwordx4 %[wvo], %[rw], s66 offen lds\n" \
"s_waitcnt vmcnt(2)\n" \
"s_add_u32 m0, %[wbase], 20480\n" \
"s_add_u32 s66, %[so0], 425984\n" \
"buffer_load_dwordx4 %[wvo], %[rw], s66 offen lds\n" \
"s_waitcnt vmcnt(2)\n" \
"s_add_u32 m0, %[wbase], 21504\n" \
"s_add_u32 s66, %[so0], 434176\n" \
"buffer_load_dwordx4 %[wvo], %[rw], s66 offen lds\n" \
"s_waitcnt vmcnt(2)\n" \
"s_add_u32 m0, %[wbase], 22528\n" \
"s_add_u32 s66, %[so0], 442368\n" \
"buffer_load_dwordx4 %[wvo], %[rw], s66 offen lds\n" \
"s_waitcnt vmcnt(2)\n" \
"s_add_u32 m0, %[wbase], 23552\n" \
"s_add_u32 s66, %[so0], 450560\n" \
"buffer_load_dwordx4 %[wvo], %[rw], s66 offen lds\n" \
"s_waitcnt vmcnt(0)\n" \
"ds_read_b64_tr_b16 a[192:193], v20 offset:16384\n" \
"ds_read_b64_tr_b16 a[194:195], v20 offset:17408\n" \
"ds_read_b64_tr_b16 a[196:197], v21 offset:16384\n" \
"ds_read_b64_tr_b16 a[198:199], v21 offset:17408\n" \
"ds_read_b64_tr_b16 a[200:201], v22 offset:16384\n" \
"ds_read_b64_tr_b16 a[202:203], v22 offset:17408\n" \
"ds_read_b64_tr_b16 a[204:205], v23 offset:16384\n" \
"ds_read_b64_tr_b16 a[206:207], v23 offset:17408\n" \
"ds_read_b64_tr_b16 a[208:209], v24 offset:16384\n" \
"ds_read_b64_tr_b16 a[210:211], v24 offset:17408\n" \
"ds_read_b64_tr_b16 a[212:213], v25 offset:16384\n" \
"ds_read_b64_tr_b16 a[214:215], v25 offset:17408\n" \
"ds_read_b64_tr_b16 a[216:217], v26 offset:16384\n" \
"ds_read_b64_tr_b16 a[218:219], v26 offset:17408\n" \
"ds_read_b64_tr_b16 a[220:221], v27 offset:16384\n" \
"ds_read_b64_tr_b16 a[222:223], v27 offset:17408\n" \
"s_waitcnt lgkmcnt(0)\n" \
"v_xor_b32 v212, 8192, %[xr]\n" \
"v_xor_b32 v213, 16384, %[xr]\n" \
"v_xor_b32 v214, 24576, %[xr]\n" \
"s_barrier\n" \
"s_add_u32 s81, %[dst], 13369344\n" \
"v_mov_b32 v220, -1\n" \
"v_mov_b32 v221, -1\n" \
"v_mov_b32 v222, -1\n" \
"v_mov_b32 v223, -1\n" \
"s_mov_b32 s80, 128\n" \
"s_mov_b32 s65, 25\n" \
"Lw4_poi_%=:\n" \
"buffer_store_dwordx4 v[220:223], %[so], %[ws], s81 offen sc1\n" \
"s_waitcnt vmcnt(2)\n" \
"s_add_u32 s81, s81, 131072\n" \
"s_sub_u32 s65, s65, 1\n" \
"s_cbranch_scc0 Lw4_poi_%=\n" \
"s_waitcnt vmcnt(0)\n" \
"s_mov_b32 s64, 262144\n" \
"s_mov_b32 s65, 0\n" \
"s_mov_b32 s74, 1\n" \
"s_mov_b32 s69, 1073741824\n" \
"s_mov_b32 s67, %[src]\n" \
"s_mov_b32 s68, %[dst]\n" \
"v_mov_b32 v202, 0\n" \
"v_mov_b32 v203, 0\n" \
"Lw4_top0_%=:\n" \
"v_cmp_gt_u32 vcc, s74, v202\n" \
"s_mov_b32 s72, s67\n" \
"s_add_u32 s73, s67, 4096\n" \
"s_cbranch_vccnz Lw4_sgo0_%=\n" \
"Lw4_go0_%=:\n" \
"buffer_load_dwordx4 v[20:23], %[lo], %[ws], s72 offen offset:0 sc1\n" \
"buffer_load_dwordx4 v[24:27], %[lo], %[ws], s72 offen offset:1024 sc1\n" \
"buffer_load_dwordx4 v[28:31], %[lo], %[ws], s72 offen offset:2048 sc1\n" \
"buffer_load_dwordx4 v[32:35], %[lo], %[ws], s72 offen offset:3072 sc1\n" \
"buffer_load_dwordx4 v[36:39], %[lo], %[ws], s73 offen offset:0 sc1\n" \
"buffer_load_dwordx4 v[40:43], %[lo], %[ws], s73 offen offset:1024 sc1\n" \
"buffer_load_dwordx4 v[44:47], %[lo], %[ws], s73 offen offset:2048 sc1\n" \
"s_waitcnt vmcnt(7)\n" \
"buffer_store_dword v203, %[fo], %[ws], s69 offen sc1\n" \
"s_mov_b32 s69, 1073741824\n" \
"Lw4_body0_%=:\n" \
"s_waitcnt vmcnt(7)\n" \
"v_mfma_f32_16x16x32_bf16 v[108:111], a[8:11], v[20:23], 0\n" \
"s_nop 2\n" \
"v_mfma_f32_16x16x32_bf16 v[112:115], a[12:15], v[20:23], 0\n" \
"s_nop 2\n" \
"v_mfma_f32_16x16x32_bf16 v[116:119], a[16:19], v[20:23], 0\n" \
"s_add_u32 s74, s74, 1\n" \
"s_add_u32 s67, s67, 114688\n" \
"s_nop 0\n" \
"v_mfma_f32_16x16x32_bf16 v[120:123], a[20:23], v[20:23], 0\n" \
"s_add_u32 s65, s65, 1\n" \
"s_nop 1\n" \
"v_mfma_f32_16x16x32_bf16 v[124:127], a[24:27], v[20:23], 0\n" \
"s_cmp_gt_u32 s65, 104\n" \
"s_cselect_b32 s79, 1073741824, 0\n" \
"s_cselect_b32 s69, 0, s69\n" \
"v_mfma_f32_16x16x32_bf16 v[128:131], a[28:31], v[20:23], 0\n" \
"s_waitcnt vmcnt(6)\n" \
"s_addc_u32 s66, s65, -1\n" \
"s_nop 0\n" \
"v_mfma_f32_16x16x32_bf16 v[108:111], a[40:43], v[24:27], v[108:111]\n" \
"s_nop 0\n" \
"buffer_load_dword v202, %[fvo], %[ws], 0 offen sc1\n" \
"v_mov_b32 v203, s66\n" \
"v_mfma_f32_16x16x32_bf16 v[112:115], a[44:47], v[24:27], v[112:115]\n" \
"s_nop 2\n" \
"v_mfma_f32_16x16x32_bf16 v[116:119], a[48:51], v[24:27], v[116:119]\n" \
"s_nop 2\n" \
"v_mfma_f32_16x16x32_bf16 v[120:123], a[52:55], v[24:27], v[120:123]\n" \
"s_nop 2\n" \
"v_mfma_f32_16x16x32_bf16 v[124:127], a[56:59], v[24:27], v[124:127]\n" \
"s_nop 2\n" \
"v_mfma_f32_16x16x32_bf16 v[128:131], a[60:63], v[24:27], v[128:131]\n" \
"s_waitcnt vmcnt(6)\n" \
"s_nop 1\n" \
"v_mfma_f32_16x16x32_bf16 v[108:111], a[72:75], v[28:31], v[108:111]\n" \
"s_nop 2\n" \
"v_mfma_f32_16x16x32_bf16 v[112:115], a[76:79], v[28:31], v[112:115]\n" \
"s_nop 2\n" \
"v_mfma_f32_16x16x32_bf16 v[116:119], a[80:83], v[28:31], v[116:119]\n" \
"s_nop 2\n" \
"v_mfma_f32_16x16x32_bf16 v[120:123], a[84:87], v[28:31], v[120:123]\n" \
"s_nop 2\n" \
"v_mfma_f32_16x16x32_bf16 v[124:127], a[88:91], v[28:31], v[124:127]\n" \
"s_nop 2\n" \
"v_mfma_f32_16x16x32_bf16 v[128:131], a[92:95], v[28:31], v[128:131]\n" \
"s_waitcnt vmcnt(5)\n" \
"s_nop 1\n" \
"v_mfma_f32_16x16x32_bf16 v[108:111], a[104:107], v[32:35], v[108:111]\n" \
"s_nop 2\n" \
"v_mfma_f32_16x16x32_bf16 v[112:115], a[108:111], v[32:35], v[112:115]\n" \
"s_nop 2\n" \
"v_mfma_f32_16x16x32_bf16 v[116:119], a[112:115], v[32:35], v[116:119]\n" \
"s_nop 2\n" \
"v_mfma_f32_16x16x32_bf16 v[120:123], a[116:119], v[32:35], v[120:123]\n" \
"s_nop 2\n" \
"v_mfma_f32_16x16x32_bf16 v[124:127], a[120:123], v[32:35], v[124:127]\n" \
"s_nop 2\n" \
"v_mfma_f32_16x16x32_bf16 v[128:131], a[124:127], v[32:35], v[128:131]\n" \
"s_waitcnt vmcnt(4)\n" \
"s_nop 1\n" \
"v_mfma_f32_16x16x32_bf16 v[108:111], a[136:139], v[36:39], v[108:111]\n" \
"s_waitcnt vmcnt(3)\n" \
"s_nop 1\n" \
"v_mfma_f32_16x16x32_bf16 v[108:111], a[168:171], v[40:43], v[108:111]\n" \
"s_waitcnt vmcnt(2)\n" \
"s_nop 1\n" \
"v_mfma_f32_16x16x32_bf16 v[108:111], a[200:203], v[44:47], v[108:111]\n" \
"s_nop 2\n" \
"v_mfma_f32_16x16x32_bf16 v[112:115], a[140:143], v[36:39], v[112:115]\n" \
"s_nop 2\n" \
"v_mfma_f32_16x16x32_bf16 v[112:115], a[172:175], v[40:43], v[112:115]\n" \
"s_nop 0\n" \
"ds_write_b128 v212, v[108:111] offset:2048\n" \
"v_mfma_f32_16x16x32_bf16 v[112:115], a[204:207], v[44:47], v[112:115]\n" \
"s_nop 0\n" \
"v_cmp_u_f32 vcc, v108, v108\n" \
"v_mfma_f32_16x16x32_bf16 v[116:119], a[144:147], v[36:39], v[116:119]\n" \
"s_cbranch_vccnz Lw4_sn0_%=\n" \
"Lw4_n0_%=:\n" \
"v_mfma_f32_16x16x32_bf16 v[116:119], a[176:179], v[40:43], v[116:119]\n" \
"s_nop 0\n" \
"s_nop 1\n" \
"ds_write_b128 v212, v[112:115] offset:3072\n" \
"v_mfma_f32_16x16x32_bf16 v[116:119], a[208:211], v[44:47], v[116:119]\n" \
"s_nop 2\n" \
"v_mfma_f32_16x16x32_bf16 v[120:123], a[148:151], v[36:39], v[120:123]\n" \
"s_nop 2\n" \
"v_mfma_f32_16x16x32_bf16 v[120:123], a[180:183], v[40:43], v[120:123]\n" \
"s_nop 0\n" \
"ds_write_b128 v213, v[116:119] offset:4096\n" \
"v_mfma_f32_16x16x32_bf16 v[120:123], a[212:215], v[44:47], v[120:123]\n" \
"s_waitcnt vmcnt(0)\n" \
"v_cmp_gt_u32 vcc, s74, v202\n" \
"v_mfma_f32_16x16x32_bf16 v[124:127], a[152:155], v[36:39], v[124:127]\n" \
"s_nop 0\n" \
"buffer_load_dword v202, %[fvo], %[ws], 0 offen sc1\n" \
"buffer_store_dword v203, %[fo], %[ws], s69 offen sc1\n" \
"v_mfma_f32_16x16x32_bf16 v[124:127], a[184:187], v[40:43], v[124:127]\n" \
"s_cmp_eq_u64 vcc, 0\n" \
"ds_write_b128 v213, v[120:123] offset:5120\n" \
"v_mfma_f32_16x16x32_bf16 v[124:127], a[216:219], v[44:47], v[124:127]\n" \
"s_cselect_b32 s72, s67, 1073741824\n" \
"buffer_load_dwordx4 v[52:55], %[lo], %[ws], s72 offen offset:0 sc1\n" \
"v_mfma_f32_16x16x32_bf16 v[128:131], a[156:159], v[36:39], v[128:131]\n" \
"s_add_u32 s73, s72, 4096\n" \
"s_nop 1\n" \
"v_mfma_f32_16x16x32_bf16 v[128:131], a[188:191], v[40:43], v[128:131]\n" \
"s_nop 0\n" \
"ds_write_b128 v214, v[124:127] offset:6144\n" \
"v_mfma_f32_16x16x32_bf16 v[128:131], a[220:223], v[44:47], v[128:131]\n" \
"s_waitcnt lgkmcnt(1)\n" \
"s_barrier\n" \
"v_mfma_f32_16x16x32_bf16 v[100:103], a[0:3], v[20:23], 0\n" \
"s_nop 0\n" \
"ds_read_b128 v[132:135], %[xr] offset:2048\n" \
"buffer_load_dwordx4 v[56:59], %[lo], %[ws], s72 offen offset:1024 sc1\n" \
"v_mfma_f32_16x16x32_bf16 v[104:107], a[4:7], v[20:23], 0\n" \
"s_nop 0\n" \
"ds_write_b128 v214, v[128:131] offset:7168\n" \
"v_mfma_f32_16x16x32_bf16 v[100:103], a[32:35], v[24:27], v[100:103]\n" \
"s_nop 0\n" \
"ds_read_b128 v[136:139], %[xr] offset:3072\n" \
"v_mfma_f32_16x16x32_bf16 v[104:107], a[36:39], v[24:27], v[104:107]\n" \
"s_nop 0\n" \
"ds_read_b128 v[140:143], %[xr] offset:4096\n" \
"v_mfma_f32_16x16x32_bf16 v[100:103], a[64:67], v[28:31], v[100:103]\n" \
"s_nop 0\n" \
"ds_read_b128 v[144:147], %[xr] offset:5120\n" \
"buffer_load_dwordx4 v[60:63], %[lo], %[ws], s72 offen offset:2048 sc1\n" \
"v_mfma_f32_16x16x32_bf16 v[104:107], a[68:71], v[28:31], v[104:107]\n" \
"s_waitcnt lgkmcnt(3)\n" \
"s_barrier\n" \
"v_mfma_f32_16x16x32_bf16 v[100:103], a[96:99], v[32:35], v[100:103]\n" \
"s_nop 0\n" \
"ds_read_b128 v[148:151], %[xr] offset:6144\n" \
"v_mfma_f32_16x16x32_bf16 v[104:107], a[100:103], v[32:35], v[104:107]\n" \
"s_nop 0\n" \
"ds_read_b128 v[152:155], %[xr] offset:7168\n" \
"v_mfma_f32_16x16x32_bf16 v[100:103], a[128:131], v[36:39], v[100:103]\n" \
"s_waitcnt lgkmcnt(3)\n" \
"buffer_load_dwordx4 v[64:67], %[lo], %[ws], s72 offen offset:3072 sc1\n" \
"v_add_f32 v132, v132, v140\n" \
"v_add_f32 v133, v133, v141\n" \
"v_mfma_f32_16x16x32_bf16 v[104:107], a[132:135], v[36:39], v[104:107]\n" \
"s_nop 0\n" \
"v_add_f32 v134, v134, v142\n" \
"v_add_f32 v135, v135, v143\n" \
"v_mfma_f32_16x16x32_bf16 v[100:103], a[160:163], v[40:43], v[100:103]\n" \
"s_waitcnt lgkmcnt(2)\n" \
"v_add_f32 v136, v136, v144\n" \
"v_add_f32 v137, v137, v145\n" \
"v_mfma_f32_16x16x32_bf16 v[104:107], a[164:167], v[40:43], v[104:107]\n" \
"s_nop 0\n" \
"v_add_f32 v138, v138, v146\n" \
"v_add_f32 v139, v139, v147\n" \
"v_mfma_f32_16x16x32_bf16 v[100:103], a[192:195], v[44:47], v[100:103]\n" \
"s_waitcnt lgkmcnt(1)\n" \
"buffer_load_dwordx4 v[68:71], %[lo], %[ws], s73 offen offset:0 sc1\n" \
"v_add_f32 v132, v132, v148\n" \
"v_add_f32 v133, v133, v149\n" \
"v_mfma_f32_16x16x32_bf16 v[104:107], a[196:199], v[44:47], v[104:107]\n" \
"s_nop 0\n" \
"v_add_f32 v134, v134, v150\n" \
"v_add_f32 v135, v135, v151\n" \
"s_waitcnt lgkmcnt(0)\n" \
"v_add_f32 v136, v136, v152\n" \
"v_add_f32 v137, v137, v153\n" \
"v_add_f32 v138, v138, v154\n" \
"v_add_f32 v139, v139, v155\n" \
"v_mov_b32 v203, s65\n" \
"s_mov_b32 s69, s79\n" \
"buffer_load_dwordx4 v[72:75], %[lo], %[ws], s73 offen offset:1024 sc1\n" \
"v_pk_add_f32 v[100:101], v[100:101], v[132:133]\n" \
"v_pk_add_f32 v[102:103], v[102:103], v[134:135]\n" \
"v_pk_add_f32 v[104:105], v[104:105], v[136:137]\n" \
"v_pk_add_f32 v[106:107], v[106:107], v[138:139]\n" \
"buffer_load_dwordx4 v[76:79], %[lo], %[ws], s73 offen offset:2048 sc1\n" \
"v_cvt_pk_bf16_f32 v204, v100, v101\n" \
"v_cvt_pk_bf16_f32 v205, v102, v103\n" \
"v_cvt_pk_bf16_f32 v206, v104, v105\n" \
"v_cvt_pk_bf16_f32 v207, v106, v107\n" \
"buffer_store_dwordx4 v[204:207], %[so], %[ws], s68 offen sc1\n" \
"s_add_u32 s68, s68, 131072\n" \
"s_cmp_eq_u32 s72, s67\n" \
"s_cbranch_scc1 Lw4_body1_%=\n" \
"s_waitcnt vmcnt(9)\n" \
"Lw4_top1_%=:\n" \
"v_cmp_gt_u32 vcc, s74, v202\n" \
"s_mov_b32 s72, s67\n" \
"s_add_u32 s73, s67, 4096\n" \
"s_cbranch_vccnz Lw4_sgo1_%=\n" \
"Lw4_go1_%=:\n" \
"buffer_load_dwordx4 v[52:55], %[lo], %[ws], s72 offen offset:0 sc1\n" \
"buffer_load_dwordx4 v[56:59], %[lo], %[ws], s72 offen offset:1024 sc1\n" \
"buffer_load_dwordx4 v[60:63], %[lo], %[ws], s72 offen offset:2048 sc1\n" \
"buffer_load_dwordx4 v[64:67], %[lo], %[ws], s72 offen offset:3072 sc1\n" \
"buffer_load_dwordx4 v[68:71], %[lo], %[ws], s73 offen offset:0 sc1\n" \
"buffer_load_dwordx4 v[72:75], %[lo], %[ws], s73 offen offset:1024 sc1\n" \
"buffer_load_dwordx4 v[76:79], %[lo], %[ws], s73 offen offset:2048 sc1\n" \
"s_waitcnt vmcnt(7)\n" \
"buffer_store_dword v203, %[fo], %[ws], s69 offen sc1\n" \
"s_mov_b32 s69, 1073741824\n" \
"Lw4_body1_%=:\n" \
"s_waitcnt vmcnt(7)\n" \
"v_mfma_f32_16x16x32_bf16 v[108:111], a[8:11], v[52:55], 0\n" \
"s_nop 2\n" \
"v_mfma_f32_16x16x32_bf16 v[112:115], a[12:15], v[52:55], 0\n" \
"s_nop 2\n" \
"v_mfma_f32_16x16x32_bf16 v[116:119], a[16:19], v[52:55], 0\n" \
"s_add_u32 s74, s74, 1\n" \
"s_add_u32 s67, s67, 114688\n" \
"s_nop 0\n" \
"v_mfma_f32_16x16x32_bf16 v[120:123], a[20:23], v[52:55], 0\n" \
"s_add_u32 s65, s65, 1\n" \
"s_nop 1\n" \
"v_mfma_f32_16x16x32_bf16 v[124:127], a[24:27], v[52:55], 0\n" \
"s_cmp_gt_u32 s65, 104\n" \
"s_cselect_b32 s79, 1073741824, 0\n" \
"s_cselect_b32 s69, 0, s69\n" \
"v_mfma_f32_16x16x32_bf16 v[128:131], a[28:31], v[52:55], 0\n" \
"s_waitcnt vmcnt(6)\n" \
"s_addc_u32 s66, s65, -1\n" \
"s_nop 0\n" \
"v_mfma_f32_16x16x32_bf16 v[108:111], a[40:43], v[56:59], v[108:111]\n" \
"s_nop 0\n" \
"buffer_load_dword v202, %[fvo], %[ws], 0 offen sc1\n" \
"v_mov_b32 v203, s66\n" \
"v_mfma_f32_16x16x32_bf16 v[112:115], a[44:47], v[56:59], v[112:115]\n" \
"s_nop 2\n" \
"v_mfma_f32_16x16x32_bf16 v[116:119], a[48:51], v[56:59], v[116:119]\n" \
"s_nop 2\n" \
"v_mfma_f32_16x16x32_bf16 v[120:123], a[52:55], v[56:59], v[120:123]\n" \
"s_nop 2\n" \
"v_mfma_f32_16x16x32_bf16 v[124:127], a[56:59], v[56:59], v[124:127]\n" \
"s_nop 2\n" \
"v_mfma_f32_16x16x32_bf16 v[128:131], a[60:63], v[56:59], v[128:131]\n" \
"s_waitcnt vmcnt(6)\n" \
"s_nop 1\n" \
"v_mfma_f32_16x16x32_bf16 v[108:111], a[72:75], v[60:63], v[108:111]\n" \
"s_nop 2\n" \
"v_mfma_f32_16x16x32_bf16 v[112:115], a[76:79], v[60:63], v[112:115]\n" \
"s_nop 2\n" \
"v_mfma_f32_16x16x32_bf16 v[116:119], a[80:83], v[60:63], v[116:119]\n" \
"s_nop 2\n" \
"v_mfma_f32_16x16x32_bf16 v[120:123], a[84:87], v[60:63], v[120:123]\n" \
"s_nop 2\n" \
"v_mfma_f32_16x16x32_bf16 v[124:127], a[88:91], v[60:63], v[124:127]\n" \
"s_nop 2\n" \
"v_mfma_f32_16x16x32_bf16 v[128:131], a[92:95], v[60:63], v[128:131]\n" \
"s_waitcnt vmcnt(5)\n" \
"s_nop 1\n" \
"v_mfma_f32_16x16x32_bf16 v[108:111], a[104:107], v[64:67], v[108:111]\n" \
"s_nop 2\n" \
"v_mfma_f32_16x16x32_bf16 v[112:115], a[108:111], v[64:67], v[112:115]\n" \
"s_nop 2\n" \
"v_mfma_f32_16x16x32_bf16 v[116:119], a[112:115], v[64:67], v[116:119]\n" \
"s_nop 2\n" \
"v_mfma_f32_16x16x32_bf16 v[120:123], a[116:119], v[64:67], v[120:123]\n" \
"s_nop 2\n" \
"v_mfma_f32_16x16x32_bf16 v[124:127], a[120:123], v[64:67], v[124:127]\n" \
"s_nop 2\n" \
"v_mfma_f32_16x16x32_bf16 v[128:131], a[124:127], v[64:67], v[128:131]\n" \
"s_waitcnt vmcnt(4)\n" \
"s_nop 1\n" \
"v_mfma_f32_16x16x32_bf16 v[108:111], a[136:139], v[68:71], v[108:111]\n" \
"s_waitcnt vmcnt(3)\n" \
"s_nop 1\n" \
"v_mfma_f32_16x16x32_bf16 v[108:111], a[168:171], v[72:75], v[108:111]\n" \
"s_waitcnt vmcnt(2)\n" \
"s_nop 1\n" \
"v_mfma_f32_16x16x32_bf16 v[108:111], a[200:203], v[76:79], v[108:111]\n" \
"s_nop 2\n" \
"v_mfma_f32_16x16x32_bf16 v[112:115], a[140:143], v[68:71], v[112:115]\n" \
"s_nop 2\n" \
"v_mfma_f32_16x16x32_bf16 v[112:115], a[172:175], v[72:75], v[112:115]\n" \
"s_nop 0\n" \
"ds_write_b128 v212, v[108:111] offset:34816\n" \
"v_mfma_f32_16x16x32_bf16 v[112:115], a[204:207], v[76:79], v[112:115]\n" \
"s_nop 0\n" \
"v_cmp_u_f32 vcc, v108, v108\n" \
"v_mfma_f32_16x16x32_bf16 v[116:119], a[144:147], v[68:71], v[116:119]\n" \
"s_cbranch_vccnz Lw4_sn1_%=\n" \
"Lw4_n1_%=:\n" \
"v_mfma_f32_16x16x32_bf16 v[116:119], a[176:179], v[72:75], v[116:119]\n" \
"s_nop 0\n" \
"s_nop 1\n" \
"ds_write_b128 v212, v[112:115] offset:35840\n" \
"v_mfma_f32_16x16x32_bf16 v[116:119], a[208:211], v[76:79], v[116:119]\n" \
"s_nop 2\n" \
"v_mfma_f32_16x16x32_bf16 v[120:123], a[148:151], v[68:71], v[120:123]\n" \
"s_nop 2\n" \
"v_mfma_f32_16x16x32_bf16 v[120:123], a[180:183], v[72:75], v[120:123]\n" \
"s_nop 0\n" \
"ds_write_b128 v213, v[116:119] offset:36864\n" \
"v_mfma_f32_16x16x32_bf16 v[120:123], a[212:215], v[76:79], v[120:123]\n" \
"s_waitcnt vmcnt(0)\n" \
"v_cmp_gt_u32 vcc, s74, v202\n" \
"v_mfma_f32_16x16x32_bf16 v[124:127], a[152:155], v[68:71], v[124:127]\n" \
"s_nop 0\n" \
"buffer_load_dword v202, %[fvo], %[ws], 0 offen sc1\n" \
"buffer_store_dword v203, %[fo], %[ws], s69 offen sc1\n" \
"v_mfma_f32_16x16x32_bf16 v[124:127], a[184:187], v[72:75], v[124:127]\n" \
"s_cmp_eq_u64 vcc, 0\n" \
"ds_write_b128 v213, v[120:123] offset:37888\n" \
"v_mfma_f32_16x16x32_bf16 v[124:127], a[216:219], v[76:79], v[124:127]\n" \
"s_cselect_b32 s72, s67, 1073741824\n" \
"buffer_load_dwordx4 v[20:23], %[lo], %[ws], s72 offen offset:0 sc1\n" \
"v_mfma_f32_16x16x32_bf16 v[128:131], a[156:159], v[68:71], v[128:131]\n" \
"s_add_u32 s73, s72, 4096\n" \
"s_nop 1\n" \
"v_mfma_f32_16x16x32_bf16 v[128:131], a[188:191], v[72:75], v[128:131]\n" \
"s_nop 0\n" \
"ds_write_b128 v214, v[124:127] offset:38912\n" \
"v_mfma_f32_16x16x32_bf16 v[128:131], a[220:223], v[76:79], v[128:131]\n" \
"s_waitcnt lgkmcnt(1)\n" \
"s_barrier\n" \
"v_mfma_f32_16x16x32_bf16 v[100:103], a[0:3], v[52:55], 0\n" \
"s_nop 0\n" \
"ds_read_b128 v[132:135], %[xr] offset:34816\n" \
"buffer_load_dwordx4 v[24:27], %[lo], %[ws], s72 offen offset:1024 sc1\n" \
"v_mfma_f32_16x16x32_bf16 v[104:107], a[4:7], v[52:55], 0\n" \
"s_nop 0\n" \
"ds_write_b128 v214, v[128:131] offset:39936\n" \
"v_mfma_f32_16x16x32_bf16 v[100:103], a[32:35], v[56:59], v[100:103]\n" \
"s_nop 0\n" \
"ds_read_b128 v[136:139], %[xr] offset:35840\n" \
"v_mfma_f32_16x16x32_bf16 v[104:107], a[36:39], v[56:59], v[104:107]\n" \
"s_nop 0\n" \
"ds_read_b128 v[140:143], %[xr] offset:36864\n" \
"v_mfma_f32_16x16x32_bf16 v[100:103], a[64:67], v[60:63], v[100:103]\n" \
"s_nop 0\n" \
"ds_read_b128 v[144:147], %[xr] offset:37888\n" \
"buffer_load_dwordx4 v[28:31], %[lo], %[ws], s72 offen offset:2048 sc1\n" \
"v_mfma_f32_16x16x32_bf16 v[104:107], a[68:71], v[60:63], v[104:107]\n" \
"s_waitcnt lgkmcnt(3)\n" \
"s_barrier\n" \
"v_mfma_f32_16x16x32_bf16 v[100:103], a[96:99], v[64:67], v[100:103]\n" \
"s_nop 0\n" \
"ds_read_b128 v[148:151], %[xr] offset:38912\n" \
"v_mfma_f32_16x16x32_bf16 v[104:107], a[100:103], v[64:67], v[104:107]\n" \
"s_nop 0\n" \
"ds_read_b128 v[152:155], %[xr] offset:39936\n" \
"v_mfma_f32_16x16x32_bf16 v[100:103], a[128:131], v[68:71], v[100:103]\n" \
"s_waitcnt lgkmcnt(3)\n" \
"buffer_load_dwordx4 v[32:35], %[lo], %[ws], s72 offen offset:3072 sc1\n" \
"v_add_f32 v132, v132, v140\n" \
"v_add_f32 v133, v133, v141\n" \
"v_mfma_f32_16x16x32_bf16 v[104:107], a[132:135], v[68:71], v[104:107]\n" \
"s_nop 0\n" \
"v_add_f32 v134, v134, v142\n" \
"v_add_f32 v135, v135, v143\n" \
"v_mfma_f32_16x16x32_bf16 v[100:103], a[160:163], v[72:75], v[100:103]\n" \
"s_waitcnt lgkmcnt(2)\n" \
"v_add_f32 v136, v136, v144\n" \
"v_add_f32 v137, v137, v145\n" \
"v_mfma_f32_16x16x32_bf16 v[104:107], a[164:167], v[72:75], v[104:107]\n" \
"s_nop 0\n" \
"v_add_f32 v138, v138, v146\n" \
"v_add_f32 v139, v139, v147\n" \
"v_mfma_f32_16x16x32_bf16 v[100:103], a[192:195], v[76:79], v[100:103]\n" \
"s_waitcnt lgkmcnt(1)\n" \
"buffer_load_dwordx4 v[36:39], %[lo], %[ws], s73 offen offset:0 sc1\n" \
"v_add_f32 v132, v132, v148\n" \
"v_add_f32 v133, v133, v149\n" \
"v_mfma_f32_16x16x32_bf16 v[104:107], a[196:199], v[76:79], v[104:107]\n" \
"s_nop 0\n" \
"v_add_f32 v134, v134, v150\n" \
"v_add_f32 v135, v135, v151\n" \
"s_waitcnt lgkmcnt(0)\n" \
"v_add_f32 v136, v136, v152\n" \
"v_add_f32 v137, v137, v153\n" \
"v_add_f32 v138, v138, v154\n" \
"v_add_f32 v139, v139, v155\n" \
"v_mov_b32 v203, s65\n" \
"s_mov_b32 s69, s79\n" \
"buffer_load_dwordx4 v[40:43], %[lo], %[ws], s73 offen offset:1024 sc1\n" \
"v_pk_add_f32 v[100:101], v[100:101], v[132:133]\n" \
"v_pk_add_f32 v[102:103], v[102:103], v[134:135]\n" \
"v_pk_add_f32 v[104:105], v[104:105], v[136:137]\n" \
"v_pk_add_f32 v[106:107], v[106:107], v[138:139]\n" \
"buffer_load_dwordx4 v[44:47], %[lo], %[ws], s73 offen offset:2048 sc1\n" \
"v_cvt_pk_bf16_f32 v204, v100, v101\n" \
"v_cvt_pk_bf16_f32 v205, v102, v103\n" \
"v_cvt_pk_bf16_f32 v206, v104, v105\n" \
"v_cvt_pk_bf16_f32 v207, v106, v107\n" \
"buffer_store_dwordx4 v[204:207], %[so], %[ws], s68 offen sc1\n" \
"s_add_u32 s68, s68, 131072\n" \
"s_cmp_eq_u32 s65, s80\n" \
"s_cbranch_scc1 Lw4_end_%=\n" \
"Lw4_on_%=:\n" \
"s_cmp_eq_u32 s72, s67\n" \
"s_cbranch_scc1 Lw4_body0_%=\n" \
"s_waitcnt vmcnt(9)\n" \
"s_branch Lw4_top0_%=\n" \
"Lw4_end_%=:\n" \
"s_endpgm\n" \
"Lw4_sgo0_%=:\n" \
"s_waitcnt vmcnt(0)\n" \
"buffer_load_dword v202, %[fvo], %[ws], 0 offen sc1\n" \
"buffer_store_dword v203, %[fo], %[ws], s69 offen sc1\n" \
"s_mov_b32 s69, 1073741824\n" \
"s_waitcnt vmcnt(1)\n" \
"s_branch Lw4_cgo0_%=\n" \
"Lw4_pgo0_%=:\n" \
"s_sub_u32 s64, s64, 1\n" \
"s_cbranch_scc1 Lw4_dead_%=\n" \
"buffer_load_dword v202, %[fvo], %[ws], 0 offen sc1\n" \
"s_waitcnt vmcnt(0)\n" \
"Lw4_cgo0_%=:\n" \
"v_cmp_gt_u32 vcc, s74, v202\n" \
"s_cbranch_vccz Lw4_go0_%=\n" \
"s_sleep 1\n" \
"s_branch Lw4_pgo0_%=\n" \
"Lw4_sn0_%=:\n" \
"v_max3_u32 v215, v20, v21, v22\n" \
"v_max3_u32 v215, v215, v23, v24\n" \
"v_max3_u32 v215, v215, v25, v26\n" \
"v_max3_u32 v215, v215, v27, v28\n" \
"v_max3_u32 v215, v215, v29, v30\n" \
"v_max3_u32 v215, v215, v31, v32\n" \
"v_max3_u32 v215, v215, v33, v34\n" \
"v_max3_u32 v215, v215, v35, v36\n" \
"v_max3_u32 v215, v215, v37, v38\n" \
"v_max3_u32 v215, v215, v39, v40\n" \
"v_max3_u32 v215, v215, v41, v42\n" \
"v_max3_u32 v215, v215, v43, v44\n" \
"v_max3_u32 v215, v215, v45, v46\n" \
"v_max3_u32 v215, v215, v47, v47\n" \
"v_cmp_eq_u32 vcc, -1, v215\n" \
"s_nop 1\n" \
"s_cbranch_vccz Lw4_n0_%=\n" \
"Lw4_rn0_%=:\n" \
"s_sub_u32 s64, s64, 1\n" \
"s_cbranch_scc1 Lw4_dead_%=\n" \
"buffer_load_dwordx4 v[20:23], %[lo], %[ws], s72 offen offset:0 sc1\n" \
"buffer_load_dwordx4 v[24:27], %[lo], %[ws], s72 offen offset:1024 sc1\n" \
"buffer_load_dwordx4 v[28:31], %[lo], %[ws], s72 offen offset:2048 sc1\n" \
"buffer_load_dwordx4 v[32:35], %[lo], %[ws], s72 offen offset:3072 sc1\n" \
"buffer_load_dwordx4 v[36:39], %[lo], %[ws], s73 offen offset:0 sc1\n" \
"buffer_load_dwordx4 v[40:43], %[lo], %[ws], s73 offen offset:1024 sc1\n" \
"buffer_load_dwordx4 v[44:47], %[lo], %[ws], s73 offen offset:2048 sc1\n" \
"s_waitcnt vmcnt(0)\n" \
"v_max3_u32 v215, v20, v21, v22\n" \
"v_max3_u32 v215, v215, v23, v24\n" \
"v_max3_u32 v215, v215, v25, v26\n" \
"v_max3_u32 v215, v215, v27, v28\n" \
"v_max3_u32 v215, v215, v29, v30\n" \
"v_max3_u32 v215, v215, v31, v32\n" \
"v_max3_u32 v215, v215, v33, v34\n" \
"v_max3_u32 v215, v215, v35, v36\n" \
"v_max3_u32 v215, v215, v37, v38\n" \
"v_max3_u32 v215, v215, v39, v40\n" \
"v_max3_u32 v215, v215, v41, v42\n" \
"v_max3_u32 v215, v215, v43, v44\n" \
"v_max3_u32 v215, v215, v45, v46\n" \
"v_max3_u32 v215, v215, v47, v47\n" \
"v_cmp_eq_u32 vcc, -1, v215\n" \
"s_nop 1\n" \
"s_cbranch_vccnz Lw4_rn0_%=\n" \
"v_mfma_f32_16x16x32_bf16 v[108:111], a[8:11], v[20:23], 0\n" \
"v_mfma_f32_16x16x32_bf16 v[112:115], a[12:15], v[20:23], 0\n" \
"v_mfma_f32_16x16x32_bf16 v[116:119], a[16:19], v[20:23], 0\n" \
"v_mfma_f32_16x16x32_bf16 v[120:123], a[20:23], v[20:23], 0\n" \
"v_mfma_f32_16x16x32_bf16 v[124:127], a[24:27], v[20:23], 0\n" \
"v_mfma_f32_16x16x32_bf16 v[128:131], a[28:31], v[20:23], 0\n" \
"v_mfma_f32_16x16x32_bf16 v[108:111], a[40:43], v[24:27], v[108:111]\n" \
"v_mfma_f32_16x16x32_bf16 v[112:115], a[44:47], v[24:27], v[112:115]\n" \
"v_mfma_f32_16x16x32_bf16 v[116:119], a[48:51], v[24:27], v[116:119]\n" \
"v_mfma_f32_16x16x32_bf16 v[120:123], a[52:55], v[24:27], v[120:123]\n" \
"v_mfma_f32_16x16x32_bf16 v[124:127], a[56:59], v[24:27], v[124:127]\n" \
"v_mfma_f32_16x16x32_bf16 v[128:131], a[60:63], v[24:27], v[128:131]\n" \
"v_mfma_f32_16x16x32_bf16 v[108:111], a[72:75], v[28:31], v[108:111]\n" \
"v_mfma_f32_16x16x32_bf16 v[112:115], a[76:79], v[28:31], v[112:115]\n" \
"v_mfma_f32_16x16x32_bf16 v[116:119], a[80:83], v[28:31], v[116:119]\n" \
"v_mfma_f32_16x16x32_bf16 v[120:123], a[84:87], v[28:31], v[120:123]\n" \
"v_mfma_f32_16x16x32_bf16 v[124:127], a[88:91], v[28:31], v[124:127]\n" \
"v_mfma_f32_16x16x32_bf16 v[128:131], a[92:95], v[28:31], v[128:131]\n" \
"v_mfma_f32_16x16x32_bf16 v[108:111], a[104:107], v[32:35], v[108:111]\n" \
"v_mfma_f32_16x16x32_bf16 v[112:115], a[108:111], v[32:35], v[112:115]\n" \
"v_mfma_f32_16x16x32_bf16 v[116:119], a[112:115], v[32:35], v[116:119]\n" \
"v_mfma_f32_16x16x32_bf16 v[120:123], a[116:119], v[32:35], v[120:123]\n" \
"v_mfma_f32_16x16x32_bf16 v[124:127], a[120:123], v[32:35], v[124:127]\n" \
"v_mfma_f32_16x16x32_bf16 v[128:131], a[124:127], v[32:35], v[128:131]\n" \
"v_mfma_f32_16x16x32_bf16 v[108:111], a[136:139], v[36:39], v[108:111]\n" \
"v_mfma_f32_16x16x32_bf16 v[108:111], a[168:171], v[40:43], v[108:111]\n" \
"v_mfma_f32_16x16x32_bf16 v[108:111], a[200:203], v[44:47], v[108:111]\n" \
"v_mfma_f32_16x16x32_bf16 v[112:115], a[140:143], v[36:39], v[112:115]\n" \
"v_mfma_f32_16x16x32_bf16 v[112:115], a[172:175], v[40:43], v[112:115]\n" \
"v_mfma_f32_16x16x32_bf16 v[112:115], a[204:207], v[44:47], v[112:115]\n" \
"v_mfma_f32_16x16x32_bf16 v[116:119], a[144:147], v[36:39], v[116:119]\n" \
"s_nop 7\n" \
"ds_write_b128 v212, v[108:111] offset:2048\n" \
"s_branch Lw4_n0_%=\n" \
"Lw4_sgo1_%=:\n" \
"s_waitcnt vmcnt(0)\n" \
"buffer_load_dword v202, %[fvo], %[ws], 0 offen sc1\n" \
"buffer_store_dword v203, %[fo], %[ws], s69 offen sc1\n" \
"s_mov_b32 s69, 1073741824\n" \
"s_waitcnt vmcnt(1)\n" \
"s_branch Lw4_cgo1_%=\n" \
"Lw4_pgo1_%=:\n" \
"s_sub_u32 s64, s64, 1\n" \
"s_cbranch_scc1 Lw4_dead_%=\n" \
"buffer_load_dword v202, %[fvo], %[ws], 0 offen sc1\n" \
"s_waitcnt vmcnt(0)\n" \
"Lw4_cgo1_%=:\n" \
"v_cmp_gt_u32 vcc, s74, v202\n" \
"s_cbranch_vccz Lw4_go1_%=\n" \
"s_sleep 1\n" \
"s_branch Lw4_pgo1_%=\n" \
"Lw4_sn1_%=:\n" \
"v_max3_u32 v215, v52, v53, v54\n" \
"v_max3_u32 v215, v215, v55, v56\n" \
"v_max3_u32 v215, v215, v57, v58\n" \
"v_max3_u32 v215, v215, v59, v60\n" \
"v_max3_u32 v215, v215, v61, v62\n" \
"v_max3_u32 v215, v215, v63, v64\n" \
"v_max3_u32 v215, v215, v65, v66\n" \
"v_max3_u32 v215, v215, v67, v68\n" \
"v_max3_u32 v215, v215, v69, v70\n" \
"v_max3_u32 v215, v215, v71, v72\n" \
"v_max3_u32 v215, v215, v73, v74\n" \
"v_max3_u32 v215, v215, v75, v76\n" \
"v_max3_u32 v215, v215, v77, v78\n" \
"v_max3_u32 v215, v215, v79, v79\n" \
"v_cmp_eq_u32 vcc, -1, v215\n" \
"s_nop 1\n" \
"s_cbranch_vccz Lw4_n1_%=\n" \
"Lw4_rn1_%=:\n" \
"s_sub_u32 s64, s64, 1\n" \
"s_cbranch_scc1 Lw4_dead_%=\n" \
"buffer_load_dwordx4 v[52:55], %[lo], %[ws], s72 offen offset:0 sc1\n" \
"buffer_load_dwordx4 v[56:59], %[lo], %[ws], s72 offen offset:1024 sc1\n" \
"buffer_load_dwordx4 v[60:63], %[lo], %[ws], s72 offen offset:2048 sc1\n" \
"buffer_load_dwordx4 v[64:67], %[lo], %[ws], s72 offen offset:3072 sc1\n" \
"buffer_load_dwordx4 v[68:71], %[lo], %[ws], s73 offen offset:0 sc1\n" \
"buffer_load_dwordx4 v[72:75], %[lo], %[ws], s73 offen offset:1024 sc1\n" \
"buffer_load_dwordx4 v[76:79], %[lo], %[ws], s73 offen offset:2048 sc1\n" \
"s_waitcnt vmcnt(0)\n" \
"v_max3_u32 v215, v52, v53, v54\n" \
"v_max3_u32 v215, v215, v55, v56\n" \
"v_max3_u32 v215, v215, v57, v58\n" \
"v_max3_u32 v215, v215, v59, v60\n" \
"v_max3_u32 v215, v215, v61, v62\n" \
"v_max3_u32 v215, v215, v63, v64\n" \
"v_max3_u32 v215, v215, v65, v66\n" \
"v_max3_u32 v215, v215, v67, v68\n" \
"v_max3_u32 v215, v215, v69, v70\n" \
"v_max3_u32 v215, v215, v71, v72\n" \
"v_max3_u32 v215, v215, v73, v74\n" \
"v_max3_u32 v215, v215, v75, v76\n" \
"v_max3_u32 v215, v215, v77, v78\n" \
"v_max3_u32 v215, v215, v79, v79\n" \
"v_cmp_eq_u32 vcc, -1, v215\n" \
"s_nop 1\n" \
"s_cbranch_vccnz Lw4_rn1_%=\n" \
"v_mfma_f32_16x16x32_bf16 v[108:111], a[8:11], v[52:55], 0\n" \
"v_mfma_f32_16x16x32_bf16 v[112:115], a[12:15], v[52:55], 0\n" \
"v_mfma_f32_16x16x32_bf16 v[116:119], a[16:19], v[52:55], 0\n" \
"v_mfma_f32_16x16x32_bf16 v[120:123], a[20:23], v[52:55], 0\n" \
"v_mfma_f32_16x16x32_bf16 v[124:127], a[24:27], v[52:55], 0\n" \
"v_mfma_f32_16x16x32_bf16 v[128:131], a[28:31], v[52:55], 0\n" \
"v_mfma_f32_16x16x32_bf16 v[108:111], a[40:43], v[56:59], v[108:111]\n" \
"v_mfma_f32_16x16x32_bf16 v[112:115], a[44:47], v[56:59], v[112:115]\n" \
"v_mfma_f32_16x16x32_bf16 v[116:119], a[48:51], v[56:59], v[116:119]\n" \
"v_mfma_f32_16x16x32_bf16 v[120:123], a[52:55], v[56:59], v[120:123]\n" \
"v_mfma_f32_16x16x32_bf16 v[124:127], a[56:59], v[56:59], v[124:127]\n" \
"v_mfma_f32_16x16x32_bf16 v[128:131], a[60:63], v[56:59], v[128:131]\n" \
"v_mfma_f32_16x16x32_bf16 v[108:111], a[72:75], v[60:63], v[108:111]\n" \
"v_mfma_f32_16x16x32_bf16 v[112:115], a[76:79], v[60:63], v[112:115]\n" \
"v_mfma_f32_16x16x32_bf16 v[116:119], a[80:83], v[60:63], v[116:119]\n" \
"v_mfma_f32_16x16x32_bf16 v[120:123], a[84:87], v[60:63], v[120:123]\n" \
"v_mfma_f32_16x16x32_bf16 v[124:127], a[88:91], v[60:63], v[124:127]\n" \
"v_mfma_f32_16x16x32_bf16 v[128:131], a[92:95], v[60:63], v[128:131]\n" \
"v_mfma_f32_16x16x32_bf16 v[108:111], a[104:107], v[64:67], v[108:111]\n" \
"v_mfma_f32_16x16x32_bf16 v[112:115], a[108:111], v[64:67], v[112:115]\n" \
"v_mfma_f32_16x16x32_bf16 v[116:119], a[112:115], v[64:67], v[116:119]\n" \
"v_mfma_f32_16x16x32_bf16 v[120:123], a[116:119], v[64:67], v[120:123]\n" \
"v_mfma_f32_16x16x32_bf16 v[124:127], a[120:123], v[64:67], v[124:127]\n" \
"v_mfma_f32_16x16x32_bf16 v[128:131], a[124:127], v[64:67], v[128:131]\n" \
"v_mfma_f32_16x16x32_bf16 v[108:111], a[136:139], v[68:71], v[108:111]\n" \
"v_mfma_f32_16x16x32_bf16 v[108:111], a[168:171], v[72:75], v[108:111]\n" \
"v_mfma_f32_16x16x32_bf16 v[108:111], a[200:203], v[76:79], v[108:111]\n" \
"v_mfma_f32_16x16x32_bf16 v[112:115], a[140:143], v[68:71], v[112:115]\n" \
"v_mfma_f32_16x16x32_bf16 v[112:115], a[172:175], v[72:75], v[112:115]\n" \
"v_mfma_f32_16x16x32_bf16 v[112:115], a[204:207], v[76:79], v[112:115]\n" \
"v_mfma_f32_16x16x32_bf16 v[116:119], a[144:147], v[68:71], v[116:119]\n" \
"s_nop 7\n" \
"ds_write_b128 v212, v[108:111] offset:34816\n" \
"s_branch Lw4_n1_%=\n" \
"Lw4_dead_%=:\n" \
"v_mov_b32 v202, 29188\n" \
"buffer_store_dword v202, off, %[ws], 0 sc1\n" \
"s_endpgm\n" \
    :  : [wvo] "v"(wvo), [tr0] "v"(tr0), [fo] "v"(fo), [xr] "v"(xr), [fvo] "v"(fvo), [lo] "v"(lo), [so] "v"(so), [ws] "s"(ws), [rw] "s"(rw), [so0] "s"(so0), [wbase] "s"(wbase), [src] "s"(src), [dst] "s"(dst), [d0] "s"(d0) \
    : CLOB20)

#define R1_ASM() asm volatile( \
"s_nop 4\n" \
"s_add_u32 s68, %[dst], 3375104\n" \
"s_add_u32 s70, s68, 4096\n" \
"s_add_u32 s66, %[os], 210944\n" \
"v_mov_b32 v72, -1\n" \
"v_mov_b32 v73, -1\n" \
"v_mov_b32 v74, -1\n" \
"v_mov_b32 v75, -1\n" \
"s_mov_b32 s65, 24\n" \
"Lr1_poi_%=:\n" \
"buffer_store_dwordx4 v[72:75], %[oo], %[ws], s68 offen sc1\n" \
"buffer_store_dwordx4 v[72:75], %[oo], %[ws], s70 offen sc1\n" \
"buffer_store_dwordx2 v[72:73], %[sto], %[ws], s66 offen sc1\n" \
"s_waitcnt vmcnt(2)\n" \
"s_add_u32 s68, s68, 32768\n" \
"s_add_u32 s70, s70, 32768\n" \
"s_add_u32 s66, s66, 2048\n" \
"s_sub_u32 s65, s65, 1\n" \
"s_cbranch_scc0 Lr1_poi_%=\n" \
"s_waitcnt vmcnt(0)\n" \
"s_mov_b32 s64, 262144\n" \
"s_mov_b32 s74, 1\n" \
"s_mov_b32 s67, %[src]\n" \
"s_mov_b32 s68, %[dst]\n" \
"s_mov_b32 s70, %[oy]\n" \
"s_mov_b32 s72, 1073741824\n" \
"s_mov_b32 s71, %[os]\n" \
"s_mov_b32 s69, 1073741824\n" \
"buffer_load_dwordx4 v[64:67], %[yo], %[rx], s70 offen offset:0\n" \
"buffer_load_dwordx4 v[68:71], %[yo], %[rx], s70 offen offset:256\n" \
"s_add_u32 s70, s70, 32768\n" \
"s_waitcnt vmcnt(1)\n" \
"v_lshlrev_b32 v72, 16, v64\n" \
"v_and_b32 v73, 0xffff0000, v64\n" \
"v_lshlrev_b32 v74, 16, v65\n" \
"v_and_b32 v75, 0xffff0000, v65\n" \
"v_lshlrev_b32 v76, 16, v66\n" \
"v_and_b32 v77, 0xffff0000, v66\n" \
"v_lshlrev_b32 v78, 16, v67\n" \
"v_and_b32 v79, 0xffff0000, v67\n" \
"s_waitcnt vmcnt(0)\n" \
"v_lshlrev_b32 v80, 16, v68\n" \
"v_and_b32 v81, 0xffff0000, v68\n" \
"v_lshlrev_b32 v82, 16, v69\n" \
"v_and_b32 v83, 0xffff0000, v69\n" \
"v_lshlrev_b32 v84, 16, v70\n" \
"v_and_b32 v85, 0xffff0000, v70\n" \
"v_lshlrev_b32 v86, 16, v71\n" \
"v_and_b32 v87, 0xffff0000, v71\n" \
"buffer_load_dwordx4 v[64:67], %[yo], %[rx], s70 offen offset:0\n" \
"buffer_load_dwordx4 v[68:71], %[yo], %[rx], s70 offen offset:256\n" \
"s_add_u32 s70, s70, 32768\n" \
"v_mov_b32 v104, 0\n" \
"v_mov_b32 v105, 0\n" \
"Lr1_top_%=:\n" \
"v_cmp_gt_u32 vcc, s74, v105\n" \
"s_cbranch_vccnz Lr1_sgo_%=\n" \
"Lr1_go_%=:\n" \
"s_add_u32 s66, s67, 4096\n" \
"buffer_load_dwordx4 v[32:35], %[po], %[ws], s67 offen offset:0 sc1\n" \
"buffer_load_dwordx4 v[36:39], %[po], %[ws], s67 offen offset:1024 sc1\n" \
"buffer_load_dwordx4 v[40:43], %[po], %[ws], s67 offen offset:2048 sc1\n" \
"buffer_load_dwordx4 v[44:47], %[po], %[ws], s67 offen offset:3072 sc1\n" \
"buffer_load_dwordx4 v[48:51], %[po], %[ws], s66 offen offset:0 sc1\n" \
"buffer_load_dwordx4 v[52:55], %[po], %[ws], s66 offen offset:1024 sc1\n" \
"buffer_load_dwordx4 v[56:59], %[po], %[ws], s66 offen offset:2048 sc1\n" \
"buffer_load_dwordx4 v[60:63], %[po], %[ws], s66 offen offset:3072 sc1\n" \
"s_add_u32 s67, s67, 131072\n" \
"buffer_store_dword v104, %[po], %[ws], s72 offen\n" \
"buffer_store_dword v104, %[po], %[ws], s72 offen\n" \
"buffer_store_dword v104, %[po], %[ws], s72 offen\n" \
"buffer_load_dword v105, %[fvo], %[ws], 0 offen sc1\n" \
"buffer_store_dword v104, %[po], %[ws], s72 offen\n" \
"buffer_store_dword v104, %[po], %[ws], s72 offen\n" \
"Lr1_body_%=:\n" \
"s_waitcnt vmcnt(13)\n" \
"v_max3_u32 v106, v32, v33, v34\n" \
"s_waitcnt vmcnt(12)\n" \
"v_max3_u32 v106, v106, v35, v36\n" \
"v_max3_u32 v106, v106, v37, v38\n" \
"v_cmp_u_f32 vcc, v106, v39\n" \
"s_cbranch_vccnz Lr1_sk1_%=\n" \
"Lr1_k1_%=:\n" \
"v_mfma_f32_16x16x32_bf16 v[72:75], %[sa], v[32:35], v[72:75]\n" \
"v_mfma_f32_16x16x32_bf16 v[76:79], %[sa], v[36:39], v[76:79]\n" \
"s_waitcnt vmcnt(11)\n" \
"v_max3_u32 v106, v40, v41, v42\n" \
"s_waitcnt vmcnt(10)\n" \
"v_max3_u32 v106, v106, v43, v44\n" \
"v_max3_u32 v106, v106, v45, v46\n" \
"v_cmp_u_f32 vcc, v106, v47\n" \
"s_cbranch_vccnz Lr1_sk2_%=\n" \
"Lr1_k2_%=:\n" \
"s_cmp_gt_u32 s74, 104\n" \
"s_cselect_b32 s79, 0, 1073741824\n" \
"s_cselect_b32 s69, 1073741824, s69\n" \
"v_mov_b32 v107, s74\n" \
"buffer_store_dword v107, %[fo], %[ws], s79 offen sc1\n" \
"v_mfma_f32_16x16x32_bf16 v[72:75], %[sb], v[40:43], v[72:75]\n" \
"v_mfma_f32_16x16x32_bf16 v[76:79], %[sb], v[44:47], v[76:79]\n" \
"s_waitcnt vmcnt(10)\n" \
"v_max3_u32 v106, v48, v49, v50\n" \
"s_waitcnt vmcnt(9)\n" \
"v_max3_u32 v106, v106, v51, v52\n" \
"v_max3_u32 v106, v106, v53, v54\n" \
"v_cmp_u_f32 vcc, v106, v55\n" \
"s_cbranch_vccnz Lr1_sk3_%=\n" \
"Lr1_k3_%=:\n" \
"v_mfma_f32_16x16x32_bf16 v[80:83], %[sa], v[48:51], v[80:83]\n" \
"v_mfma_f32_16x16x32_bf16 v[84:87], %[sa], v[52:55], v[84:87]\n" \
"s_waitcnt vmcnt(8)\n" \
"v_max3_u32 v106, v56, v57, v58\n" \
"s_waitcnt vmcnt(7)\n" \
"v_max3_u32 v106, v106, v59, v60\n" \
"v_max3_u32 v106, v106, v61, v62\n" \
"v_cmp_u_f32 vcc, v106, v63\n" \
"s_cbranch_vccnz Lr1_sk4_%=\n" \
"Lr1_k4_%=:\n" \
"v_mfma_f32_16x16x32_bf16 v[80:83], %[sb], v[56:59], v[80:83]\n" \
"v_mfma_f32_16x16x32_bf16 v[84:87], %[sb], v[60:63], v[84:87]\n" \
"s_add_u32 s74, s74, 1\n" \
"s_waitcnt vmcnt(3)\n" \
"v_cmp_gt_u32 vcc, s74, v105\n" \
"buffer_store_dword v104, %[fo], %[ws], s69 offen sc1\n" \
"s_cbranch_vccnz Lr1_skip_%=\n" \
"s_add_u32 s66, s67, 4096\n" \
"buffer_load_dwordx4 v[32:35], %[po], %[ws], s67 offen offset:0 sc1\n" \
"buffer_load_dwordx4 v[36:39], %[po], %[ws], s67 offen offset:1024 sc1\n" \
"buffer_load_dwordx4 v[40:43], %[po], %[ws], s67 offen offset:2048 sc1\n" \
"buffer_load_dwordx4 v[44:47], %[po], %[ws], s67 offen offset:3072 sc1\n" \
"buffer_load_dwordx4 v[48:51], %[po], %[ws], s66 offen offset:0 sc1\n" \
"buffer_load_dwordx4 v[52:55], %[po], %[ws], s66 offen offset:1024 sc1\n" \
"buffer_load_dwordx4 v[56:59], %[po], %[ws], s66 offen offset:2048 sc1\n" \
"buffer_load_dwordx4 v[60:63], %[po], %[ws], s66 offen offset:3072 sc1\n" \
"s_add_u32 s67, s67, 131072\n" \
"Lr1_skip_%=:\n" \
"s_nop 3\n" \
"v_cvt_pk_bf16_f32 v88, v72, v73\n" \
"v_cvt_pk_bf16_f32 v89, v74, v75\n" \
"v_cvt_pk_bf16_f32 v90, v76, v77\n" \
"v_cvt_pk_bf16_f32 v91, v78, v79\n" \
"buffer_store_dwordx4 v[88:91], %[oo], %[ws], s68 offen sc1\n" \
"s_add_u32 s68, s68, 4096\n" \
"v_pk_mul_f32 v[98:99], v[72:73], v[72:73]\n" \
"v_pk_fma_f32 v[98:99], v[74:75], v[74:75], v[98:99]\n" \
"v_pk_add_f32 v[96:97], v[74:75], v[72:73]\n" \
"v_pk_fma_f32 v[98:99], v[76:77], v[76:77], v[98:99]\n" \
"v_pk_add_f32 v[96:97], v[76:77], v[96:97]\n" \
"v_pk_fma_f32 v[98:99], v[78:79], v[78:79], v[98:99]\n" \
"v_pk_add_f32 v[96:97], v[78:79], v[96:97]\n" \
"v_cvt_pk_bf16_f32 v92, v80, v81\n" \
"v_cvt_pk_bf16_f32 v93, v82, v83\n" \
"v_cvt_pk_bf16_f32 v94, v84, v85\n" \
"v_cvt_pk_bf16_f32 v95, v86, v87\n" \
"buffer_store_dwordx4 v[92:95], %[oo], %[ws], s68 offen sc1\n" \
"s_add_u32 s68, s68, 28672\n" \
"v_pk_fma_f32 v[98:99], v[80:81], v[80:81], v[98:99]\n" \
"v_pk_add_f32 v[96:97], v[80:81], v[96:97]\n" \
"v_pk_fma_f32 v[98:99], v[82:83], v[82:83], v[98:99]\n" \
"v_pk_add_f32 v[96:97], v[82:83], v[96:97]\n" \
"v_pk_fma_f32 v[98:99], v[84:85], v[84:85], v[98:99]\n" \
"v_pk_add_f32 v[96:97], v[84:85], v[96:97]\n" \
"v_pk_fma_f32 v[98:99], v[86:87], v[86:87], v[98:99]\n" \
"v_pk_add_f32 v[96:97], v[86:87], v[96:97]\n" \
"v_add_f32 v96, v96, v97\n" \
"v_add_f32 v97, v98, v99\n" \
"s_nop 0\n" \
"v_add_f32_dpp v96, v96, v96 row_ror:4 row_mask:0xf bank_mask:0xf\n" \
"v_add_f32_dpp v97, v97, v97 row_ror:4 row_mask:0xf bank_mask:0xf\n" \
"s_nop 0\n" \
"v_add_f32_dpp v96, v96, v96 row_ror:8 row_mask:0xf bank_mask:0xf\n" \
"v_add_f32_dpp v97, v97, v97 row_ror:8 row_mask:0xf bank_mask:0xf\n" \
"buffer_store_dwordx2 v[96:97], %[sto], %[ws], s71 offen sc1\n" \
"s_add_u32 s71, s71, 2048\n" \
"v_add_u32 v104, -1, s74\n" \
"s_xor_b32 s69, s79, 1073741824\n" \
"buffer_load_dword v105, %[fvo], %[ws], 0 offen sc1\n" \
"s_waitcnt vmcnt(7)\n" \
"v_lshlrev_b32 v72, 16, v64\n" \
"v_and_b32 v73, 0xffff0000, v64\n" \
"v_lshlrev_b32 v74, 16, v65\n" \
"v_and_b32 v75, 0xffff0000, v65\n" \
"v_lshlrev_b32 v76, 16, v66\n" \
"v_and_b32 v77, 0xffff0000, v66\n" \
"v_lshlrev_b32 v78, 16, v67\n" \
"v_and_b32 v79, 0xffff0000, v67\n" \
"s_waitcnt vmcnt(6)\n" \
"v_lshlrev_b32 v80, 16, v68\n" \
"v_and_b32 v81, 0xffff0000, v68\n" \
"v_lshlrev_b32 v82, 16, v69\n" \
"v_and_b32 v83, 0xffff0000, v69\n" \
"v_lshlrev_b32 v84, 16, v70\n" \
"v_and_b32 v85, 0xffff0000, v70\n" \
"v_lshlrev_b32 v86, 16, v71\n" \
"v_and_b32 v87, 0xffff0000, v71\n" \
"buffer_load_dwordx4 v[64:67], %[yo], %[rx], s70 offen offset:0\n" \
"buffer_load_dwordx4 v[68:71], %[yo], %[rx], s70 offen offset:256\n" \
"s_add_u32 s70, s70, 32768\n" \
"s_cbranch_vccz Lr1_body_%=\n" \
"s_waitcnt vmcnt(2)\n" \
"buffer_store_dword v104, %[fo], %[ws], s69 offen sc1\n" \
"s_mov_b32 s69, 1073741824\n" \
"s_cmp_lg_u32 s74, 129\n" \
"s_cbranch_scc1 Lr1_top_%=\n" \
"s_endpgm\n" \
"Lr1_sgo_%=:\n" \
"Lr1_pgo_%=:\n" \
"s_sub_u32 s64, s64, 1\n" \
"s_cbranch_scc1 Lr1_dead_%=\n" \
"buffer_load_dword v105, %[fvo], %[ws], 0 offen sc1\n" \
"s_waitcnt vmcnt(0)\n" \
"Lr1_cgo_%=:\n" \
"v_cmp_gt_u32 vcc, s74, v105\n" \
"s_cbranch_vccz Lr1_go_%=\n" \
"s_sleep 1\n" \
"s_branch Lr1_pgo_%=\n" \
"Lr1_sk1_%=:\n" \
"v_max3_u32 v106, v32, v33, v34\n" \
"v_max3_u32 v106, v106, v35, v36\n" \
"v_max3_u32 v106, v106, v37, v38\n" \
"v_max3_u32 v106, v106, v39, v39\n" \
"v_cmp_eq_u32 vcc, -1, v106\n" \
"s_nop 1\n" \
"s_cbranch_vccz Lr1_k1_%=\n" \
"s_sub_u32 s76, s67, 131072\n" \
"s_add_u32 s77, s76, 4096\n" \
"s_sub_u32 s80, s70, 32768\n" \
"s_add_u32 s81, s80, 4096\n" \
"Lr1_rk1_%=:\n" \
"s_sub_u32 s64, s64, 1\n" \
"s_cbranch_scc1 Lr1_dead_%=\n" \
"buffer_load_dwordx4 v[32:35], %[po], %[ws], s76 offen offset:0 sc1\n" \
"buffer_load_dwordx4 v[36:39], %[po], %[ws], s76 offen offset:1024 sc1\n" \
"buffer_load_dwordx4 v[40:43], %[po], %[ws], s76 offen offset:2048 sc1\n" \
"buffer_load_dwordx4 v[44:47], %[po], %[ws], s76 offen offset:3072 sc1\n" \
"buffer_load_dwordx4 v[48:51], %[po], %[ws], s77 offen offset:0 sc1\n" \
"buffer_load_dwordx4 v[52:55], %[po], %[ws], s77 offen offset:1024 sc1\n" \
"buffer_load_dwordx4 v[56:59], %[po], %[ws], s77 offen offset:2048 sc1\n" \
"buffer_load_dwordx4 v[60:63], %[po], %[ws], s77 offen offset:3072 sc1\n" \
"s_waitcnt vmcnt(0)\n" \
"v_max3_u32 v106, v32, v33, v34\n" \
"v_max3_u32 v106, v106, v35, v36\n" \
"v_max3_u32 v106, v106, v37, v38\n" \
"v_max3_u32 v106, v106, v39, v39\n" \
"v_cmp_eq_u32 vcc, -1, v106\n" \
"s_nop 1\n" \
"s_cbranch_vccnz Lr1_rk1_%=\n" \
"s_branch Lr1_k1_%=\n" \
"Lr1_sk2_%=:\n" \
"v_max3_u32 v106, v40, v41, v42\n" \
"v_max3_u32 v106, v106, v43, v44\n" \
"v_max3_u32 v106, v106, v45, v46\n" \
"v_max3_u32 v106, v106, v47, v47\n" \
"v_cmp_eq_u32 vcc, -1, v106\n" \
"s_nop 1\n" \
"s_cbranch_vccz Lr1_k2_%=\n" \
"s_sub_u32 s76, s67, 131072\n" \
"s_add_u32 s77, s76, 4096\n" \
"s_sub_u32 s80, s70, 32768\n" \
"s_add_u32 s81, s80, 4096\n" \
"Lr1_rk2_%=:\n" \
"s_sub_u32 s64, s64, 1\n" \
"s_cbranch_scc1 Lr1_dead_%=\n" \
"buffer_load_dwordx4 v[40:43], %[po], %[ws], s76 offen offset:2048 sc1\n" \
"buffer_load_dwordx4 v[44:47], %[po], %[ws], s76 offen offset:3072 sc1\n" \
"buffer_load_dwordx4 v[48:51], %[po], %[ws], s77 offen offset:0 sc1\n" \
"buffer_load_dwordx4 v[52:55], %[po], %[ws], s77 offen offset:1024 sc1\n" \
"buffer_load_dwordx4 v[56:59], %[po], %[ws], s77 offen offset:2048 sc1\n" \
"buffer_load_dwordx4 v[60:63], %[po], %[ws], s77 offen offset:3072 sc1\n" \
"s_waitcnt vmcnt(0)\n" \
"v_max3_u32 v106, v40, v41, v42\n" \
"v_max3_u32 v106, v106, v43, v44\n" \
"v_max3_u32 v106, v106, v45, v46\n" \
"v_max3_u32 v106, v106, v47, v47\n" \
"v_cmp_eq_u32 vcc, -1, v106\n" \
"s_nop 1\n" \
"s_cbranch_vccnz Lr1_rk2_%=\n" \
"s_branch Lr1_k2_%=\n" \
"Lr1_sk3_%=:\n" \
"v_max3_u32 v106, v48, v49, v50\n" \
"v_max3_u32 v106, v106, v51, v52\n" \
"v_max3_u32 v106, v106, v53, v54\n" \
"v_max3_u32 v106, v106, v55, v55\n" \
"v_cmp_eq_u32 vcc, -1, v106\n" \
"s_nop 1\n" \
"s_cbranch_vccz Lr1_k3_%=\n" \
"s_sub_u32 s76, s67, 131072\n" \
"s_add_u32 s77, s76, 4096\n" \
"s_sub_u32 s80, s70, 32768\n" \
"s_add_u32 s81, s80, 4096\n" \
"Lr1_rk3_%=:\n" \
"s_sub_u32 s64, s64, 1\n" \
"s_cbranch_scc1 Lr1_dead_%=\n" \
"buffer_load_dwordx4 v[48:51], %[po], %[ws], s77 offen offset:0 sc1\n" \
"buffer_load_dwordx4 v[52:55], %[po], %[ws], s77 offen offset:1024 sc1\n" \
"buffer_load_dwordx4 v[56:59], %[po], %[ws], s77 offen offset:2048 sc1\n" \
"buffer_load_dwordx4 v[60:63], %[po], %[ws], s77 offen offset:3072 sc1\n" \
"s_waitcnt vmcnt(0)\n" \
"v_max3_u32 v106, v48, v49, v50\n" \
"v_max3_u32 v106, v106, v51, v52\n" \
"v_max3_u32 v106, v106, v53, v54\n" \
"v_max3_u32 v106, v106, v55, v55\n" \
"v_cmp_eq_u32 vcc, -1, v106\n" \
"s_nop 1\n" \
"s_cbranch_vccnz Lr1_rk3_%=\n" \
"s_branch Lr1_k3_%=\n" \
"Lr1_sk4_%=:\n" \
"v_max3_u32 v106, v56, v57, v58\n" \
"v_max3_u32 v106, v106, v59, v60\n" \
"v_max3_u32 v106, v106, v61, v62\n" \
"v_max3_u32 v106, v106, v63, v63\n" \
"v_cmp_eq_u32 vcc, -1, v106\n" \
"s_nop 1\n" \
"s_cbranch_vccz Lr1_k4_%=\n" \
"s_sub_u32 s76, s67, 131072\n" \
"s_add_u32 s77, s76, 4096\n" \
"s_sub_u32 s80, s70, 32768\n" \
"s_add_u32 s81, s80, 4096\n" \
"Lr1_rk4_%=:\n" \
"s_sub_u32 s64, s64, 1\n" \
"s_cbranch_scc1 Lr1_dead_%=\n" \
"buffer_load_dwordx4 v[56:59], %[po], %[ws], s77 offen offset:2048 sc1\n" \
"buffer_load_dwordx4 v[60:63], %[po], %[ws], s77 offen offset:3072 sc1\n" \
"s_waitcnt vmcnt(0)\n" \
"v_max3_u32 v106, v56, v57, v58\n" \
"v_max3_u32 v106, v106, v59, v60\n" \
"v_max3_u32 v106, v106, v61, v62\n" \
"v_max3_u32 v106, v106, v63, v63\n" \
"v_cmp_eq_u32 vcc, -1, v106\n" \
"s_nop 1\n" \
"s_cbranch_vccnz Lr1_rk4_%=\n" \
"s_branch Lr1_k4_%=\n" \
"Lr1_dead_%=:\n" \
"v_mov_b32 v105, 29441\n" \
"buffer_store_dword v105, off, %[ws], 0 sc1\n" \
"s_endpgm\n" \
    :  : [sa] "v"(sa), [sb] "v"(sb), [po] "v"(po), [yo] "v"(yo), [oo] "v"(oo), [fvo] "v"(fvo), [fo] "v"(fo), [sto] "v"(sto), [ws] "s"(ws), [src] "s"(src), [dst] "s"(dst), [oy] "s"(oy), [rx] "s"(rx), [os] "s"(os) \
    : CLOB32)

#define R2_ASM() asm volatile( \
"s_nop 4\n" \
"s_mov_b32 s64, 262144\n" \
"s_mov_b32 s74, 1\n" \
"s_mov_b32 s67, %[src]\n" \
"s_mov_b32 s68, %[dst]\n" \
"s_mov_b32 s70, %[oy]\n" \
"s_mov_b32 s72, 1073741824\n" \
"v_mov_b32 v105, 0\n" \
"Lr2_top_%=:\n" \
"v_cmp_gt_u32 vcc, s74, v105\n" \
"s_cbranch_vccnz Lr2_sgo_%=\n" \
"Lr2_go_%=:\n" \
"buffer_load_dwordx4 v[64:67], %[yo], %[ws], s70 offen sc1\n" \
"s_add_u32 s70, s70, 4096\n" \
"buffer_load_dwordx4 v[68:71], %[yo], %[ws], s70 offen sc1\n" \
"s_add_u32 s70, s70, 28672\n" \
"s_add_u32 s66, s67, 4096\n" \
"buffer_load_dwordx4 v[32:35], %[po], %[ws], s67 offen offset:0 sc1\n" \
"buffer_load_dwordx4 v[36:39], %[po], %[ws], s67 offen offset:1024 sc1\n" \
"buffer_load_dwordx4 v[40:43], %[po], %[ws], s67 offen offset:2048 sc1\n" \
"buffer_load_dwordx4 v[44:47], %[po], %[ws], s67 offen offset:3072 sc1\n" \
"buffer_load_dwordx4 v[48:51], %[po], %[ws], s66 offen offset:0 sc1\n" \
"buffer_load_dwordx4 v[52:55], %[po], %[ws], s66 offen offset:1024 sc1\n" \
"buffer_load_dwordx4 v[56:59], %[po], %[ws], s66 offen offset:2048 sc1\n" \
"buffer_load_dwordx4 v[60:63], %[po], %[ws], s66 offen offset:3072 sc1\n" \
"s_add_u32 s67, s67, 131072\n" \
"buffer_store_dword v104, %[po], %[ws], s72 offen\n" \
"buffer_store_dword v104, %[po], %[ws], s72 offen\n" \
"buffer_load_dword v105, %[fvo], %[ws], 0 offen sc1\n" \
"Lr2_body_%=:\n" \
"s_waitcnt vmcnt(12)\n" \
"v_max3_u32 v106, v64, v65, v66\n" \
"s_waitcnt vmcnt(11)\n" \
"v_max3_u32 v106, v106, v67, v68\n" \
"v_max3_u32 v106, v106, v69, v70\n" \
"v_cmp_u_f32 vcc, v106, v71\n" \
"s_cbranch_vccnz Lr2_sk0_%=\n" \
"Lr2_k0_%=:\n" \
"v_lshlrev_b32 v72, 16, v64\n" \
"v_and_b32 v73, 0xffff0000, v64\n" \
"v_lshlrev_b32 v74, 16, v65\n" \
"v_and_b32 v75, 0xffff0000, v65\n" \
"v_lshlrev_b32 v76, 16, v66\n" \
"v_and_b32 v77, 0xffff0000, v66\n" \
"v_lshlrev_b32 v78, 16, v67\n" \
"v_and_b32 v79, 0xffff0000, v67\n" \
"v_lshlrev_b32 v80, 16, v68\n" \
"v_and_b32 v81, 0xffff0000, v68\n" \
"v_lshlrev_b32 v82, 16, v69\n" \
"v_and_b32 v83, 0xffff0000, v69\n" \
"v_lshlrev_b32 v84, 16, v70\n" \
"v_and_b32 v85, 0xffff0000, v70\n" \
"v_lshlrev_b32 v86, 16, v71\n" \
"v_and_b32 v87, 0xffff0000, v71\n" \
"s_waitcnt vmcnt(10)\n" \
"v_max3_u32 v106, v32, v33, v34\n" \
"s_waitcnt vmcnt(9)\n" \
"v_max3_u32 v106, v106, v35, v36\n" \
"v_max3_u32 v106, v106, v37, v38\n" \
"v_cmp_u_f32 vcc, v106, v39\n" \
"s_cbranch_vccnz Lr2_sk1_%=\n" \
"Lr2_k1_%=:\n" \
"v_mfma_f32_16x16x32_bf16 v[72:75], %[sa], v[32:35], v[72:75]\n" \
"v_mfma_f32_16x16x32_bf16 v[76:79], %[sa], v[36:39], v[76:79]\n" \
"s_waitcnt vmcnt(8)\n" \
"v_max3_u32 v106, v40, v41, v42\n" \
"s_waitcnt vmcnt(7)\n" \
"v_max3_u32 v106, v106, v43, v44\n" \
"v_max3_u32 v106, v106, v45, v46\n" \
"v_cmp_u_f32 vcc, v106, v47\n" \
"s_cbranch_vccnz Lr2_sk2_%=\n" \
"Lr2_k2_%=:\n" \
"v_mfma_f32_16x16x32_bf16 v[72:75], %[sb], v[40:43], v[72:75]\n" \
"v_mfma_f32_16x16x32_bf16 v[76:79], %[sb], v[44:47], v[76:79]\n" \
"s_waitcnt vmcnt(6)\n" \
"v_max3_u32 v106, v48, v49, v50\n" \
"s_waitcnt vmcnt(5)\n" \
"v_max3_u32 v106, v106, v51, v52\n" \
"v_max3_u32 v106, v106, v53, v54\n" \
"v_cmp_u_f32 vcc, v106, v55\n" \
"s_cbranch_vccnz Lr2_sk3_%=\n" \
"Lr2_k3_%=:\n" \
"v_mfma_f32_16x16x32_bf16 v[80:83], %[sa], v[48:51], v[80:83]\n" \
"v_mfma_f32_16x16x32_bf16 v[84:87], %[sa], v[52:55], v[84:87]\n" \
"s_waitcnt vmcnt(4)\n" \
"v_max3_u32 v106, v56, v57, v58\n" \
"s_waitcnt vmcnt(3)\n" \
"v_max3_u32 v106, v106, v59, v60\n" \
"v_max3_u32 v106, v106, v61, v62\n" \
"v_cmp_u_f32 vcc, v106, v63\n" \
"s_cbranch_vccnz Lr2_sk4_%=\n" \
"Lr2_k4_%=:\n" \
"v_mfma_f32_16x16x32_bf16 v[80:83], %[sb], v[56:59], v[80:83]\n" \
"v_mfma_f32_16x16x32_bf16 v[84:87], %[sb], v[60:63], v[84:87]\n" \
"s_add_u32 s74, s74, 1\n" \
"s_waitcnt vmcnt(0)\n" \
"v_cmp_gt_u32 vcc, s74, v105\n" \
"s_cbranch_vccnz Lr2_skip_%=\n" \
"buffer_load_dwordx4 v[64:67], %[yo], %[ws], s70 offen sc1\n" \
"s_add_u32 s70, s70, 4096\n" \
"buffer_load_dwordx4 v[68:71], %[yo], %[ws], s70 offen sc1\n" \
"s_add_u32 s70, s70, 28672\n" \
"s_add_u32 s66, s67, 4096\n" \
"buffer_load_dwordx4 v[32:35], %[po], %[ws], s67 offen offset:0 sc1\n" \
"buffer_load_dwordx4 v[36:39], %[po], %[ws], s67 offen offset:1024 sc1\n" \
"buffer_load_dwordx4 v[40:43], %[po], %[ws], s67 offen offset:2048 sc1\n" \
"buffer_load_dwordx4 v[44:47], %[po], %[ws], s67 offen offset:3072 sc1\n" \
"buffer_load_dwordx4 v[48:51], %[po], %[ws], s66 offen offset:0 sc1\n" \
"buffer_load_dwordx4 v[52:55], %[po], %[ws], s66 offen offset:1024 sc1\n" \
"buffer_load_dwordx4 v[56:59], %[po], %[ws], s66 offen offset:2048 sc1\n" \
"buffer_load_dwordx4 v[60:63], %[po], %[ws], s66 offen offset:3072 sc1\n" \
"s_add_u32 s67, s67, 131072\n" \
"Lr2_skip_%=:\n" \
"s_nop 3\n" \
"v_cvt_pk_bf16_f32 v88, v72, v73\n" \
"v_cvt_pk_bf16_f32 v89, v74, v75\n" \
"v_cvt_pk_bf16_f32 v90, v76, v77\n" \
"v_cvt_pk_bf16_f32 v91, v78, v79\n" \
"buffer_store_dwordx4 v[88:91], %[oo], %[rx], s68 offen offset:0 sc1\n" \
"v_cvt_pk_bf16_f32 v92, v80, v81\n" \
"v_cvt_pk_bf16_f32 v93, v82, v83\n" \
"v_cvt_pk_bf16_f32 v94, v84, v85\n" \
"v_cvt_pk_bf16_f32 v95, v86, v87\n" \
"buffer_store_dwordx4 v[92:95], %[oo], %[rx], s68 offen offset:256 sc1\n" \
"s_add_u32 s68, s68, 32768\n" \
"buffer_load_dword v105, %[fvo], %[ws], 0 offen sc1\n" \
"s_cbranch_vccz Lr2_body_%=\n" \
"s_waitcnt vmcnt(0)\n" \
"s_cmp_lg_u32 s74, 129\n" \
"s_cbranch_scc1 Lr2_top_%=\n" \
"s_endpgm\n" \
"Lr2_sgo_%=:\n" \
"Lr2_pgo_%=:\n" \
"s_sub_u32 s64, s64, 1\n" \
"s_cbranch_scc1 Lr2_dead_%=\n" \
"buffer_load_dword v105, %[fvo], %[ws], 0 offen sc1\n" \
"s_waitcnt vmcnt(0)\n" \
"Lr2_cgo_%=:\n" \
"v_cmp_gt_u32 vcc, s74, v105\n" \
"s_cbranch_vccz Lr2_go_%=\n" \
"s_sleep 1\n" \
"s_branch Lr2_pgo_%=\n" \
"Lr2_sk0_%=:\n" \
"v_max3_u32 v106, v64, v65, v66\n" \
"v_max3_u32 v106, v106, v67, v68\n" \
"v_max3_u32 v106, v106, v69, v70\n" \
"v_max3_u32 v106, v106, v71, v71\n" \
"v_cmp_eq_u32 vcc, -1, v106\n" \
"s_nop 1\n" \
"s_cbranch_vccz Lr2_k0_%=\n" \
"s_sub_u32 s76, s67, 131072\n" \
"s_add_u32 s77, s76, 4096\n" \
"s_sub_u32 s80, s70, 32768\n" \
"s_add_u32 s81, s80, 4096\n" \
"Lr2_rk0_%=:\n" \
"s_sub_u32 s64, s64, 1\n" \
"s_cbranch_scc1 Lr2_dead_%=\n" \
"buffer_load_dwordx4 v[64:67], %[yo], %[ws], s80 offen sc1\n" \
"buffer_load_dwordx4 v[68:71], %[yo], %[ws], s81 offen sc1\n" \
"buffer_load_dwordx4 v[32:35], %[po], %[ws], s76 offen offset:0 sc1\n" \
"buffer_load_dwordx4 v[36:39], %[po], %[ws], s76 offen offset:1024 sc1\n" \
"buffer_load_dwordx4 v[40:43], %[po], %[ws], s76 offen offset:2048 sc1\n" \
"buffer_load_dwordx4 v[44:47], %[po], %[ws], s76 offen offset:3072 sc1\n" \
"buffer_load_dwordx4 v[48:51], %[po], %[ws], s77 offen offset:0 sc1\n" \
"buffer_load_dwordx4 v[52:55], %[po], %[ws], s77 offen offset:1024 sc1\n" \
"buffer_load_dwordx4 v[56:59], %[po], %[ws], s77 offen offset:2048 sc1\n" \
"buffer_load_dwordx4 v[60:63], %[po], %[ws], s77 offen offset:3072 sc1\n" \
"s_waitcnt vmcnt(0)\n" \
"v_max3_u32 v106, v64, v65, v66\n" \
"v_max3_u32 v106, v106, v67, v68\n" \
"v_max3_u32 v106, v106, v69, v70\n" \
"v_max3_u32 v106, v106, v71, v71\n" \
"v_cmp_eq_u32 vcc, -1, v106\n" \
"s_nop 1\n" \
"s_cbranch_vccnz Lr2_rk0_%=\n" \
"s_branch Lr2_k0_%=\n" \
"Lr2_sk1_%=:\n" \
"v_max3_u32 v106, v32, v33, v34\n" \
"v_max3_u32 v106, v106, v35, v36\n" \
"v_max3_u32 v106, v106, v37, v38\n" \
"v_max3_u32 v106, v106, v39, v39\n" \
"v_cmp_eq_u32 vcc, -1, v106\n" \
"s_nop 1\n" \
"s_cbranch_vccz Lr2_k1_%=\n" \
"s_sub_u32 s76, s67, 131072\n" \
"s_add_u32 s77, s76, 4096\n" \
"s_sub_u32 s80, s70, 32768\n" \
"s_add_u32 s81, s80, 4096\n" \
"Lr2_rk1_%=:\n" \
"s_sub_u32 s64, s64, 1\n" \
"s_cbranch_scc1 Lr2_dead_%=\n" \
"buffer_load_dwordx4 v[32:35], %[po], %[ws], s76 offen offset:0 sc1\n" \
"buffer_load_dwordx4 v[36:39], %[po], %[ws], s76 offen offset:1024 sc1\n" \
"buffer_load_dwordx4 v[40:43], %[po], %[ws], s76 offen offset:2048 sc1\n" \
"buffer_load_dwordx4 v[44:47], %[po], %[ws], s76 offen offset:3072 sc1\n" \
"buffer_load_dwordx4 v[48:51], %[po], %[ws], s77 offen offset:0 sc1\n" \
"buffer_load_dwordx4 v[52:55], %[po], %[ws], s77 offen offset:1024 sc1\n" \
"buffer_load_dwordx4 v[56:59], %[po], %[ws], s77 offen offset:2048 sc1\n" \
"buffer_load_dwordx4 v[60:63], %[po], %[ws], s77 offen offset:3072 sc1\n" \
"s_waitcnt vmcnt(0)\n" \
"v_max3_u32 v106, v32, v33, v34\n" \
"v_max3_u32 v106, v106, v35, v36\n" \
"v_max3_u32 v106, v106, v37, v38\n" \
"v_max3_u32 v106, v106, v39, v39\n" \
"v_cmp_eq_u32 vcc, -1, v106\n" \
"s_nop 1\n" \
"s_cbranch_vccnz Lr2_rk1_%=\n" \
"s_branch Lr2_k1_%=\n" \
"Lr2_sk2_%=:\n" \
"v_max3_u32 v106, v40, v41, v42\n" \
"v_max3_u32 v106, v106, v43, v44\n" \
"v_max3_u32 v106, v106, v45, v46\n" \
"v_max3_u32 v106, v106, v47, v47\n" \
"v_cmp_eq_u32 vcc, -1, v106\n" \
"s_nop 1\n" \
"s_cbranch_vccz Lr2_k2_%=\n" \
"s_sub_u32 s76, s67, 131072\n" \
"s_add_u32 s77, s76, 4096\n" \
"s_sub_u32 s80, s70, 32768\n" \
"s_add_u32 s81, s80, 4096\n" \
"Lr2_rk2_%=:\n" \
"s_sub_u32 s64, s64, 1\n" \
"s_cbranch_scc1 Lr2_dead_%=\n" \
"buffer_load_dwordx4 v[40:43], %[po], %[ws], s76 offen offset:2048 sc1\n" \
"buffer_load_dwordx4 v[44:47], %[po], %[ws], s76 offen offset:3072 sc1\n" \
"buffer_load_dwordx4 v[48:51], %[po], %[ws], s77 offen offset:0 sc1\n" \
"buffer_load_dwordx4 v[52:55], %[po], %[ws], s77 offen offset:1024 sc1\n" \
"buffer_load_dwordx4 v[56:59], %[po], %[ws], s77 offen offset:2048 sc1\n" \
"buffer_load_dwordx4 v[60:63], %[po], %[ws], s77 offen offset:3072 sc1\n" \
"s_waitcnt vmcnt(0)\n" \
"v_max3_u32 v106, v40, v41, v42\n" \
"v_max3_u32 v106, v106, v43, v44\n" \
"v_max3_u32 v106, v106, v45, v46\n" \
"v_max3_u32 v106, v106, v47, v47\n" \
"v_cmp_eq_u32 vcc, -1, v106\n" \
"s_nop 1\n" \
"s_cbranch_vccnz Lr2_rk2_%=\n" \
"s_branch Lr2_k2_%=\n" \
"Lr2_sk3_%=:\n" \
"v_max3_u32 v106, v48, v49, v50\n" \
"v_max3_u32 v106, v106, v51, v52\n" \
"v_max3_u32 v106, v106, v53, v54\n" \
"v_max3_u32 v106, v106, v55, v55\n" \
"v_cmp_eq_u32 vcc, -1, v106\n" \
"s_nop 1\n" \
"s_cbranch_vccz Lr2_k3_%=\n" \
"s_sub_u32 s76, s67, 131072\n" \
"s_add_u32 s77, s76, 4096\n" \
"s_sub_u32 s80, s70, 32768\n" \
"s_add_u32 s81, s80, 4096\n" \
"Lr2_rk3_%=:\n" \
"s_sub_u32 s64, s64, 1\n" \
"s_cbranch_scc1 Lr2_dead_%=\n" \
"buffer_load_dwordx4 v[48:51], %[po], %[ws], s77 offen offset:0 sc1\n" \
"buffer_load_dwordx4 v[52:55], %[po], %[ws], s77 offen offset:1024 sc1\n" \
"buffer_load_dwordx4 v[56:59], %[po], %[ws], s77 offen offset:2048 sc1\n" \
"buffer_load_dwordx4 v[60:63], %[po], %[ws], s77 offen offset:3072 sc1\n" \
"s_waitcnt vmcnt(0)\n" \
"v_max3_u32 v106, v48, v49, v50\n" \
"v_max3_u32 v106, v106, v51, v52\n" \
"v_max3_u32 v106, v106, v53, v54\n" \
"v_max3_u32 v106, v106, v55, v55\n" \
"v_cmp_eq_u32 vcc, -1, v106\n" \
"s_nop 1\n" \
"s_cbranch_vccnz Lr2_rk3_%=\n" \
"s_branch Lr2_k3_%=\n" \
"Lr2_sk4_%=:\n" \
"v_max3_u32 v106, v56, v57, v58\n" \
"v_max3_u32 v106, v106, v59, v60\n" \
"v_max3_u32 v106, v106, v61, v62\n" \
"v_max3_u32 v106, v106, v63, v63\n" \
"v_cmp_eq_u32 vcc, -1, v106\n" \
"s_nop 1\n" \
"s_cbranch_vccz Lr2_k4_%=\n" \
"s_sub_u32 s76, s67, 131072\n" \
"s_add_u32 s77, s76, 4096\n" \
"s_sub_u32 s80, s70, 32768\n" \
"s_add_u32 s81, s80, 4096\n" \
"Lr2_rk4_%=:\n" \
"s_sub_u32 s64, s64, 1\n" \
"s_cbranch_scc1 Lr2_dead_%=\n" \
"buffer_load_dwordx4 v[56:59], %[po], %[ws], s77 offen offset:2048 sc1\n" \
"buffer_load_dwordx4 v[60:63], %[po], %[ws], s77 offen offset:3072 sc1\n" \
"s_waitcnt vmcnt(0)\n" \
"v_max3_u32 v106, v56, v57, v58\n" \
"v_max3_u32 v106, v106, v59, v60\n" \
"v_max3_u32 v106, v106, v61, v62\n" \
"v_max3_u32 v106, v106, v63, v63\n" \
"v_cmp_eq_u32 vcc, -1, v106\n" \
"s_nop 1\n" \
"s_cbranch_vccnz Lr2_rk4_%=\n" \
"s_branch Lr2_k4_%=\n" \
"Lr2_dead_%=:\n" \
"v_mov_b32 v105, 29442\n" \
"buffer_store_dword v105, off, %[ws], 0 sc1\n" \
"s_endpgm\n" \
    :  : [sa] "v"(sa), [sb] "v"(sb), [po] "v"(po), [yo] "v"(yo), [oo] "v"(oo), [fvo] "v"(fvo), [ws] "s"(ws), [src] "s"(src), [dst] "s"(dst), [oy] "s"(oy), [rx] "s"(rx) \
    : CLOB32)

constexpr int L_STAGE = 128 << 10;
constexpr int L_X = 144 << 10;
DI u4 make_sel_add(int lane) {
  const int x = (lane & 15) - 8 * ((lane >> 4) & 1);
  u4 s;
  for (int d = 0; d < 4; ++d) s[d] = (x == 2 * d ? 0x3F80u : 0u) | (x == 2 * d + 1 ? 0x3F800000u : 0u);
  return s;
}
DI int wide_rows(int lane, int ldb) { return (lane >> 4) * ldb + ((lane & 15) ^ ((lane >> 4) << 2)) * 16; }

DI int help256(int h, int w) { return (h & 3) == 1 ? OOB : ((h & ~3) | 1) * 256 + (h & 2) * 64 + (16 * w + 8 * (h & 1)) * 8192; }

DI void role_qk(const Params& p, int w, int h, int d0, int d1) {
  const int lane = lane_id();
  const rsrc_t ws = mkrs(p.ws, WS_END), rq = mkrs(p.Wq, 8 << 20), rk = mkrs(p.Wk, 8 << 20);
  const int wbase = w * 32768, quad = lane >> 4, hi = (lane >> 1) & 1, tok = tok_of(lane);
  const int wl = wbase + lane * 16, str = L_STAGE + lane * 16, stw = L_STAGE + w * 1024 + lane * 16;
  const int mvo = w * 2048 + (lane >> 5) * 1024 + (lane & 24) * 16 + (lane & 1) * 64 + (lane & 6) * 8;
  const int wvo = wide_rows(lane, 4 * 8192), so0 = h * 256 + ((w & 1) + 8 * (w & 2)) * 8192;
  const u4 sel = make_sel_add(lane);
  const unsigned st = quad != (lane >> 2 & 3) ? 0u : lane & 1 ? 0x3F800000u : 0x3F80u;
  const u2 selT = {st, st};
  const int idx = (lane & 2) ? 0xEEEE : 0x4444;
  int fvo = ((lane & 2) ? FV : FK) + fa((8 * h + 2 * w + (lane & 1)) / 3), fo = w ? OOB : FKM + h * 128;
  const int xr = L_X + hi * 4096 + lane * 16, ma = L_X + 8192 + lane * 8 + hi * 4, vmo = L_X + 8192 + w * 512 + lane * 8;
  const int hb = (B_OM + (h >> 2)) * BLK + (h & 3) * 2048, ovm = (B_VM - B_OM) * BLK + hb - (L_X + 8192), okm = __builtin_amdgcn_readfirstlane(WS_KM + h * 2048 + w * 512);
  const int kmo = hi ? OOB : quad * 128 + tok * 16, omo = hi ? OOB : (w >> 1) * 1024 + (lane >> 3) * 128 + (w & 1) * 64 + (lane & 4) * 8 + (lane & 1) * 16;
  const int okx = WS_KX + h * 8192 + w * 1024 - wbase, ovx = WS_VX + h * 8192 + w * 2048 - wbase;
  const int odb = w ? OOB : B_DB * BLK + h * DBS, dvm = WS_VM - WS_KM - (L_X + 8192) - w * 512;
  const int op0 = (w & 2 ? (B_VM - B_OM) * BLK : 0) + hb + (w & 1) * 1024 - wbase, op1 = w == 0 ? (h >> 3) * BLK + (h & 7) * 1024 : w == 1 && h < DBS / 16 ? B_DB * BLK + h * 1024 - wbase : OOB;
  const int hs = HS_R + blockIdx.x * 4, hp = help256(h, w);
  QK_ASM();
}
DI void role_vo(const Params& p, int w, int j, int d0, int d1) {
  const int lane = lane_id();
  const rsrc_t ws = mkrs(p.ws, WS_END), rv = mkrs(p.Wv, 8 << 20), ro = mkrs(p.Wo, 8 << 20);
  const int wbase = w * 32768, g = j >> 4, nb = j & 15, quad = lane >> 4, c = lane & 15;
  const int wl = wbase + lane * 16, str = L_STAGE + lane * 16, stw = L_STAGE + w * 1024 + lane * 16;
  const int mvo = w * 2048 + (lane >> 5) * 1024 + (lane & 24) * 16 + (lane & 1) * 64 + (lane & 6) * 8;
  const int vov = wide_rows(lane, 4 * 8192);
  const int voo = (32 * (quad & 1) + ((lane >> 3) & 1) + 4 * (lane >> 5)) * 2048 + ((lane & 7) ^ ((quad & 1) << 2)) * 16;
  const int sov = j * 256 + ((w & 1) + 8 * (w & 2)) * 8192, soo = (g * 2048 + w * 512) * 2048 + nb * 128;
  const u4 sel = make_sel_add(lane);
  const int idx = (lane & 2) ? 0xEEEE : 0x4444;
  const int xw = L_X + w * 2048 + lane * 8, ol = (lane >> 3) * 128 + (lane & 2) * 32 + (lane & 4) * 8 + ((lane ^ w) & 1) * 16;
  const int vb = (B_VM + (j >> 2)) * BLK + (j & 3) * 2048, l8 = w * 512 + lane * 8, l4 = (nb >> 2) * BLK + (nb & 3) * 2048 + g * 1024 + (w >> 1) * 512 + (c >> 3) * 256 + (quad >> 1) * 128 + (w & 1) * 64 + (quad & 1) * 32 + (c & 7) * 4;
  const int odb = w ? OOB : B_DB * BLK + (32 + j) * DBS;
  const int oom = (B_OM + 4 * g + w) * BLK;
  const int hs = HS_R + blockIdx.x * 4, hp = help256(j, w), ho = nb & 1 ? OOB : (g * 2048 + w * 512 + 8 * (nb & 4) + 4 * (nb & 2)) * 2048 + ((nb & 8) | 3) * 128;
  VO_ASM();
}
#define ROLE_COMMON                                                                                             \
  const int lane = lane_id();                      \
  const rsrc_t ws = mkrs(p.ws, WS_END);                                                                         \
  const int wbase = w * 32768, l16 = lane * 16, q = lane >> 4, c = lane & 15;                                   \
  const int wrow = (4 * (w & 1) + 32 * (w >> 1)) * 8192;                                                        \
  const int dbg = WS_DBG + (blockIdx.x * 4 + w) * 2048; (void)dbg;
template <bool ISV> DI void role_kv(const Params& p, int w, int u) {
  ROLE_COMMON
  const rsrc_t rw = mkrs(ISV ? p.Wv : p.Wk, 8 << 20);
  const int wvo = (8 * (lane >> 5) + ((lane >> 3) & 3)) * 8192 + ((lane & 7) ^ ((q & 1) << 2)) * 16;
  const int so0 = u * 384 + wrow, src = WS_XN + w * 16384, dst = ISV ? WS_VX : WS_KX;
  int tr[3], so[3];
#pragma unroll
  for (int t = 0; t < 3; ++t) {
    const int r = 3 * w + t, T = 12 * u + r;
    tr[t] = (r >> 2) * 2048 + (lane >> 2) * 128 + (((r >> 1) ^ (lane >> 3)) & 1) * 64 + (ISV ? (lane & 3) * 16 + (r & 1) * 8 : (r & 1) * 32 + (lane & 3) * 8);
    so[t] = T >= 256 ? OOB : ISV ? T * 1024 + l16 : (T >> 3) * 8192 + (q & 1) * 4096 + ((T >> 1) & 3) * 1024 + ((2 * (T & 1) + (q >> 1)) * 16 + c) * 16;
  }
  const int tr0 = tr[0], tr1 = tr[1], tr2 = tr[2], so1 = so[0], so2 = so[1], so3 = so[2];
  int fo = lane == 0 ? (ISV ? FV : FK) + fa(u * 4 + w) : OOB;
  const int hs = HS_R + blockIdx.x * 4, om = ISV ? WS_VM : WS_KM, pt = (12 * u + 3 * w) * 256 + l16, po = q < 3 && pt < 65536 ? pt : OOB;
  const int at = 3 * u + 4, hw = 4 * (u - (at >> 3)) + w, hv = (at & 7) > 4 || lane > 3 ? OOB : (hw % 7 * 9 + lane) * 8192 + (hw / 7 * 8 + 3) * 128;
  if constexpr (ISV) V_ASM(); else K_ASM();
}
DI void role_qa(const Params& p, int w, int h, int d0, int cx) {
  ROLE_COMMON
  const rsrc_t rw = mkrs(p.Wq, 8 << 20);
  const int rt = w >> 1, eh = w & 1, wvo = wide_rows(lane, 8192), so0 = h * 256 + wrow, tr0 = wbase + l16, src = WS_XN + w * 16384;
  const int dst = WS_OX + rt * 131072 + (4 * h + 2 * eh) * 1024, lo = (c >> 2) * 256 + q * 64 + (c & 3) * 16, fo = lane == 0 ? (FQA ^ cx) + fa(h * 4 + w) : OOB;
  const int dd = 16 * rt + c - 4 * q, fkv = (((lane & 8) ? FV : FK) ^ cx) + fa((8 * h + (lane & 7)) / 3), fm = (FKM ^ cx) + h * 128 + (lane & 0);
  const int qw = (128 << 10) + w * 1024 + l16, qr = (128 << 10) + rt * 4096 + l16, kr = (136 << 10) + l16, vr = (144 << 10) + eh * 4096 + l16;
  const int kmo = (c & 2) ? OOB : q * 128 + tok_of(c) * 16, vmo = lane * 8;
  const int okx = WS_KX + h * 8192 + w * 2048, dvx = WS_VX - WS_KX, okm = WS_KM + h * 2048, ovm = WS_VM + h * 2048 + eh * 1024, stm = 65536;
  QA_ASM();
}
#define E3_COMMON(LDB, PART)                                                                                    \
  const int lane = lane_id();                                                                                   \
  const rsrc_t ws = mkrs(p.ws, WS_END);                                                                         \
  const int wbase = w * 32768, q = lane >> 4, c = lane & 15, lo = (c >> 2) * 256 + q * 64 + (c & 3) * 16, xr = w * 8192 + lane * 16;        \
  const int wvo = wide_rows(lane, LDB);                                                                         \
  const int tr0 = wbase + q * 2048 + (c >> 2) * 256 + ((((PART) ? w & 2 : w) ^ (c >> 2)) * 64) + (c & 3) * 16 + ((PART) ? (w & 1) * 8 : 0); \
  const int sw = c * 32 + (lane >> 5) * 16 + w * 4, sr = c * 32, s1 = (c >> 2) * 128 + q * 32 + (c & 3) * 8;    \
  const int dbg = WS_DBG + (blockIdx.x * 4 + w) * 2048; (void)dbg;
DI void role_wo(const Params& p, int w, int u, int d0, int cx) {
  E3_COMMON(2048, 1)
  const rsrc_t rw = mkrs(p.Wo, 8 << 20);
  const int g = u & 3, nb = u >> 2, so0 = (g * 1024 + w * 256) * 2048 + nb * 256, src = WS_OX + (32 * g + 8 * w) * 1024, dst = WS_P1 + g * 32768 + (4 * nb + w) * 1024, so = lo;
  const int fvo = (FQA ^ cx) + fa(32 * g + 8 * w + (lane & 7)), fo = lane == 0 ? (FP1 ^ cx) + fa(u * 4 + w) : OOB;
  WO_ASM();
}
template <int ROT> DI void role_w1(const Params& p, int w, int nb, int d0, int cx) {
  E3_COMMON(2048, 0)
  const rsrc_t rw = mkrs(p.w1, 2 << 20);
  const int so0 = w * 256 * 2048 + nb * 256, src = WS_H + w * 8192, dst = WS_G - (ROT > 8 ? 32768 : 0) + (4 * nb + w) * 1024, ox0 = WS_S1 + w * 512, sl = s1, so = lo;
  const int fvo = (FH ^ cx) + fa(4 * (lane & 3) + w), fo = lane == 0 ? (FG ^ cx) + fa(nb * 4 + w) : OOB;
  W1_ASM();
}
DI void role_w2(const Params& p, int w, int nb, int d0, int cx) {
  E3_COMMON(2048, 0)
  const rsrc_t rw = mkrs(p.w2, 2 << 20);
  const int so0 = w * 256 * 2048 + nb * 256, src = WS_G + w * 8192, dst = WS_X1 + (4 * nb + w) * 1024, so = lo, yo = lo + (WS_Y - WS_X1), sl = s1;
  const int ox0 = WS_S1 + w * 512, ox1 = WS_H + (4 * nb + w) * 1024, ox2 = WS_S2 + (4 * nb + w) * 128, sto = q & 1 ? OOB : c * 8 + q * 2;
  const int fvo = (FG ^ cx) + fa(8 * w + (lane & 7)), fo = lane == 0 ? (FX1 ^ cx) + fa(nb * 4 + w) : OOB;
  W2_ASM();
}
DI void role_w3(const Params& p, int w, int u, int d0, int cx) {
  E3_COMMON(7168, 0)
  const rsrc_t rw = mkrs(p.w3, 7 << 20);
  const int so0 = w * 256 * 7168 + u * 256, src = WS_X1 + w * 8192, dst = WS_U + (4 * u + w) * 1024, ox0 = WS_S2 + w * 1024, sl = lane * 8, so = lo;
  const int fvo = (FX1 ^ cx) + fa(8 * w + (lane & 7)), fo = lane == 0 ? (FU ^ cx) + fa(u * 4 + w) : OOB;
  W3_ASM();
}
DI void role_w4(const Params& p, int w, int u, int d0, int cx) {
  E3_COMMON(2048, 1)
  const rsrc_t rw = mkrs(p.w4, 7 << 20);
  const int g = u & 3, nb = u >> 2, so0 = (g * 896 + w * 224) * 2048 + nb * 256, src = WS_U + (28 * g + 7 * w) * 1024, dst = WS_P2 + g * 32768 + (4 * nb + w) * 1024, so = lo;
  const int fvo = (FU ^ cx) + fa(28 * g + 7 * w + min(lane & 7, 6)), fo = lane == 0 ? (FP2 ^ cx) + fa(u * 4 + w) : OOB;
  W4_ASM();
}
DI u4 sel_at(int x) { u4 s; for (int d = 0; d < 4; ++d) s[d] = (x == 2 * d ? 0x3F80u : 0u) | (x == 2 * d + 1 ? 0x3F800000u : 0u); return s; }
#define SUM_COMMON                                                                                              \
  const int lane = lane_id();                                                                                   \
  const rsrc_t ws = mkrs(p.ws, WS_END);                                                                         \
  const int q = lane >> 4, c = lane & 15, po = q * 32768 + u * 256 + c * 16, fr = q * 1024 + u * 256 + c * 16;  \
  const int rm = (4 * u + (c & 3)) * 2048 + w * 512 + q * 64 + (c >> 2) * 16;                                   \
  const u4 sa = sel_at(c), sb = sel_at(c - 8);                                                                  \
  const int dbg = WS_DBG + (blockIdx.x * 4 + w) * 2048; (void)dbg;
DI void role_r1(const Params& p, int w, int u, int cx) {
  SUM_COMMON
  const rsrc_t rx = mkrs(p.x, 4 << 20);
  const int fvo = (FP1 ^ cx) + fa(32 * w + (lane & 31)), fo = lane == 0 ? (FH ^ cx) + fa(u * 4 + w) : OOB, yo = rm, oo = fr, sto = c < 4 ? u * 128 + q * 32 + c * 8 : OOB;
  const int src = WS_P1 + w * 8192, dst = WS_H + w * 8192, oy = 0, os = WS_S1 + w * 512;
  R1_ASM();
}
DI void role_r2(const Params& p, int w, int u, int cx) {
  SUM_COMMON
  const rsrc_t rx = mkrs(p.out, 4 << 20);
  const int fvo = (FP2 ^ cx) + fa(32 * w + (lane & 31)), yo = fr, oo = rm;
  const int src = WS_P2 + w * 8192, dst = 0, oy = WS_Y + w * 8192;
  R2_ASM();
}
DI void set_up(const Params& p, int w, int n) {
  const int lane = lane_id(), i = n >> 1, q = lane >> 4, ks = 8 * w + ((lane >> 3) & 1), r0 = 16 * (n & 1) + (lane & 7);
  const rsrc_t ws = mkrs(p.ws, WS_END), rx = mkrs(p.x, 4 << 20);
  u4 xv[2][4];
#pragma unroll
  for (int j = 0; j < 8; ++j) xv[j >> 2][j & 3] = ld16p(rx, (r0 + (j >> 2) * 8) * 2048 + ks * 64 + q * 16 + (j & 3) * 128, i * 65536);
  const u4 z = {0, 0, 0, 0}, po = {SENT, SENT, SENT, SENT};
  if (w == 3 && lane < 16) {
    const int m = n * 16 + lane, k = m >> 7, g = (m >> 6) & 1, tok = (m & 2) * 2 + (m >> 2 & 3);
    u4 o = z;
    if (!g)
#pragma unroll
      for (int x = 0; x < 8; ++x) o[x >> 1] |= (unsigned)p.mem0[tok * 1024 + k * 64 + (m & 48) + 8 * (m & 1) + 4 * (x & 1) + (x >> 1)] << (16 * (x & 1));
    st16(ws, (m >> 9) * BLK + (m & 511) * 16, M_FIRST, o);
  }
  const int c = (w * 128 + n) * 1024 + lane * 16;
  if (c < B_END * BLK)
#pragma unroll
    for (int s = 0; s < 2; ++s) st16(ws, c, MB0 + s * SLOT, po);
  if (c >= FL0 - MB0 && c < FL0 - MB0 + FL_SIZE) st16(ws, c, MB0, z);
#pragma unroll
  for (int u = 0; u < 2; ++u) {
    float h[32];
#pragma unroll
    for (int j = 0; j < 4; ++j)
#pragma unroll
      for (int k = 0; k < 4; ++k) { h[8 * j + 2 * k] = blo(xv[u][j][k]); h[8 * j + 2 * k + 1] = bhi(xv[u][j][k]); }
    float mean, var;
    row_stats(h, (LAS f2*)(1024 + u * 256), lane, w, mean, var);
    const float r1 = rsqrtf(var + 1e-5f);
#pragma unroll
    for (int j = 0; j < 4; ++j) {
      u4 a;
#pragma unroll
      for (int k = 0; k < 4; ++k) a[k] = cvtpk((h[8 * j + 2 * k] - mean) * r1, (h[8 * j + 2 * k + 1] - mean) * r1);
      st16(ws, ks * 2048 + q * 256 + (n & 1) * 1024 + ((lane & 7) + 8 * u) * 16, WS_XN + i * 65536 + j * 4096, a);
    }
  }
}
DI int share_of(int b) { const int x = b & 7, i = b >> 3; return x < 2 ? -1 : !(x & 2) ? (i < 22 ? -1 : 10 * x + i - 62) : i < 27 ? 20 + 27 * ((x >> 1 & 2) + (x & 1)) + i : -1; }
DI void pull(const Params& p, int w) {
  const rsrc_t rw = mkrs(blockIdx.x & 1 ? p.Wv : p.Wk, 8 << 20);
  const int lane = lane_id(), o = (64 * (16 - HELP) + 8 * ((blockIdx.x >> 3) - 22) + (lane >> 3)) * 8192 + ((8 * (lane & 7) + 3) / 3 * 3 + w - 1) * 128;
  unsigned a = 0;
#pragma unroll
  for (int n = 0; n < (HELP * 8 + 9) / 10; ++n) a |= __builtin_amdgcn_raw_buffer_load_b32(rw, o + n * 80 * 8192, 0, 0);
  asm volatile("" :: "v"(a));
}
DI void pull2(const Params& p, int w) {
  const bool o = blockIdx.x & 1;
  const rsrc_t rw = mkrs(o ? p.Wo : p.Wq, 8 << 20);
  const int t0 = (blockIdx.x >> 2) * 3 + w - 1 + (o ? 0 : 64 * PULL);
  unsigned a = 0;
#pragma unroll
  for (int n = 0; n < 6; ++n) {
    const int t = t0 + 192 * n, r = o ? (t & 15) + (0x61754320 >> (t >> 5 & 28) & 7) * 16 + (t & 112) * 8 : (t & 63) + (int)(0xd80fecba97654321ull >> (t >> 4 & 60) & 15) * 64;
    a |= __builtin_amdgcn_raw_buffer_load_b32(rw, t < 1024 ? lane_id() * 128 : OOB, r * 8192, 0);
  }
  asm volatile("" :: "v"(a));
}
DI int lead(const Params& p, int w) {
  const int lane = lane_id();
  const rsrc_t ws = mkrs(p.ws, WS_END);
  volatile LAS unsigned* bc = (volatile LAS unsigned*)1024;
  if (w == 0 && lane < 4) bc[lane] = lane < 2 ? 0u : ~0u;
  lds_barrier();
  if (w) {
#pragma unroll 1
    for (int n = w; n < 1 << 18 && !bc[0]; ++n) {
      const unsigned t0 = (unsigned)__builtin_amdgcn_s_memtime();
      st4(ws, lane ? OOB : ((n >> 1) * 5 + w * 11 & 31) * 256, MB0 + 30 * BLK + (n & 1) * 8192, 0);
      drain();
      const unsigned d = (unsigned)__builtin_amdgcn_s_memtime() - t0;
      if (!lane) { __hip_atomic_fetch_min((LAS unsigned*)bc + 2 + (n & 1), d, __ATOMIC_RELAXED, __HIP_MEMORY_SCOPE_WORKGROUP); __hip_atomic_fetch_add((LAS unsigned*)bc + 1, 1u, __ATOMIC_RELAXED, __HIP_MEMORY_SCOPE_WORKGROUP); }
    }
  } else {
    st16(ws, lane * 16, HS_D, u4{0, 0, lane == 63 ? 2u : 0u, 0});
    drain();
    unsigned r = 0;
#pragma unroll 1
    for (int n = 0; n < 1 << 18; ++n) {
      const u4 d = ld16(ws, lane * 16, HS_D);
      bool miss = false, open = false;
#pragma unroll
      for (int k = 0; k < 4; ++k) { miss |= d[k] == 0 && share_of(4 * lane + k) >= 0; open |= d[k] != 2; }
      fence_c();
      if (__builtin_amdgcn_ballot_w64(open) == 0) break;
      if (r) __builtin_amdgcn_s_sleep(8);
      else if (__builtin_amdgcn_ballot_w64(miss) == 0 && bc[1] >= 24) bc[0] = r = bc[2] < bc[3] ? 8193 : 1;
      if (r) st16(ws, lane * 16, HS_R, u4{r, r, r, r});
    }
  }
  lds_barrier();
  return __builtin_amdgcn_readfirstlane(bc[0]) & 8192;
}
DI int follow(const Params& p, int w, int n) {
  const int lane = lane_id(), o = lane ? OOB : blockIdx.x * 4;
  const rsrc_t ws = mkrs(p.ws, WS_END);
  LAS int* bc = (LAS int*)2048;
  if (n >= 0) set_up(p, w, n);
  drain(); lds_barrier();
  if (w == 0) {
    unsigned r = 0;
#pragma unroll 1
    for (int k = 0; k < 1 << 18 && !r; ++k) {
      if (n >= 0 && !(k & 3)) st4(ws, o, HS_D, 1);
      __builtin_amdgcn_s_sleep(8);
      fence_c();
      r = __builtin_amdgcn_readfirstlane(ld4(ws, o, HS_R));
      drain();
    }
    st4(ws, o, HS_D, 2);
    *bc = r;
  } else {
    __builtin_amdgcn_s_sleep(4);
    if (HELP && !(blockIdx.x & 2)) pull(p, w);
    if (PULL && (blockIdx.x & 2)) pull2(p, w);
  }
  lds_barrier();
  const int r = __builtin_amdgcn_readfirstlane(*bc);
  lds_barrier();
  return r & 8192;
}
struct Tune { int dq, dm, dov, dl; };
__global__ __launch_bounds__(256, 1) void k_main(Params p, Tune tn) {
  const int x = blockIdx.x & 7, i = blockIdx.x >> 3, w = __builtin_amdgcn_readfirstlane(threadIdx.x >> 6);
#define RUN(call) { call; __builtin_unreachable(); }
  p.ws = (unsigned char*)(((size_t)p.ws + MB0 - 1) & ~(size_t)(MB0 - 1));
  st4(mkrs(p.ws, WS_END), threadIdx.x ? OOB : blockIdx.x * 4, HS_R, 0);
  fence_c();
  if (x < 2 && i < 16) RUN(role_qk(p, w, 16 * x + i, tn.dq, tn.dq - DKV))
  if (x < 2) RUN(role_vo(p, w, 16 * (x ^ VFLIP) + i - 16, tn.dm, tn.dov))
  if (x == 4 && i < 22) RUN(role_kv<false>(p, w, i))
  if (x == 5 && i < 22) RUN(role_kv<true>(p, w, i))
  const int cx = blockIdx.x == 254 ? lead(p, w) : follow(p, w, share_of(blockIdx.x));
  const int d0 = tn.dl & 255, d1 = tn.dl >> 8 & 255, d2 = tn.dl >> 16 & 255;
  if (x == 2) RUN(role_qa(p, w, i, d1, cx))
  if (x == 3) RUN(role_wo(p, w, i, d2, cx))
  if (x == 5 && i < 30) RUN(role_w2(p, w, i - 22, d0, cx))
  if (x == 4 && i < 24) RUN(role_w1<1>(p, w, i - 22, d0, cx))
  if (x == 4 && i < 26) RUN(role_w1<15>(p, w, i - 22, d0, cx))
  if (x == 4 && i < 28) RUN(role_w1<2>(p, w, i - 22, d0, cx))
  if (x == 4 && i < 30) RUN(role_w1<14>(p, w, i - 22, d0, cx))
  if (x == 6 && i < 28) RUN(role_w3(p, w, i, d0, cx))
  if (x == 7) RUN(role_w4(p, w, i, d0, cx))
  if (x == 6) RUN(role_r1(p, w, i - 28, cx))
  role_r2(p, w, 2 * x + i - 38, cx);
}
static void launch_all(void* const* d_in, void* d_out, void* d_ws, hipStream_t stream, Tune tn) {
  Params p{(const u16*)d_in[0], (const u16*)d_in[1], (const u16*)d_in[2], (const u16*)d_in[3], (const u16*)d_in[4], (const u16*)d_in[5],
           (const u16*)d_in[8], (const u16*)d_in[9], (const u16*)d_in[10], (const u16*)d_in[11], (u16*)d_out, (unsigned char*)d_ws};
  (void)hipFuncSetAttribute((const void*)k_main, hipFuncAttributeMaxDynamicSharedMemorySize, 160 << 10);
  k_main<<<256, 256, 160 << 10, stream>>>(p, tn);
}
extern "C" void kernel_launch(void* const* d_in, const int* in_sizes, int n_in, void* d_out, int out_size, void* d_ws, size_t ws_size, hipStream_t stream) {
  launch_all(d_in, d_out, d_ws, stream, Tune{3330, 3280, 1500, 0x300000});
}
```
